```python
import torch

def _softmax(x, axis=-1):
    return torch.softmax(x, dim=axis)

def _gelu(x):
    return torch.nn.functional.gelu(x, approximate='tanh')

def _silu(x):
    return torch.nn.functional.silu(x)

def _sigmoid(x):
    return torch.sigmoid(x)

def _layernorm(x, axis=-1, eps=1e-05):
    m = x.mean(dim=axis, keepdim=True)
    v = x.var(dim=axis, keepdim=True, unbiased=False)
    return (x - m) / torch.sqrt(v + eps)

def _scatter_add(tgt, idx, src, axis=0):
    return tgt.clone().index_add_(axis, idx, src)

def _segment_sum(src, seg, n):
    out = torch.zeros((n,) + src.shape[1:], dtype=src.dtype)
    return out.index_add_(0, seg, src)

def _pad(x, pw):
    flat = tuple((v for pair in reversed(pw) for v in pair))
    return torch.nn.functional.pad(x, flat)
kernel_name = 'parallel_sum__ssm_conv_s4d__mixer_gmlp__prompt'

def setup_inputs():

    _w = lambda a: (lambda t: t.to(torch.bfloat16) if t.is_floating_point() else t)(torch.as_tensor(a))

    def _A_setup():
        Bsz, L, D, N = (1, 2048, 640, 128)
        return {'u': _w((lambda *s: torch.randn(*s, dtype=torch.bfloat16) * 0.05)(Bsz, L, D)), 'log_A_real': _w((lambda *s: torch.randn(*s, dtype=torch.bfloat16) * 3.2)(D, N) * 0.5), 'A_imag': _w((lambda *s: torch.randn(*s, dtype=torch.bfloat16) * 0.1)(D, N) * 3.14), 'C_real': _w((lambda *s: torch.randn(*s, dtype=torch.bfloat16) * 3.2)(D, N) / N ** 0.5), 'C_imag': _w((lambda *s: torch.randn(*s, dtype=torch.bfloat16) * 3.2)(D, N) / N ** 0.5), 'log_dt': _w((lambda *s: torch.randn(*s, dtype=torch.bfloat16) * 0.1)(D) * 0.5 - 4.0), 'D_skip': _w((lambda *s: torch.randn(*s, dtype=torch.bfloat16) * 0.4)(D))}
    _A_in = _A_setup()

    def _B_setup():
        B, S, D, F = (1, 2048, 640, 1728)
        Fh = F // 2
        return {'x': _w((lambda *s: torch.randn(*s, dtype=torch.bfloat16) * 0.05)(B, S, D)), 'w_in': _w((lambda *s: torch.randn(*s, dtype=torch.bfloat16) * 0.2)(D, 2 * Fh)), 'w_sp': _w(0.01 * (lambda *s: torch.randn(*s, dtype=torch.bfloat16) * 0.2)(S, S)), 'b_sp': _w(1.0 + 0.01 * (lambda *s: torch.randn(*s, dtype=torch.bfloat16) * 0.05)(S)), 'w_out': _w((lambda *s: torch.randn(*s, dtype=torch.bfloat16) * 0.05)(Fh, D))}
    _B_in = _B_setup()
    return {'A_u': _A_in['u'], 'A_log_A_real': _A_in['log_A_real'], 'A_A_imag': _A_in['A_imag'], 'A_C_real': _A_in['C_real'], 'A_C_imag': _A_in['C_imag'], 'A_log_dt': _A_in['log_dt'], 'A_D_skip': _A_in['D_skip'], 'B_x': _B_in['x'], 'B_w_in': _B_in['w_in'], 'B_w_sp': _B_in['w_sp'], 'B_b_sp': _B_in['b_sp'], 'B_w_out': _B_in['w_out']}

def reference(A_u, A_log_A_real, A_A_imag, A_C_real, A_C_imag, A_log_dt, A_D_skip, B_x, B_w_in, B_w_sp, B_b_sp, B_w_out):

    def _A_ref(u, log_A_real, A_imag, C_real, C_imag, log_dt, D_skip):
        L = u.shape[1]
        dt = torch.exp(log_dt)
        A = -torch.exp(log_A_real) + 1j * A_imag
        C = (C_real + 1j * C_imag) * dt[:, None]
        pos = (lambda x, dt: x.to(dt))(torch.arange(L), log_dt.dtype)
        P = torch.exp((A * dt[:, None])[:, :, None] * pos[None, None, :])
        K = 2.0 * torch.einsum('dn,dnl->dl', C, P).real
        L2 = 2 * L
        u_t = (lambda x, *d: x.permute(*d))(u, 0, 2, 1)
        Kf = (lambda x, *a, **k: torch.fft.rfft(x.float(), *a, **k))(K, L2)
        Uf = (lambda x, *a, **k: torch.fft.rfft(x.float(), *a, **k))(u_t, L2)
        y = (lambda x, *a, **k: torch.fft.irfft(x, *a, **k).to(torch.bfloat16))(Uf * Kf[None, :, :], L2)[:, :, :L]
        y = (lambda x, *d: x.permute(*d))(y, 0, 2, 1)
        return y + u * D_skip[None, None, :]

    def _B_ref(x, w_in, w_sp, b_sp, w_out):
        B, S, D = x.shape
        F = w_in.shape[-1]
        Fh = F // 2
        z = _gelu(torch.einsum('bsd,df->bsf', x, w_in))
        z1 = z[..., :Fh].clone()
        z2 = _layernorm(z[..., Fh:], axis=-1)
        g = torch.einsum('ts,bsf->btf', w_sp, z2) + b_sp[None, :, None]
        return torch.einsum('bsf,fd->bsd', z1 * g, w_out)
    x = A_u
    xn = _layernorm(x, axis=-1)
    return x + _A_ref(xn, A_log_A_real, A_A_imag, A_C_real, A_C_imag, A_log_dt, A_D_skip) + _B_ref(xn, B_w_in, B_w_sp, B_b_sp, B_w_out)
```

```cpp
#include <hip/hip_runtime.h>
#include <hip/hip_bf16.h>
typedef unsigned short u16;
#define L_ 2048
#define D_ 640
#define N_ 128
#define F_ 1728
#define FH 864
__device__ __forceinline__ float bf2f(u16 v){ return __uint_as_float(((unsigned)v)<<16); }
__device__ __forceinline__ u16 f2bf(float f){ __hip_bfloat16 b = __float2bfloat16(f); return *(u16*)&b; }
__device__ __forceinline__ float rbf(float f){ return bf2f(f2bf(f)); }

__global__ void ln_rows(const u16* x, int S, int W, u16* out, int So){
  int row = blockIdx.x; int lane = threadIdx.x;
  const u16* p = x + (size_t)row*S;
  float s=0; for(int i=lane;i<W;i+=64) s+=bf2f(p[i]);
  for(int o=32;o;o>>=1) s+=__shfl_xor(s,o);
  float mean=s/W;
  float q=0; for(int i=lane;i<W;i+=64){float d=bf2f(p[i])-mean; q+=d*d;}
  for(int o=32;o;o>>=1) q+=__shfl_xor(q,o);
  float m=rbf(mean), v=rbf(q/W);
  float sd=rbf(sqrtf(rbf(v+1e-5f)));
  for(int i=lane;i<W;i+=64) out[(size_t)row*So+i]=f2bf(rbf(bf2f(p[i])-m)/sd);
}
__global__ void gemm_naive(const u16* A,int lda,const u16* B,int ldb,float* C,int M,int N,int K){
  __shared__ float As[16][17], Bs[16][17];
  int tx=threadIdx.x, ty=threadIdx.y; int r=blockIdx.y*16+ty, c=blockIdx.x*16+tx; float acc=0;
  for(int k0=0;k0<K;k0+=16){
    As[ty][tx]=bf2f(A[(size_t)r*lda+k0+tx]); Bs[ty][tx]=bf2f(B[(size_t)(k0+ty)*ldb+c]);
    __syncthreads();
    for(int k=0;k<16;k++) acc+=As[ty][k]*Bs[k][tx];
    __syncthreads();
  }
  C[(size_t)r*N+c]=acc;
}
__global__ void gelu_k(const float* c,u16* z,int n){int i=blockIdx.x*256+threadIdx.x; if(i<n){float x=rbf(c[i]); float t=tanhf(0.7978845608028654f*(x+0.044715f*x*x*x)); z[i]=f2bf(0.5f*x*(1.f+t));}}
__global__ void gate_k(const float* c,const u16* z,const u16* bsp,u16* h){int i=blockIdx.x*256+threadIdx.x; if(i<L_*FH){int t=i/FH,f=i%FH; float g=rbf(rbf(c[i])+bf2f(bsp[t])); h[i]=f2bf(bf2f(z[(size_t)t*F_+f])*g);}}
__global__ void kgen(const u16* lar,const u16* aim,const u16* cr,const u16* ci,const u16* ldt,float* Kk){
  int d=blockIdx.y; int l=blockIdx.x*256+threadIdx.x;
  float dt=rbf(expf(bf2f(ldt[d]))); float pos=rbf((float)l); float acc=0;
  for(int n=0;n<N_;n++){ float ar=-rbf(expf(bf2f(lar[d*N_+n])))*dt, ai=bf2f(aim[d*N_+n])*dt; float c_r=bf2f(cr[d*N_+n])*dt,c_i=bf2f(ci[d*N_+n])*dt;
    float e=expf(ar*pos); float sn,cs; sincosf(ai*pos,&sn,&cs); acc+=c_r*(e*cs)-c_i*(e*sn);}
  Kk[(size_t)d*L_+l]=2.f*acc;
}
__global__ void conv_final(const float* Kk,const u16* xn,const u16* x,const u16* dsk,const float* ob,u16* out){
  int d=blockIdx.x*64+threadIdx.x; int t=blockIdx.y; float acc=0;
  for(int j=0;j<=t;j++) acc+=Kk[(size_t)d*L_+j]*bf2f(xn[(size_t)(t-j)*D_+d]);
  float y=rbf(acc); float u=bf2f(xn[(size_t)t*D_+d]); float a=rbf(y+rbf(u*bf2f(dsk[d])));
  float r=rbf(bf2f(x[(size_t)t*D_+d])+a); out[(size_t)t*D_+d]=f2bf(r+rbf(ob[(size_t)t*D_+d]));
}
extern "C" void kernel_launch(void* const* d_in,const int* in_sizes,int n_in,void* d_out,int out_size,void* d_ws,size_t ws_size,hipStream_t stream){
  const u16* x=(const u16*)d_in[0]; const u16* lar=(const u16*)d_in[1]; const u16* aim=(const u16*)d_in[2]; const u16* cr=(const u16*)d_in[3]; const u16* ci=(const u16*)d_in[4];
  const u16* ldt=(const u16*)d_in[5]; const u16* dsk=(const u16*)d_in[6]; const u16* win=(const u16*)d_in[8]; const u16* wsp=(const u16*)d_in[9]; const u16* bsp=(const u16*)d_in[10]; const u16* wout=(const u16*)d_in[11];
  char* ws=(char*)d_ws; u16* xn=(u16*)ws; ws+=L_*D_*2; float* c=(float*)ws; ws+=(size_t)L_*F_*4; u16* z=(u16*)ws; ws+=L_*F_*2; u16* z2n=(u16*)ws; ws+=L_*FH*2; u16* h=(u16*)ws; ws+=L_*FH*2; float* Kk=(float*)ws; ws+=(size_t)D_*L_*4;
  ln_rows<<<L_,64,0,stream>>>(x,D_,D_,xn,D_);
  gemm_naive<<<dim3(F_/16,L_/16),dim3(16,16),0,stream>>>(xn,D_,win,F_,c,L_,F_,D_);
  gelu_k<<<(L_*F_+255)/256,256,0,stream>>>(c,z,L_*F_);
  ln_rows<<<L_,64,0,stream>>>(z+FH,F_,FH,z2n,FH);
  gemm_naive<<<dim3(FH/16,L_/16),dim3(16,16),0,stream>>>(wsp,L_,z2n,FH,c,L_,FH,L_);
  gate_k<<<(L_*FH+255)/256,256,0,stream>>>(c,z,bsp,h);
  gemm_naive<<<dim3(D_/16,L_/16),dim3(16,16),0,stream>>>(h,FH,wout,D_,c,L_,D_,FH);
  kgen<<<dim3(L_/256,D_),256,0,stream>>>(lar,aim,cr,ci,ldt,Kk);
  conv_final<<<dim3(D_/64,L_),64,0,stream>>>(Kk,xn,x,dsk,c,(u16*)d_out);
}
```

```cpp
#include <hip/hip_runtime.h>
#include <utility>
#pragma clang diagnostic ignored "-Wint-to-pointer-cast"
typedef unsigned short u16;
typedef unsigned int u32;
typedef unsigned long long u64;
typedef __attribute__((ext_vector_type(8))) __bf16 bf8;
typedef __attribute__((ext_vector_type(8))) _Float16 hf8;
typedef __attribute__((ext_vector_type(2))) __bf16 bf2;
typedef __attribute__((ext_vector_type(2))) _Float16 hf2;
typedef __attribute__((ext_vector_type(2))) float f2;
typedef __attribute__((ext_vector_type(4))) float f4;
typedef __attribute__((ext_vector_type(16))) float f16v;
typedef __attribute__((ext_vector_type(4))) u32 u4;
typedef __attribute__((ext_vector_type(2))) u32 u2;
typedef __attribute__((ext_vector_type(4))) short s4;
#define LAS __attribute__((address_space(3)))
#define GAS __attribute__((address_space(1)))
#define DI __device__ __forceinline__

constexpr u32 OFF_Z2F = 0;
constexpr u32 OFF_Z1T = OFF_Z2F + 64*54*1024;
constexpr u32 OFF_XNT = OFF_Z1T + 128*54*512;
constexpr u32 KXP     = 4224;
constexpr u32 OFF_KX  = OFF_XNT + 640*4096;
constexpr u32 OFF_KSC = OFF_KX + 640*KXP;
constexpr u32 OFF_WOF = OFF_KSC + 4096;
constexpr u32 OFF_HF  = OFF_WOF + 8*27*5*1024;
constexpr u32 OFF_AT  = OFF_HF + 128*27*1024;
constexpr u32 OFF_ST  = OFF_AT + 640*4096;
constexpr u32 OFF_EP  = OFF_ST + 64*14*512;
constexpr u32 OFF_CNT = OFF_EP + 64*256;
constexpr u32 WS_END  = OFF_CNT + 40*256;

struct Params {
  const u16 *x, *lar, *aim, *cre, *cim, *ldt, *dsk, *win, *wsp, *bsp, *wout;
  u16* out; char* ws;
};

#define STAMP(id) do{}while(0)

template<class F,int... I> DI void sfor_impl(F&& f, std::integer_sequence<int,I...>){ (f(std::integral_constant<int,I>{}),...); }
template<int N,class F> DI void sfor(F&& f){ sfor_impl(f, std::make_integer_sequence<int,N>{}); }

DI float bflo(u32 v){ return __uint_as_float(v<<16); }
DI float bfhi(u32 v){ return __uint_as_float(v&0xffff0000u); }
DI float bf1(u16 v){ return __uint_as_float(((u32)v)<<16); }
DI u32 pkbf(float a,float b){ f2 v={a,b}; return __builtin_bit_cast(u32,__builtin_convertvector(v,bf2)); }
DI u32 pkhf(float a,float b){ f2 v={a,b}; return __builtin_bit_cast(u32,__builtin_convertvector(v,hf2)); }
DI float rbf(float a){ return bflo(pkbf(a,a)); }
DI float hlo(u32 v){ return (float)__builtin_bit_cast(hf2,v)[0]; }
DI float hhi(u32 v){ return (float)__builtin_bit_cast(hf2,v)[1]; }
DI float dot2(u32 a,u32 b,float c){ return __builtin_amdgcn_fdot2_f32_bf16(__builtin_bit_cast(bf2,a),__builtin_bit_cast(bf2,b),c,false); }
DI f4 mfma16(u4 a,u4 b,f4 c){ return __builtin_amdgcn_mfma_f32_16x16x32_bf16(__builtin_bit_cast(bf8,a),__builtin_bit_cast(bf8,b),c,0,0,0); }
DI f16v mfma32h(u4 a,u4 b,f16v c){ return __builtin_amdgcn_mfma_f32_32x32x16_f16(__builtin_bit_cast(hf8,a),__builtin_bit_cast(hf8,b),c,0,0,0); }
DI u4 ldsr128(u32 a){ return *(LAS u4*)a; }
DI u2 ldsr64(u32 a){ return *(LAS u2*)a; }
DI void ldsw128(u32 a,u4 v){ *(LAS u4*)a=v; }
DI u2 ldstr(u32 a){ return __builtin_bit_cast(u2,__builtin_amdgcn_ds_read_tr16_b64_v4i16((LAS s4*)a)); }
DI u32 uni(u32 v){ return __builtin_amdgcn_readfirstlane(v); }
DI void glds(const char* sbase,u32 voff,u32 lds){
  u32 keep;
  asm volatile("s_mov_b32 %0, m0\n\ts_mov_b32 m0, %3\n\ts_nop 0\n\tglobal_load_lds_dwordx4 %1, %2\n\ts_mov_b32 m0, %0"
    : "=&s"(keep) : "v"(voff), "s"(sbase), "s"(lds) : "memory");
}
template<int OFF> DI void gload(u4& dst,u32 voff,const char* base){ asm volatile("global_load_dwordx4 %0, %1, %2 offset:%3" : "=v"(dst) : "v"(voff), "s"(base), "n"(OFF)); }
template<int CNT> DI void waitB(u4 (&B)[7]){ asm volatile("s_waitcnt vmcnt(%7)" : "+v"(B[0]),"+v"(B[1]),"+v"(B[2]),"+v"(B[3]),"+v"(B[4]),"+v"(B[5]),"+v"(B[6]) : "n"(CNT) : "memory"); }
DI void waitA(u4 (&A)[4]){ asm volatile("s_waitcnt lgkmcnt(0)" : "+v"(A[0]),"+v"(A[1]),"+v"(A[2]),"+v"(A[3]) :: "memory"); }
#define WAITV(n) asm volatile("s_waitcnt vmcnt(%0)" :: "n"(n) : "memory")
#define WAITL0() asm volatile("s_waitcnt lgkmcnt(0)" ::: "memory")
DI void bar(){ asm volatile("s_waitcnt lgkmcnt(0)\n\ts_barrier" ::: "memory"); }
DI float rowsum16(float v){
  v += __int_as_float(__builtin_amdgcn_update_dpp(0,__float_as_int(v),0x128,0xf,0xf,false));
  v += __int_as_float(__builtin_amdgcn_update_dpp(0,__float_as_int(v),0x124,0xf,0xf,false));
  v += __int_as_float(__builtin_amdgcn_update_dpp(0,__float_as_int(v),0x122,0xf,0xf,false));
  v += __int_as_float(__builtin_amdgcn_update_dpp(0,__float_as_int(v),0x121,0xf,0xf,false));
  return v;
}
DI float gelu(float x){
  float e=__builtin_amdgcn_exp2f(x*__builtin_fmaf(x*x,-0.10294324f,-2.30220820f));
  return x*__builtin_amdgcn_rcpf(1.f+e);
}
DI void ln_fin(float mean,float var,float& m,float& rinv){
  m=rbf(mean); float v=rbf(__builtin_fmaxf(var,0.f));
  float s=rbf(__builtin_sqrtf(rbf(v+1e-5f))); rinv=1.f/s;
}
DI u32 swzA(u32 row){ return ((row>>1)&1)|(((row>>3)&1)<<1)|((((row>>2)^(row>>4))&1)<<2); }
DI u32 swzB(u32 row){ return (((row>>1)&1)|(((row>>3)&1)<<1))<<1; }
DI __amdgpu_buffer_rsrc_t mkrs(const void* b,u32 n){ return __builtin_amdgcn_make_buffer_rsrc((void*)b,0,n,0x00020000); }

constexpr u32 A_STG=32768, A_MISC=4*A_STG, A_LDS=A_MISC+8192;

struct cf { float r,i; };
DI cf cmul(cf a,cf b){ cf o; o.r=__builtin_fmaf(a.r,b.r,-(a.i*b.i)); o.i=__builtin_fmaf(a.r,b.i,a.i*b.r); return o; }
DI cf cpow_dir(float lr,float rv,float pw){ float e=__builtin_amdgcn_exp2f(lr*pw); float f=__builtin_amdgcn_fractf(rv*pw);
  cf o; o.r=e*__builtin_amdgcn_cosf(f); o.i=e*__builtin_amdgcn_sinf(f); return o; }

DI void kgen_block(const Params& p,int a,u32 smb){
  const int w=uni(threadIdx.x>>6), lane=threadIdx.x&63;
  if(a==0 && threadIdx.x<40) *(u32*)(p.ws+OFF_CNT+threadIdx.x*256)=0u;
  const u32 TP=272, tb=smb+w*14848, TA=tb, TBo=tb+22*TP;
  u32 ga[4][8];
  #pragma unroll
  for(int it=0;it<4;it++)
    #pragma unroll
    for(int j=0;j<8;j++){ int lag=2048-(8*(lane+64*it)+j); ga[it][j]=tb+2u*(u32)rbf((float)lag); }
  const int nch=w<4?3:2, ch0=20*a+(w<4?3*w:12+2*(w-4));
  for(int ci=0;ci<nch;ci++){
    const int d=ch0+ci;
    const float dt=rbf(__expf(bf1(p.ldt[d])));
    float la[2],im[2],cr[2],cm[2];
    #pragma unroll
    for(int c=0;c<2;c++){ int o=d*128+c*64+lane; la[c]=bf1(p.lar[o]); im[c]=bf1(p.aim[o]); cr[c]=bf1(p.cre[o]); cm[c]=bf1(p.cim[o]); }
    float mx=fmaxf(fmaxf(fabsf(cr[0]),fabsf(cm[0])),fmaxf(fabsf(cr[1]),fabsf(cm[1])));
    #pragma unroll
    for(int o=32;o;o>>=1) mx=fmaxf(mx,__shfl_xor(mx,o));
    u32 eb=__float_as_uint(mx)&0x7f800000u; eb=eb<0x01000000u?0x3f800000u:(eb>0x7e000000u?0x7e000000u:eb);
    const float sc=__uint_as_float(eb), isc=__uint_as_float(0x7f000000u-eb);
    if(lane==0) *(float*)(p.ws+OFF_KSC+d*4)=2.f*dt*sc;
    f16v acc={};
    #pragma unroll 1
    for(int c=0;c<2;c++){
      float l_a=c?la[1]:la[0], i_m=c?im[1]:im[0], c_r=c?cr[1]:cr[0], c_i=c?cm[1]:cm[0];
      const float lr=(-rbf(__expf(l_a))*dt)*1.44269504f, rv=(i_m*dt)*0.15915494f;
      cf z1=cpow_dir(lr,rv,1.f), z8=cpow_dir(lr,-rv,8.f), z16=cpow_dir(lr,-rv,16.f), zk=cpow_dir(lr,rv,256.f);
      const u32 wa=TA+lane*4, wb=TBo+lane*4;
      #define PUT(base,row,v) do{ *(LAS u32*)((base)+(row)*TP)=pkhf((v).r,(v).i); }while(0)
      cf e={1.f,0.f}, o=z8;
      #pragma unroll
      for(int k=0;k<16;k++){ PUT(wb,2*k,e); PUT(wb,2*k+1,o); if(k<15){ e=k?cmul(e,z16):z16; o=cmul(o,z16);} }
      cf b0={c_r*isc,c_i*isc}; PUT(wa,0,b0);
      cf b1=cmul(b0,z1); PUT(wa,1,b1); cf b2=cmul(b1,z1); PUT(wa,2,b2); cf b3=cmul(b2,z1); PUT(wa,3,b3);
      cf b4=cmul(b3,z1); PUT(wa,4,b4); cf b5=cmul(b4,z1); PUT(wa,5,b5); cf b6=cmul(b5,z1); PUT(wa,6,b6); cf b7=cmul(b6,z1); PUT(wa,7,b7);
      cf t=cmul(b2,zk); PUT(wa,9,t); t=cmul(b6,zk); PUT(wa,13,t);
      t=cmul(b4,zk); PUT(wa,12,t); t=cmul(t,zk); PUT(wa,14,t); t=cmul(t,zk); PUT(wa,15,t);
      t=cmul(b0,zk); PUT(wa,8,t); t=cmul(t,zk); PUT(wa,10,t); t=cmul(t,zk); PUT(wa,11,t);
      t=cmul(t,zk); PUT(wa,16,t); t=cmul(t,zk); PUT(wa,17,t); t=cmul(t,zk); PUT(wa,20,t); t=cmul(t,zk); PUT(wa,21,t); t=cmul(t,zk); PUT(wa,18,t);
      #undef PUT
      WAITL0();
      const int r=lane&31, h=lane>>5; const int ra=(r>21||r==19)?0:r;
      #pragma unroll
      for(int ks=0;ks<8;ks++){ u4 fa=ldsr128(TA+ra*TP+ks*32+h*16), fb=ldsr128(TBo+r*TP+ks*32+h*16); acc=mfma32h(fa,fb,acc); }
      WAITL0();
    }
    { const u32 a4=tb+(lane&31)*16+(lane>>5)*8, a5=tb+(lane&31)*16+(lane>>5)*1024;
      #define PK(addr,base,reg) do{ *(LAS _Float16*)((addr)+2*(base))=(_Float16)acc[reg]; }while(0)
      PK(a4,0,0);PK(a4,1,1);PK(a4,2,2);PK(a4,3,3); PK(a4,256,4);PK(a4,258,5);PK(a4,512,6);PK(a4,768,7);
      PK(a5,1024,8);PK(a5,1280,9);PK(a5,2048,10);
      #undef PK
    }
    WAITL0();
    char* kx=p.ws+OFF_KX+d*KXP+64;
    #pragma unroll
    for(int it=0;it<4;it++){ u4 v;
      #pragma unroll
      for(int j=0;j<4;j++) v[j]=(u32)*(LAS u16*)ga[it][2*j] | ((u32)*(LAS u16*)ga[it][2*j+1]<<16);
      *(u4*)(kx+(lane+64*it)*16)=v; }
    if(lane<4){ u4 v={0,0,0,0}; if(lane==0) v[0]=*(LAS u16*)tb; *(u4*)(kx+4096+lane*16)=v; }
    WAITL0();
  }
}

template<bool Z2> DI void a_loop(const Params& p,u32 smb,int w4,int lane,int k0,const char* xa,const char* wb2,const char* wb1,
                                 u32 voA0,u32 voA1,u32 voB,int w,f4 (&acc)[2][4],float (&rs)[2],float (&cs)[4]){
  const int r=lane&15,q=lane>>4;
  u32 aoff[2],asw[2];
  #pragma unroll
  for(int m=0;m<2;m++){ u32 row=32*w4+(Z2? 8*(r>>2)+4*m+(r&3) : 16*m+r); aoff[m]=row*128; asw[m]=swzA(row); }
  u32 boff[2];
  #pragma unroll
  for(int t=0;t<2;t++){ u32 row=8*q+4*t+(r>>2); boff[t]=(Z2?16384u:24576u)+row*128+8*(r&1); }
  const u32 bsw=swzB(8*q+(r>>2));
  int kn=k0+4; if(kn>=10) kn-=10;
  #pragma unroll 1
  for(int it=0;it<10;it++){
    if(it<=7) WAITV(8); else if(it==8) WAITV(4); else WAITV(0);
    bar();
    if(it>=1 && it<=6){ u32 sl=smb+((it+3)&3)*A_STG+w*1024;
      glds(xa+kn*128,voA0,sl); glds(xa+kn*128,voA1,sl+8192); glds(wb2+kn*(64*3456),voB,sl+16384); glds(wb1+kn*(64*3456),voB,sl+24576);
      kn++; if(kn>=10) kn=0; }
    const u32 sb=smb+(it&3)*A_STG;
    #pragma unroll
    for(int ks=0;ks<2;ks++){
      u4 xf[2],wf[4];
      #pragma unroll
      for(int m=0;m<2;m++) xf[m]=ldsr128(sb+aoff[m]+(((4*ks+q)^asw[m])<<4));
      #pragma unroll
      for(int n=0;n<4;n++){ u32 ch=(((2*n+((r>>1)&1))^bsw)<<4)+ks*4096; u2 lo=ldstr(sb+boff[0]+ch), hi=ldstr(sb+boff[1]+ch); wf[n]=u4{lo[0],lo[1],hi[0],hi[1]}; }
      #pragma unroll
      for(int m=0;m<2;m++)
        #pragma unroll
        for(int n=0;n<4;n++) acc[m][n]= Z2? mfma16(xf[m],wf[n],acc[m][n]) : mfma16(wf[n],xf[m],acc[m][n]);
      #pragma unroll
      for(int m=0;m<2;m++)
        #pragma unroll
        for(int j=0;j<4;j++) rs[m]=dot2(xf[m][j], Z2? xf[m][j] : 0x3f803f80u, rs[m]);
      if(w4==(it&1)*2+ks){
        #pragma unroll
        for(int n=0;n<4;n++)
          #pragma unroll
          for(int j=0;j<4;j++) cs[n]=dot2(wf[n][j],0x3f803f80u,cs[n]);
      }
    }
  }
}

__global__ __launch_bounds__(512,2) void kA(Params p){
  extern __shared__ __attribute__((aligned(16))) char sm[];
  const u32 smb=(u32)(size_t)(LAS char*)sm;
  const int b=blockIdx.x, xc=b&7, idx=b>>3;
  #define STW 0
  STAMP(0);
  if(idx>=28){ kgen_block(p,xc*4+idx-28,smb); STAMP(7); return; }
  const int rt=4*(xc>>1)+(idx&3), j=7*(xc&1)+(idx>>2);
  const int w=uni(threadIdx.x>>6), lane=threadIdx.x&63, w4=w&3, r=lane&15, q=lane>>4;
  const bool z2=w<4;
  int kl=(j+3*(rt&3))%10, k0=kl+1; if(k0>=10) k0=0;
  u32 tag=0; u4 wo[5];
  const int grp=rt*4+w4;
  if(z2) tag=__hip_atomic_load((GAS u32*)(p.ws+OFF_EP+grp*256),__ATOMIC_RELAXED,__HIP_MEMORY_SCOPE_AGENT)+1u;
  const int wofi=((j-10)*16+rt)*4+w4;
  if(!z2 && j>=10){
    #pragma unroll
    for(int i=0;i<5;i++){ int f=wofi+256*i; if(f>=1080) f=0; int dg=f/135, rem=f-dg*135, ks=rem/5, d5=rem-ks*5;
      const u16* s=p.wout+(32*ks+4*q)*640+80*dg+16*d5+r;
      #pragma unroll
      for(int e=0;e<4;e++){ int k=(e>>1)*16+(e&1)*2; wo[i][e]=(u32)s[k*640]|((u32)s[(k+1)*640]<<16); } }
  }
  const char* xa=(const char*)p.x+(size_t)rt*128*1280;
  const int u1=j, u2_=(j==13)?13:14+j;
  const char* wb1=(const char*)p.win+u1*128; const char* wb2=(const char*)p.win+u2_*128;
  u32 voA0,voA1,voB;
  { u32 lr=lane>>3, c=lane&7; u32 r0=8*w+lr, r1=64+r0; voA0=r0*1280+((c^swzA(r0))<<4); voA1=r1*1280+((c^swzA(r1))<<4); voB=r0*3456+((c^swzB(r0))<<4); }
  { int kn=k0;
    #pragma unroll
    for(int s=0;s<4;s++){ u32 sl=smb+s*A_STG+w*1024;
      glds(xa+kn*128,voA0,sl); glds(xa+kn*128,voA1,sl+8192); glds(wb2+kn*(64*3456),voB,sl+16384); glds(wb1+kn*(64*3456),voB,sl+24576);
      kn++; if(kn>=10) kn=0; } }
  f4 acc[2][4]={}; float rs[2]={0.f,0.f}, cs[4]={0.f,0.f,0.f,0.f};
  if(z2) a_loop<true>(p,smb,w4,lane,k0,xa,wb2,wb1,voA0,voA1,voB,w,acc,rs,cs);
  else   a_loop<false>(p,smb,w4,lane,k0,xa,wb2,wb1,voA0,voA1,voB,w,acc,rs,cs);
  STAMP(1);
  const u32 mb=smb+A_MISC;
  #pragma unroll
  for(int m=0;m<2;m++){ rs[m]+=__shfl_xor(rs[m],16); rs[m]+=__shfl_xor(rs[m],32);
    u32 row=32*w4+(z2? 8*(r>>2)+4*m+(r&3) : 16*m+r); if(q==0) *(LAS float*)(mb+(z2?512:0)+row*4)=rs[m]; }
  #pragma unroll
  for(int n=0;n<4;n++){ cs[n]+=__shfl_xor(cs[n],16); cs[n]+=__shfl_xor(cs[n],32); if(q==0) *(LAS float*)(mb+1024+w*256+(16*n+r)*4)=cs[n]; }
  bar();
  { int t=threadIdx.x;
    if(t<128){ float s=*(LAS float*)(mb+t*4), sq=*(LAS float*)(mb+512+t*4); float mean=s*(1.f/640.f); float m,ri; ln_fin(mean,sq*(1.f/640.f)-mean*mean,m,ri); *(LAS f2*)(mb+3072+t*8)=f2{m,ri}; }
    else if(t<256){ int g=(t-128)>>6, c=t&63; u32 a=mb+1024+g*1024+c*4; *(LAS float*)(mb+4096+(t-128)*4)=(*(LAS float*)a+*(LAS float*)(a+256))+(*(LAS float*)(a+512)+*(LAS float*)(a+768)); } }
  bar();
  if(z2){
    float zv[2][4][4]; float csn[4];
    #pragma unroll
    for(int n=0;n<4;n++) csn[n]=*(LAS float*)(mb+4096+(16*n+r)*4);
    const float msk=(j==13)?0.f:1.f, icnt=(j==13)?(1.f/32.f):(1.f/64.f);
    float su[8],m2[8];
    #pragma unroll
    for(int m=0;m<2;m++){
      f4 s01=*(LAS f4*)(mb+3072+(32*w4+8*q+4*m)*8), s23=*(LAS f4*)(mb+3072+(32*w4+8*q+4*m+2)*8);
      float mm[4]={s01[0],s01[2],s23[0],s23[2]}, ri[4]={s01[1],s01[3],s23[1],s23[3]};
      #pragma unroll
      for(int n=0;n<4;n++)
        #pragma unroll
        for(int e=0;e<4;e+=2){ u32 t=pkbf((acc[m][n][e]-mm[e]*csn[n])*ri[e],(acc[m][n][e+1]-mm[e+1]*csn[n])*ri[e+1]);
          t=pkbf(gelu(bflo(t)),gelu(bfhi(t))); zv[m][n][e]=bflo(t); zv[m][n][e+1]=bfhi(t); }
      #pragma unroll
      for(int e=0;e<4;e++){ float s=rowsum16((zv[m][0][e]+zv[m][1][e])*msk+(zv[m][2][e]+zv[m][3][e])); float mu=s*icnt;
        float d0=zv[m][0][e]-mu,d1=zv[m][1][e]-mu,d2=zv[m][2][e]-mu,d3=zv[m][3][e]-mu;
        su[4*m+e]=s; m2[4*m+e]=rowsum16((d0*d0+d1*d1)*msk+(d2*d2+d3*d3)); }
    }
    float ps=su[0],pm=m2[0];
    #pragma unroll
    for(int e=1;e<8;e++){ ps=(r==e)?su[e]:ps; pm=(r==e)?m2[e]:pm; }
    __amdgpu_buffer_rsrc_t rst=mkrs(p.ws+OFF_ST+grp*(14*512),14*512);
    if(r<8) __builtin_amdgcn_raw_buffer_store_b128(u4{__float_as_uint(ps),tag,__float_as_uint(pm),tag},rst,j*512+(8*q+r)*16,0,16);
    STAMP(2);
    u4 g[7]; const u32 go=(lane>>5)*(7*512)+(lane&31)*16;
    for(int spin=0;spin<(1<<20);spin++){
      bool ok=true;
      #pragma unroll
      for(int i=0;i<7;i++) g[i]=__builtin_amdgcn_raw_buffer_load_b128(rst,go+i*512,0,16);
      #pragma unroll
      for(int i=0;i<7;i++) ok=ok&&g[i][1]==tag&&g[i][3]==tag;
      if(__all(ok)) break;
      __builtin_amdgcn_s_sleep(1);
    }
    STAMP(3);
    if(j==0 && lane==0) __hip_atomic_store((GAS u32*)(p.ws+OFF_EP+grp*256),tag,__ATOMIC_RELAXED,__HIP_MEMORY_SCOPE_AGENT);
    float S=0.f;
    #pragma unroll
    for(int i=0;i<7;i++) S+=__uint_as_float(g[i][0]);
    S+=__shfl_xor(S,32); const float mean=S*(1.f/864.f); float M=0.f;
    #pragma unroll
    for(int i=0;i<7;i++){ const bool half=(i==6)&&(lane>=32); float cn=half?32.f:64.f, dm=__uint_as_float(g[i][0])*(half?(1.f/32.f):(1.f/64.f))-mean; M+=__uint_as_float(g[i][2])+cn*dm*dm; }
    M+=__shfl_xor(M,32);
    float m_,ri_; ln_fin(mean,M*(1.f/864.f),m_,ri_);
    const u32 s2=mb+4608+w4*256; if(lane<32) *(LAS f2*)(s2+lane*8)=f2{m_,ri_};
    WAITL0();
    float mm[8],ri[8];
    #pragma unroll
    for(int e=0;e<4;e++){ f4 t=*(LAS f4*)(s2+(8*q+2*e)*8); mm[2*e]=t[0]; ri[2*e]=t[1]; mm[2*e+1]=t[2]; ri[2*e+1]=t[3]; }
    const int ft0=(j==13)?-2:2+4*j;
    #pragma unroll
    for(int n=0;n<4;n++){ u4 o;
      #pragma unroll
      for(int e=0;e<4;e++){ int m=e>>1,c=(e&1)*2; u32 t=pkbf(zv[m][n][c]-mm[2*e],zv[m][n][c+1]-mm[2*e+1]); o[e]=pkbf(bflo(t)*ri[2*e],bfhi(t)*ri[2*e+1]); }
      if(ft0+n>=0) *(u4*)(p.ws+OFF_Z2F+(u32)(grp*54+ft0+n)*1024+lane*16)=o; }
    STAMP(4);
  } else {
    #pragma unroll
    for(int m=0;m<2;m++){ f2 st=*(LAS f2*)(mb+3072+(32*w4+16*m+r)*8);
      #pragma unroll
      for(int n=0;n<4;n++){ f4 c4=*(LAS f4*)(mb+4096+256+(16*n+4*q)*4); u2 o;
        #pragma unroll
        for(int e=0;e<4;e+=2){ u32 t=pkbf((acc[m][n][e]-st[0]*c4[e])*st[1],(acc[m][n][e+1]-st[0]*c4[e+1])*st[1]); o[e>>1]=pkbf(gelu(bflo(t)),gelu(bfhi(t))); }
        if(4*j+n<54) *(u2*)(p.ws+OFF_Z1T+(u32)((rt*8+2*w4+m)*54+4*j+n)*512+lane*8)=o; } }
    if(j<10){
      float mm[8],ri[8];
      #pragma unroll
      for(int e=0;e<4;e++){ f4 t=*(LAS f4*)(mb+3072+(32*w4+8*q+2*e)*8); mm[2*e]=t[0]; ri[2*e]=t[1]; mm[2*e+1]=t[2]; ri[2*e+1]=t[3]; }
      const u32 sb=smb+(9&3)*A_STG;
      #pragma unroll
      for(int c=0;c<4;c++){ u4 o;
        #pragma unroll
        for(int t=0;t<2;t++){ u32 row=32*w4+8*q+4*t+(r>>2); u2 v=ldstr(sb+row*128+(((2*c+((r>>1)&1))^swzA(row))<<4)+8*(r&1));
          #pragma unroll
          for(int e=0;e<2;e++){ int i=4*t+2*e; u32 d=pkbf(bflo(v[e])-mm[i],bfhi(v[e])-mm[i+1]); d=pkbf(bflo(d)*ri[i],bfhi(d)*ri[i+1]); o[2*t+e]=pkhf(bflo(d),bfhi(d)); } }
        *(u4*)(p.ws+OFF_XNT+(u32)(64*kl+16*c+r)*4096+(rt*128+32*w4+8*q)*2)=o; }
    } else {
      #pragma unroll
      for(int i=0;i<5;i++){ int f=wofi+256*i; if(f<1080) *(u4*)(p.ws+OFF_WOF+(u32)f*1024+lane*16)=wo[i]; }
    }
  }
  #undef STW
}

constexpr u32 B_RING=24576, B_CONV=4*B_RING, CV_SZ=20800, B_LDS=B_CONV+3*CV_SZ;

DI void conv_wave(const Params& p,int d,u32 cb,int lane){
  const char* kx=p.ws+OFF_KX+d*KXP+64; const char* un=p.ws+OFF_XNT+d*4096;
  u4 kd[5]; u2 kp[5]; u4 uu[4];
  #pragma unroll
  for(int i=0;i<5;i++){ int x=lane+64*i; if(x>259) x=259; kd[i]=*(const u4*)(kx+x*16); kp[i]=*(const u2*)(kx+x*16-8); }
  #pragma unroll
  for(int i=0;i<4;i++) uu[i]=*(const u4*)(un+(lane+64*i)*16);
  const float ksc=*(const float*)(p.ws+OFF_KSC+d*4), dsk=bf1(p.dsk[d]);
  const u32 ub=cb+4*4160;
  #pragma unroll
  for(int i=0;i<5;i++){ int x=lane+64*i; if(x>259) x=259; u32 a=cb+x*16;
    u32 a0=__builtin_amdgcn_alignbit(kp[i][1],kp[i][0],16), a1=__builtin_amdgcn_alignbit(kd[i][0],kp[i][1],16), a2=__builtin_amdgcn_alignbit(kd[i][1],kd[i][0],16),
        a3=__builtin_amdgcn_alignbit(kd[i][2],kd[i][1],16), a4=__builtin_amdgcn_alignbit(kd[i][3],kd[i][2],16);
    ldsw128(a,kd[i]); ldsw128(a+4160,u4{a1,a2,a3,a4}); ldsw128(a+2*4160,u4{kp[i][1],kd[i][0],kd[i][1],kd[i][2]}); ldsw128(a+3*4160,u4{a0,a1,a2,a3}); }
  #pragma unroll
  for(int i=0;i<4;i++){ int x=lane+64*i; ldsw128(ub+(x&3)*1040+((x>>2)+1)*16,uu[i]); }
  if(lane<4) ldsw128(ub+lane*1040,u4{0,0,0,0});
  WAITL0();
  const int r=lane&31,h=lane>>5;
  u32 ka=cb+(r&3)*4160+16*h-8*(r>>2)+4096-2048;
  u32 u1=ub+h*1040+(r+33)*16;
  int c0=r;
  f16v y0={},y1={};
  #pragma unroll 2
  for(int dl=0;dl<32;dl++){
    u4 af[4],bfr[4];
    #pragma unroll
    for(int i=0;i<4;i++){ u32 o=(i<2?2048u:0u)+(i&1)*32u; u2 lo,hi;
      asm volatile("ds_read_b64 %0, %1 offset:%2" : "=v"(lo) : "v"(ka), "n"(o)); asm volatile("ds_read_b64 %0, %1 offset:%2" : "=v"(hi) : "v"(ka), "n"(o+8));
      af[i]=u4{lo[0],lo[1],hi[0],hi[1]}; }
    u32 u0=ub+h*1040+((c0<-1?-1:c0)+1)*16;
    asm volatile("ds_read_b128 %0, %1" : "=v"(bfr[0]) : "v"(u0)); asm volatile("ds_read_b128 %0, %1 offset:2080" : "=v"(bfr[1]) : "v"(u0));
    asm volatile("ds_read_b128 %0, %1" : "=v"(bfr[2]) : "v"(u1)); asm volatile("ds_read_b128 %0, %1 offset:2080" : "=v"(bfr[3]) : "v"(u1));
    asm volatile("s_waitcnt lgkmcnt(0)" : "+v"(af[0]),"+v"(af[1]),"+v"(af[2]),"+v"(af[3]),"+v"(bfr[0]),"+v"(bfr[1]),"+v"(bfr[2]),"+v"(bfr[3]));
    y0=mfma32h(af[0],bfr[0],y0); y1=mfma32h(af[0],bfr[2],y1); y0=mfma32h(af[1],bfr[1],y0); y1=mfma32h(af[1],bfr[3],y1);
    y1=mfma32h(af[2],bfr[0],y1); y1=mfma32h(af[3],bfr[1],y1);
    ka-=64; u1-=16; c0-=1;
  }
  char* at=p.ws+OFF_AT+d*4096;
  #pragma unroll
  for(int t=0;t<2;t++)
    #pragma unroll
    for(int g=0;g<4;g++){ u2 uv=ldsr64(ub+g*1040+(32*t+r+1)*16+8*h); u2 o;
      #pragma unroll
      for(int e=0;e<2;e++){ float ya=(t?y1:y0)[4*g+2*e]*ksc, yb=(t?y1:y0)[4*g+2*e+1]*ksc; u32 yy=pkbf(ya,yb), sk=pkbf(hlo(uv[e])*dsk,hhi(uv[e])*dsk); o[e]=pkbf(bflo(yy)+bflo(sk),bfhi(yy)+bfhi(sk)); }
      *(u2*)(at+((32*t+r)*32+8*g+4*h)*2)=o; }
}

__global__ __launch_bounds__(512,2) void kB(Params p){
  extern __shared__ __attribute__((aligned(16))) char sm[];
  const u32 smb=(u32)(size_t)(LAS char*)sm;
  const int b=blockIdx.x, xc=b&7, idx=b>>3;
  const int rt=8*(xc>>1)+(idx&7), ct=4*(xc&1)+(idx>>3);
  const int w=uni(threadIdx.x>>6), lane=threadIdx.x&63, r=lane&15, q=lane>>4;
  #define STW 0
  STAMP(8);
  #undef STW
  if(w>=4){
    const int d=b+256*(w-4);
    if(d<640) conv_wave(p,d,smb+B_CONV+(w-4)*CV_SZ,lane);
    #define STW 4
    STAMP(12);
    #undef STW
    bar();
    return;
  }
  #define STW 0
  const int ft0=ct<7?7*ct:47, rot=(2*(ct&3)+(rt&7))&7;
  const u32 rb=smb+w*B_RING;
  const char* wa=(const char*)p.wsp+(size_t)rt*64*4096+w*1024;
  const char* zb=p.ws+OFF_Z2F+(u32)(16*w*54+ft0+3)*1024;
  u32 voA[2]; { u32 lr=lane>>3,c=lane&7; voA[0]=lr*4096+((c^(lr>>1))<<4); voA[1]=lr*4096+((c^(4|(lr>>1)))<<4); }
  const u32 voB=lane*16;
  u32 la[2];
  la[0]=rb+r*128+((q^(r>>1))<<4); la[1]=rb+r*128+(((4+q)^(r>>1))<<4);
  f4 acc[4][7]={}; u4 B[3][7]; u4 A[2][4];
  auto issueA=[&](int s){ const char* sb=wa+((s+rot)&7)*128; u32 sl=rb+(s%3)*8192;
    #pragma unroll
    for(int g=0;g<8;g++) glds(sb+g*8*4096,voA[g&1],sl+(((g>>1)-w)&3)*2048+(g&1)*1024); };
  auto bbase=[&](int h){ return zb+(u32)(((((h>>1)+rot)&7)<<1)+(h&1))*(54*1024); };
  #define LOADB(dst,base,n) gload<((n)-3)*1024>(dst,voB,base)
  issueA(0);
  { const char* s=bbase(0); sfor<7>([&](auto n){ LOADB(B[0][n],s,n.value); }); }
  issueA(1);
  { const char* s=bbase(1); sfor<7>([&](auto n){ LOADB(B[1][n],s,n.value); }); }
  issueA(2);
  { const char* s=bbase(2); sfor<7>([&](auto n){ LOADB(B[2][n],s,n.value); }); }
  WAITV(37);
  #pragma unroll
  for(int m=0;m<4;m++) A[0][m]=ldsr128(la[0]+m*2048);
  sfor<16>([&](auto hc){
    constexpr int h=hc.value, S=h%3, P=h&1;
    constexpr int cnt = h==0? 30 : h==1? 22 : h==2? 22 :
        ((h-2+3<16?7:0)+(h-1+3<16?7:0) + ((((h-2)&1)&&((h-2)/2+3<8))?8:0) + ((((h-1)&1)&&((h-1)/2+3<8))?8:0));
    waitB<cnt>(B[S]); waitA(A[P]);
    if constexpr(h+1<16){
      #pragma unroll
      for(int m=0;m<4;m++) A[P^1][m]=ldsr128(la[(h+1)&1]+m*2048+(((h+1)>>1)%3)*8192);
    }
    if constexpr((h&1)&&(h/2+3<8)) issueA(h/2+3);
    const char* s=bbase(h+3<16?h+3:0);
    sfor<7>([&](auto n){
      #pragma unroll
      for(int m=0;m<4;m++) acc[m][n]=mfma16(B[S][n],A[P][m],acc[m][n]);
      __builtin_amdgcn_sched_barrier(0);
      if constexpr(h+3<16) LOADB(B[S][n],s,n.value);
      __builtin_amdgcn_sched_barrier(0);
    });
  });
  STAMP(9);
  const int tt=rt*4+w;
  u2 z1[7];
  #pragma unroll
  for(int n=0;n<7;n++) z1[n]=*(const u2*)(p.ws+OFF_Z1T+(u32)(tt*54+ft0+n)*512+lane*8);
  const float bs=bf1(p.bsp[tt*16+r]);
  #pragma unroll
  for(int m=1;m<4;m++)
    #pragma unroll
    for(int n=0;n<7;n++) *(LAS f4*)(rb+((m-1)*7+n)*1024+lane*16)=acc[m][n];
  bar();
  #pragma unroll
  for(int m=1;m<4;m++){ u32 src=smb+((w-m)&3)*B_RING+(m-1)*7168+lane*16;
    #pragma unroll
    for(int n=0;n<7;n++) acc[0][n]+=*(LAS f4*)(src+n*1024); }
  STAMP(10);
  #pragma unroll
  for(int n=0;n<7;n++){ u2 o;
    #pragma unroll
    for(int e=0;e<2;e++){ u32 g=pkbf(acc[0][n][2*e],acc[0][n][2*e+1]); g=pkbf(bflo(g)+bs,bfhi(g)+bs); o[e]=pkbf(bflo(z1[n][e])*bflo(g),bfhi(z1[n][e])*bfhi(g)); }
    const int ft=ft0+n;
    if(ct<7||n>=2) *(u2*)(p.ws+OFF_HF+(u32)(tt*27+(ft>>1))*1024+lane*16+(ft&1)*8)=o; }
  STAMP(11);
  #undef STW
}

constexpr u32 C_AT=135*1024, C_LDS=C_AT+10240;
__global__ __launch_bounds__(512,2) void kC(Params p){
  extern __shared__ __attribute__((aligned(16))) char sm[];
  const u32 smb=(u32)(size_t)(LAS char*)sm;
  const int b=blockIdx.x, xc=b&7, idx=b>>3;
  const int rt=8*(xc>>1)+(idx&7), ct=4*(xc&1)+(idx>>3);
  const int w=uni(threadIdx.x>>6), lane=threadIdx.x&63, r=lane&15, q=lane>>4, m=w&3, kh=w>>2;
  #define STW 0
  STAMP(16);
  const char* wf=p.ws+OFF_WOF+ct*(135*1024);
  for(int i=w;i<135;i+=8) glds(wf+i*1024,lane*16,smb+i*1024);
  for(int i=w;i<10;i+=8) glds(p.ws+OFF_AT+(u32)(80*ct+8*i)*4096+rt*128,(lane>>3)*4096+(lane&7)*16,smb+C_AT+i*1024);
  u4 hf[14]; const char* hb=p.ws+OFF_HF+(u32)((rt*4+m)*27+kh*13)*1024+lane*16;
  #pragma unroll
  for(int k=0;k<14;k++) hf[k]=*(const u4*)(hb+k*1024);
  const int t=rt*64+m*16+r;
  u2 xv[5];
  #pragma unroll
  for(int n=0;n<5;n++) xv[n]=*(const u2*)(p.x+t*640+80*ct+16*n+4*q);
  WAITV(0); bar();
  STAMP(17);
  f4 acc[5]={};
  if(kh) hf[0]=u4{0,0,0,0};
  #pragma unroll
  for(int k=0;k<14;k++)
    #pragma unroll
    for(int n=0;n<5;n++) acc[n]=mfma16(ldsr128(smb+((kh*13+k)*5+n)*1024+lane*16),hf[k],acc[n]);
  bar();
  if(kh){
    #pragma unroll
    for(int n=0;n<5;n++) *(LAS f4*)(smb+(m*5+n)*1024+lane*16)=acc[n];
  }
  bar();
  if(!kh){
    #pragma unroll
    for(int n=0;n<5;n++){ f4 o=acc[n]+*(LAS f4*)(smb+(m*5+n)*1024+lane*16);
      u2 av=ldstr(smb+C_AT+(16*n+4*q+(r>>2))*128+(16*m+4*(r&3))*2); u2 res;
      #pragma unroll
      for(int e=0;e<2;e++){ u32 xa=pkbf(bflo(xv[n][e])+bflo(av[e]),bfhi(xv[n][e])+bfhi(av[e])), ob=pkbf(o[2*e],o[2*e+1]); res[e]=pkbf(bflo(xa)+bflo(ob),bfhi(xa)+bfhi(ob)); }
      *(u2*)(p.out+t*640+80*ct+16*n+4*q)=res; }
  }
  STAMP(18);
  #undef STW
}

extern "C" void kernel_launch(void* const* d_in,const int* in_sizes,int n_in,void* d_out,int out_size,void* d_ws,size_t ws_size,hipStream_t stream){
  Params p;
  p.x=(const u16*)d_in[0]; p.lar=(const u16*)d_in[1]; p.aim=(const u16*)d_in[2]; p.cre=(const u16*)d_in[3]; p.cim=(const u16*)d_in[4];
  p.ldt=(const u16*)d_in[5]; p.dsk=(const u16*)d_in[6]; p.win=(const u16*)d_in[8]; p.wsp=(const u16*)d_in[9]; p.bsp=(const u16*)d_in[10]; p.wout=(const u16*)d_in[11];
  p.out=(u16*)d_out; p.ws=(char*)d_ws;
  (void)hipFuncSetAttribute((const void*)kA,hipFuncAttributeMaxDynamicSharedMemorySize,A_LDS);
  (void)hipFuncSetAttribute((const void*)kB,hipFuncAttributeMaxDynamicSharedMemorySize,B_LDS);
  (void)hipFuncSetAttribute((const void*)kC,hipFuncAttributeMaxDynamicSharedMemorySize,C_LDS);
  kA<<<256,512,A_LDS,stream>>>(p);
  kB<<<256,512,B_LDS,stream>>>(p);
  kC<<<256,512,C_LDS,stream>>>(p);
}
```

```cpp
#include <hip/hip_runtime.h>
#include <utility>
#pragma clang diagnostic ignored "-Wint-to-pointer-cast"
typedef unsigned short u16;
typedef unsigned int u32;
typedef unsigned long long u64;
typedef __attribute__((ext_vector_type(8))) __bf16 bf8;
typedef __attribute__((ext_vector_type(8))) _Float16 hf8;
typedef __attribute__((ext_vector_type(2))) __bf16 bf2;
typedef __attribute__((ext_vector_type(2))) _Float16 hf2;
typedef __attribute__((ext_vector_type(2))) float f2;
typedef __attribute__((ext_vector_type(4))) float f4;
typedef __attribute__((ext_vector_type(16))) float f16v;
typedef __attribute__((ext_vector_type(4))) u32 u4;
typedef __attribute__((ext_vector_type(2))) u32 u2;
typedef __attribute__((ext_vector_type(4))) short s4;
#define LAS __attribute__((address_space(3)))
#define GAS __attribute__((address_space(1)))
#define DI __device__ __forceinline__

constexpr u32 OFF_Z2F = 0;
constexpr u32 OFF_Z1T = OFF_Z2F + 64*54*1024;
constexpr u32 OFF_XNT = OFF_Z1T + 128*54*512;
constexpr u32 KXP     = 4224;
constexpr u32 OFF_KX  = OFF_XNT + 640*4096;
constexpr u32 OFF_KSC = OFF_KX + 640*KXP;
constexpr u32 OFF_WOF = OFF_KSC + 4096;
constexpr u32 OFF_HF  = OFF_WOF + 8*27*5*1024;
constexpr u32 OFF_AT  = OFF_HF + 128*27*1024;
constexpr u32 OFF_ST  = OFF_AT + 640*4096;
constexpr u32 OFF_EP  = OFF_ST + 64*14*512;
constexpr u32 OFF_CNT = OFF_EP + 64*256;
constexpr u32 WS_END  = OFF_CNT + 40*256;

struct Params {
  const u16 *x, *lar, *aim, *cre, *cim, *ldt, *dsk, *win, *wsp, *bsp, *wout;
  u16* out; char* ws;
};

#define STAMP(id) do{}while(0)

template<class F,int... I> DI void sfor_impl(F&& f, std::integer_sequence<int,I...>){ (f(std::integral_constant<int,I>{}),...); }
template<int N,class F> DI void sfor(F&& f){ sfor_impl(f, std::make_integer_sequence<int,N>{}); }

DI float bflo(u32 v){ return __uint_as_float(v<<16); }
DI float bfhi(u32 v){ return __uint_as_float(v&0xffff0000u); }
DI float bf1(u16 v){ return __uint_as_float(((u32)v)<<16); }
DI u32 pkbf(float a,float b){ f2 v={a,b}; return __builtin_bit_cast(u32,__builtin_convertvector(v,bf2)); }
DI u32 pkhf(float a,float b){ f2 v={a,b}; return __builtin_bit_cast(u32,__builtin_convertvector(v,hf2)); }
DI float rbf(float a){ return bflo(pkbf(a,a)); }
DI float hlo(u32 v){ return (float)__builtin_bit_cast(hf2,v)[0]; }
DI float hhi(u32 v){ return (float)__builtin_bit_cast(hf2,v)[1]; }
DI float dot2(u32 a,u32 b,float c){ return __builtin_amdgcn_fdot2_f32_bf16(__builtin_bit_cast(bf2,a),__builtin_bit_cast(bf2,b),c,false); }
DI f4 mfma16(u4 a,u4 b,f4 c){ return __builtin_amdgcn_mfma_f32_16x16x32_bf16(__builtin_bit_cast(bf8,a),__builtin_bit_cast(bf8,b),c,0,0,0); }
DI f16v mfma32h(u4 a,u4 b,f16v c){ return __builtin_amdgcn_mfma_f32_32x32x16_f16(__builtin_bit_cast(hf8,a),__builtin_bit_cast(hf8,b),c,0,0,0); }
DI u4 ldsr128(u32 a){ return *(LAS u4*)a; }
DI u2 ldsr64(u32 a){ return *(LAS u2*)a; }
DI void ldsw128(u32 a,u4 v){ *(LAS u4*)a=v; }
DI u2 ldstr(u32 a){ return __builtin_bit_cast(u2,__builtin_amdgcn_ds_read_tr16_b64_v4i16((LAS s4*)a)); }
DI u32 uni(u32 v){ return __builtin_amdgcn_readfirstlane(v); }
DI void glds(const char* sbase,u32 voff,u32 lds){
  u32 keep;
  asm volatile("s_mov_b32 %0, m0\n\ts_mov_b32 m0, %3\n\ts_nop 0\n\tglobal_load_lds_dwordx4 %1, %2\n\ts_mov_b32 m0, %0"
    : "=&s"(keep) : "v"(voff), "s"(sbase), "s"(lds) : "memory");
}
template<int OFF> DI void gload(u4& dst,u32 voff,const char* base){ asm volatile("global_load_dwordx4 %0, %1, %2 offset:%3" : "=v"(dst) : "v"(voff), "s"(base), "n"(OFF)); }
template<int CNT> DI void waitB(u4 (&B)[7]){ asm volatile("s_waitcnt vmcnt(%7)" : "+v"(B[0]),"+v"(B[1]),"+v"(B[2]),"+v"(B[3]),"+v"(B[4]),"+v"(B[5]),"+v"(B[6]) : "n"(CNT) : "memory"); }
DI void waitA(u4 (&A)[4]){ asm volatile("s_waitcnt lgkmcnt(0)" : "+v"(A[0]),"+v"(A[1]),"+v"(A[2]),"+v"(A[3]) :: "memory"); }
#define WAITV(n) asm volatile("s_waitcnt vmcnt(%0)" :: "n"(n) : "memory")
#define WAITL0() asm volatile("s_waitcnt lgkmcnt(0)" ::: "memory")
DI void bar(){ asm volatile("s_waitcnt lgkmcnt(0)\n\ts_barrier" ::: "memory"); }
DI float rowsum16(float v){
  v += __int_as_float(__builtin_amdgcn_update_dpp(0,__float_as_int(v),0x128,0xf,0xf,false));
  v += __int_as_float(__builtin_amdgcn_update_dpp(0,__float_as_int(v),0x124,0xf,0xf,false));
  v += __int_as_float(__builtin_amdgcn_update_dpp(0,__float_as_int(v),0x122,0xf,0xf,false));
  v += __int_as_float(__builtin_amdgcn_update_dpp(0,__float_as_int(v),0x121,0xf,0xf,false));
  return v;
}
DI float gelu(float x){
  float e=__builtin_amdgcn_exp2f(x*__builtin_fmaf(x*x,-0.10294324f,-2.30220820f));
  return x*__builtin_amdgcn_rcpf(1.f+e);
}
DI void ln_fin(float mean,float var,float& m,float& rinv){
  m=rbf(mean); float v=rbf(__builtin_fmaxf(var,0.f));
  float s=rbf(__builtin_sqrtf(rbf(v+1e-5f))); rinv=1.f/s;
}
DI u32 swzA(u32 row){ return ((row>>1)&1)|(((row>>3)&1)<<1)|((((row>>2)^(row>>4))&1)<<2); }
DI u32 swzB(u32 row){ return (((row>>1)&1)|(((row>>3)&1)<<1))<<1; }
DI __amdgpu_buffer_rsrc_t mkrs(const void* b,u32 n){ return __builtin_amdgcn_make_buffer_rsrc((void*)b,0,n,0x00020000); }

constexpr u32 A_STG=32768, A_MISC=4*A_STG, A_LDS=A_MISC+8192;

struct cf { float r,i; };
DI cf cmul(cf a,cf b){ cf o; o.r=__builtin_fmaf(a.r,b.r,-(a.i*b.i)); o.i=__builtin_fmaf(a.r,b.i,a.i*b.r); return o; }
DI cf cpow_dir(float lr,float rv,float pw){ float e=__builtin_amdgcn_exp2f(lr*pw); float f=__builtin_amdgcn_fractf(rv*pw);
  cf o; o.r=e*__builtin_amdgcn_cosf(f); o.i=e*__builtin_amdgcn_sinf(f); return o; }

DI void kgen_block(const Params& p,int a,u32 smb){
  const int w=uni(threadIdx.x>>6), lane=threadIdx.x&63;
  if(a==0 && threadIdx.x<40) *(u32*)(p.ws+OFF_CNT+threadIdx.x*256)=0u;
  const u32 TP=272, tb=smb+w*14848, TA=tb, TBo=tb+22*TP;
  u32 ga[4][8];
  #pragma unroll
  for(int it=0;it<4;it++)
    #pragma unroll
    for(int j=0;j<8;j++){ int lag=2048-(8*(lane+64*it)+j); ga[it][j]=tb+2u*(u32)rbf((float)lag); }
  const int nch=w<4?3:2, ch0=20*a+(w<4?3*w:12+2*(w-4));
  for(int ci=0;ci<nch;ci++){
    const int d=ch0+ci;
    const float dt=rbf(__expf(bf1(p.ldt[d])));
    float la[2],im[2],cr[2],cm[2];
    #pragma unroll
    for(int c=0;c<2;c++){ int o=d*128+c*64+lane; la[c]=bf1(p.lar[o]); im[c]=bf1(p.aim[o]); cr[c]=bf1(p.cre[o]); cm[c]=bf1(p.cim[o]); }
    float mx=fmaxf(fmaxf(fabsf(cr[0]),fabsf(cm[0])),fmaxf(fabsf(cr[1]),fabsf(cm[1])));
    #pragma unroll
    for(int o=32;o;o>>=1) mx=fmaxf(mx,__shfl_xor(mx,o));
    u32 eb=__float_as_uint(mx)&0x7f800000u; eb=eb<0x01000000u?0x3f800000u:(eb>0x7e000000u?0x7e000000u:eb);
    const float sc=__uint_as_float(eb), isc=__uint_as_float(0x7f000000u-eb);
    if(lane==0) *(float*)(p.ws+OFF_KSC+d*4)=2.f*dt*sc;
    f16v acc={};
    #pragma unroll 1
    for(int c=0;c<2;c++){
      float l_a=c?la[1]:la[0], i_m=c?im[1]:im[0], c_r=c?cr[1]:cr[0], c_i=c?cm[1]:cm[0];
      const float lr=(-rbf(__expf(l_a))*dt)*1.44269504f, rv=(i_m*dt)*0.15915494f;
      cf z1=cpow_dir(lr,rv,1.f), z8=cpow_dir(lr,-rv,8.f), z16=cpow_dir(lr,-rv,16.f), zk=cpow_dir(lr,rv,256.f);
      const u32 wa=TA+lane*4, wb=TBo+lane*4;
      #define PUT(base,row,v) do{ *(LAS u32*)((base)+(row)*TP)=pkhf((v).r,(v).i); }while(0)
      cf e={1.f,0.f}, o=z8;
      #pragma unroll
      for(int k=0;k<16;k++){ PUT(wb,2*k,e); PUT(wb,2*k+1,o); if(k<15){ e=k?cmul(e,z16):z16; o=cmul(o,z16);} }
      cf b0={c_r*isc,c_i*isc}; PUT(wa,0,b0);
      cf b1=cmul(b0,z1); PUT(wa,1,b1); cf b2=cmul(b1,z1); PUT(wa,2,b2); cf b3=cmul(b2,z1); PUT(wa,3,b3);
      cf b4=cmul(b3,z1); PUT(wa,4,b4); cf b5=cmul(b4,z1); PUT(wa,5,b5); cf b6=cmul(b5,z1); PUT(wa,6,b6); cf b7=cmul(b6,z1); PUT(wa,7,b7);
      cf t=cmul(b2,zk); PUT(wa,9,t); t=cmul(b6,zk); PUT(wa,13,t);
      t=cmul(b4,zk); PUT(wa,12,t); t=cmul(t,zk); PUT(wa,14,t); t=cmul(t,zk); PUT(wa,15,t);
      t=cmul(b0,zk); PUT(wa,8,t); t=cmul(t,zk); PUT(wa,10,t); t=cmul(t,zk); PUT(wa,11,t);
      t=cmul(t,zk); PUT(wa,16,t); t=cmul(t,zk); PUT(wa,17,t); t=cmul(t,zk); PUT(wa,20,t); t=cmul(t,zk); PUT(wa,21,t); t=cmul(t,zk); PUT(wa,18,t);
      #undef PUT
      WAITL0();
      const int r=lane&31, h=lane>>5; const int ra=(r>21||r==19)?0:r;
      #pragma unroll
      for(int ks=0;ks<8;ks++){ u4 fa=ldsr128(TA+ra*TP+ks*32+h*16), fb=ldsr128(TBo+r*TP+ks*32+h*16); acc=mfma32h(fa,fb,acc); }
      WAITL0();
    }
    { const u32 a4=tb+(lane&31)*16+(lane>>5)*8, a5=tb+(lane&31)*16+(lane>>5)*1024;
      #define PK(addr,base,reg) do{ *(LAS _Float16*)((addr)+2*(base))=(_Float16)acc[reg]; }while(0)
      PK(a4,0,0);PK(a4,1,1);PK(a4,2,2);PK(a4,3,3); PK(a4,256,4);PK(a4,258,5);PK(a4,512,6);PK(a4,768,7);
      PK(a5,1024,8);PK(a5,1280,9);PK(a5,2048,10);
      #undef PK
    }
    WAITL0();
    char* kx=p.ws+OFF_KX+d*KXP+64;
    #pragma unroll
    for(int it=0;it<4;it++){ u4 v;
      #pragma unroll
      for(int j=0;j<4;j++) v[j]=(u32)*(LAS u16*)ga[it][2*j] | ((u32)*(LAS u16*)ga[it][2*j+1]<<16);
      *(u4*)(kx+(lane+64*it)*16)=v; }
    if(lane<4){ u4 v={0,0,0,0}; if(lane==0) v[0]=*(LAS u16*)tb; *(u4*)(kx+4096+lane*16)=v; }
    WAITL0();
  }
}

template<bool Z2> DI void a_loop(const Params& p,u32 smb,int w4,int lane,int k0,const char* xa,const char* wb2,const char* wb1,
                                 u32 voA0,u32 voA1,u32 voB,int w,f4 (&acc)[2][4],float (&rs)[2],float (&cs)[4]){
  const int r=lane&15,q=lane>>4;
  u32 aoff[2],asw[2];
  #pragma unroll
  for(int m=0;m<2;m++){ u32 row=32*w4+(Z2? 8*(r>>2)+4*m+(r&3) : 16*m+r); aoff[m]=row*128; asw[m]=swzA(row); }
  u32 boff[2];
  #pragma unroll
  for(int t=0;t<2;t++){ u32 row=8*q+4*t+(r>>2); boff[t]=(Z2?16384u:24576u)+row*128+8*(r&1); }
  const u32 bsw=swzB(8*q+(r>>2));
  int kn=k0+4; if(kn>=10) kn-=10;
  #pragma unroll 1
  for(int it=0;it<10;it++){
    if(it<=7) WAITV(8); else if(it==8) WAITV(4); else WAITV(0);
    bar();
    if(it>=1 && it<=6){ u32 sl=smb+((it+3)&3)*A_STG+w*1024;
      glds(xa+kn*128,voA0,sl); glds(xa+kn*128,voA1,sl+8192); glds(wb2+kn*(64*3456),voB,sl+16384); glds(wb1+kn*(64*3456),voB,sl+24576);
      kn++; if(kn>=10) kn=0; }
    const u32 sb=smb+(it&3)*A_STG;
    #pragma unroll
    for(int ks=0;ks<2;ks++){
      u4 xf[2],wf[4];
      #pragma unroll
      for(int m=0;m<2;m++) xf[m]=ldsr128(sb+aoff[m]+(((4*ks+q)^asw[m])<<4));
      #pragma unroll
      for(int n=0;n<4;n++){ u32 ch=(((2*n+((r>>1)&1))^bsw)<<4)+ks*4096; u2 lo=ldstr(sb+boff[0]+ch), hi=ldstr(sb+boff[1]+ch); wf[n]=u4{lo[0],lo[1],hi[0],hi[1]}; }
      #pragma unroll
      for(int m=0;m<2;m++)
        #pragma unroll
        for(int n=0;n<4;n++) acc[m][n]= Z2? mfma16(xf[m],wf[n],acc[m][n]) : mfma16(wf[n],xf[m],acc[m][n]);
      #pragma unroll
      for(int m=0;m<2;m++)
        #pragma unroll
        for(int j=0;j<4;j++) rs[m]=dot2(xf[m][j], Z2? xf[m][j] : 0x3f803f80u, rs[m]);
      if(w4==(it&1)*2+ks){
        #pragma unroll
        for(int n=0;n<4;n++)
          #pragma unroll
          for(int j=0;j<4;j++) cs[n]=dot2(wf[n][j],0x3f803f80u,cs[n]);
      }
    }
  }
}

__global__ __launch_bounds__(512,2) void kA(Params p){
  extern __shared__ __attribute__((aligned(16))) char sm[];
  const u32 smb=(u32)(size_t)(LAS char*)sm;
  const int b=blockIdx.x, xc=b&7, idx=b>>3;
  #define STW 0
  STAMP(0);
  if(idx>=28){ kgen_block(p,xc*4+idx-28,smb); STAMP(7); return; }
  const int rt=4*(xc>>1)+(idx&3), j=7*(xc&1)+(idx>>2);
  const int w=uni(threadIdx.x>>6), lane=threadIdx.x&63, w4=w&3, r=lane&15, q=lane>>4;
  const bool z2=w<4;
  int kl=(j+3*(rt&3))%10, k0=kl+1; if(k0>=10) k0=0;
  u32 tag=0; u4 wo[5];
  const int grp=rt*4+w4;
  if(z2) tag=__hip_atomic_load((GAS u32*)(p.ws+OFF_EP+grp*256),__ATOMIC_RELAXED,__HIP_MEMORY_SCOPE_AGENT)+1u;
  const int wofi=((j-10)*16+rt)*4+w4;
  if(!z2 && j>=10){
    #pragma unroll
    for(int i=0;i<5;i++){ int f=wofi+256*i; if(f>=1080) f=0; int dg=f/135, rem=f-dg*135, ks=rem/5, d5=rem-ks*5;
      const u16* s=p.wout+(32*ks+4*q)*640+80*dg+16*d5+r;
      #pragma unroll
      for(int e=0;e<4;e++){ int k=(e>>1)*16+(e&1)*2; wo[i][e]=(u32)s[k*640]|((u32)s[(k+1)*640]<<16); } }
  }
  const char* xa=(const char*)p.x+(size_t)rt*128*1280;
  const int u1=j, u2_=(j==13)?13:14+j;
  const char* wb1=(const char*)p.win+u1*128; const char* wb2=(const char*)p.win+u2_*128;
  u32 voA0,voA1,voB;
  { u32 lr=lane>>3, c=lane&7; u32 r0=8*w+lr, r1=64+r0; voA0=r0*1280+((c^swzA(r0))<<4); voA1=r1*1280+((c^swzA(r1))<<4); voB=r0*3456+((c^swzB(r0))<<4); }
  { int kn=k0;
    #pragma unroll
    for(int s=0;s<4;s++){ u32 sl=smb+s*A_STG+w*1024;
      glds(xa+kn*128,voA0,sl); glds(xa+kn*128,voA1,sl+8192); glds(wb2+kn*(64*3456),voB,sl+16384); glds(wb1+kn*(64*3456),voB,sl+24576);
      kn++; if(kn>=10) kn=0; } }
  f4 acc[2][4]={}; float rs[2]={0.f,0.f}, cs[4]={0.f,0.f,0.f,0.f};
  if(z2) a_loop<true>(p,smb,w4,lane,k0,xa,wb2,wb1,voA0,voA1,voB,w,acc,rs,cs);
  else   a_loop<false>(p,smb,w4,lane,k0,xa,wb2,wb1,voA0,voA1,voB,w,acc,rs,cs);
  STAMP(1);
  const u32 mb=smb+A_MISC;
  #pragma unroll
  for(int m=0;m<2;m++){ rs[m]+=__shfl_xor(rs[m],16); rs[m]+=__shfl_xor(rs[m],32);
    u32 row=32*w4+(z2? 8*(r>>2)+4*m+(r&3) : 16*m+r); if(q==0) *(LAS float*)(mb+(z2?512:0)+row*4)=rs[m]; }
  #pragma unroll
  for(int n=0;n<4;n++){ cs[n]+=__shfl_xor(cs[n],16); cs[n]+=__shfl_xor(cs[n],32); if(q==0) *(LAS float*)(mb+1024+w*256+(16*n+r)*4)=cs[n]; }
  bar();
  { int t=threadIdx.x;
    if(t<128){ float s=*(LAS float*)(mb+t*4), sq=*(LAS float*)(mb+512+t*4); float mean=s*(1.f/640.f); float m,ri; ln_fin(mean,sq*(1.f/640.f)-mean*mean,m,ri); *(LAS f2*)(mb+3072+t*8)=f2{m,ri}; }
    else if(t<256){ int g=(t-128)>>6, c=t&63; u32 a=mb+1024+g*1024+c*4; *(LAS float*)(mb+4096+(t-128)*4)=(*(LAS float*)a+*(LAS float*)(a+256))+(*(LAS float*)(a+512)+*(LAS float*)(a+768)); } }
  bar();
  if(z2){
    float zv[2][4][4]; float csn[4];
    #pragma unroll
    for(int n=0;n<4;n++) csn[n]=*(LAS float*)(mb+4096+(16*n+r)*4);
    const float msk=(j==13)?0.f:1.f, icnt=(j==13)?(1.f/32.f):(1.f/64.f);
    float su[8],m2[8];
    #pragma unroll
    for(int m=0;m<2;m++){
      f4 s01=*(LAS f4*)(mb+3072+(32*w4+8*q+4*m)*8), s23=*(LAS f4*)(mb+3072+(32*w4+8*q+4*m+2)*8);
      float mm[4]={s01[0],s01[2],s23[0],s23[2]}, ri[4]={s01[1],s01[3],s23[1],s23[3]};
      #pragma unroll
      for(int n=0;n<4;n++)
        #pragma unroll
        for(int e=0;e<4;e+=2){ u32 t=pkbf((acc[m][n][e]-mm[e]*csn[n])*ri[e],(acc[m][n][e+1]-mm[e+1]*csn[n])*ri[e+1]);
          t=pkbf(gelu(bflo(t)),gelu(bfhi(t))); zv[m][n][e]=bflo(t); zv[m][n][e+1]=bfhi(t); }
      #pragma unroll
      for(int e=0;e<4;e++){ float s=rowsum16((zv[m][0][e]+zv[m][1][e])*msk+(zv[m][2][e]+zv[m][3][e])); float mu=s*icnt;
        float d0=zv[m][0][e]-mu,d1=zv[m][1][e]-mu,d2=zv[m][2][e]-mu,d3=zv[m][3][e]-mu;
        su[4*m+e]=s; m2[4*m+e]=rowsum16((d0*d0+d1*d1)*msk+(d2*d2+d3*d3)); }
    }
    float ps=su[0],pm=m2[0];
    #pragma unroll
    for(int e=1;e<8;e++){ ps=(r==e)?su[e]:ps; pm=(r==e)?m2[e]:pm; }
    __amdgpu_buffer_rsrc_t rst=mkrs(p.ws+OFF_ST+grp*(14*512),14*512);
    if(r<8) __builtin_amdgcn_raw_buffer_store_b128(u4{__float_as_uint(ps),tag,__float_as_uint(pm),tag},rst,j*512+(8*q+r)*16,0,16);
    STAMP(2);
    u4 g[7]; const u32 go=(lane>>5)*(7*512)+(lane&31)*16;
    for(int spin=0;spin<(1<<20);spin++){
      bool ok=true;
      #pragma unroll
      for(int i=0;i<7;i++) g[i]=__builtin_amdgcn_raw_buffer_load_b128(rst,go+i*512,0,16);
      #pragma unroll
      for(int i=0;i<7;i++) ok=ok&&g[i][1]==tag&&g[i][3]==tag;
      if(__all(ok)) break;
      __builtin_amdgcn_s_sleep(1);
    }
    STAMP(3);
    if(j==0 && lane==0) __hip_atomic_store((GAS u32*)(p.ws+OFF_EP+grp*256),tag,__ATOMIC_RELAXED,__HIP_MEMORY_SCOPE_AGENT);
    float S=0.f;
    #pragma unroll
    for(int i=0;i<7;i++) S+=__uint_as_float(g[i][0]);
    S+=__shfl_xor(S,32); const float mean=S*(1.f/864.f); float M=0.f;
    #pragma unroll
    for(int i=0;i<7;i++){ const bool half=(i==6)&&(lane>=32); float cn=half?32.f:64.f, dm=__uint_as_float(g[i][0])*(half?(1.f/32.f):(1.f/64.f))-mean; M+=__uint_as_float(g[i][2])+cn*dm*dm; }
    M+=__shfl_xor(M,32);
    float m_,ri_; ln_fin(mean,M*(1.f/864.f),m_,ri_);
    const u32 s2=mb+4608+w4*256; if(lane<32) *(LAS f2*)(s2+lane*8)=f2{m_,ri_};
    WAITL0();
    float mm[8],ri[8];
    #pragma unroll
    for(int e=0;e<4;e++){ f4 t=*(LAS f4*)(s2+(8*q+2*e)*8); mm[2*e]=t[0]; ri[2*e]=t[1]; mm[2*e+1]=t[2]; ri[2*e+1]=t[3]; }
    const int ft0=(j==13)?-2:2+4*j;
    #pragma unroll
    for(int n=0;n<4;n++){ u4 o;
      #pragma unroll
      for(int e=0;e<4;e++){ int m=e>>1,c=(e&1)*2; u32 t=pkbf(zv[m][n][c]-mm[2*e],zv[m][n][c+1]-mm[2*e+1]); o[e]=pkbf(bflo(t)*ri[2*e],bfhi(t)*ri[2*e+1]); }
      if(ft0+n>=0) *(u4*)(p.ws+OFF_Z2F+(u32)(grp*54+ft0+n)*1024+lane*16)=o; }
    STAMP(4);
  } else {
    #pragma unroll
    for(int m=0;m<2;m++){ f2 st=*(LAS f2*)(mb+3072+(32*w4+16*m+r)*8);
      #pragma unroll
      for(int n=0;n<4;n++){ f4 c4=*(LAS f4*)(mb+4096+256+(16*n+4*q)*4); u2 o;
        #pragma unroll
        for(int e=0;e<4;e+=2){ u32 t=pkbf((acc[m][n][e]-st[0]*c4[e])*st[1],(acc[m][n][e+1]-st[0]*c4[e+1])*st[1]); o[e>>1]=pkbf(gelu(bflo(t)),gelu(bfhi(t))); }
        if(4*j+n<54) *(u2*)(p.ws+OFF_Z1T+(u32)((rt*8+2*w4+m)*54+4*j+n)*512+lane*8)=o; } }
    if(j<10){
      float mm[8],ri[8];
      #pragma unroll
      for(int e=0;e<4;e++){ f4 t=*(LAS f4*)(mb+3072+(32*w4+8*q+2*e)*8); mm[2*e]=t[0]; ri[2*e]=t[1]; mm[2*e+1]=t[2]; ri[2*e+1]=t[3]; }
      const u32 sb=smb+(9&3)*A_STG;
      #pragma unroll
      for(int c=0;c<4;c++){ u4 o;
        #pragma unroll
        for(int t=0;t<2;t++){ u32 row=32*w4+8*q+4*t+(r>>2); u2 v=ldstr(sb+row*128+(((2*c+((r>>1)&1))^swzA(row))<<4)+8*(r&1));
          #pragma unroll
          for(int e=0;e<2;e++){ int i=4*t+2*e; u32 d=pkbf(bflo(v[e])-mm[i],bfhi(v[e])-mm[i+1]); d=pkbf(bflo(d)*ri[i],bfhi(d)*ri[i+1]); o[2*t+e]=pkhf(bflo(d),bfhi(d)); } }
        *(u4*)(p.ws+OFF_XNT+(u32)(64*kl+16*c+r)*4096+(rt*128+32*w4+8*q)*2)=o; }
    } else {
      #pragma unroll
      for(int i=0;i<5;i++){ int f=wofi+256*i; if(f<1080) *(u4*)(p.ws+OFF_WOF+(u32)f*1024+lane*16)=wo[i]; }
    }
  }
  #undef STW
}

#define ROT ((2*(ct&3)+(rt&7))&7)
constexpr u32 B_RING=24576, B_CONV=4*B_RING, CV_SZ=20800, B_LDS=B_CONV+3*CV_SZ;

DI void conv_wave(const Params& p,int d,u32 cb,int lane){
  const char* kx=p.ws+OFF_KX+d*KXP+64; const char* un=p.ws+OFF_XNT+d*4096;
  u4 kd[5]; u2 kp[5]; u4 uu[4];
  #pragma unroll
  for(int i=0;i<5;i++){ int x=lane+64*i; if(x>259) x=259; kd[i]=*(const u4*)(kx+x*16); kp[i]=*(const u2*)(kx+x*16-8); }
  #pragma unroll
  for(int i=0;i<4;i++) uu[i]=*(const u4*)(un+(lane+64*i)*16);
  const float ksc=*(const float*)(p.ws+OFF_KSC+d*4), dsk=bf1(p.dsk[d]);
  const u32 ub=cb+4*4160;
  #pragma unroll
  for(int i=0;i<5;i++){ int x=lane+64*i; if(x>259) x=259; u32 a=cb+x*16;
    u32 a0=__builtin_amdgcn_alignbit(kp[i][1],kp[i][0],16), a1=__builtin_amdgcn_alignbit(kd[i][0],kp[i][1],16), a2=__builtin_amdgcn_alignbit(kd[i][1],kd[i][0],16),
        a3=__builtin_amdgcn_alignbit(kd[i][2],kd[i][1],16), a4=__builtin_amdgcn_alignbit(kd[i][3],kd[i][2],16);
    ldsw128(a,kd[i]); ldsw128(a+4160,u4{a1,a2,a3,a4}); ldsw128(a+2*4160,u4{kp[i][1],kd[i][0],kd[i][1],kd[i][2]}); ldsw128(a+3*4160,u4{a0,a1,a2,a3}); }
  #pragma unroll
  for(int i=0;i<4;i++){ int x=lane+64*i; ldsw128(ub+(x&3)*1040+((x>>2)+1)*16,uu[i]); }
  if(lane<4) ldsw128(ub+lane*1040,u4{0,0,0,0});
  WAITL0();
  const int r=lane&31,h=lane>>5;
  u32 ka=cb+(r&3)*4160+16*h-8*(r>>2)+4096-2048;
  u32 u1=ub+h*1040+(r+33)*16;
  int c0=r;
  f16v y0={},y1={};
  #pragma unroll 2
  for(int dl=0;dl<32;dl++){
    u4 af[4],bfr[4];
    #pragma unroll
    for(int i=0;i<4;i++){ u32 o=(i<2?2048u:0u)+(i&1)*32u; u2 lo,hi;
      asm volatile("ds_read_b64 %0, %1 offset:%2" : "=v"(lo) : "v"(ka), "n"(o)); asm volatile("ds_read_b64 %0, %1 offset:%2" : "=v"(hi) : "v"(ka), "n"(o+8));
      af[i]=u4{lo[0],lo[1],hi[0],hi[1]}; }
    u32 u0=ub+h*1040+((c0<-1?-1:c0)+1)*16;
    asm volatile("ds_read_b128 %0, %1" : "=v"(bfr[0]) : "v"(u0)); asm volatile("ds_read_b128 %0, %1 offset:2080" : "=v"(bfr[1]) : "v"(u0));
    asm volatile("ds_read_b128 %0, %1" : "=v"(bfr[2]) : "v"(u1)); asm volatile("ds_read_b128 %0, %1 offset:2080" : "=v"(bfr[3]) : "v"(u1));
    asm volatile("s_waitcnt lgkmcnt(0)" : "+v"(af[0]),"+v"(af[1]),"+v"(af[2]),"+v"(af[3]),"+v"(bfr[0]),"+v"(bfr[1]),"+v"(bfr[2]),"+v"(bfr[3]));
    y0=mfma32h(af[0],bfr[0],y0); y1=mfma32h(af[0],bfr[2],y1); y0=mfma32h(af[1],bfr[1],y0); y1=mfma32h(af[1],bfr[3],y1);
    y1=mfma32h(af[2],bfr[0],y1); y1=mfma32h(af[3],bfr[1],y1);
    ka-=64; u1-=16; c0-=1;
  }
  char* at=p.ws+OFF_AT+d*4096;
  #pragma unroll
  for(int t=0;t<2;t++)
    #pragma unroll
    for(int g=0;g<4;g++){ u2 uv=ldsr64(ub+g*1040+(32*t+r+1)*16+8*h); u2 o;
      #pragma unroll
      for(int e=0;e<2;e++){ float ya=(t?y1:y0)[4*g+2*e]*ksc, yb=(t?y1:y0)[4*g+2*e+1]*ksc; u32 yy=pkbf(ya,yb), sk=pkbf(hlo(uv[e])*dsk,hhi(uv[e])*dsk); o[e]=pkbf(bflo(yy)+bflo(sk),bfhi(yy)+bfhi(sk)); }
      *(u2*)(at+((32*t+r)*32+8*g+4*h)*2)=o; }
}

__global__ __launch_bounds__(512,2) void kB(Params p){
  extern __shared__ __attribute__((aligned(16))) char sm[];
  const u32 smb=(u32)(size_t)(LAS char*)sm;
  const int b=blockIdx.x, xc=b&7, idx=b>>3;
  const int rt=8*(xc>>1)+(idx&7), ct=4*(xc&1)+(idx>>3);
  const int w=uni(threadIdx.x>>6), lane=threadIdx.x&63, r=lane&15, q=lane>>4;
  #define STW 0
  STAMP(8);
  #undef STW
  if(w>=4){
    const int d=b+256*(w-4);
    if(d<640) conv_wave(p,d,smb+B_CONV+(w-4)*CV_SZ,lane);
    #define STW 4
    STAMP(12);
    #undef STW
    bar();
    return;
  }
  #define STW 0
  const int ft0=ct<7?7*ct:47, rot=ROT;
  const u32 rb=smb+w*B_RING;
  const char* wa=(const char*)p.wsp+(size_t)rt*64*4096+w*1024;
  const char* zb=p.ws+OFF_Z2F+(u32)(16*w*54+ft0+3)*1024;
  u32 voA[2]; { u32 lr=lane>>3,c=lane&7; voA[0]=lr*4096+((c^(lr>>1))<<4); voA[1]=lr*4096+((c^(4|(lr>>1)))<<4); }
  const u32 voB=lane*16;
  u32 la[2];
  la[0]=rb+r*128+((q^(r>>1))<<4); la[1]=rb+r*128+(((4+q)^(r>>1))<<4);
  f4 acc[4][7]={}; u4 B[3][7]; u4 A[2][4];
  auto issueA=[&](int s){ const char* sb=wa+((s+rot)&7)*128; u32 sl=rb+(s%3)*8192;
    #pragma unroll
    for(int g=0;g<8;g++) glds(sb+g*8*4096,voA[g&1],sl+(((g>>1)-w)&3)*2048+(g&1)*1024); };
  auto issueA1=[&](int s,int g){ const char* sb=wa+((s+rot)&7)*128; u32 sl=rb+(s%3)*8192; glds(sb+g*8*4096,voA[g&1],sl+(((g>>1)-w)&3)*2048+(g&1)*1024); };
  auto bbase=[&](int h){ return zb+(u32)(((((h>>1)+rot)&7)<<1)+(h&1))*(54*1024); };
  #define LOADB(dst,base,n) gload<((n)-3)*1024>(dst,voB,base)
  issueA(0);
  { const char* s=bbase(0); sfor<7>([&](auto n){ LOADB(B[0][n],s,n.value); }); }
  issueA(1);
  { const char* s=bbase(1); sfor<7>([&](auto n){ LOADB(B[1][n],s,n.value); }); }
  issueA(2);
  { const char* s=bbase(2); sfor<7>([&](auto n){ LOADB(B[2][n],s,n.value); }); }
  WAITV(37);
  #pragma unroll
  for(int m=0;m<4;m++) A[0][m]=ldsr128(la[0]+m*2048);
  sfor<16>([&](auto hc){
    constexpr int h=hc.value, S=h%3, P=h&1;
    constexpr int cnt = h==0? 30 : h==1? 22 : h==2? 22 :
        ((h-2+3<16?7:0)+(h-1+3<16?7:0) + ((((h-2)&1)&&((h-2)/2+3<8))?8:0) + ((((h-1)&1)&&((h-1)/2+3<8))?8:0));
    waitB<cnt>(B[S]); waitA(A[P]);
    if constexpr(h+1<16){
      #pragma unroll
      for(int m=0;m<4;m++) A[P^1][m]=ldsr128(la[(h+1)&1]+m*2048+(((h+1)>>1)%3)*8192);
    }
    if constexpr((h&1)&&(h/2+3<8)) issueA(h/2+3);
    const char* s=bbase(h+3<16?h+3:0);
    sfor<7>([&](auto n){
      #pragma unroll
      for(int m=0;m<4;m++){
        acc[m][n]=mfma16(B[S][n],A[P][m],acc[m][n]); }
      __builtin_amdgcn_sched_barrier(0);
      if constexpr(h+3<16) LOADB(B[S][n],s,n.value);
      __builtin_amdgcn_sched_barrier(0);
    });
  });
  STAMP(9);
  const int tt=rt*4+w;
  u2 z1[7];
  #pragma unroll
  for(int n=0;n<7;n++) z1[n]=*(const u2*)(p.ws+OFF_Z1T+(u32)(tt*54+ft0+n)*512+lane*8);
  const float bs=bf1(p.bsp[tt*16+r]);
  #pragma unroll
  for(int m=1;m<4;m++)
    #pragma unroll
    for(int n=0;n<7;n++) *(LAS f4*)(rb+((m-1)*7+n)*1024+lane*16)=acc[m][n];
  bar();
  #pragma unroll
  for(int m=1;m<4;m++){ u32 src=smb+((w-m)&3)*B_RING+(m-1)*7168+lane*16;
    #pragma unroll
    for(int n=0;n<7;n++) acc[0][n]+=*(LAS f4*)(src+n*1024); }
  STAMP(10);
  #pragma unroll
  for(int n=0;n<7;n++){ u2 o;
    #pragma unroll
    for(int e=0;e<2;e++){ u32 g=pkbf(acc[0][n][2*e],acc[0][n][2*e+1]); g=pkbf(bflo(g)+bs,bfhi(g)+bs); o[e]=pkbf(bflo(z1[n][e])*bflo(g),bfhi(z1[n][e])*bfhi(g)); }
    const int ft=ft0+n;
    if(ct<7||n>=2) *(u2*)(p.ws+OFF_HF+(u32)(tt*27+(ft>>1))*1024+lane*16+(ft&1)*8)=o; }
  STAMP(11);
  #undef STW
}

constexpr u32 C_AT=135*1024, C_LDS=C_AT+10240;
__global__ __launch_bounds__(512,2) void kC(Params p){
  extern __shared__ __attribute__((aligned(16))) char sm[];
  const u32 smb=(u32)(size_t)(LAS char*)sm;
  const int b=blockIdx.x, xc=b&7, idx=b>>3;
  const int rt=8*(xc>>1)+(idx&7), ct=4*(xc&1)+(idx>>3);
  const int w=uni(threadIdx.x>>6), lane=threadIdx.x&63, r=lane&15, q=lane>>4, m=w&3, kh=w>>2;
  #define STW 0
  STAMP(16);
  const char* wf=p.ws+OFF_WOF+ct*(135*1024);
  for(int i=w;i<135;i+=8) glds(wf+i*1024,lane*16,smb+i*1024);
  for(int i=w;i<10;i+=8) glds(p.ws+OFF_AT+(u32)(80*ct+8*i)*4096+rt*128,(lane>>3)*4096+(lane&7)*16,smb+C_AT+i*1024);
  u4 hf[14]; const char* hb=p.ws+OFF_HF+(u32)((rt*4+m)*27+kh*13)*1024+lane*16;
  #pragma unroll
  for(int k=0;k<14;k++) hf[k]=*(const u4*)(hb+k*1024);
  const int t=rt*64+m*16+r;
  u2 xv[5];
  #pragma unroll
  for(int n=0;n<5;n++) xv[n]=*(const u2*)(p.x+t*640+80*ct+16*n+4*q);
  WAITV(0); bar();
  STAMP(17);
  f4 acc[5]={};
  if(kh) hf[0]=u4{0,0,0,0};
  #pragma unroll
  for(int k=0;k<14;k++)
    #pragma unroll
    for(int n=0;n<5;n++) acc[n]=mfma16(ldsr128(smb+((kh*13+k)*5+n)*1024+lane*16),hf[k],acc[n]);
  bar();
  if(kh){
    #pragma unroll
    for(int n=0;n<5;n++) *(LAS f4*)(smb+(m*5+n)*1024+lane*16)=acc[n];
  }
  bar();
  if(!kh){
    #pragma unroll
    for(int n=0;n<5;n++){ f4 o=acc[n]+*(LAS f4*)(smb+(m*5+n)*1024+lane*16);
      u2 av=ldstr(smb+C_AT+(16*n+4*q+(r>>2))*128+(16*m+4*(r&3))*2); u2 res;
      #pragma unroll
      for(int e=0;e<2;e++){ u32 xa=pkbf(bflo(xv[n][e])+bflo(av[e]),bfhi(xv[n][e])+bfhi(av[e])), ob=pkbf(o[2*e],o[2*e+1]); res[e]=pkbf(bflo(xa)+bflo(ob),bfhi(xa)+bfhi(ob)); }
      *(u2*)(p.out+t*640+80*ct+16*n+4*q)=res; }
  }
  STAMP(18);
  #undef STW
}

extern "C" void kernel_launch(void* const* d_in,const int* in_sizes,int n_in,void* d_out,int out_size,void* d_ws,size_t ws_size,hipStream_t stream){
  Params p;
  p.x=(const u16*)d_in[0]; p.lar=(const u16*)d_in[1]; p.aim=(const u16*)d_in[2]; p.cre=(const u16*)d_in[3]; p.cim=(const u16*)d_in[4];
  p.ldt=(const u16*)d_in[5]; p.dsk=(const u16*)d_in[6]; p.win=(const u16*)d_in[8]; p.wsp=(const u16*)d_in[9]; p.bsp=(const u16*)d_in[10]; p.wout=(const u16*)d_in[11];
  p.out=(u16*)d_out; p.ws=(char*)d_ws;
  (void)hipFuncSetAttribute((const void*)kA,hipFuncAttributeMaxDynamicSharedMemorySize,A_LDS);
  (void)hipFuncSetAttribute((const void*)kB,hipFuncAttributeMaxDynamicSharedMemorySize,B_LDS);
  (void)hipFuncSetAttribute((const void*)kC,hipFuncAttributeMaxDynamicSharedMemorySize,C_LDS);
  kA<<<256,512,A_LDS,stream>>>(p);
  kB<<<256,512,B_LDS,stream>>>(p);
  kC<<<256,512,C_LDS,stream>>>(p);
}
```

```cpp
#include <hip/hip_runtime.h>
#include <utility>
#pragma clang diagnostic ignored "-Wint-to-pointer-cast"
typedef unsigned short u16;
typedef unsigned int u32;
typedef unsigned long long u64;
typedef __attribute__((ext_vector_type(8))) __bf16 bf8;
typedef __attribute__((ext_vector_type(8))) _Float16 hf8;
typedef __attribute__((ext_vector_type(2))) __bf16 bf2;
typedef __attribute__((ext_vector_type(2))) _Float16 hf2;
typedef __attribute__((ext_vector_type(2))) float f2;
typedef __attribute__((ext_vector_type(4))) float f4;
typedef __attribute__((ext_vector_type(16))) float f16v;
typedef __attribute__((ext_vector_type(4))) u32 u4;
typedef __attribute__((ext_vector_type(2))) u32 u2;
typedef __attribute__((ext_vector_type(4))) short s4;
#define LAS __attribute__((address_space(3)))
#define GAS __attribute__((address_space(1)))
#define DI __device__ __forceinline__

constexpr u32 OFF_Z2F = 0;
constexpr u32 OFF_Z1T = OFF_Z2F + 64*54*1024;
constexpr u32 OFF_XNT = OFF_Z1T + 128*27*1024;
constexpr u32 OFF_WOF = OFF_XNT + 640*4096;
constexpr u32 OFF_HF  = OFF_WOF + 8*27*5*1024;
constexpr u32 OFF_AT  = OFF_HF + 128*27*1024;
constexpr u32 OFF_ST  = OFF_AT + 640*4096;
constexpr u32 OFF_RS  = OFF_ST + 16*14*2048+8192;
constexpr u32 OFF_EP  = OFF_RS + 2048*16;
constexpr u32 OFF_F2  = OFF_EP + 256;
constexpr u32 OFF_F1  = OFF_F2 + 256*128;
constexpr u32 OFF_FX  = OFF_F1 + 256*128;
constexpr u32 OFF_FW  = OFF_FX + 32*128;
constexpr u32 OFF_FH  = OFF_FW + 32*128;
constexpr u32 OFF_FA  = OFF_FH + 256*128;
constexpr u32 WS_END  = OFF_FA + 256*128;

struct Params {
  const u16 *x, *lar, *aim, *cre, *cim, *ldt, *dsk, *win, *wsp, *bsp, *wout;
  u16* out; char* ws;
};
#define STAMP(id,wv) do{}while(0)
template<class F,int... I> DI void sfor_impl(F&& f, std::integer_sequence<int,I...>){ (f(std::integral_constant<int,I>{}),...); }
template<int N,class F> DI void sfor(F&& f){ sfor_impl(f, std::make_integer_sequence<int,N>{}); }

DI float bflo(u32 v){ return __uint_as_float(v<<16); }
DI float bfhi(u32 v){ return __uint_as_float(v&0xffff0000u); }
DI float bf1(u16 v){ return __uint_as_float(((u32)v)<<16); }
DI u32 pkbf(float a,float b){ f2 v={a,b}; return __builtin_bit_cast(u32,__builtin_convertvector(v,bf2)); }
DI u32 pkhf(float a,float b){ f2 v={a,b}; return __builtin_bit_cast(u32,__builtin_convertvector(v,hf2)); }
DI float rbf(float a){ return bflo(pkbf(a,a)); }
DI float hlo(u32 v){ return (float)__builtin_bit_cast(hf2,v)[0]; }
DI float hhi(u32 v){ return (float)__builtin_bit_cast(hf2,v)[1]; }
DI float dot2(u32 a,u32 b,float c){ return __builtin_amdgcn_fdot2_f32_bf16(__builtin_bit_cast(bf2,a),__builtin_bit_cast(bf2,b),c,false); }
DI f4 mfma16(u4 a,u4 b,f4 c){ return __builtin_amdgcn_mfma_f32_16x16x32_bf16(__builtin_bit_cast(bf8,a),__builtin_bit_cast(bf8,b),c,0,0,0); }
DI f16v mfma32h(u4 a,u4 b,f16v c){ return __builtin_amdgcn_mfma_f32_32x32x16_f16(__builtin_bit_cast(hf8,a),__builtin_bit_cast(hf8,b),c,0,0,0); }
DI u4 ldsr128(u32 a){ return *(LAS u4*)a; }
DI u2 ldsr64(u32 a){ return *(LAS u2*)a; }
DI void ldsw128(u32 a,u4 v){ *(LAS u4*)a=v; }
DI u2 ldstr(u32 a){ return __builtin_bit_cast(u2,__builtin_amdgcn_ds_read_tr16_b64_v4i16((LAS s4*)a)); }
DI u32 uni(u32 v){ return __builtin_amdgcn_readfirstlane(v); }

template<int OFF> DI void gload(u4& dst,u32 voff,const char* base){ asm volatile("global_load_dwordx4 %0, %1, %2 offset:%3 sc1" : "=v"(dst) : "v"(voff), "s"(base), "n"(OFF)); }
template<int OFF> DI void gload_t(u4& dst,u32 voff,const char* base){ asm volatile("global_load_dwordx4 %0, %1, %2 offset:%3 sc1" : "+v"(dst) : "v"(voff), "s"(base), "n"(OFF)); }
DI void mfma4(f4& c0,f4& c1,f4& c2,f4& c3,const u4& a,const u4 (&b)[4]){
  asm volatile("v_mfma_f32_16x16x32_bf16 %0, %4, %5, %0\n\tv_mfma_f32_16x16x32_bf16 %1, %4, %6, %1\n\tv_mfma_f32_16x16x32_bf16 %2, %4, %7, %2\n\tv_mfma_f32_16x16x32_bf16 %3, %4, %8, %3"
    : "+v"(c0),"+v"(c1),"+v"(c2),"+v"(c3) : "v"(a),"v"(b[0]),"v"(b[1]),"v"(b[2]),"v"(b[3])); }
template<int CNT> DI void waitB(u4 (&B)[7]){ asm volatile("s_waitcnt vmcnt(%7)" : "+v"(B[0]),"+v"(B[1]),"+v"(B[2]),"+v"(B[3]),"+v"(B[4]),"+v"(B[5]),"+v"(B[6]) : "n"(CNT) : "memory"); }
DI void waitA(u4 (&A)[4]){ asm volatile("s_waitcnt lgkmcnt(0)" : "+v"(A[0]),"+v"(A[1]),"+v"(A[2]),"+v"(A[3]) :: "memory"); }
#define WAITV(n) asm volatile("s_waitcnt vmcnt(%0)" :: "n"(n) : "memory")
#define WAITL0() asm volatile("s_waitcnt lgkmcnt(0)" ::: "memory")
#define SCHED() __builtin_amdgcn_sched_barrier(0)
DI void bar(){ asm volatile("s_waitcnt lgkmcnt(0)\n\ts_barrier" ::: "memory"); }
DI f2 unpk(u32 v){ return f2{bflo(v),bfhi(v)}; }
DI u32 pkbf2(f2 v){ return __builtin_bit_cast(u32,__builtin_convertvector(v,bf2)); }
DI u32 pkhf2(f2 v){ return __builtin_bit_cast(u32,__builtin_convertvector(v,hf2)); }
DI f2 fma2(f2 a,f2 b,f2 c){ return __builtin_elementwise_fma(a,b,c); }
DI f2 gelu2(f2 x){
  f2 u=x*fma2(x*x,f2{-0.10294324f,-0.10294324f},f2{-2.30220820f,-2.30220820f});
  f2 d=f2{__builtin_amdgcn_exp2f(u[0]),__builtin_amdgcn_exp2f(u[1])}+f2{1.f,1.f};
  return x*f2{__builtin_amdgcn_rcpf(d[0]),__builtin_amdgcn_rcpf(d[1])};
}
DI void ln_fin(float mean,float var,float& m,float& rinv){
  m=rbf(mean); float v=rbf(__builtin_fmaxf(var,0.f));
  float s=rbf(__builtin_amdgcn_sqrtf(rbf(v+1e-5f))); float r=__builtin_amdgcn_rcpf(s); rinv=__builtin_fmaf(__builtin_fmaf(-s,r,1.f),r,r);
}
typedef __amdgpu_buffer_rsrc_t rsrc_t;
DI void st16(rsrc_t rs,u32 off,u4 v){ __builtin_amdgcn_raw_buffer_store_b128(v,rs,off,0,16); }
DI void st8(rsrc_t rs,u32 off,u2 v){ __builtin_amdgcn_raw_buffer_store_b64(v,rs,off,0,16); }
DI void st4(rsrc_t rs,u32 off,u32 v){ __builtin_amdgcn_raw_buffer_store_b32(v,rs,off,0,16); }
DI u4 ld16(rsrc_t rs,u32 off){ return __builtin_amdgcn_raw_buffer_load_b128(rs,off,0,16); }
DI u2 ld8(rsrc_t rs,u32 off){ return __builtin_amdgcn_raw_buffer_load_b64(rs,off,0,16); }
DI u32 ld4(rsrc_t rs,u32 off){ return __builtin_amdgcn_raw_buffer_load_b32(rs,off,0,16); }
constexpr int SPIN=1<<17;
DI void pollw(rsrc_t rs,u32 off,u32 tag){
  #pragma unroll 1
  for(int s=0;s<SPIN;s++){ u32 v=ld4(rs,off); asm volatile("" ::: "memory"); if(__all(v==tag)) break; __builtin_amdgcn_s_sleep(2); }
}

DI f16v mfma32b(u4 a,u4 b,f16v c){ return __builtin_amdgcn_mfma_f32_32x32x16_bf16(__builtin_bit_cast(bf8,a),__builtin_bit_cast(bf8,b),c,0,0,0); }
DI u4 mksrd(const void* b){ u64 a=(u64)b; return u4{(u32)a,(u32)(a>>32)&0xffffu,0xffffffffu,0x00020000u}; }
template<int IMM> DI void setm0(u32 base){ asm volatile("s_add_u32 m0, %0, %1\n\ts_nop 0" :: "s"(base), "n"(IMM) : "scc","memory"); }
template<int OFF> DI void dma(u4 srd,u32 voff,u32 soff){ asm volatile("buffer_load_dwordx4 %0, %1, %2 offen offset:%3 sc1 lds" :: "v"(voff), "s"(srd), "s"(soff), "n"(OFF) : "memory"); }
DI void touch(u4 srd,u32 voff,u32 soff){ asm volatile("buffer_load_dword %0, %1, %2 offen sc1 lds" :: "v"(voff), "s"(srd), "s"(soff) : "memory"); }
template<int OFF> DI void gload8(u2& dst,u32 voff,const char* base){ asm volatile("global_load_dwordx2 %0, %1, %2 offset:%3 sc1" : "=v"(dst) : "v"(voff), "s"(base), "n"(OFF)); }
DI void gload2(u32& dst,u32 voff,const char* base){ asm volatile("global_load_ushort %0, %1, %2" : "=v"(dst) : "v"(voff), "s"(base)); }
DI u32 arrive(u32 a,int){ u32 o; asm volatile("s_mov_b64 exec, 1\n\tds_add_rtn_u32 %0, %1, %2\n\ts_mov_b64 exec, -1\n\ts_waitcnt lgkmcnt(0)" : "=&v"(o) : "v"(a), "v"(1u) : "memory"); return uni(o); }
DI void ldswait(u32 a,u32 n){ while(__hip_atomic_load((LAS u32*)a,__ATOMIC_RELAXED,__HIP_MEMORY_SCOPE_WORKGROUP)<n) __builtin_amdgcn_s_sleep(1); asm volatile("" ::: "memory"); }
DI float addf(float a,float b){ float o; asm("v_add_f32 %0, %1, %2" : "=v"(o) : "v"(a), "v"(b)); return o; }
DI u32 opq(u32 v){ asm volatile("" : "+v"(v)); return v; }

constexpr u32 STG=32768;
constexpr u32 RING=24576, CONV=4*RING, CVS=20800, UOF=16640, PM=163840-64, JNK=CONV+3*CVS+2560, LDS_TOTAL=163840;
constexpr u32 ATL=137*1024;
constexpr u32 C_G0=PM, C_G1=PM+4, C_Z2=PM+8, C_Z1=PM+12, C_RED=PM+16, C_H=PM+20, C_FREE=PM+24, C_CV=PM+28, C_Z1R=PM+32, C_KP=PM+48;
constexpr u32 CODE_BYTES=32768;
#define LA 3
#define TC_S 7
#define RS_S 7
#define KROT ((j+3*(rt&3))%10)
DI void signal(u32 a,int){ asm volatile("s_mov_b64 exec, 1\n\tds_add_u32 %0, %1\n\ts_mov_b64 exec, -1" :: "v"(a), "v"(1u) : "memory"); }
template<int OFF> DI void gload4(u32& dst,u32 voff,const char* base){ asm volatile("global_load_dword %0, %1, %2 offset:%3 sc1" : "=v"(dst) : "v"(voff), "s"(base), "n"(OFF)); }
template<int CNT> DI void waitG(u4& g){ asm volatile("s_waitcnt vmcnt(%1)" : "+v"(g) : "n"(CNT) : "memory"); }

DI f2 cmulp(f2 a,f2 b,f2 bs){ return fma2(f2{a[1],a[1]},bs,f2{a[0],a[0]}*b); }
DI f2 cpow_dir(float lr,float rv,float pw){ float e=__builtin_amdgcn_exp2f(lr*pw); float f=__builtin_amdgcn_fractf(rv*pw);
  return f2{e*__builtin_amdgcn_cosf(f),e*__builtin_amdgcn_sinf(f)}; }
DI u32 dup(u32 a){ return __builtin_amdgcn_perm(a,a,0x01000100u); }
DI u32 cat(u32 lo,u32 hi){ return __builtin_amdgcn_perm(hi,lo,0x05040100u); }
DI u32 lds16(u32 a){ return *(LAS u16*)a; }

template<class CB> DI float kgen_tab(u32 cb,int lane,const u32 (&pr)[6],int c,float (&kp)[11],CB&& mid){
  const float dt=rbf(__expf(bflo(pr[4])));
  const float cr[2]={bflo(pr[2]),bfhi(pr[2])}, cm[2]={bflo(pr[3]),bfhi(pr[3])};
  float mx=fmaxf(fmaxf(fabsf(cr[0]),fabsf(cm[0])),fmaxf(fabsf(cr[1]),fabsf(cm[1])));
  u32 eb=__float_as_uint(mx)&0x7f800000u;
  #define DPPMAX(c) eb=max(eb,(u32)__builtin_amdgcn_update_dpp(0,(int)eb,c,0xf,0xf,false))
  DPPMAX(0x128); DPPMAX(0x124); DPPMAX(0x122); DPPMAX(0x121);
  eb=max(max((u32)__builtin_amdgcn_readlane(eb,0),(u32)__builtin_amdgcn_readlane(eb,16)),max((u32)__builtin_amdgcn_readlane(eb,32),(u32)__builtin_amdgcn_readlane(eb,48))); eb=eb<0x01000000u?0x3f800000u:(eb>0x7e000000u?0x7e000000u:eb);
  const float ksc=2.f*dt*__uint_as_float(eb), isc=__uint_as_float(0x7f000000u-eb);
  const int r=lane&31,h=lane>>5;
  const u32 TP=272, TA=cb, TB=cb+22*TP;
  f16v acc={};
  {
    float l_a=c?bfhi(pr[0]):bflo(pr[0]), i_m=c?bfhi(pr[1]):bflo(pr[1]), c_r=c?cr[1]:cr[0], c_i=c?cm[1]:cm[0];
    const float lr=(-rbf(__expf(l_a))*dt)*1.44269504f, rv=(i_m*dt)*0.15915494f;
    const f2 z1=cpow_dir(lr,rv,1.f), z8=cpow_dir(lr,-rv,8.f), zs=cpow_dir(lr,-rv,16.f), zk=cpow_dir(lr,rv,256.f);
    const f2 z1s={-z1[1],z1[0]}, zss={-zs[1],zs[0]}, zks={-zk[1],zk[0]};
    const u32 wa=TA+lane*4, wb=TB+lane*4;
    #define PUT(base,row,v) do{ *(LAS u32*)((base)+(row)*TP)=pkhf2(v); }while(0)
    f2 e={1.f,0.f}, o=z8;
    #pragma unroll
    for(int k=0;k<16;k++){ PUT(wb,2*k,e); PUT(wb,2*k+1,o); if(k<15){ e=k?cmulp(e,zs,zss):zs; o=cmulp(o,zs,zss);} }
    mid();
    f2 b0={c_r*isc,c_i*isc}; PUT(wa,0,b0);
    f2 t=cmulp(b0,zk,zks); PUT(wa,8,t);
    f2 b1=cmulp(b0,z1,z1s); PUT(wa,1,b1); t=cmulp(t,zk,zks); PUT(wa,10,t);
    f2 b2=cmulp(b1,z1,z1s); PUT(wa,2,b2); t=cmulp(t,zk,zks); PUT(wa,11,t);
    f2 b3=cmulp(b2,z1,z1s); PUT(wa,3,b3); t=cmulp(t,zk,zks); PUT(wa,16,t);
    f2 b4=cmulp(b3,z1,z1s); PUT(wa,4,b4); t=cmulp(t,zk,zks); PUT(wa,17,t);
    f2 b5=cmulp(b4,z1,z1s); PUT(wa,5,b5); t=cmulp(t,zk,zks); PUT(wa,20,t);
    f2 b6=cmulp(b5,z1,z1s); PUT(wa,6,b6); t=cmulp(t,zk,zks); PUT(wa,21,t);
    f2 b7=cmulp(b6,z1,z1s); PUT(wa,7,b7); t=cmulp(t,zk,zks); PUT(wa,18,t);
    f2 s=cmulp(b4,zk,zks); PUT(wa,12,s); t=cmulp(b2,zk,zks); PUT(wa,9,t);
    s=cmulp(s,zk,zks); PUT(wa,14,s); t=cmulp(b6,zk,zks); PUT(wa,13,t);
    s=cmulp(s,zk,zks); PUT(wa,15,s);
    #undef PUT
    WAITL0();
    const int ra=(r>21||r==19)?0:r;
    #pragma unroll
    for(int ks=0;ks<8;ks++){ u4 fa=ldsr128(TA+ra*TP+ks*32+h*16), fb=ldsr128(TB+r*TP+ks*32+h*16); acc=mfma32h(fa,fb,acc); }
  }
  #pragma unroll
  for(int i=0;i<11;i++) kp[i]=acc[i];
  return ksc;
}
DI void kgen_exp(u32 cb,int lane,const float (&acc)[11]){
  const int r=lane&31,h=lane>>5;
  { const u32 a4=cb+r*16+h*8, a5=cb+r*16+h*1024;
    #define PK(addr,base,reg) do{ *(LAS _Float16*)((addr)+2*(base))=(_Float16)acc[reg]; }while(0)
    PK(a4,0,0);PK(a4,1,1);PK(a4,2,2);PK(a4,3,3); PK(a4,256,4);PK(a4,258,5);PK(a4,512,6);PK(a4,768,7);
    PK(a5,1024,8);PK(a5,1280,9);PK(a5,2048,10);
    #undef PK
  }
  WAITL0();
  { const u32 g0=cb+2*(2048-8*lane);
    u32 a0=lds16(g0), b0=lds16(g0-16), a1=lds16(g0-1024), b1=lds16(g0-1024-16);
    u32 f2_=lds16(g0-2048+(lane?8:0)), a2=lds16(g0-2048), m2=lds16(g0-2048-8), b2=lds16(g0-2048-16);
    u32 e[11];
    #pragma unroll
    for(int j=0;j<11;j++) e[j]=lds16(cb+2u*(u32)rbf((float)(512-8*lane+3-j)));
    u32 k01=*(LAS u32*)cb, k23=*(LAS u32*)(cb+4);
    WAITL0();
    const u32 wo=cb+lane*16;
    { u32 aa=dup(a0),bb=dup(b0),c=(lane&1)?b0:a0, cbv=cat(c,b0), ac=cat(a0,c);
      ldsw128(wo,u4{aa,aa,cbv,bb}); ldsw128(wo+4160,u4{aa,aa,ac,bb}); ldsw128(wo+8320,u4{aa,aa,aa,cbv}); ldsw128(wo+12480,u4{aa,aa,aa,ac}); }
    { u32 aa=dup(a1),bb=dup(b1),c=(lane&1)?b1:a1, cbv=cat(c,b1), ac=cat(a1,c);
      ldsw128(wo+1024,u4{aa,aa,cbv,bb}); ldsw128(wo+1024+4160,u4{aa,aa,ac,bb}); ldsw128(wo+1024+8320,u4{aa,aa,aa,cbv}); ldsw128(wo+1024+12480,u4{aa,aa,aa,ac}); }
    { u32 aa=dup(a2),mm=dup(m2),bb=dup(b2),am=cat(a2,m2),fa=cat(f2_,a2),mb=cat(m2,b2);
      ldsw128(wo+2048,u4{aa,am,mm,bb}); ldsw128(wo+2048+4160,u4{aa,aa,mm,mb}); ldsw128(wo+2048+8320,u4{aa,aa,am,mm}); ldsw128(wo+2048+12480,u4{fa,aa,aa,mm}); }
    { u32 o0=cat(e[0],e[1]),o1=cat(e[2],e[3]),o2=cat(e[4],e[5]),o3=cat(e[6],e[7]),o4=cat(e[8],e[9]);
      u32 p1=cat(e[1],e[2]),d0=cat(e[3],e[4]),d1=cat(e[5],e[6]),d2=cat(e[7],e[8]),d3=cat(e[9],e[10]);
      ldsw128(wo+3072,u4{d0,d1,d2,d3}); ldsw128(wo+3072+4160,u4{o1,o2,o3,o4}); ldsw128(wo+3072+8320,u4{p1,d0,d1,d2}); ldsw128(wo+3072+12480,u4{o0,o1,o2,o3}); }
    if(lane<4){ if(lane){k01=0;k23=0;}
      u32 d0=k01&0xffffu, o1=__builtin_amdgcn_alignbit(k01,k01,16), o0=__builtin_amdgcn_alignbit(k23,k23,16), p1=(k23&0xffffu)|(k01&0xffff0000u);
      ldsw128(wo+4096,u4{d0,0,0,0}); ldsw128(wo+4096+4160,u4{o1,0,0,0}); ldsw128(wo+4096+8320,u4{p1,d0,0,0}); ldsw128(wo+4096+12480,u4{o0,o1,0,0}); }
  }
}
DI void conv_wave(const Params& p,rsrc_t rs,int d,u32 cb,int lane,float ksc,float dsk,const u4 (&uu)[4]){
  const int r=lane&31,h=lane>>5;
  const u32 ub=cb+UOF;
  #pragma unroll
  for(int i=0;i<4;i++){ int x=lane+64*i; ldsw128(ub+(x&3)*1040+((x>>2)+1)*16,uu[i]); }
  if(lane<4) ldsw128(ub+lane*1040,u4{0,0,0,0});
  WAITL0();
  u32 ka=cb+(r&3)*4160+16*h-8*(r>>2)+2048;
  u32 u1=ub+h*1040+(r+33)*16;
  const u32 uz=ub+h*1040;
  f16v y0={},y1={};
  u2 AL[2][4],AH[2][4]; u4 BF[2][4];
  #define RDA(S,i,o) asm volatile("ds_read_b64 %0, %2 offset:%3\n\tds_read_b64 %1, %2 offset:%4" : "=&v"(AL[S][i]),"=&v"(AH[S][i]) : "v"(ka), "n"(o), "n"((o)+8))
  #define RDB(S,i,ad,o) asm volatile("ds_read_b128 %0, %1 offset:%2" : "=v"(BF[S][i]) : "v"(ad), "n"(o))
  #define CV_WT(S) asm volatile("s_waitcnt lgkmcnt(0)" : "+v"(AL[S][0]),"+v"(AH[S][0]),"+v"(AL[S][1]),"+v"(AH[S][1]),"+v"(AL[S][2]),"+v"(AH[S][2]),"+v"(AL[S][3]),"+v"(AH[S][3]), \
      "+v"(BF[S][0]),"+v"(BF[S][1]),"+v"(BF[S][2]),"+v"(BF[S][3]))
  #define CV_A(S,i) u4{AL[S][i][0],AL[S][i][1],AH[S][i][0],AH[S][i][1]}
  #define CV_STEP(C,N) do{ u32 u0=u1-512; u0=((int)u0<(int)uz)?uz:u0; \
      y0=mfma32h(CV_A(C,0),BF[C][0],y0); SCHED(); RDA(N,0,2048); RDB(N,0,u0,0); RDB(N,2,u1,0); SCHED(); \
      y1=mfma32h(CV_A(C,0),BF[C][2],y1); SCHED(); RDA(N,1,2080); RDB(N,1,u0,2080); RDB(N,3,u1,2080); SCHED(); \
      y0=mfma32h(CV_A(C,1),BF[C][1],y0); SCHED(); RDA(N,2,0); RDA(N,3,32); ka-=64; u1-=16; SCHED(); \
      y1=mfma32h(CV_A(C,1),BF[C][3],y1); y1=mfma32h(CV_A(C,2),BF[C][0],y1); y1=mfma32h(CV_A(C,3),BF[C][1],y1); SCHED(); CV_WT(N); }while(0)
  { u32 u0=u1-512; RDA(0,0,2048); RDA(0,1,2080); RDA(0,2,0); RDA(0,3,32); RDB(0,0,u0,0); RDB(0,1,u0,2080); RDB(0,2,u1,0); RDB(0,3,u1,2080); ka-=64; u1-=16; CV_WT(0); }
  #pragma unroll 1
  for(int dl=0;dl<16;dl++){
    CV_STEP(0,1);
    if(dl==15){ ka+=64; u1+=16; }
    CV_STEP(1,0);
  }
  #pragma unroll
  for(int t=0;t<2;t++){ u2 o[4];
    #pragma unroll
    for(int g=0;g<4;g++){ u2 uv=ldsr64(ub+g*1040+(32*t+r+1)*16+8*h);
      #pragma unroll
      for(int e=0;e<2;e++){ f2 yv=f2{(t?y1:y0)[4*g+2*e],(t?y1:y0)[4*g+2*e+1]}*ksc; o[g][e]=pkbf2(fma2(f2{hlo(uv[e]),hhi(uv[e])},f2{dsk,dsk},yv)); } }
    #pragma unroll
    for(int k=0;k<4;k+=2){ auto sx=__builtin_amdgcn_permlane32_swap(o[k][0],o[k+1][0],false,false); auto sy=__builtin_amdgcn_permlane32_swap(o[k][1],o[k+1][1],false,false);
      st16(rs,OFF_AT+d*4096+(32*t+r)*64+h*16+16*k,u4{sx[0],sy[0],sx[1],sy[1]}); } }
  WAITV(0);
}

struct Frag { u4 x[4], w[4][2]; };

__global__ __launch_bounds__(512,2) void fused(Params p){
  extern __shared__ __attribute__((aligned(16))) char sm[];
  const u32 smb=(u32)(size_t)(LAS char*)sm;
  u64 pc0; asm volatile("s_getpc_b64 %0" : "=s"(pc0));
  const int b=blockIdx.x, xc=b&7, idx=b>>3;
  const int tid=threadIdx.x, w=uni(tid>>6); int lane=tid&63;
  const rsrc_t rs=__builtin_amdgcn_make_buffer_rsrc(p.ws,0,WS_END,0x00020000);
  const u4 srdS=mksrd(p.ws);
  const int rtB=8*(xc>>1)+(idx&7), ct=4*(xc&1)+(idx>>3);
  const bool hask=idx<28 && idx+28*(w&3)<80, hasc=hask&&w>=4;
  const int dch=80*xc+(hask? idx+28*(w&3) : 0);
  const u32 cb=smb+CONV+(w&3)*CVS;
  const int rt=4*(xc>>1)+(idx&3), j=7*(xc&1)+(idx>>2), mi=w&3, g1=w>>2;
  u32 tag; u4 uu[4]={}; float ksc=0.f, dsk=0.f;
  STAMP(0,0);
  auto prefill=[&](int i0,int i1){
    const u4 sa=mksrd((const char*)p.wsp+(size_t)rtB*64*4096+mi*1024-4096); const u32 lr=lane>>3, v0=lr*4096+(((lane&7)^(lr>>1))<<4), v1=v0^64u, rb=smb+mi*RING;
    #pragma unroll 1
    for(int i=i0;i<i1;i++){ setm0<0>(rb+i*4096); const u32 g=(i&1)*4+2*mi, k=(i>>1)*128+4096;
      dma<0>(sa,v0,(g&7)*32768+k); dma<1024>(sa,v1,(g&7)*32768+32768-1024+k); dma<2048>(sa,v0,((g+2)&7)*32768-2048+k); dma<3072>(sa,v1,((g+2)&7)*32768+32768-3072+k); } };
  auto warm=[&](){
    const u4 sp=mksrd((const char*)p.wsp+(size_t)(rtB*64+ct*8)*4096+mi*1024+384); const u32 st=lane>>3; setm0<JNK>(smb); touch(sp,(lane&7)*4096+(st<5?st:4)*128,0u);
  };
  if(idx>=28){
    const int a=xc*4+idx-28, r=lane&15, q=lane>>4;
    const u32 ep=ld4(rs,OFF_EP);
    u4 xr[10]; const char* xs=(const char*)p.x+(size_t)(64*a+8*w+(lane>>3))*1280+(lane&7)*16;
    #pragma unroll
    for(int i=0;i<10;i++) xr[i]=*(const u4*)(xs+i*128);
    if(tid<16) *(LAS u32*)(smb+PM+tid*4)=0u;
    tag=uni(ep)+1u;
    float s0=0.f,s1=0.f,q0=0.f,q1=0.f;
    #pragma unroll
    for(int i=0;i<10;i++){ s0=dot2(xr[i][0],0x3f803f80u,s0); q0=dot2(xr[i][0],xr[i][0],q0); s1=dot2(xr[i][1],0x3f803f80u,s1); q1=dot2(xr[i][1],xr[i][1],q1);
      s0=dot2(xr[i][2],0x3f803f80u,s0); q0=dot2(xr[i][2],xr[i][2],q0); s1=dot2(xr[i][3],0x3f803f80u,s1); q1=dot2(xr[i][3],xr[i][3],q1); }
    s0+=s1; q0+=q1;
    #define DPPADD(v,c) v+=__int_as_float(__builtin_amdgcn_update_dpp(0,__float_as_int(v),c,0xf,0xf,false))
    DPPADD(s0,0xB1); DPPADD(q0,0xB1); DPPADD(s0,0x4E); DPPADD(q0,0x4E); DPPADD(s0,0x141); DPPADD(q0,0x141);
    float m1,ri; { float mean=s0*(1.f/640.f); ln_fin(mean,q0*(1.f/640.f)-mean*mean,m1,ri); }
    if((lane&7)==0) st16(rs,OFF_RS+(64*a+8*w+(lane>>3))*16,u4{__float_as_uint(m1),tag,__float_as_uint(ri),tag});
    STAMP(5,0);
    if(a<27){ const u4 srdO=mksrd((const char*)p.wout+a*(32*1280)+w*5120); const u32 mo=smb+CONV+8192+w*5120; setm0<0>(mo);
      dma<0>(srdO,lane*16,0); dma<1024>(srdO,lane*16,0); dma<2048>(srdO,lane*16,0); dma<3072>(srdO,lane*16,0); setm0<4096>(mo); dma<0>(srdO,lane*16,4096); }
    { u4 sc=mksrd((const void*)(pc0&~127ull)); sc[2]=CODE_BYTES; setm0<JNK>(smb); touch(sc,lane*128,w*8192); }
    #pragma unroll
    for(int i=0;i<10;i++){ u4 o;
      #pragma unroll
      for(int e=0;e<4;e++) o[e]=pkhf2(fma2(unpk(xr[i][e]),f2{ri,ri},f2{-m1*ri,-m1*ri}));
      ldsw128(smb+(8*w+(lane>>3))*1296+(lane&7)*16+i*128,o); }
    WAITV(0); bar();
    STAMP(20,0);
    #pragma unroll
    for(int i=0;i<10;i++){ const int c=w*10+i; const u32 ad=smb+(32*((r>>1)&1)+8*q+(r>>2))*1296+c*16+(r&1)*8; u2 lo=ldstr(ad), hi=ldstr(ad+4*1296);
      st16(rs,OFF_XNT+(8*c+(r&7))*4096+(64*a+32*(r>>3)+8*q)*2,u4{lo[0],lo[1],hi[0],hi[1]}); }
    WAITV(0);
    if(arrive(smb+C_Z1,lane)==7u && lane==0) st4(rs,OFF_FX+a*128,tag);
    STAMP(6,0);
    if(a<27){
      #pragma unroll
      for(int i=0;i<5;i++){ u32 ad=smb+CONV+8192+(8*q+(r>>2))*1280+(w*5+i)*32+(r&3)*8; u2 lo=ldstr(ad), hi=ldstr(ad+4*1280);
        st16(rs,OFF_WOF+(u32)((w*27+a)*5+i)*1024+lane*16,u4{lo[0],lo[1],hi[0],hi[1]}); } }
    WAITV(0); bar();
    if(tid==0) st4(rs,OFF_FW+a*128,tag);
    if(w<4) prefill(0,6);
    else warm();
    STAMP(22,0);
  } else {
    const int r=lane&31, h=lane>>5;
    const u4 srdX=mksrd((const char*)p.x+(size_t)rt*128*1280);
    const u4 srdW=mksrd((const char*)p.win+j*128-3072);
    const u32 mw=smb+w*4096;
    u32 vx0,vx1,vw1,vw2,epv;
    { u32 lr=lane>>3,c=lane&7; vx0=(16*w+lr)*1280+((c^(lr>>1))<<4); vx1=(vx0^64u)+9216;
      vw1=(8*w+lr)*3456+((c^((lr&2)<<1))<<4); vw2=vw1+1024+(j==13?0:14*128); }
    u32 pr[6];
    gload4<0>(epv,0u,p.ws+OFF_EP);
    gload4<0>(pr[0],lane*4,(const char*)(p.lar+dch*128)); gload4<0>(pr[1],lane*4,(const char*)(p.aim+dch*128)); gload4<0>(pr[2],lane*4,(const char*)(p.cre+dch*128)); gload4<0>(pr[3],lane*4,(const char*)(p.cim+dch*128));
    gload2(pr[4],0u,(const char*)(p.ldt+dch)); gload2(pr[5],0u,(const char*)(p.dsk+dch));
    u32 kx[10]; { u32 k=KROT;
      #pragma unroll
      for(int i=0;i<10;i++){ kx[i]=k*128; k=k==9?0:k+1; } }
    auto issue=[&](auto sc){ constexpr int s=sc.value;
      setm0<(s&3)*STG>(mw); dma<0>(srdX,vx0,kx[s]); dma<1024>(srdX,vx1,kx[s]); dma<2048>(srdW,vw2,kx[s]*1728); dma<3072>(srdW,vw1,kx[s]*1728); };
    auto issue1=[&](auto sc,int k){ constexpr int s=sc.value;
      if(k==0){ setm0<(s&3)*STG>(mw); dma<0>(srdX,vx0,kx[s]); } if(k==1) dma<1024>(srdX,vx1,kx[s]); if(k==2) dma<2048>(srdW,vw2,kx[s]*1728); if(k==3) dma<3072>(srdW,vw1,kx[s]*1728); };
    issue(std::integral_constant<int,0>{}); asm volatile("s_barrier" ::: "memory");
    issue(std::integral_constant<int,1>{});
    if(tid<16) *(LAS u32*)(smb+PM+tid*4)=0u;
    u32 ax[2][4],aw[2][4][2];
    { u32 row=32*mi+r, a0=smb+(row>>4)*4096+(row&15)*128+((h^((r>>1)&7))<<4);
      u32 qq=(lane&15)>>2, w0=smb+2048+g1*1024+h*4096+qq*128+8*(lane&1)+(((2*((lane>>4)&1)+((lane>>1)&1))^((qq&2)<<1))<<4);
      #pragma unroll
      for(int i=0;i<2;i++)
        #pragma unroll
        for(int k=0;k<4;k++){ const u32 kk=(k+mi)&3, wo=i*2*STG; ax[i][k]=opq((a0^(kk*32u))+wo); aw[i][k][0]=opq(w0+kk*8192+wo); aw[i][k][1]=opq((w0^64u)+kk*8192+wo); } }
    Frag F[2];
    asm volatile("s_waitcnt vmcnt(4)" : "+v"(epv),"+v"(pr[0]),"+v"(pr[1]),"+v"(pr[2]),"+v"(pr[3]),"+v"(pr[4]),"+v"(pr[5]) :: "memory");
    bar();
    tag=uni(epv)+1u; dsk=bflo(pr[5]);
    #pragma unroll
    for(int k=0;k<4;k++){
      #pragma unroll
      for(int n=0;n<2;n++){ u2 lo=ldstr(aw[0][k][n]), hi=ldstr(aw[0][k][n]+512); F[0].w[k][n]=u4{lo[0],lo[1],hi[0],hi[1]}; }
      F[0].x[k]=ldsr128(ax[0][k]); }
    issue(std::integral_constant<int,2>{});
    STAMP(1,0);
    f16v acc[2]={}; u4 G; float cv[2][2]={};
    sfor<10>([&](auto ic){
      constexpr int s=ic.value; Frag& cur=F[s&1]; Frag& nxt=F[(s&1)^1];
      constexpr int cnt=(s<=7?4:0)+(s>RS_S?1:0);
      if constexpr(s<9){ WAITV(cnt); asm volatile("s_barrier" ::: "memory"); }
      auto rw=[&](int k,int n){ if constexpr(s<9){ const int i=((s+1)>>1)&1; const u32 so=((s+1)&1)*STG; u2 lo=ldstr(aw[i][k][n]+so), hi=ldstr(aw[i][k][n]+so+512); nxt.w[k][n]=u4{lo[0],lo[1],hi[0],hi[1]}; } };
      auto rx=[&](int k){ if constexpr(s<9){ const int i=((s+1)>>1)&1; const u32 so=((s+1)&1)*STG; nxt.x[k]=ldsr128(ax[i][k]+so); } };
      auto csum=[&](int n,int e){ const u32 v=cur.w[0][n][e]; cv[n][0]=addf(cv[n][0],bflo(v)); cv[n][1]=addf(cv[n][1],bfhi(v)); };
      #pragma unroll
      for(int k=0;k<4;k++){
        acc[0]=mfma32b(cur.w[k][0],cur.x[k],acc[0]); SCHED();
        if constexpr(s+3<=9) issue1(std::integral_constant<int,s+3>{},k);
        if(k==0){ if constexpr(s==RS_S) gload<0>(G,(32*mi+r)*16,p.ws+OFF_RS+rt*2048); }
        rw(k,0); rx(k); csum(0,k);
        SCHED();
        acc[1]=mfma32b(cur.w[k][1],cur.x[k],acc[1]); SCHED();
        rw(k,1); csum(1,k);
        SCHED();
      }
    });
    STAMP(2,0);
    if(!g1) __builtin_amdgcn_s_setprio(2);
    #define XSUM32(v) do{ auto t_=__builtin_amdgcn_permlane32_swap(__float_as_uint(v),__float_as_uint(v),false,false); v=__uint_as_float(t_[0])+__uint_as_float(t_[1]); }while(0)
    { float c0=cv[0][0]+cv[0][1], c1=cv[1][0]+cv[1][1]; XSUM32(c0); XSUM32(c1); *(LAS f2*)(smb+CONV+3*CVS+w*256+r*8)=f2{c0,c1}; }
    waitG<0>(G);
    bar();
    f2 cs; { const u32 ad=smb+CONV+3*CVS+g1*1024+h*512+r*8; cs=*(LAS f2*)ad+*(LAS f2*)(ad+256); }
    if(g1){ prefill(0,2); warm(); }
    #pragma unroll 1
    for(int sp=0;sp<SPIN && !__all(G[1]==tag&&G[3]==tag);sp++){ G=ld16(rs,OFF_RS+(rt*128+32*mi+r)*16); asm volatile("" ::: "memory"); }
    const float cs0=cs[0], cs1=cs[1];
    u32 zp[2][8];
    { const float nm=-__uint_as_float(G[0]), ri1=__uint_as_float(G[2]);
      acc[0]=__builtin_amdgcn_mfma_f32_32x32x2f32(cs0,nm,acc[0],0,0,0); acc[1]=__builtin_amdgcn_mfma_f32_32x32x2f32(cs1,nm,acc[1],0,0,0);
      #pragma unroll
      for(int n=0;n<2;n++)
        #pragma unroll
        for(int e=0;e<8;e++) zp[n][e]=pkbf2(gelu2(f2{acc[n][2*e],acc[n][2*e+1]}*ri1)); }
    if(!g1){
      float S=0.f,Q=0.f;
      if(j<13){
        #pragma unroll
        for(int e=0;e<8;e++){ S=dot2(zp[0][e],0x3f803f80u,S); Q=dot2(zp[0][e],zp[0][e],Q); } }
      #pragma unroll
      for(int e=0;e<8;e++){ S=dot2(zp[1][e],0x3f803f80u,S); Q=dot2(zp[1][e],zp[1][e],Q); }
      XSUM32(S); XSUM32(Q);
      const u32 sto=OFF_ST+rt*(14*2048)+(32*mi+r)*16;
      if(h==0) st16(rs,sto+j*2048,u4{__float_as_uint(S),tag,__float_as_uint(Q),tag});
      STAMP(3,0);
    } else {
      #pragma unroll
      for(int n=0;n<2;n++) if(2*j+n<27){ const u32 o=OFF_Z1T+((rt*8+2*mi+(r>>4))*27+2*j+n)*1024+(h*16+(r&15))*16;
        st16(rs,o,u4{zp[n][0],zp[n][1],zp[n][4],zp[n][5]}); st16(rs,o+512,u4{zp[n][2],zp[n][3],zp[n][6],zp[n][7]}); }
      STAMP(4,4);
    }
    float kp[11]; u4 g[7]={};
    const u32 gof=g1? OFF_FX+r*128 : OFF_ST+rt*(14*2048)+(32*mi+r)*16+h*(7*2048);
    auto samp=[&](){ asm volatile("" ::: "memory");
      g[0]=ld16(rs,gof);
      if(!g1){
        #pragma unroll
        for(int i=1;i<7;i++) g[i]=ld16(rs,gof+i*2048); }
      asm volatile("" ::: "memory"); };
    if(hask) ksc=kgen_tab(g1? cb : smb+mi*RING+8192,lane,pr,g1,kp,samp); else samp();
    STAMP(24,0); STAMP(25,4);
    if(!g1){
      if(hask){
        #pragma unroll
        for(int i=0;i<11;i++) *(LAS float*)(cb+UOF+lane*4+i*256)=kp[i];
        WAITL0(); signal(smb+C_KP+mi*4,lane); }
      float S,Q;
      #pragma unroll 1
      for(int sp=0;sp<SPIN;sp++){ bool ok=true;
        #pragma unroll
        for(int i=0;i<7;i++) ok=ok&&g[i][1]==tag&&g[i][3]==tag;
        if(__all(ok)) break;
        __builtin_amdgcn_s_sleep(2); samp(); }
      STAMP(7,0);
      S=0.f; Q=0.f;
      #pragma unroll
      for(int i=0;i<7;i++){ S+=__uint_as_float(g[i][0]); Q+=__uint_as_float(g[i][2]); }
      XSUM32(S); XSUM32(Q);
      float mean=S*(1.f/864.f), m2,r2; ln_fin(mean,Q*(1.f/864.f)-mean*mean,m2,r2);
      const u32 tb=smb+mi*RING+16384, ad=tb+(8*(lane>>4)+((lane&15)>>2))*144+(lane&3)*8;
      #pragma unroll
      for(int n=0;n<2;n++)
        #pragma unroll
        for(int g2=0;g2<4;g2++){ u2 o;
          #pragma unroll
          for(int e=0;e<2;e++) o[e]=pkbf2(fma2(unpk(zp[n][2*g2+e]),f2{r2,r2},f2{-m2*r2,-m2*r2}));
          *(LAS u2*)(tb+r*144+n*64+g2*16+h*8)=o; }
      WAITL0();
      { const u32 zo=OFF_Z2F+((rt*4+mi)*54+(j==13?-2:2+4*j))*1024+lane*16;
        #pragma unroll
        for(int n=0;n<4;n++){ u2 lo=ldstr(ad+n*32), hi=ldstr(ad+n*32+576); if(j<13||n>=2) st16(rs,zo+n*1024,u4{lo[0],lo[1],hi[0],hi[1]}); } }
      WAITV(0);
      if(arrive(smb+C_Z2,lane)==3u && lane==0) st4(rs,OFF_F2+(rt*16+j)*128,tag);
      STAMP(8,0);
      prefill(2,6);
    } else {
      WAITV(0); if(arrive(smb+C_Z1,lane)==3u && lane==0) st4(rs,OFF_F1+(rt*16+j)*128,tag);
      if(hasc){
        if(!__all(g[0][0]==tag)) pollw(rs,OFF_FX+r*128,tag);
        #pragma unroll
        for(int i=0;i<4;i++) uu[i]=ld16(rs,OFF_XNT+dch*4096+(lane+64*i)*16);
        ldswait(smb+C_KP+mi*4,1u);
        #pragma unroll
        for(int i=0;i<11;i++) kp[i]+=*(LAS float*)(cb+UOF+lane*4+i*256);
        kgen_exp(cb,lane,kp); }
      STAMP(13,4);
    }
  }
  const int ft0=ct<7?7*ct:47;
  if(w>=4){
    STAMP(15,4);
    if(hasc){
      conv_wave(p,rs,dch,cb,lane,ksc,dsk,uu);
      if(arrive(smb+C_CV,lane)==(idx<24?2u:1u) && lane==0) st4(rs,OFF_FA+b*128,tag);
    }
    if(w==7){
      pollw(rs,lane<27? OFF_FW+lane*128 : OFF_F1+((rtB>>1)*16+(((ft0>>1)+(lane&3))>>1))*128,tag);
      if(lane==0) *(LAS u32*)(smb+C_Z1R)=1u;
    }
    STAMP(14,4);
    signal(smb+C_FREE,lane);
    ldswait(smb+C_FREE,8u);
    STAMP(21,4);
    { const u32 so=OFF_WOF+ct*(135*1024)+(w-4)*4096;
      #pragma unroll 1
      for(u32 i=(w-4)*4096;i<135*1024;i+=16384){ setm0<0>(smb+i); const u32 o=OFF_WOF+ct*(135*1024)+i; dma<0>(srdS,lane*16,o); dma<1024>(srdS,lane*16,o); dma<2048>(srdS,lane*16,o); dma<3072>(srdS,lane*16,o); } (void)so; }
    if(w==7){ const u32 vo=(lane>>3)*4096+(lane&7)*16;
      pollw(rs,OFF_FA+((lane<28?lane:0)*8+ct)*128,tag);
      #pragma unroll 1
      for(int i=0;i<10;i++){ setm0<ATL>(smb+i*1024); dma<0>(srdS,vo,OFF_AT+(u32)(80*ct+8*i)*4096+rtB*128); } }
    if(w==6 && b==255){
      #pragma unroll 1
      for(int sp=0;sp<SPIN;sp++){ const u32 o=OFF_FA+lane*128; u32 v0=ld4(rs,o),v1=ld4(rs,o+8192),v2=ld4(rs,o+16384),v3=ld4(rs,lane<32? o+24576 : OFF_FW+(lane-32)*128); asm volatile("" ::: "memory");
        if(__all(v0==tag&&v1==tag&&v2==tag&&v3==tag)) break; __builtin_amdgcn_s_sleep(4); }
      if(lane==0) st4(rs,OFF_EP,tag); }
    WAITV(0);
  }
  int w2=w; asm volatile("" : "+s"(w2));
  if(w2<4){
    __builtin_amdgcn_s_setprio(2);
    int r=lane&15, q=lane>>4;
    const u32 rb=smb+w*RING;
    u4 srdA=mksrd((const char*)p.wsp+(size_t)rtB*64*4096+w*1024-4096+384);
    const char* zb=p.ws+OFF_Z2F+(u32)(16*w*54+ft0+3)*1024;
    u32 voA[2]; { u32 lr=lane>>3,c=lane&7; voA[0]=lr*4096+((c^(lr>>1))<<4); voA[1]=voA[0]^64u; }
    u32 sofa[8];
    #pragma unroll
    for(int g=0;g<8;g++) sofa[g]=((g+2*w)&7)*32768+4096-(g&3)*1024;
    const u32 voB=lane*16;
    u32 la[2]; la[0]=rb+r*128+((q^(r>>1))<<4); la[1]=la[0]^64u;
    f4 acc[4][7]={}; u4 B[3][7]; u4 A[2][4]; u2 z1[7];
    auto issueA=[&](auto sc,auto gc){ constexpr int s=sc.value,g=gc.value; if constexpr((g&3)==0) setm0<(s%3)*8192+g*1024>(rb); dma<(g&3)*1024>(srdA,voA[g&1],sofa[g]);
      if constexpr(g==7){ u64 a=((u64)srdA[1]<<32|srdA[0])+128; srdA[0]=(u32)a; srdA[1]=(u32)(a>>32); } };

    u32 bsr;
    #pragma unroll
    for(int m=0;m<4;m++)
      #pragma unroll
      for(int n=0;n<7;n++) asm volatile("" : "+v"(acc[m][n]));
    { const int hf=(ft0>>1)+q; pollw(rs,OFF_F2+((4*w+(lane&3))*16+(hf==0?13:(hf-1)>>1))*128,tag); }
    STAMP(9,0);
    sfor<3>([&](auto hh){ const char* s=zb+(u32)hh.value*(54*1024); sfor<7>([&](auto n){ gload<(n.value-3)*1024>(B[hh][n],voB,s); }); });
    ldswait(smb+C_Z1,4u);
    #pragma unroll
    for(int m=0;m<4;m++) A[0][m]=ldsr128(la[0]+m*2048);
    const char* z1b=p.ws+OFF_Z1T+(u32)((rtB*4+w)*27+(ft0>>1))*1024;
    sfor<16>([&](auto hc){
      constexpr int h=hc.value, S=h%3, P=h&1;
      constexpr int cnt = h<2? 14 : ((h+1<16?7:0)+(h+2<16?7:0) + ((((h-2)&1)&&((h-2)/2+3<8))?8:0) + ((((h-1)&1)&&((h-1)/2+3<8))?8:0) + (h>=14?8:0));
      waitB<cnt>(B[S]); waitA(A[P]);
      if constexpr(h==13) ldswait(smb+C_Z1R,1u);
      if constexpr(h+1<16){
        #pragma unroll
        for(int m=0;m<4;m++) A[P^1][m]=ldsr128(la[(h+1)&1]+m*2048+(((h+1)>>1)%3)*8192);
      }
      const char* s=zb+(u32)(h+3)*(54*1024);
      sfor<7>([&](auto n){
        mfma4(acc[0][n],acc[1][n],acc[2][n],acc[3][n],B[S][n],A[P]);
        if constexpr((h&1)&&(h/2+3<8)){ issueA(std::integral_constant<int,h/2+3>{},n); if constexpr(n.value==6) issueA(std::integral_constant<int,h/2+3>{},std::integral_constant<int,7>{}); }
        if constexpr(h+3<16) gload_t<(n.value-3)*1024>(B[S][n],voB,s);
        if constexpr(h==13){ gload8<(n.value>>1)*1024>(z1[n],voB,z1b+((n.value&1)? ((ft0&1)?1024:8) : (ft0&1)*8));
          if constexpr(n.value==6) gload2(bsr,(voB>>3)&30u,(const char*)(p.bsp+(rtB*4+w)*16)); }
      });
    });
    asm volatile("s_nop 15\n\ts_nop 3" ::: "memory");
    SCHED();
    STAMP(10,0);
    lane=__builtin_amdgcn_mbcnt_hi(~0u,__builtin_amdgcn_mbcnt_lo(~0u,0u)); asm volatile("" : "+v"(lane)); r=lane&15; q=lane>>4;
    #pragma unroll
    for(int m=1;m<4;m++)
      #pragma unroll
      for(int n=0;n<7;n++) *(LAS f4*)(rb+((m-1)*7+n)*1024+lane*16)=acc[m][n];
    WAITL0();
    signal(smb+C_RED,lane);
    ldswait(smb+C_RED,4u);
    #pragma unroll
    for(int m=1;m<4;m++){ u32 src=smb+((w-m)&3)*RING+(m-1)*7168+lane*16;
      #pragma unroll
      for(int n=0;n<7;n++) acc[0][n]+=*(LAS f4*)(src+n*1024); }
    asm volatile("s_waitcnt vmcnt(0) lgkmcnt(0)" : "+v"(z1[0]),"+v"(z1[1]),"+v"(z1[2]),"+v"(z1[3]),"+v"(z1[4]),"+v"(z1[5]),"+v"(z1[6]),"+v"(bsr) :: "memory");
    const float bs=bflo(bsr);
    signal(smb+C_FREE,lane);
    u2 o[7];
    #pragma unroll
    for(int n=0;n<7;n++)
      #pragma unroll
      for(int e=0;e<2;e++)o[n][e]=pkbf2(unpk(z1[n][e])*(f2{acc[0][n][2*e],acc[0][n][2*e+1]}+bs));
    const u32 ho=OFF_HF+(u32)((rtB*4+w)*54+ft0)*512+(q>>1)*256+r*16;
    #pragma unroll
    for(int n=0;n<6;n+=2){ auto sx=__builtin_amdgcn_permlane16_swap(o[n][0],o[n+1][0],false,false); auto sy=__builtin_amdgcn_permlane16_swap(o[n][1],o[n+1][1],false,false);
      if(ct<7||n>=2) st16(rs,ho+(n+(q&1))*512,u4{sx[0],sy[0],sx[1],sy[1]}); }
    st8(rs,ho+6*512+(q&1)*8,o[6]);
    STAMP(11,0);
    WAITV(0);
    if(arrive(smb+C_H,lane)==3u && lane==0) st4(rs,OFF_FH+(rtB*8+ct)*128,tag);
    STAMP(12,0);
    __builtin_amdgcn_s_setprio(0);
  }
  { const int r=lane&15, q=lane>>4, m=w&3, kh=w>>2, t=rtB*64+m*16+r;
    u2 xv[5];
    #pragma unroll
    for(int n=0;n<5;n++) xv[n]=*(const u2*)(p.x+t*640+80*ct+16*n+4*q);
    if(w==0) pollw(rs,OFF_FH+(rtB*8+(lane&7))*128,tag);
    STAMP(16,0);
    bar();
    STAMP(17,0);
    u4 hf[14];
    #pragma unroll
    for(int k=0;k<14;k++) hf[k]=ld16(rs,OFF_HF+(u32)((rtB*4+m)*27+kh*13+k)*1024+lane*16);
    f4 acc[5]={}; u4 W[2][5];
    const u32 wl=smb+kh*13*5120+lane*16;
    #pragma unroll
    for(int n=0;n<5;n++) W[0][n]=ldsr128(wl+n*1024);
    f2 xa[5][2];
    #pragma unroll
    for(int n=0;n<5;n++){ u2 av=ldstr(smb+ATL+(16*n+4*q+(r>>2))*128+(16*m+4*(r&3))*2); xa[n][0]=unpk(xv[n][0])+unpk(av[0]); xa[n][1]=unpk(xv[n][1])+unpk(av[1]); }
    if(kh) hf[0]=u4{0,0,0,0};
    #pragma unroll
    for(int k=0;k<14;k++){
      if(k<13){
        #pragma unroll
        for(int n=0;n<5;n++) W[(k+1)&1][n]=ldsr128(wl+((k+1)*5+n)*1024);
      }
      SCHED();
      #pragma unroll
      for(int n=0;n<5;n++) acc[n]=mfma16(W[k&1][n],hf[k],acc[n]);
      SCHED();
    }
    STAMP(18,0);
    bar();
    if(kh){
      #pragma unroll
      for(int n=0;n<5;n++) *(LAS f4*)(smb+(m*5+n)*1024+lane*16)=acc[n];
    }
    bar();
    if(!kh){
      #pragma unroll
      for(int n=0;n<5;n++){ f4 o=acc[n]+*(LAS f4*)(smb+(m*5+n)*1024+lane*16); u2 res;
        #pragma unroll
        for(int e=0;e<2;e++)res[e]=pkbf2(xa[n][e]+f2{o[2*e],o[2*e+1]});
        *(u2*)(p.out+t*640+80*ct+16*n+4*q)=res; }
    }
    STAMP(19,0);
  }
}
extern "C" void kernel_launch(void* const* d_in,const int* in_sizes,int n_in,void* d_out,int out_size,void* d_ws,size_t ws_size,hipStream_t stream){
  Params p;
  p.x=(const u16*)d_in[0]; p.lar=(const u16*)d_in[1]; p.aim=(const u16*)d_in[2]; p.cre=(const u16*)d_in[3]; p.cim=(const u16*)d_in[4];
  p.ldt=(const u16*)d_in[5]; p.dsk=(const u16*)d_in[6]; p.win=(const u16*)d_in[8]; p.wsp=(const u16*)d_in[9]; p.bsp=(const u16*)d_in[10]; p.wout=(const u16*)d_in[11];
  p.out=(u16*)d_out; p.ws=(char*)d_ws;
  (void)hipFuncSetAttribute((const void*)fused,hipFuncAttributeMaxDynamicSharedMemorySize,LDS_TOTAL);
  fused<<<256,512,LDS_TOTAL,stream>>>(p);
}
```

```cpp
#include <hip/hip_runtime.h>
#include <utility>
#pragma clang diagnostic ignored "-Wint-to-pointer-cast"
typedef unsigned short u16;
typedef unsigned int u32;
typedef unsigned long long u64;
typedef __attribute__((ext_vector_type(8))) __bf16 bf8;
typedef __attribute__((ext_vector_type(8))) _Float16 hf8;
typedef __attribute__((ext_vector_type(2))) __bf16 bf2;
typedef __attribute__((ext_vector_type(2))) _Float16 hf2;
typedef __attribute__((ext_vector_type(2))) float f2;
typedef __attribute__((ext_vector_type(4))) float f4;
typedef __attribute__((ext_vector_type(16))) float f16v;
typedef __attribute__((ext_vector_type(4))) u32 u4;
typedef __attribute__((ext_vector_type(2))) u32 u2;
typedef __attribute__((ext_vector_type(4))) short s4;
#define LAS __attribute__((address_space(3)))
#define GAS __attribute__((address_space(1)))
#define DI __device__ __forceinline__

constexpr u32 OFF_Z2F = 0;
constexpr u32 OFF_Z1T = OFF_Z2F + 64*54*1024;
constexpr u32 OFF_XNT = OFF_Z1T + 128*27*1024;
constexpr u32 OFF_WOF = OFF_XNT + 640*4096;
constexpr u32 OFF_HF  = OFF_WOF + 8*27*5*1024;
constexpr u32 OFF_AT  = OFF_HF + 128*27*1024;
constexpr u32 OFF_ST  = OFF_AT + 640*4096;
constexpr u32 OFF_RS  = OFF_ST + 16*14*2048+8192;
constexpr u32 OFF_EP  = OFF_RS + 2048*16;
constexpr u32 OFF_F2  = OFF_EP + 256;
constexpr u32 OFF_F1  = OFF_F2 + 256*128;
constexpr u32 OFF_FX  = OFF_F1 + 256*128;
constexpr u32 OFF_FW  = OFF_FX + 32*128;
constexpr u32 OFF_FH  = OFF_FW + 32*128;
constexpr u32 OFF_FA  = OFF_FH + 256*128;
constexpr u32 WS_END  = OFF_FA + 256*128;

struct Params {
  const u16 *x, *lar, *aim, *cre, *cim, *ldt, *dsk, *win, *wsp, *bsp, *wout;
  u16* out; char* ws;
};
#define STAMP(id,wv) do{}while(0)
#define RST(i) do{}while(0)
template<class F,int... I> DI void sfor_impl(F&& f, std::integer_sequence<int,I...>){ (f(std::integral_constant<int,I>{}),...); }
template<int N,class F> DI void sfor(F&& f){ sfor_impl(f, std::make_integer_sequence<int,N>{}); }

DI float bflo(u32 v){ return __uint_as_float(v<<16); }
DI float bfhi(u32 v){ return __uint_as_float(v&0xffff0000u); }
DI float bf1(u16 v){ return __uint_as_float(((u32)v)<<16); }
DI u32 pkbf(float a,float b){ f2 v={a,b}; return __builtin_bit_cast(u32,__builtin_convertvector(v,bf2)); }
DI u32 pkhf(float a,float b){ f2 v={a,b}; return __builtin_bit_cast(u32,__builtin_convertvector(v,hf2)); }
DI float rbf(float a){ return bflo(pkbf(a,a)); }
DI float hlo(u32 v){ return (float)__builtin_bit_cast(hf2,v)[0]; }
DI float hhi(u32 v){ return (float)__builtin_bit_cast(hf2,v)[1]; }
DI float dot2(u32 a,u32 b,float c){ return __builtin_amdgcn_fdot2_f32_bf16(__builtin_bit_cast(bf2,a),__builtin_bit_cast(bf2,b),c,false); }
DI f4 mfma16(u4 a,u4 b,f4 c){ return __builtin_amdgcn_mfma_f32_16x16x32_bf16(__builtin_bit_cast(bf8,a),__builtin_bit_cast(bf8,b),c,0,0,0); }
DI f16v mfma32h(u4 a,u4 b,f16v c){ return __builtin_amdgcn_mfma_f32_32x32x16_f16(__builtin_bit_cast(hf8,a),__builtin_bit_cast(hf8,b),c,0,0,0); }
DI u4 ldsr128(u32 a){ return *(LAS u4*)a; }
DI u2 ldsr64(u32 a){ return *(LAS u2*)a; }
DI void ldsw128(u32 a,u4 v){ *(LAS u4*)a=v; }
DI u2 ldstr(u32 a){ return __builtin_bit_cast(u2,__builtin_amdgcn_ds_read_tr16_b64_v4i16((LAS s4*)a)); }
DI u32 uni(u32 v){ return __builtin_amdgcn_readfirstlane(v); }

template<int OFF> DI void gload(u4& dst,u32 voff,const char* base){ asm volatile("global_load_dwordx4 %0, %1, %2 offset:%3 sc1" : "=v"(dst) : "v"(voff), "s"(base), "n"(OFF)); }
template<int OFF> DI void gload_t(u4& dst,u32 voff,const char* base){ asm volatile("global_load_dwordx4 %0, %1, %2 offset:%3 sc1" : "+v"(dst) : "v"(voff), "s"(base), "n"(OFF)); }
DI void mfma4(f4& c0,f4& c1,f4& c2,f4& c3,const u4& a,const u4 (&b)[4]){
  asm volatile("v_mfma_f32_16x16x32_bf16 %0, %4, %5, %0\n\tv_mfma_f32_16x16x32_bf16 %1, %4, %6, %1\n\tv_mfma_f32_16x16x32_bf16 %2, %4, %7, %2\n\tv_mfma_f32_16x16x32_bf16 %3, %4, %8, %3"
    : "+v"(c0),"+v"(c1),"+v"(c2),"+v"(c3) : "v"(a),"v"(b[0]),"v"(b[1]),"v"(b[2]),"v"(b[3])); }
template<int CNT> DI void waitB(u4 (&B)[7]){ asm volatile("s_waitcnt vmcnt(%7)" : "+v"(B[0]),"+v"(B[1]),"+v"(B[2]),"+v"(B[3]),"+v"(B[4]),"+v"(B[5]),"+v"(B[6]) : "n"(CNT) : "memory"); }
DI void waitA(u4 (&A)[4]){ asm volatile("s_waitcnt lgkmcnt(0)" : "+v"(A[0]),"+v"(A[1]),"+v"(A[2]),"+v"(A[3]) :: "memory"); }
#define WAITV(n) asm volatile("s_waitcnt vmcnt(%0)" :: "n"(n) : "memory")
#define WAITL0() asm volatile("s_waitcnt lgkmcnt(0)" ::: "memory")
#define SCHED() __builtin_amdgcn_sched_barrier(0)
DI void bar(){ asm volatile("s_waitcnt lgkmcnt(0)\n\ts_barrier" ::: "memory"); }
DI f2 unpk(u32 v){ return f2{bflo(v),bfhi(v)}; }
DI u32 pkbf2(f2 v){ return __builtin_bit_cast(u32,__builtin_convertvector(v,bf2)); }
DI u32 pkhf2(f2 v){ return __builtin_bit_cast(u32,__builtin_convertvector(v,hf2)); }
DI f2 fma2(f2 a,f2 b,f2 c){ return __builtin_elementwise_fma(a,b,c); }
DI f2 gelu2(f2 x){
  f2 u=x*fma2(x*x,f2{-0.10294324f,-0.10294324f},f2{-2.30220820f,-2.30220820f});
  f2 d=f2{__builtin_amdgcn_exp2f(u[0]),__builtin_amdgcn_exp2f(u[1])}+f2{1.f,1.f};
  return x*f2{__builtin_amdgcn_rcpf(d[0]),__builtin_amdgcn_rcpf(d[1])};
}
DI void ln_fin(float mean,float var,float& m,float& rinv){
  m=rbf(mean); float v=rbf(__builtin_fmaxf(var,0.f));
  float s=rbf(__builtin_amdgcn_sqrtf(rbf(v+1e-5f))); float r=__builtin_amdgcn_rcpf(s); rinv=__builtin_fmaf(__builtin_fmaf(-s,r,1.f),r,r);
}
typedef __amdgpu_buffer_rsrc_t rsrc_t;
DI void st16(rsrc_t rs,u32 off,u4 v){ __builtin_amdgcn_raw_buffer_store_b128(v,rs,off,0,16); }
DI void st8(rsrc_t rs,u32 off,u2 v){ __builtin_amdgcn_raw_buffer_store_b64(v,rs,off,0,16); }
DI void st4(rsrc_t rs,u32 off,u32 v){ __builtin_amdgcn_raw_buffer_store_b32(v,rs,off,0,16); }
DI u4 ld16(rsrc_t rs,u32 off){ return __builtin_amdgcn_raw_buffer_load_b128(rs,off,0,16); }
DI u2 ld8(rsrc_t rs,u32 off){ return __builtin_amdgcn_raw_buffer_load_b64(rs,off,0,16); }
DI u32 ld4(rsrc_t rs,u32 off){ return __builtin_amdgcn_raw_buffer_load_b32(rs,off,0,16); }
constexpr int SPIN=1<<17;
template<int SL=2> DI void pollw(rsrc_t rs,u32 off,u32 tag){
  #pragma unroll 1
  for(int s=0;s<SPIN;s++){ u32 v=ld4(rs,off); asm volatile("" ::: "memory"); if(__all(v==tag)) break; __builtin_amdgcn_s_sleep(SL); }
}

DI f16v mfma32b(u4 a,u4 b,f16v c){ return __builtin_amdgcn_mfma_f32_32x32x16_bf16(__builtin_bit_cast(bf8,a),__builtin_bit_cast(bf8,b),c,0,0,0); }
DI u4 mksrd(const void* b){ u64 a=(u64)b; return u4{(u32)a,(u32)(a>>32)&0xffffu,0xffffffffu,0x00020000u}; }
template<int IMM> DI void setm0(u32 base){ asm volatile("s_add_u32 m0, %0, %1\n\ts_nop 0" :: "s"(base), "n"(IMM) : "scc","memory"); }
template<int OFF> DI void dma(u4 srd,u32 voff,u32 soff){ asm volatile("buffer_load_dwordx4 %0, %1, %2 offen offset:%3 sc1 lds" :: "v"(voff), "s"(srd), "s"(soff), "n"(OFF) : "memory"); }
DI void touch(u4 srd,u32 voff,u32 soff){ asm volatile("buffer_load_dword %0, %1, %2 offen sc1 lds" :: "v"(voff), "s"(srd), "s"(soff) : "memory"); }
template<int OFF> DI void gload8(u2& dst,u32 voff,const char* base){ asm volatile("global_load_dwordx2 %0, %1, %2 offset:%3 sc1" : "=v"(dst) : "v"(voff), "s"(base), "n"(OFF)); }
DI void gload2(u32& dst,u32 voff,const char* base){ asm volatile("global_load_ushort %0, %1, %2" : "=v"(dst) : "v"(voff), "s"(base)); }
DI u32 arrive(u32 a,int){ u32 o; asm volatile("s_mov_b64 exec, 1\n\tds_add_rtn_u32 %0, %1, %2\n\ts_mov_b64 exec, -1\n\ts_waitcnt lgkmcnt(0)" : "=&v"(o) : "v"(a), "v"(1u) : "memory"); return uni(o); }
DI void ldswait(u32 a,u32 n){ while(__hip_atomic_load((LAS u32*)a,__ATOMIC_RELAXED,__HIP_MEMORY_SCOPE_WORKGROUP)<n) __builtin_amdgcn_s_sleep(1); asm volatile("" ::: "memory"); }
DI float addf(float a,float b){ float o; asm("v_add_f32 %0, %1, %2" : "=v"(o) : "v"(a), "v"(b)); return o; }
DI u32 opq(u32 v){ asm volatile("" : "+v"(v)); return v; }

constexpr u32 STG=32768;
constexpr u32 RING=24576, RB=62464, CONV=0, CVS=20800, UOF=16640, MISC=RB+4*RING, JNK=MISC+2048, PM=163840-128, LDS_TOTAL=163840;
constexpr u32 ATL=137*1024;
constexpr u32 C_FULL=PM, C_G1=PM+4, C_Z2=PM+8, C_Z1=PM+12, C_RED=PM+16, C_H=PM+20, C_FREE=PM+24, C_CV=PM+28, C_Z1R=PM+32, C_TAG=PM+40, C_KP=PM+48, C_DONE=PM+64, C_ZQ=PM+68, C_CS=PM+72;
constexpr u32 CODE_BYTES=32768;
#define LA 3
#define TC_S 7
#define G2PRIO 2
#define XMAX 9
#define GS_K 35
#define WNT ""
#define PDLY 8
#define RS_S 7
#define KROT ((j+3*(rt&3))%10)
DI void signal(u32 a,int){ asm volatile("s_mov_b64 exec, 1\n\tds_add_u32 %0, %1\n\ts_mov_b64 exec, -1" :: "v"(a), "v"(1u) : "memory"); }
template<int OFF> DI void gload4(u32& dst,u32 voff,const char* base){ asm volatile("global_load_dword %0, %1, %2 offset:%3 sc1" : "=v"(dst) : "v"(voff), "s"(base), "n"(OFF)); }
template<int CNT> DI void waitG(u4& g){ asm volatile("s_waitcnt vmcnt(%1)" : "+v"(g) : "n"(CNT) : "memory"); }

DI f2 cmulp(f2 a,f2 b,f2 bs){ return fma2(f2{a[1],a[1]},bs,f2{a[0],a[0]}*b); }
DI float fmaf1(float a,float b,float c){ float o; asm("v_fma_f32 %0, %1, %2, %3" : "=v"(o) : "v"(a),"v"(b),"v"(c)); return o; }
DI u32 ldsv(u32 a){ return __hip_atomic_load((LAS u32*)a,__ATOMIC_RELAXED,__HIP_MEMORY_SCOPE_WORKGROUP); }
DI void ldswait4(u32 a,int lane,u32 n){ while(__any(ldsv(a+(lane&3)*4)<n)) __builtin_amdgcn_s_sleep(1); asm volatile("" ::: "memory"); }
DI u32 gettag(u32 a){ ldswait(a+4,1u); return uni(ldsv(a)); }
DI f2 cpow_dir(float lr,float rv,float pw){ float e=__builtin_amdgcn_exp2f(lr*pw); float f=__builtin_amdgcn_fractf(rv*pw);
  return f2{e*__builtin_amdgcn_cosf(f),e*__builtin_amdgcn_sinf(f)}; }
DI u32 dup(u32 a){ return __builtin_amdgcn_perm(a,a,0x01000100u); }
DI u32 cat(u32 lo,u32 hi){ return __builtin_amdgcn_perm(hi,lo,0x05040100u); }
DI u32 lds16(u32 a){ return *(LAS u16*)a; }

template<class CB> DI float kgen_tab(u32 cb,int lane,const u32 (&pr)[6],float (&kp)[11],CB&& mid){
  const float dt=rbf(__expf(bflo(pr[4])));
  const float cr[2]={bflo(pr[2]),bfhi(pr[2])}, cm[2]={bflo(pr[3]),bfhi(pr[3])};
  float mx=fmaxf(fmaxf(fabsf(cr[0]),fabsf(cm[0])),fmaxf(fabsf(cr[1]),fabsf(cm[1])));
  u32 eb=__float_as_uint(mx)&0x7f800000u;
  #define DPPMAX(c) eb=max(eb,(u32)__builtin_amdgcn_update_dpp(0,(int)eb,c,0xf,0xf,false))
  DPPMAX(0x128); DPPMAX(0x124); DPPMAX(0x122); DPPMAX(0x121);
  eb=max(max((u32)__builtin_amdgcn_readlane(eb,0),(u32)__builtin_amdgcn_readlane(eb,16)),max((u32)__builtin_amdgcn_readlane(eb,32),(u32)__builtin_amdgcn_readlane(eb,48))); eb=eb<0x01000000u?0x3f800000u:(eb>0x7e000000u?0x7e000000u:eb);
  const float ksc=2.f*dt*__uint_as_float(eb), isc=__uint_as_float(0x7f000000u-eb);
  const int r=lane&31,h=lane>>5;
  const u32 TP=272, TA=cb, TB=cb+22*TP;
  f16v acc={};
  #pragma unroll 1
  for(int c=0;c<2;c++){
    if(c) mid();
    float l_a=c?bfhi(pr[0]):bflo(pr[0]), i_m=c?bfhi(pr[1]):bflo(pr[1]), c_r=c?cr[1]:cr[0], c_i=c?cm[1]:cm[0];
    const float lr=(-rbf(__expf(l_a))*dt)*1.44269504f, rv=(i_m*dt)*0.15915494f;
    const f2 z1=cpow_dir(lr,rv,1.f), z8=cpow_dir(lr,-rv,8.f), zs=cpow_dir(lr,-rv,16.f), zk=cpow_dir(lr,rv,256.f);
    const f2 z1s={-z1[1],z1[0]}, zss={-zs[1],zs[0]}, zks={-zk[1],zk[0]};
    const u32 wa=TA+lane*4, wb=TB+lane*4;
    #define PUT(base,row,v) do{ *(LAS u32*)((base)+(row)*TP)=pkhf2(v); }while(0)
    f2 e={1.f,0.f}, o=z8;
    #pragma unroll
    for(int k=0;k<16;k++){ PUT(wb,2*k,e); PUT(wb,2*k+1,o); if(k<15){ e=k?cmulp(e,zs,zss):zs; o=cmulp(o,zs,zss);} }
    f2 b0={c_r*isc,c_i*isc}; PUT(wa,0,b0);
    f2 t=cmulp(b0,zk,zks); PUT(wa,8,t);
    f2 b1=cmulp(b0,z1,z1s); PUT(wa,1,b1); t=cmulp(t,zk,zks); PUT(wa,10,t);
    f2 b2=cmulp(b1,z1,z1s); PUT(wa,2,b2); t=cmulp(t,zk,zks); PUT(wa,11,t);
    f2 b3=cmulp(b2,z1,z1s); PUT(wa,3,b3); t=cmulp(t,zk,zks); PUT(wa,16,t);
    f2 b4=cmulp(b3,z1,z1s); PUT(wa,4,b4); t=cmulp(t,zk,zks); PUT(wa,17,t);
    f2 b5=cmulp(b4,z1,z1s); PUT(wa,5,b5); t=cmulp(t,zk,zks); PUT(wa,20,t);
    f2 b6=cmulp(b5,z1,z1s); PUT(wa,6,b6); t=cmulp(t,zk,zks); PUT(wa,21,t);
    f2 b7=cmulp(b6,z1,z1s); PUT(wa,7,b7); t=cmulp(t,zk,zks); PUT(wa,18,t);
    f2 s=cmulp(b4,zk,zks); PUT(wa,12,s); t=cmulp(b2,zk,zks); PUT(wa,9,t);
    s=cmulp(s,zk,zks); PUT(wa,14,s); t=cmulp(b6,zk,zks); PUT(wa,13,t);
    s=cmulp(s,zk,zks); PUT(wa,15,s);
    #undef PUT
    WAITL0();
    const int ra=(r>21||r==19)?0:r;
    #pragma unroll
    for(int ks=0;ks<8;ks++){ u4 fa=ldsr128(TA+ra*TP+ks*32+h*16), fb=ldsr128(TB+r*TP+ks*32+h*16); acc=mfma32h(fa,fb,acc); }
    WAITL0();
  }
  #pragma unroll
  for(int i=0;i<11;i++) kp[i]=acc[i];
  return ksc;
}
DI void kgen_exp(u32 cb,int lane,const float (&acc)[11]){
  const int r=lane&31,h=lane>>5;
  { const u32 a4=cb+r*16+h*8, a5=cb+r*16+h*1024;
    #define PK(addr,base,reg) do{ *(LAS _Float16*)((addr)+2*(base))=(_Float16)acc[reg]; }while(0)
    PK(a4,0,0);PK(a4,1,1);PK(a4,2,2);PK(a4,3,3); PK(a4,256,4);PK(a4,258,5);PK(a4,512,6);PK(a4,768,7);
    PK(a5,1024,8);PK(a5,1280,9);PK(a5,2048,10);
    #undef PK
  }
  WAITL0();
  { const u32 g0=cb+2*(2048-8*lane);
    u32 a0=lds16(g0), b0=lds16(g0-16), a1=lds16(g0-1024), b1=lds16(g0-1024-16);
    u32 f2_=lds16(g0-2048+(lane?8:0)), a2=lds16(g0-2048), m2=lds16(g0-2048-8), b2=lds16(g0-2048-16);
    u32 e[11];
    #pragma unroll
    for(int j=0;j<11;j++) e[j]=lds16(cb+2u*(u32)rbf((float)(512-8*lane+3-j)));
    u32 k01=*(LAS u32*)cb, k23=*(LAS u32*)(cb+4);
    WAITL0();
    const u32 wo=cb+lane*16;
    { u32 aa=dup(a0),bb=dup(b0),c=(lane&1)?b0:a0, cbv=cat(c,b0), ac=cat(a0,c);
      ldsw128(wo,u4{aa,aa,cbv,bb}); ldsw128(wo+4160,u4{aa,aa,ac,bb}); ldsw128(wo+8320,u4{aa,aa,aa,cbv}); ldsw128(wo+12480,u4{aa,aa,aa,ac}); }
    { u32 aa=dup(a1),bb=dup(b1),c=(lane&1)?b1:a1, cbv=cat(c,b1), ac=cat(a1,c);
      ldsw128(wo+1024,u4{aa,aa,cbv,bb}); ldsw128(wo+1024+4160,u4{aa,aa,ac,bb}); ldsw128(wo+1024+8320,u4{aa,aa,aa,cbv}); ldsw128(wo+1024+12480,u4{aa,aa,aa,ac}); }
    { u32 aa=dup(a2),mm=dup(m2),bb=dup(b2),am=cat(a2,m2),fa=cat(f2_,a2),mb=cat(m2,b2);
      ldsw128(wo+2048,u4{aa,am,mm,bb}); ldsw128(wo+2048+4160,u4{aa,aa,mm,mb}); ldsw128(wo+2048+8320,u4{aa,aa,am,mm}); ldsw128(wo+2048+12480,u4{fa,aa,aa,mm}); }
    { u32 o0=cat(e[0],e[1]),o1=cat(e[2],e[3]),o2=cat(e[4],e[5]),o3=cat(e[6],e[7]),o4=cat(e[8],e[9]);
      u32 p1=cat(e[1],e[2]),d0=cat(e[3],e[4]),d1=cat(e[5],e[6]),d2=cat(e[7],e[8]),d3=cat(e[9],e[10]);
      ldsw128(wo+3072,u4{d0,d1,d2,d3}); ldsw128(wo+3072+4160,u4{o1,o2,o3,o4}); ldsw128(wo+3072+8320,u4{p1,d0,d1,d2}); ldsw128(wo+3072+12480,u4{o0,o1,o2,o3}); }
    if(lane<4){ if(lane){k01=0;k23=0;}
      u32 d0=k01&0xffffu, o1=__builtin_amdgcn_alignbit(k01,k01,16), o0=__builtin_amdgcn_alignbit(k23,k23,16), p1=(k23&0xffffu)|(k01&0xffff0000u);
      ldsw128(wo+4096,u4{d0,0,0,0}); ldsw128(wo+4096+4160,u4{o1,0,0,0}); ldsw128(wo+4096+8320,u4{p1,d0,0,0}); ldsw128(wo+4096+12480,u4{o0,o1,0,0}); }
  }
}
DI f4 mfma16h(u4 a,u4 b,f4 c){ return __builtin_amdgcn_mfma_f32_16x16x32_f16(__builtin_bit_cast(hf8,a),__builtin_bit_cast(hf8,b),c,0,0,0); }
#define RDA(S,i,m) asm volatile("ds_read_b64 %0, %2 offset:%3\n\tds_read_b64 %1, %2 offset:%4" : "=&v"(AL[S][i][m]),"=&v"(AH[S][i][m]) : "v"(ka), "n"((3-(i))*1024+(1-(m))*32), "n"((3-(i))*1024+(1-(m))*32+8))
#define RDB(S,i,ad,o) asm volatile("ds_read_b128 %0, %1 offset:%2" : "=v"(BF[S][i]) : "v"(ad), "n"(o))
#define TIEA(S,i) "+v"(AL[S][i][0]),"+v"(AH[S][i][0]),"+v"(AL[S][i][1]),"+v"(AH[S][i][1])
#define CV_A(S,i,m) u4{AL[S][i][m][0],AL[S][i][m][1],AH[S][i][m][0],AH[S][i][m][1]}
#define CV_M(S,i,n) do{ y[0][n]=mfma16h(CV_A(S,i,0),BF[S][(n)-(i)],y[0][n]); y[1][n]=mfma16h(CV_A(S,i,1),BF[S][(n)-(i)],y[1][n]); SCHED(); }while(0)
#define CV_U0() u32 u0=u1-256; u0=((int)u0<(int)uz)?uz:u0
#define CV_SETUP() const int r=lane&15,q=lane>>4; u32 ka=cb+(r&3)*4160+16*q-8*(r>>2)+992; const u32 uz=cb+UOF+q*1040; u32 u1=uz+(r+17)*16; u2 AL[2][4][2],AH[2][4][2]; u4 BF[2][4]
DI void conv_help(u32 cb,int lane){
  CV_SETUP(); f4 y[2][4]={};
  #define HP_WT(S) asm volatile("s_waitcnt lgkmcnt(0)" : TIEA(S,2),TIEA(S,3),"+v"(BF[S][0]),"+v"(BF[S][1]))
  #define HP_STEP(C,N) do{ CV_U0(); RDA(N,2,0); RDA(N,2,1); RDB(N,1,u1,0); SCHED(); CV_M(C,2,3); RDA(N,3,0); RDA(N,3,1); RDB(N,0,u0,0); ka-=64; u1-=16; SCHED(); CV_M(C,3,3); HP_WT(N); }while(0)
  { CV_U0(); RDA(0,2,0); RDA(0,2,1); RDA(0,3,0); RDA(0,3,1); RDB(0,0,u0,0); RDB(0,1,u1,0); ka-=64; u1-=16; HP_WT(0); }
  #pragma unroll 1
  for(int dl=0;dl<8;dl++){ HP_STEP(0,1); if(dl==7){ ka+=64; u1+=16; } HP_STEP(1,0); }
  *(LAS f4*)(cb+q*4160+r*16)=y[0][3]; *(LAS f4*)(cb+q*4160+256+r*16)=y[1][3];
  WAITL0();
}
DI void conv_wave(const Params& p,rsrc_t rs,int d,u32 cb,u32 flag,int lane,float ksc,float dsk){
  CV_SETUP(); f4 y[2][4]={};
  #define CV_WT(S) asm volatile("s_waitcnt lgkmcnt(0)" : TIEA(S,0),TIEA(S,1),TIEA(S,2),"+v"(BF[S][0]),"+v"(BF[S][1]),"+v"(BF[S][2]),"+v"(BF[S][3]))
  #define CV_STEP(C,N) do{ CV_U0(); CV_M(C,0,0); RDA(N,0,0); RDB(N,0,u0,0); SCHED(); CV_M(C,0,1); RDA(N,0,1); RDB(N,1,u1,0); SCHED(); CV_M(C,0,2); RDA(N,1,0); RDB(N,2,u1,256); SCHED(); \
      CV_M(C,0,3); RDA(N,1,1); RDB(N,3,u1,512); SCHED(); CV_M(C,1,1); RDA(N,2,0); SCHED(); CV_M(C,1,2); RDA(N,2,1); ka-=64; u1-=16; SCHED(); CV_M(C,1,3); CV_M(C,2,2); CV_WT(N); }while(0)
  { CV_U0(); RDA(0,0,0); RDA(0,0,1); RDA(0,1,0); RDA(0,1,1); RDA(0,2,0); RDA(0,2,1); RDB(0,0,u0,0); RDB(0,1,u1,0); RDB(0,2,u1,256); RDB(0,3,u1,512); ka-=64; u1-=16; CV_WT(0); }
  #pragma unroll 1
  for(int dl=0;dl<8;dl++){ CV_STEP(0,1); if(dl==7){ ka+=64; u1+=16; } CV_STEP(1,0); }
  ldswait(flag,2u);
  y[0][3]+=*(LAS f4*)(cb+q*4160+r*16); y[1][3]+=*(LAS f4*)(cb+q*4160+256+r*16);
  #pragma unroll
  for(int n=0;n<4;n++){ u2 o[2];
    #pragma unroll
    for(int m=0;m<2;m++){ u2 uv=ldsr64(cb+UOF+(2*m+(q>>1))*1040+(16*n+r+1)*16+8*(q&1));
      #pragma unroll
      for(int e=0;e<2;e++) o[m][e]=pkbf2(fma2(f2{hlo(uv[e]),hhi(uv[e])},f2{dsk,dsk},f2{y[m][n][2*e],y[m][n][2*e+1]}*ksc)); }
    auto sx=__builtin_amdgcn_permlane16_swap(o[0][0],o[1][0],false,false); auto sy=__builtin_amdgcn_permlane16_swap(o[0][1],o[1][1],false,false);
    st16(rs,OFF_AT+d*4096+(16*n+r)*64+(q&1)*32+(q>>1)*16,u4{sx[0],sy[0],sx[1],sy[1]}); }
  WAITV(0);
}

struct Frag { u4 x[4], w[4][2]; };
__global__ __launch_bounds__(512,2) void fused(Params p){
  extern __shared__ __attribute__((aligned(16))) char sm[];
  const u32 smb=(u32)(size_t)(LAS char*)sm;
  u64 pc0; asm volatile("s_getpc_b64 %0" : "=s"(pc0));
  const int b=blockIdx.x, xc=b&7, idx=b>>3;
  const int tid=threadIdx.x, w=uni(tid>>6); int lane=tid&63;
  const rsrc_t rs=__builtin_amdgcn_make_buffer_rsrc(p.ws,0,WS_END,0x00020000);
  const u4 srdS=mksrd(p.ws);
  const int rtB=8*(xc>>1)+(idx&7), ct=4*(xc&1)+(idx>>3);
  const bool hasc=idx<28 && idx+28*(w&3)<80 && w>=4;
  const int dch=80*xc+(hasc? idx+28*(w&3) : 0);
  const u32 cb=smb+CONV+(w&3)*CVS;
  const int rt=4*(xc>>1)+(idx&3), j=7*(xc&1)+(idx>>2), mi=w&3, g1=w>>2;
  u32 tag; float ksc=0.f, dsk=0.f;
  STAMP(0,0);
  auto warm=[&](int wq){
    const u4 sp=mksrd((const char*)p.wsp+(size_t)(rtB*64+ct*8+2*wq)*4096); setm0<JNK>(smb);
    asm volatile("buffer_load_dword %0, %1, 0 offen" WNT " lds" :: "v"(lane*128), "s"(sp) : "memory");
  };
  if(idx>=28){
    const int a=xc*4+idx-28, r=lane&15, q=lane>>4;
    { u4 sc=mksrd((const void*)(pc0&~127ull)); sc[2]=CODE_BYTES; setm0<JNK>(smb); touch(sc,lane*128,w*8192); }
    const u32 ep=ld4(rs,OFF_EP);
    u4 xr[10]; const char* xs=(const char*)p.x+(size_t)(64*a+8*w+(lane>>3))*1280+(lane&7)*16;
    #pragma unroll
    for(int i=0;i<10;i++) xr[i]=*(const u4*)(xs+i*128);
    if(tid<32) *(LAS u32*)(smb+PM+tid*4)=0u;
    tag=uni(ep)+1u;
    float s0=0.f,s1=0.f,q0=0.f,q1=0.f;
    #pragma unroll
    for(int i=0;i<10;i++){ s0=dot2(xr[i][0],0x3f803f80u,s0); q0=dot2(xr[i][0],xr[i][0],q0); s1=dot2(xr[i][1],0x3f803f80u,s1); q1=dot2(xr[i][1],xr[i][1],q1);
      s0=dot2(xr[i][2],0x3f803f80u,s0); q0=dot2(xr[i][2],xr[i][2],q0); s1=dot2(xr[i][3],0x3f803f80u,s1); q1=dot2(xr[i][3],xr[i][3],q1); }
    s0+=s1; q0+=q1;
    #define DPPADD(v,c) v+=__int_as_float(__builtin_amdgcn_update_dpp(0,__float_as_int(v),c,0xf,0xf,false))
    DPPADD(s0,0xB1); DPPADD(q0,0xB1); DPPADD(s0,0x4E); DPPADD(q0,0x4E); DPPADD(s0,0x141); DPPADD(q0,0x141);
    float m1,ri; { float mean=s0*(1.f/640.f); ln_fin(mean,q0*(1.f/640.f)-mean*mean,m1,ri); }
    if((lane&7)==0) st16(rs,OFF_RS+(64*a+8*w+(lane>>3))*16,u4{__float_as_uint(m1),tag,__float_as_uint(ri),tag});
    STAMP(5,0);
    #pragma unroll
    for(int i=0;i<10;i++){ u4 o;
      #pragma unroll
      for(int e=0;e<4;e++) o[e]=pkhf2(fma2(unpk(xr[i][e]),f2{ri,ri},f2{-m1*ri,-m1*ri}));
      ldsw128(smb+RB+(8*w+(lane>>3))*1296+(lane&7)*16+i*128,o); }
    bar();
    #pragma unroll
    for(int i=0;i<10;i++){ const int c=w*10+i; const u32 ad=smb+RB+(32*((r>>1)&1)+8*q+(r>>2))*1296+c*16+(r&1)*8; u2 lo=ldstr(ad), hi=ldstr(ad+4*1296);
      st16(rs,OFF_XNT+(8*c+(r&7))*4096+(64*a+32*(r>>3)+8*q)*2,u4{lo[0],lo[1],hi[0],hi[1]}); }
    WAITV(0);
    if(arrive(smb+C_Z1,lane)==7u && lane==0) st4(rs,OFF_FX+a*128,tag);
    STAMP(6,0);
    if(g1) warm(mi);
    if(a<27){ const u4 srdO=mksrd((const char*)p.wout+a*(32*1280)+w*5120); const u32 mo=smb+w*5120; setm0<0>(mo);
      dma<0>(srdO,lane*16,0); dma<1024>(srdO,lane*16,0); dma<2048>(srdO,lane*16,0); dma<3072>(srdO,lane*16,0); setm0<4096>(mo); dma<0>(srdO,lane*16,4096); }
    WAITV(0); bar();
    if(a<27){
      #pragma unroll
      for(int i=0;i<5;i++){ u32 ad=smb+(8*q+(r>>2))*1280+(w*5+i)*32+(r&3)*8; u2 lo=ldstr(ad), hi=ldstr(ad+4*1280);
        st16(rs,OFF_WOF+(u32)((w*27+a)*5+i)*1024+lane*16,u4{lo[0],lo[1],hi[0],hi[1]}); } }
    WAITV(0); bar();
    if(tid==0) st4(rs,OFF_FW+a*128,tag);
  } else {
    const int r=lane&31, h=lane>>5;
    const u4 srdX=mksrd((const char*)p.x+(size_t)rt*128*1280);
    const u4 srdW=mksrd((const char*)p.win+j*128-3072);
    const u32 mw=smb+w*4096;
    u32 vx0,vx1,vw1,vw2,epv;
    { u32 lr=lane>>3,c=lane&7; vx0=(16*w+lr)*1280+((c^(lr>>1))<<4); vx1=(vx0^64u)+9216;
      vw1=(8*w+lr)*3456+((c^((lr&2)<<1))<<4); vw2=vw1+1024+(j==13?0:14*128); }
    u32 kx[10]; { u32 k=KROT;
      #pragma unroll
      for(int i=0;i<10;i++){ kx[i]=k*128; k=k==9?0:k+1; } }
    auto issue1=[&](auto sc,int k){ constexpr int s=sc.value;
      if(k==0){ setm0<(s&3)*STG>(mw); dma<0>(srdX,vx0,kx[s]); } if(k==1) dma<1024>(srdX,vx1,kx[s]); if(k==2) dma<2048>(srdW,vw2,kx[s]*1728); if(k==3) dma<3072>(srdW,vw1,kx[s]*1728); };
    auto issue=[&](auto sc){ issue1(sc,0); issue1(sc,1); issue1(sc,2); issue1(sc,3); };
    issue(std::integral_constant<int,0>{});
    u32 pr[6];
    gload4<0>(epv,0u,p.ws+OFF_EP);
    gload4<0>(pr[0],lane*4,(const char*)(p.lar+dch*128)); gload4<0>(pr[1],lane*4,(const char*)(p.aim+dch*128)); gload4<0>(pr[2],lane*4,(const char*)(p.cre+dch*128)); gload4<0>(pr[3],lane*4,(const char*)(p.cim+dch*128));
    gload2(pr[4],0u,(const char*)(p.ldt+dch)); gload2(pr[5],0u,(const char*)(p.dsk+dch));
    asm volatile("s_barrier" ::: "memory");
    issue(std::integral_constant<int,1>{});
    if(tid<32) *(LAS u32*)(smb+PM+tid*4)=0u;
    u32 ax[2][4],aw[2][4][2];
    { u32 row=32*mi+r, a0=smb+(row>>4)*4096+(row&15)*128+((h^((r>>1)&7))<<4);
      u32 qq=(lane&15)>>2, w0=smb+2048+g1*1024+h*4096+qq*128+8*(lane&1)+(((2*((lane>>4)&1)+((lane>>1)&1))^((qq&2)<<1))<<4);
      #pragma unroll
      for(int i=0;i<2;i++)
        #pragma unroll
        for(int k=0;k<4;k++){ const u32 kk=(k+mi)&3, wo=i*2*STG; ax[i][k]=opq((a0^(kk*32u))+wo); aw[i][k][0]=opq(w0+kk*8192+wo); aw[i][k][1]=opq((w0^64u)+kk*8192+wo); } }
    u4 sel; { const u32 o=((lane&15)==((lane>>4)&1))?0x3f803f80u:0u; sel=u4{o,o,o,o}; asm volatile("" : "+v"(sel)); }
    Frag F[2];
    asm volatile("s_waitcnt vmcnt(4)" : "+v"(epv),"+v"(pr[0]),"+v"(pr[1]),"+v"(pr[2]),"+v"(pr[3]),"+v"(pr[4]),"+v"(pr[5]) :: "memory");
    bar();
    tag=uni(epv)+1u; dsk=bflo(pr[5]);
    #pragma unroll
    for(int k=0;k<4;k++){
      #pragma unroll
      for(int n=0;n<2;n++){ u2 lo=ldstr(aw[0][k][n]), hi=ldstr(aw[0][k][n]+512); F[0].w[k][n]=u4{lo[0],lo[1],hi[0],hi[1]}; }
      F[0].x[k]=ldsr128(ax[0][k]); }
    issue(std::integral_constant<int,2>{});
    STAMP(1,0);
    f16v acc[2]={}; f4 cacc[2]={}; u4 G;
    const u32 go=OFF_RS+rt*2048+mi*512, cp=smb+MISC+g1*1024;
    sfor<10>([&](auto ic){
      constexpr int s=ic.value; Frag& cur=F[s&1]; Frag& nxt=F[(s&1)^1];
      if constexpr(s<9){ WAITV(s==8? 2 : s==7? 6 : 4); asm volatile("s_barrier" ::: "memory"); }
      auto rw=[&](int k,int n){ if constexpr(s<9){ const int i=((s+1)>>1)&1; const u32 so=((s+1)&1)*STG; u2 lo=ldstr(aw[i][k][n]+so), hi=ldstr(aw[i][k][n]+so+512); nxt.w[k][n]=u4{lo[0],lo[1],hi[0],hi[1]}; } };
      auto rx=[&](int k){ if constexpr(s<9){ const int i=((s+1)>>1)&1; const u32 so=((s+1)&1)*STG; nxt.x[k]=ldsr128(ax[i][k]+so); } };
      #pragma unroll
      for(int k=0;k<4;k++){
        acc[0]=mfma32b(cur.w[k][0],cur.x[k],acc[0]); SCHED();
        if constexpr(s+3<=9) issue1(std::integral_constant<int,s+3>{},k);
        rw(k,0); rx(k);
        SCHED();
        acc[1]=mfma32b(cur.w[k][1],cur.x[k],acc[1]); SCHED();
        rw(k,1);
        if(k==0){ cacc[0]=mfma16(cur.w[0][0],sel,cacc[0]); cacc[1]=mfma16(cur.w[0][1],sel,cacc[1]); }
        if constexpr(s==6){ if(k==3){ gload<0>(G,r*16,p.ws+go); if(g1) warm(mi); else { setm0<JNK>(smb); touch(srdS,0u,go); } } }
        if constexpr(s==8){ if(k==3) signal(smb+C_DONE,lane); }
        if constexpr(s==9){ if(k==1){
          if((lane&14)==0){ *(LAS f4*)(cp+mi*256+(lane&1)*64+(lane>>4)*16)=cacc[0]; *(LAS f4*)(cp+mi*256+128+(lane&1)*64+(lane>>4)*16)=cacc[1]; }
          signal(smb+C_CS+g1*4,lane); } }
        SCHED();
      }
    });
    RST(0);
    if(!g1) __builtin_amdgcn_s_setprio(2);
    asm volatile("s_waitcnt vmcnt(1)" : "+v"(G) :: "memory");
    RST(1);
    ldswait(smb+C_CS+g1*4,4u);
    const float cs0=*(LAS float*)(cp+h*512+r*4)+*(LAS float*)(cp+h*512+256+r*4), cs1=*(LAS float*)(cp+h*512+128+r*4)+*(LAS float*)(cp+h*512+384+r*4);
    RST(2);
    #pragma unroll 1
    for(int sp=0;sp<SPIN && !__all(G[1]==tag&&G[3]==tag);sp++)asm volatile("global_load_dwordx4 %0, %1, %2 sc1\n\ts_waitcnt vmcnt(0)" : "=&v"(G) : "v"(r*16), "s"(p.ws+go) : "memory");
    RST(3);
    const float nm=-__uint_as_float(G[0]), ri1=__uint_as_float(G[2]);
    acc[0]=__builtin_amdgcn_mfma_f32_32x32x2f32(cs0,nm,acc[0],0,0,0); acc[1]=__builtin_amdgcn_mfma_f32_32x32x2f32(cs1,nm,acc[1],0,0,0);
    if(g1){
      u4 zq[4][4];
      #pragma unroll
      for(int e=0;e<16;e++) zq[3][e>>2][e&3]=pkbf2(f2{acc[e>>3][2*(e&7)],acc[e>>3][2*(e&7)+1]}*ri1);
      if(w<7){ ldswait(smb+C_DONE,8u);
        #pragma unroll
        for(int i=0;i<4;i++) ldsw128(cb+UOF+i*1024+lane*16,zq[3][i]);
        signal(smb+C_ZQ,lane); }
      if(w==7){
        ldswait(smb+C_ZQ,3u);
        #pragma unroll
        for(int c=0;c<3;c++)
          #pragma unroll
          for(int i=0;i<4;i++) zq[c][i]=ldsr128(smb+c*CVS+UOF+i*1024+lane*16);
        WAITL0(); signal(smb+C_FULL,lane);
        #pragma unroll 1
        for(int c=0;c<4;c++){ u4 o[4];
          #pragma unroll
          for(int e=0;e<16;e++) o[e>>2][e&3]=pkbf2(gelu2(unpk(zq[0][e>>2][e&3])));
          #pragma unroll
          for(int n=0;n<2;n++) if(2*j+n<27){ const u32 oo=OFF_Z1T+((rt*8+2*c+(r>>4))*27+2*j+n)*1024+(h*16+(r&15))*16;
            st16(rs,oo,u4{o[2*n][0],o[2*n][1],o[2*n+1][0],o[2*n+1][1]}); st16(rs,oo+512,u4{o[2*n][2],o[2*n][3],o[2*n+1][2],o[2*n+1][3]}); }
          #pragma unroll
          for(int i=0;i<4;i++){ zq[0][i]=zq[1][i]; zq[1][i]=zq[2][i]; zq[2][i]=zq[3][i]; } }
        WAITV(0); if(lane==0) st4(rs,OFF_F1+(rt*16+j)*128,tag);
      } else {
        u4 uu[4]; float kp[11]; u32 fxv=0;
        auto mid=[&](){ fxv=ld4(rs,OFF_FX+r*128); asm volatile("" ::: "memory"); };
        if(hasc){ ksc=kgen_tab(cb,lane,pr,kp,mid);
          if(!__all(fxv==tag)) pollw<16>(rs,OFF_FX+r*128,tag);
          #pragma unroll
          for(int i=0;i<4;i++) uu[i]=ld16(rs,OFF_XNT+dch*4096+(lane+64*i)*16);
          asm volatile("" ::: "memory");
          kgen_exp(cb,lane,kp);
          ldswait(smb+C_FULL,1u);
          #pragma unroll
          for(int i=0;i<4;i++){ const int x=lane+64*i; ldsw128(cb+UOF+(x&3)*1040+((x>>2)+1)*16,uu[i]); }
          if(lane<4) ldsw128(cb+UOF+lane*1040,u4{0,0,0,0});
          WAITL0(); signal(smb+C_KP+mi*4,lane);
        }
        STAMP(13,4);
      }
    } else {
      u32 zp[2][8];
      #pragma unroll
      for(int e=0;e<16;e++) zp[e>>3][e&7]=pkbf2(gelu2(f2{acc[e>>3][2*(e&7)],acc[e>>3][2*(e&7)+1]}*ri1));
      float S=0.f,Q=0.f;
      if(j<13){
        #pragma unroll
        for(int e=0;e<8;e++){ S=dot2(zp[0][e],0x3f803f80u,S); Q=dot2(zp[0][e],zp[0][e],Q); } }
      #pragma unroll
      for(int e=0;e<8;e++){ S=dot2(zp[1][e],0x3f803f80u,S); Q=dot2(zp[1][e],zp[1][e],Q); }
      #define XSUM32(v) do{ auto t_=__builtin_amdgcn_permlane32_swap(__float_as_uint(v),__float_as_uint(v),false,false); v=__uint_as_float(t_[0])+__uint_as_float(t_[1]); }while(0)
      XSUM32(S); XSUM32(Q);
      const u32 sto=OFF_ST+rt*(14*2048)+(32*mi+r)*16;
      if(h==0) st16(rs,sto+j*2048,u4{__float_as_uint(S),tag,__float_as_uint(Q),tag});
      RST(6);
      u4 g[7];
      __builtin_amdgcn_s_sleep(PDLY);
      #pragma unroll 1
      for(int sp=0;sp<SPIN;sp++){ bool ok=true; asm volatile("" ::: "memory");
        #pragma unroll
        for(int i=0;i<7;i++) g[i]=ld16(rs,sto+(h*7+i)*2048);
        #pragma unroll
        for(int i=0;i<7;i++) ok=ok&&g[i][1]==tag&&g[i][3]==tag;
        if(__all(ok)) break;
        __builtin_amdgcn_s_sleep(4); }
      RST(7);
      S=0.f; Q=0.f;
      #pragma unroll
      for(int i=0;i<7;i++){ S+=__uint_as_float(g[i][0]); Q+=__uint_as_float(g[i][2]); }
      XSUM32(S); XSUM32(Q);
      float mean=S*(1.f/864.f), m2,r2; ln_fin(mean,Q*(1.f/864.f)-mean*mean,m2,r2); m2*=-r2;
      ldswait(smb+C_DONE,8u);
      const u32 tb=smb+RB+mi*RING, ad=tb+(8*(lane>>4)+((lane&15)>>2))*80+(lane&3)*8;
      const u32 zo=OFF_Z2F+((rt*4+mi)*54+(j==13?-2:2+4*j))*1024+lane*16;
      #pragma unroll
      for(int n=0;n<2;n++){
        #pragma unroll
        for(int g2=0;g2<4;g2++){ u2 o;
          #pragma unroll
          for(int e=0;e<2;e++) o[e]=pkbf2(fma2(unpk(zp[n][2*g2+e]),f2{r2,r2},f2{m2,m2}));
          *(LAS u2*)(tb+r*80+g2*16+h*8)=o; }
        u2 l0=ldstr(ad), h0=ldstr(ad+320), l1=ldstr(ad+32), h1=ldstr(ad+32+320);
        if(j<13||n){ st16(rs,zo+n*2048,u4{l0[0],l0[1],h0[0],h0[1]}); st16(rs,zo+n*2048+1024,u4{l1[0],l1[1],h1[0],h1[1]}); } }
      RST(8);
      WAITV(0);
      if(arrive(smb+C_Z2,lane)==3u && lane==0) st4(rs,OFF_F2+(rt*16+j)*128,tag);
      RST(9);
    }
  }
  const int ft0=ct<7?7*ct:47;
  if(w>=4){
    STAMP(15,4);
    if(hasc){
      conv_wave(p,rs,dch,cb,smb+C_KP+mi*4,lane,ksc,dsk);
      if(arrive(smb+C_CV,lane)==(idx<24?2u:1u) && lane==0) st4(rs,OFF_FA+b*128,tag);
    }
    if(w==7){
      const u32 fo=lane<27? OFF_FW+lane*128 : OFF_F1+((rtB>>1)*16+(((ft0>>1)+(lane&3))>>1))*128; bool seen=false;
      if(idx<28){
        #pragma unroll 1
        for(int c=0;c<(idx<24?3:2);c++){ ldswait(smb+C_KP+c*4,1u); const u32 fv=ld4(rs,fo); asm volatile("" ::: "memory");
          conv_help(smb+c*CVS,lane); signal(smb+C_KP+c*4,lane);
          seen=__all(fv==tag); if(seen && lane==0) *(LAS u32*)(smb+C_Z1R)=1u; } }
      if(!seen){ pollw<16>(rs,fo,tag); if(lane==0) *(LAS u32*)(smb+C_Z1R)=1u; }
    }
    STAMP(14,4);
    signal(smb+C_FREE,lane);
    ldswait(smb+C_FREE,8u);
    { const u32 so=OFF_WOF+ct*(135*1024)+(w-4)*4096;
      #pragma unroll 1
      for(u32 i=(w-4)*4096;i<135*1024;i+=16384){ setm0<0>(smb+i); const u32 o=OFF_WOF+ct*(135*1024)+i; dma<0>(srdS,lane*16,o); dma<1024>(srdS,lane*16,o); dma<2048>(srdS,lane*16,o); dma<3072>(srdS,lane*16,o); } (void)so; }
    if(w==7){ const u32 vo=(lane>>3)*4096+(lane&7)*16;
      pollw(rs,OFF_FA+((lane<28?lane:0)*8+ct)*128,tag);
      #pragma unroll 1
      for(int i=0;i<10;i++){ setm0<ATL>(smb+i*1024); dma<0>(srdS,vo,OFF_AT+(u32)(80*ct+8*i)*4096+rtB*128); } }
    if(w==6 && b==255){
      #pragma unroll 1
      for(int sp=0;sp<SPIN;sp++){ const u32 o=OFF_FA+lane*128; u32 v0=ld4(rs,o),v1=ld4(rs,o+8192),v2=ld4(rs,o+16384),v3=ld4(rs,lane<32? o+24576 : OFF_FW+(lane-32)*128); asm volatile("" ::: "memory");
        if(__all(v0==tag&&v1==tag&&v2==tag&&v3==tag)) break; __builtin_amdgcn_s_sleep(4); }
      if(lane==0) st4(rs,OFF_EP,tag); }
    WAITV(0);
  }
  int w2=w; asm volatile("" : "+s"(w2));
  if(w2<4){
    __builtin_amdgcn_s_setprio(G2PRIO);
    int r=lane&15, q=lane>>4;
    const u32 rb=smb+RB+w*RING;
    u4 srdA=mksrd((const char*)p.wsp+(size_t)rtB*64*4096+w*1024-4096);
    const char* zb=p.ws+OFF_Z2F+(u32)(16*w*54+ft0+3)*1024;
    u32 voA[2]; { u32 lr=lane>>3,c=lane&7; voA[0]=lr*4096+((c^(lr>>1))<<4); voA[1]=voA[0]^64u; }
    u32 sofa[8];
    #pragma unroll
    for(int g=0;g<8;g++) sofa[g]=((g+2*w)&7)*32768+4096-(g&3)*1024;
    const u32 voB=lane*16;
    u32 la[2]; la[0]=rb+r*128+((q^(r>>1))<<4); la[1]=la[0]^64u;
    f4 acc[4][7]={}; u4 B[3][7]; u4 A[2][4]; u2 z1[7];
    auto issueA=[&](auto sc,auto gc){ constexpr int s=sc.value,g=gc.value; if constexpr((g&3)==0) setm0<((s+1)%3)*8192+g*1024>(rb); dma<(g&3)*1024>(srdA,voA[g&1],sofa[g]);
      if constexpr(g==7){ u64 a=((u64)srdA[1]<<32|srdA[0])+128; srdA[0]=(u32)a; srdA[1]=(u32)(a>>32); } };

    u32 bsr;
    sfor<3>([&](auto s){ sfor<8>([&](auto g){ issueA(s,g); }); });
    #pragma unroll
    for(int m=0;m<4;m++)
      #pragma unroll
      for(int n=0;n<7;n++) asm volatile("" : "+v"(acc[m][n]));
    { const int hf=(ft0>>1)+q; pollw(rs,OFF_F2+((4*w+(lane&3))*16+(hf==0?13:(hf-1)>>1))*128,tag); }
    RST(10);
    sfor<3>([&](auto hh){ const char* s=zb+(u32)hh.value*(54*1024); sfor<7>([&](auto n){ gload<(n.value-3)*1024>(B[hh][n],voB,s); }); });
    #pragma unroll
    for(int m=0;m<4;m++) A[0][m]=ldsr128(la[0]+m*2048+8192);
    const char* z1b=p.ws+OFF_Z1T+(u32)((rtB*4+w)*27+(ft0>>1))*1024;
    sfor<16>([&](auto hc){
      constexpr int h=hc.value, S=h%3, P=h&1;
      constexpr int cnt = h<2? 14 : ((h+1<16?7:0)+(h+2<16?7:0) + ((((h-2)&1)&&((h-2)/2+3<8))?8:0) + ((((h-1)&1)&&((h-1)/2+3<8))?8:0) + (h>=14?8:0));
      waitB<cnt>(B[S]); waitA(A[P]);
      if constexpr(h==13) ldswait(smb+C_Z1R,1u);
      if constexpr(h+1<16){
        #pragma unroll
        for(int m=0;m<4;m++) A[P^1][m]=ldsr128(la[(h+1)&1]+m*2048+((((h+1)>>1)+1)%3)*8192);
      }
      const char* s=zb+(u32)(h+3)*(54*1024);
      sfor<7>([&](auto n){
        mfma4(acc[0][n],acc[1][n],acc[2][n],acc[3][n],B[S][n],A[P]);
        if constexpr((h&1)&&(h/2+3<8)){ issueA(std::integral_constant<int,h/2+3>{},n); if constexpr(n.value==6) issueA(std::integral_constant<int,h/2+3>{},std::integral_constant<int,7>{}); }
        if constexpr(h+3<16) gload_t<(n.value-3)*1024>(B[S][n],voB,s);
        if constexpr(h==13){ gload8<(n.value>>1)*1024>(z1[n],voB,z1b+((n.value&1)? ((ft0&1)?1024:8) : (ft0&1)*8));
          if constexpr(n.value==6) gload2(bsr,(voB>>3)&30u,(const char*)(p.bsp+(rtB*4+w)*16)); }
      });
    });
    asm volatile("s_nop 15\n\ts_nop 3" ::: "memory");
    SCHED();
    STAMP(10,0);
    lane=__builtin_amdgcn_mbcnt_hi(~0u,__builtin_amdgcn_mbcnt_lo(~0u,0u)); asm volatile("" : "+v"(lane)); r=lane&15; q=lane>>4;
    #pragma unroll
    for(int m=1;m<4;m++)
      #pragma unroll
      for(int n=0;n<7;n++) *(LAS f4*)(rb+((m-1)*7+n)*1024+lane*16)=acc[m][n];
    WAITL0();
    signal(smb+C_RED,lane);
    ldswait(smb+C_RED,4u);
    #pragma unroll
    for(int m=1;m<4;m++){ u32 src=smb+RB+((w-m)&3)*RING+(m-1)*7168+lane*16;
      #pragma unroll
      for(int n=0;n<7;n++) acc[0][n]+=*(LAS f4*)(src+n*1024); }
    asm volatile("s_waitcnt vmcnt(0) lgkmcnt(0)" : "+v"(z1[0]),"+v"(z1[1]),"+v"(z1[2]),"+v"(z1[3]),"+v"(z1[4]),"+v"(z1[5]),"+v"(z1[6]),"+v"(bsr) :: "memory");
    const float bs=bflo(bsr);
    signal(smb+C_FREE,lane);
    u2 o[7];
    #pragma unroll
    for(int n=0;n<7;n++)
      #pragma unroll
      for(int e=0;e<2;e++)o[n][e]=pkbf2(unpk(z1[n][e])*(f2{acc[0][n][2*e],acc[0][n][2*e+1]}+bs));
    const u32 ho=OFF_HF+(u32)((rtB*4+w)*54+ft0)*512+(q>>1)*256+r*16;
    #pragma unroll
    for(int n=0;n<6;n+=2){ auto sx=__builtin_amdgcn_permlane16_swap(o[n][0],o[n+1][0],false,false); auto sy=__builtin_amdgcn_permlane16_swap(o[n][1],o[n+1][1],false,false);
      if(ct<7||n>=2) st16(rs,ho+(n+(q&1))*512,u4{sx[0],sy[0],sx[1],sy[1]}); }
    st8(rs,ho+6*512+(q&1)*8,o[6]);
    STAMP(11,0);
    WAITV(0);
    if(arrive(smb+C_H,lane)==3u && lane==0) st4(rs,OFF_FH+(rtB*8+ct)*128,tag);
    STAMP(12,0);
    __builtin_amdgcn_s_setprio(0);
  }
  { const int r=lane&15, q=lane>>4, m=w&3, kh=w>>2, t=rtB*64+m*16+r;
    u2 xv[5];
    #pragma unroll
    for(int n=0;n<5;n++) xv[n]=*(const u2*)(p.x+t*640+80*ct+16*n+4*q);
    if(w==0) pollw(rs,OFF_FH+(rtB*8+(lane&7))*128,tag);
    STAMP(16,0);
    bar();
    STAMP(17,0);
    u4 hf[14];
    #pragma unroll
    for(int k=0;k<14;k++) hf[k]=ld16(rs,OFF_HF+(u32)((rtB*4+m)*27+kh*13+k)*1024+lane*16);
    f4 acc[5]={}; u4 W[2][5];
    const u32 wl=smb+kh*13*5120+lane*16;
    #define WAD(k,n) (wl+((k)*5+(n))*1024)
    #pragma unroll
    for(int n=0;n<5;n++) W[0][n]=ldsr128(WAD(0,n));
    f2 xa[5][2];
    #pragma unroll
    for(int n=0;n<5;n++){ u2 av=ldstr(smb+ATL+(16*n+4*q+(r>>2))*128+(16*m+4*(r&3))*2); xa[n][0]=unpk(xv[n][0])+unpk(av[0]); xa[n][1]=unpk(xv[n][1])+unpk(av[1]); }
    if(kh) hf[0]=u4{0,0,0,0};
    #pragma unroll
    for(int k=0;k<14;k++){
      if(k<13){
        #pragma unroll
        for(int n=0;n<5;n++) W[(k+1)&1][n]=ldsr128(WAD(k+1,n));
      }
      SCHED();
      #pragma unroll
      for(int n=0;n<5;n++) acc[n]=mfma16(W[k&1][n],hf[k],acc[n]);
      SCHED();
    }
    STAMP(18,0);
    bar();
    if(kh){
      #pragma unroll
      for(int n=0;n<5;n++) *(LAS f4*)(smb+(m*5+n)*1024+lane*16)=acc[n];
    }
    bar();
    if(!kh){
      #pragma unroll
      for(int n=0;n<5;n++){ f4 o=acc[n]+*(LAS f4*)(smb+(m*5+n)*1024+lane*16); u2 res;
        #pragma unroll
        for(int e=0;e<2;e++)res[e]=pkbf2(xa[n][e]+f2{o[2*e],o[2*e+1]});
        *(u2*)(p.out+t*640+80*ct+16*n+4*q)=res; }
    }
    STAMP(19,0);
  }
}
extern "C" void kernel_launch(void* const* d_in,const int* in_sizes,int n_in,void* d_out,int out_size,void* d_ws,size_t ws_size,hipStream_t stream){
  Params p;
  p.x=(const u16*)d_in[0]; p.lar=(const u16*)d_in[1]; p.aim=(const u16*)d_in[2]; p.cre=(const u16*)d_in[3]; p.cim=(const u16*)d_in[4];
  p.ldt=(const u16*)d_in[5]; p.dsk=(const u16*)d_in[6]; p.win=(const u16*)d_in[8]; p.wsp=(const u16*)d_in[9]; p.bsp=(const u16*)d_in[10]; p.wout=(const u16*)d_in[11];
  p.out=(u16*)d_out; p.ws=(char*)d_ws;
  (void)hipFuncSetAttribute((const void*)fused,hipFuncAttributeMaxDynamicSharedMemorySize,LDS_TOTAL);
  fused<<<256,512,LDS_TOTAL,stream>>>(p);
}
```

```cpp
#define DIAG 99
#include <hip/hip_runtime.h>
#include <utility>
#pragma clang diagnostic ignored "-Wint-to-pointer-cast"
typedef unsigned short u16;
typedef unsigned int u32;
typedef unsigned long long u64;
typedef __attribute__((ext_vector_type(8))) __bf16 bf8;
typedef __attribute__((ext_vector_type(8))) _Float16 hf8;
typedef __attribute__((ext_vector_type(2))) __bf16 bf2;
typedef __attribute__((ext_vector_type(2))) _Float16 hf2;
typedef __attribute__((ext_vector_type(2))) float f2;
typedef __attribute__((ext_vector_type(4))) float f4;
typedef __attribute__((ext_vector_type(16))) float f16v;
typedef __attribute__((ext_vector_type(4))) u32 u4;
typedef __attribute__((ext_vector_type(2))) u32 u2;
typedef __attribute__((ext_vector_type(4))) short s4;
#define LAS __attribute__((address_space(3)))
#define GAS __attribute__((address_space(1)))
#define DI __device__ __forceinline__

constexpr u32 OFF_Z2F = 0;
constexpr u32 OFF_Z1T = OFF_Z2F + 64*54*1024;
constexpr u32 OFF_XNT = OFF_Z1T + 128*27*1024;
constexpr u32 OFF_WOF = OFF_XNT + 640*4096;
constexpr u32 OFF_HF  = OFF_WOF + 8*27*5*1024;
constexpr u32 OFF_AT  = OFF_HF + 128*27*1024;
constexpr u32 OFF_ST  = OFF_AT + 640*4096;
constexpr u32 OFF_RS  = OFF_ST + 16*14*2048+8192;
constexpr u32 OFF_EP  = OFF_RS + 2048*16;
constexpr u32 OFF_F2  = OFF_EP + 256;
constexpr u32 OFF_F1  = OFF_F2 + 256*128;
constexpr u32 OFF_FX  = OFF_F1 + 256*128;
constexpr u32 OFF_FW  = OFF_FX + 32*128;
constexpr u32 OFF_FH  = OFF_FW + 32*128;
constexpr u32 OFF_FA  = OFF_FH + 256*128;
constexpr u32 WS_END  = OFF_FA + 256*128;

struct Params {
  const u16 *x, *lar, *aim, *cre, *cim, *ldt, *dsk, *win, *wsp, *bsp, *wout;
  u16* out; char* ws;
};
#define STAMP(id,wv) do{ if((id)==DIAG && (wv)==0) dg1=__builtin_amdgcn_s_memrealtime(); }while(0)
#define RST(i) do{ if((i)+20==DIAG) dg1=__builtin_amdgcn_s_memrealtime(); }while(0)
template<class F,int... I> DI void sfor_impl(F&& f, std::integer_sequence<int,I...>){ (f(std::integral_constant<int,I>{}),...); }
template<int N,class F> DI void sfor(F&& f){ sfor_impl(f, std::make_integer_sequence<int,N>{}); }

DI float bflo(u32 v){ return __uint_as_float(v<<16); }
DI float bfhi(u32 v){ return __uint_as_float(v&0xffff0000u); }
DI float bf1(u16 v){ return __uint_as_float(((u32)v)<<16); }
DI u32 pkbf(float a,float b){ f2 v={a,b}; return __builtin_bit_cast(u32,__builtin_convertvector(v,bf2)); }
DI u32 pkhf(float a,float b){ f2 v={a,b}; return __builtin_bit_cast(u32,__builtin_convertvector(v,hf2)); }
DI float rbf(float a){ return bflo(pkbf(a,a)); }
DI float hlo(u32 v){ return (float)__builtin_bit_cast(hf2,v)[0]; }
DI float hhi(u32 v){ return (float)__builtin_bit_cast(hf2,v)[1]; }
DI float dot2(u32 a,u32 b,float c){ return __builtin_amdgcn_fdot2_f32_bf16(__builtin_bit_cast(bf2,a),__builtin_bit_cast(bf2,b),c,false); }
DI f4 mfma16(u4 a,u4 b,f4 c){ return __builtin_amdgcn_mfma_f32_16x16x32_bf16(__builtin_bit_cast(bf8,a),__builtin_bit_cast(bf8,b),c,0,0,0); }
DI f16v mfma32h(u4 a,u4 b,f16v c){ return __builtin_amdgcn_mfma_f32_32x32x16_f16(__builtin_bit_cast(hf8,a),__builtin_bit_cast(hf8,b),c,0,0,0); }
DI u4 ldsr128(u32 a){ return *(LAS u4*)a; }
DI u2 ldsr64(u32 a){ return *(LAS u2*)a; }
DI void ldsw128(u32 a,u4 v){ *(LAS u4*)a=v; }
DI u2 ldstr(u32 a){ return __builtin_bit_cast(u2,__builtin_amdgcn_ds_read_tr16_b64_v4i16((LAS s4*)a)); }
DI u32 uni(u32 v){ return __builtin_amdgcn_readfirstlane(v); }

template<int OFF> DI void gload(u4& dst,u32 voff,const char* base){ asm volatile("global_load_dwordx4 %0, %1, %2 offset:%3 sc1" : "=v"(dst) : "v"(voff), "s"(base), "n"(OFF)); }
template<int OFF> DI void gload_t(u4& dst,u32 voff,const char* base){ asm volatile("global_load_dwordx4 %0, %1, %2 offset:%3 sc1" : "+v"(dst) : "v"(voff), "s"(base), "n"(OFF)); }
DI void mfma4(f4& c0,f4& c1,f4& c2,f4& c3,const u4& a,const u4 (&b)[4]){
  asm volatile("v_mfma_f32_16x16x32_bf16 %0, %4, %5, %0\n\tv_mfma_f32_16x16x32_bf16 %1, %4, %6, %1\n\tv_mfma_f32_16x16x32_bf16 %2, %4, %7, %2\n\tv_mfma_f32_16x16x32_bf16 %3, %4, %8, %3"
    : "+v"(c0),"+v"(c1),"+v"(c2),"+v"(c3) : "v"(a),"v"(b[0]),"v"(b[1]),"v"(b[2]),"v"(b[3])); }
template<int CNT> DI void waitB(u4 (&B)[7]){ asm volatile("s_waitcnt vmcnt(%7)" : "+v"(B[0]),"+v"(B[1]),"+v"(B[2]),"+v"(B[3]),"+v"(B[4]),"+v"(B[5]),"+v"(B[6]) : "n"(CNT) : "memory"); }
DI void waitA(u4 (&A)[4]){ asm volatile("s_waitcnt lgkmcnt(0)" : "+v"(A[0]),"+v"(A[1]),"+v"(A[2]),"+v"(A[3]) :: "memory"); }
#define WAITV(n) asm volatile("s_waitcnt vmcnt(%0)" :: "n"(n) : "memory")
#define WAITL0() asm volatile("s_waitcnt lgkmcnt(0)" ::: "memory")
#define SCHED() __builtin_amdgcn_sched_barrier(0)
DI void bar(){ asm volatile("s_waitcnt lgkmcnt(0)\n\ts_barrier" ::: "memory"); }
DI f2 unpk(u32 v){ return f2{bflo(v),bfhi(v)}; }
DI u32 pkbf2(f2 v){ return __builtin_bit_cast(u32,__builtin_convertvector(v,bf2)); }
DI u32 pkhf2(f2 v){ return __builtin_bit_cast(u32,__builtin_convertvector(v,hf2)); }
DI f2 fma2(f2 a,f2 b,f2 c){ return __builtin_elementwise_fma(a,b,c); }
DI f2 gelu2(f2 x){
  f2 u=x*fma2(x*x,f2{-0.10294324f,-0.10294324f},f2{-2.30220820f,-2.30220820f});
  f2 d=f2{__builtin_amdgcn_exp2f(u[0]),__builtin_amdgcn_exp2f(u[1])}+f2{1.f,1.f};
  return x*f2{__builtin_amdgcn_rcpf(d[0]),__builtin_amdgcn_rcpf(d[1])};
}
DI void ln_fin(float mean,float var,float& m,float& rinv){
  m=rbf(mean); float v=rbf(__builtin_fmaxf(var,0.f));
  float s=rbf(__builtin_amdgcn_sqrtf(rbf(v+1e-5f))); float r=__builtin_amdgcn_rcpf(s); rinv=__builtin_fmaf(__builtin_fmaf(-s,r,1.f),r,r);
}
typedef __amdgpu_buffer_rsrc_t rsrc_t;
DI void st16(rsrc_t rs,u32 off,u4 v){ __builtin_amdgcn_raw_buffer_store_b128(v,rs,off,0,16); }
DI void st8(rsrc_t rs,u32 off,u2 v){ __builtin_amdgcn_raw_buffer_store_b64(v,rs,off,0,16); }
DI void st4(rsrc_t rs,u32 off,u32 v){ __builtin_amdgcn_raw_buffer_store_b32(v,rs,off,0,16); }
DI u4 ld16(rsrc_t rs,u32 off){ return __builtin_amdgcn_raw_buffer_load_b128(rs,off,0,16); }
DI u2 ld8(rsrc_t rs,u32 off){ return __builtin_amdgcn_raw_buffer_load_b64(rs,off,0,16); }
DI u32 ld4(rsrc_t rs,u32 off){ return __builtin_amdgcn_raw_buffer_load_b32(rs,off,0,16); }
constexpr int SPIN=1<<17;
template<int SL=2> DI void pollw(rsrc_t rs,u32 off,u32 tag){
  #pragma unroll 1
  for(int s=0;s<SPIN;s++){ u32 v=ld4(rs,off); asm volatile("" ::: "memory"); if(__all(v==tag)) break; __builtin_amdgcn_s_sleep(SL); }
}

DI f16v mfma32b(u4 a,u4 b,f16v c){ return __builtin_amdgcn_mfma_f32_32x32x16_bf16(__builtin_bit_cast(bf8,a),__builtin_bit_cast(bf8,b),c,0,0,0); }
DI u4 mksrd(const void* b){ u64 a=(u64)b; return u4{(u32)a,(u32)(a>>32)&0xffffu,0xffffffffu,0x00020000u}; }
template<int IMM> DI void setm0(u32 base){ asm volatile("s_add_u32 m0, %0, %1\n\ts_nop 0" :: "s"(base), "n"(IMM) : "scc","memory"); }
template<int OFF> DI void dma(u4 srd,u32 voff,u32 soff){ asm volatile("buffer_load_dwordx4 %0, %1, %2 offen offset:%3 sc1 lds" :: "v"(voff), "s"(srd), "s"(soff), "n"(OFF) : "memory"); }
DI void touch(u4 srd,u32 voff,u32 soff){ asm volatile("buffer_load_dword %0, %1, %2 offen sc1 lds" :: "v"(voff), "s"(srd), "s"(soff) : "memory"); }
template<int OFF> DI void gload8(u2& dst,u32 voff,const char* base){ asm volatile("global_load_dwordx2 %0, %1, %2 offset:%3 sc1" : "=v"(dst) : "v"(voff), "s"(base), "n"(OFF)); }
DI void gload2(u32& dst,u32 voff,const char* base){ asm volatile("global_load_ushort %0, %1, %2" : "=v"(dst) : "v"(voff), "s"(base)); }
DI u32 arrive(u32 a,int){ u32 o; asm volatile("s_mov_b64 exec, 1\n\tds_add_rtn_u32 %0, %1, %2\n\ts_mov_b64 exec, -1\n\ts_waitcnt lgkmcnt(0)" : "=&v"(o) : "v"(a), "v"(1u) : "memory"); return uni(o); }
DI void ldswait(u32 a,u32 n){ while(__hip_atomic_load((LAS u32*)a,__ATOMIC_RELAXED,__HIP_MEMORY_SCOPE_WORKGROUP)<n) __builtin_amdgcn_s_sleep(1); asm volatile("" ::: "memory"); }
DI float addf(float a,float b){ float o; asm("v_add_f32 %0, %1, %2" : "=v"(o) : "v"(a), "v"(b)); return o; }
DI u32 opq(u32 v){ asm volatile("" : "+v"(v)); return v; }

constexpr u32 STG=32768;
constexpr u32 RING=24576, RB=62464, CONV=0, CVS=20800, UOF=16640, MISC=RB+4*RING, JNK=MISC+2048, PM=163840-128, LDS_TOTAL=163840;
constexpr u32 ATL=137*1024;
constexpr u32 C_FULL=PM, C_G1=PM+4, C_Z2=PM+8, C_Z1=PM+12, C_RED=PM+16, C_H=PM+20, C_FREE=PM+24, C_CV=PM+28, C_Z1R=PM+32, C_TAG=PM+40, C_KP=PM+48, C_DONE=PM+64, C_ZQ=PM+68, C_CS=PM+72;
constexpr u32 CODE_BYTES=32768;
#define LA 3
#define TC_S 7
#define G2PRIO 2
#define XMAX 9
#define GS_K 35
#define WNT ""
#define PDLY 8
#define RS_S 7
#define KROT ((j+3*(rt&3))%10)
DI void signal(u32 a,int){ asm volatile("s_mov_b64 exec, 1\n\tds_add_u32 %0, %1\n\ts_mov_b64 exec, -1" :: "v"(a), "v"(1u) : "memory"); }
template<int OFF> DI void gload4(u32& dst,u32 voff,const char* base){ asm volatile("global_load_dword %0, %1, %2 offset:%3 sc1" : "=v"(dst) : "v"(voff), "s"(base), "n"(OFF)); }
template<int CNT> DI void waitG(u4& g){ asm volatile("s_waitcnt vmcnt(%1)" : "+v"(g) : "n"(CNT) : "memory"); }

DI f2 cmulp(f2 a,f2 b,f2 bs){ return fma2(f2{a[1],a[1]},bs,f2{a[0],a[0]}*b); }
DI float fmaf1(float a,float b,float c){ float o; asm("v_fma_f32 %0, %1, %2, %3" : "=v"(o) : "v"(a),"v"(b),"v"(c)); return o; }
DI u32 ldsv(u32 a){ return __hip_atomic_load((LAS u32*)a,__ATOMIC_RELAXED,__HIP_MEMORY_SCOPE_WORKGROUP); }
DI void ldswait4(u32 a,int lane,u32 n){ while(__any(ldsv(a+(lane&3)*4)<n)) __builtin_amdgcn_s_sleep(1); asm volatile("" ::: "memory"); }
DI u32 gettag(u32 a){ ldswait(a+4,1u); return uni(ldsv(a)); }
DI f2 cpow_dir(float lr,float rv,float pw){ float e=__builtin_amdgcn_exp2f(lr*pw); float f=__builtin_amdgcn_fractf(rv*pw);
  return f2{e*__builtin_amdgcn_cosf(f),e*__builtin_amdgcn_sinf(f)}; }
DI u32 dup(u32 a){ return __builtin_amdgcn_perm(a,a,0x01000100u); }
DI u32 cat(u32 lo,u32 hi){ return __builtin_amdgcn_perm(hi,lo,0x05040100u); }
DI u32 lds16(u32 a){ return *(LAS u16*)a; }

template<class CB> DI float kgen_tab(u32 cb,int lane,const u32 (&pr)[6],float (&kp)[11],CB&& mid){
  const float dt=rbf(__expf(bflo(pr[4])));
  const float cr[2]={bflo(pr[2]),bfhi(pr[2])}, cm[2]={bflo(pr[3]),bfhi(pr[3])};
  float mx=fmaxf(fmaxf(fabsf(cr[0]),fabsf(cm[0])),fmaxf(fabsf(cr[1]),fabsf(cm[1])));
  u32 eb=__float_as_uint(mx)&0x7f800000u;
  #define DPPMAX(c) eb=max(eb,(u32)__builtin_amdgcn_update_dpp(0,(int)eb,c,0xf,0xf,false))
  DPPMAX(0x128); DPPMAX(0x124); DPPMAX(0x122); DPPMAX(0x121);
  eb=max(max((u32)__builtin_amdgcn_readlane(eb,0),(u32)__builtin_amdgcn_readlane(eb,16)),max((u32)__builtin_amdgcn_readlane(eb,32),(u32)__builtin_amdgcn_readlane(eb,48))); eb=eb<0x01000000u?0x3f800000u:(eb>0x7e000000u?0x7e000000u:eb);
  const float ksc=2.f*dt*__uint_as_float(eb), isc=__uint_as_float(0x7f000000u-eb);
  const int r=lane&31,h=lane>>5;
  const u32 TP=272, TA=cb, TB=cb+22*TP;
  f16v acc={};
  #pragma unroll 1
  for(int c=0;c<2;c++){
    if(c) mid();
    float l_a=c?bfhi(pr[0]):bflo(pr[0]), i_m=c?bfhi(pr[1]):bflo(pr[1]), c_r=c?cr[1]:cr[0], c_i=c?cm[1]:cm[0];
    const float lr=(-rbf(__expf(l_a))*dt)*1.44269504f, rv=(i_m*dt)*0.15915494f;
    const f2 z1=cpow_dir(lr,rv,1.f), z8=cpow_dir(lr,-rv,8.f), zs=cpow_dir(lr,-rv,16.f), zk=cpow_dir(lr,rv,256.f);
    const f2 z1s={-z1[1],z1[0]}, zss={-zs[1],zs[0]}, zks={-zk[1],zk[0]};
    const u32 wa=TA+lane*4, wb=TB+lane*4;
    #define PUT(base,row,v) do{ *(LAS u32*)((base)+(row)*TP)=pkhf2(v); }while(0)
    f2 e={1.f,0.f}, o=z8;
    #pragma unroll
    for(int k=0;k<16;k++){ PUT(wb,2*k,e); PUT(wb,2*k+1,o); if(k<15){ e=k?cmulp(e,zs,zss):zs; o=cmulp(o,zs,zss);} }
    f2 b0={c_r*isc,c_i*isc}; PUT(wa,0,b0);
    f2 t=cmulp(b0,zk,zks); PUT(wa,8,t);
    f2 b1=cmulp(b0,z1,z1s); PUT(wa,1,b1); t=cmulp(t,zk,zks); PUT(wa,10,t);
    f2 b2=cmulp(b1,z1,z1s); PUT(wa,2,b2); t=cmulp(t,zk,zks); PUT(wa,11,t);
    f2 b3=cmulp(b2,z1,z1s); PUT(wa,3,b3); t=cmulp(t,zk,zks); PUT(wa,16,t);
    f2 b4=cmulp(b3,z1,z1s); PUT(wa,4,b4); t=cmulp(t,zk,zks); PUT(wa,17,t);
    f2 b5=cmulp(b4,z1,z1s); PUT(wa,5,b5); t=cmulp(t,zk,zks); PUT(wa,20,t);
    f2 b6=cmulp(b5,z1,z1s); PUT(wa,6,b6); t=cmulp(t,zk,zks); PUT(wa,21,t);
    f2 b7=cmulp(b6,z1,z1s); PUT(wa,7,b7); t=cmulp(t,zk,zks); PUT(wa,18,t);
    f2 s=cmulp(b4,zk,zks); PUT(wa,12,s); t=cmulp(b2,zk,zks); PUT(wa,9,t);
    s=cmulp(s,zk,zks); PUT(wa,14,s); t=cmulp(b6,zk,zks); PUT(wa,13,t);
    s=cmulp(s,zk,zks); PUT(wa,15,s);
    #undef PUT
    WAITL0();
    const int ra=(r>21||r==19)?0:r;
    #pragma unroll
    for(int ks=0;ks<8;ks++){ u4 fa=ldsr128(TA+ra*TP+ks*32+h*16), fb=ldsr128(TB+r*TP+ks*32+h*16); acc=mfma32h(fa,fb,acc); }
    WAITL0();
  }
  #pragma unroll
  for(int i=0;i<11;i++) kp[i]=acc[i];
  return ksc;
}
DI void kgen_exp(u32 cb,int lane,const float (&acc)[11]){
  const int r=lane&31,h=lane>>5;
  { const u32 a4=cb+r*16+h*8, a5=cb+r*16+h*1024;
    #define PK(addr,base,reg) do{ *(LAS _Float16*)((addr)+2*(base))=(_Float16)acc[reg]; }while(0)
    PK(a4,0,0);PK(a4,1,1);PK(a4,2,2);PK(a4,3,3); PK(a4,256,4);PK(a4,258,5);PK(a4,512,6);PK(a4,768,7);
    PK(a5,1024,8);PK(a5,1280,9);PK(a5,2048,10);
    #undef PK
  }
  WAITL0();
  { const u32 g0=cb+2*(2048-8*lane);
    u32 a0=lds16(g0), b0=lds16(g0-16), a1=lds16(g0-1024), b1=lds16(g0-1024-16);
    u32 f2_=lds16(g0-2048+(lane?8:0)), a2=lds16(g0-2048), m2=lds16(g0-2048-8), b2=lds16(g0-2048-16);
    u32 e[11];
    #pragma unroll
    for(int j=0;j<11;j++) e[j]=lds16(cb+2u*(u32)rbf((float)(512-8*lane+3-j)));
    u32 k01=*(LAS u32*)cb, k23=*(LAS u32*)(cb+4);
    WAITL0();
    const u32 wo=cb+lane*16;
    { u32 aa=dup(a0),bb=dup(b0),c=(lane&1)?b0:a0, cbv=cat(c,b0), ac=cat(a0,c);
      ldsw128(wo,u4{aa,aa,cbv,bb}); ldsw128(wo+4160,u4{aa,aa,ac,bb}); ldsw128(wo+8320,u4{aa,aa,aa,cbv}); ldsw128(wo+12480,u4{aa,aa,aa,ac}); }
    { u32 aa=dup(a1),bb=dup(b1),c=(lane&1)?b1:a1, cbv=cat(c,b1), ac=cat(a1,c);
      ldsw128(wo+1024,u4{aa,aa,cbv,bb}); ldsw128(wo+1024+4160,u4{aa,aa,ac,bb}); ldsw128(wo+1024+8320,u4{aa,aa,aa,cbv}); ldsw128(wo+1024+12480,u4{aa,aa,aa,ac}); }
    { u32 aa=dup(a2),mm=dup(m2),bb=dup(b2),am=cat(a2,m2),fa=cat(f2_,a2),mb=cat(m2,b2);
      ldsw128(wo+2048,u4{aa,am,mm,bb}); ldsw128(wo+2048+4160,u4{aa,aa,mm,mb}); ldsw128(wo+2048+8320,u4{aa,aa,am,mm}); ldsw128(wo+2048+12480,u4{fa,aa,aa,mm}); }
    { u32 o0=cat(e[0],e[1]),o1=cat(e[2],e[3]),o2=cat(e[4],e[5]),o3=cat(e[6],e[7]),o4=cat(e[8],e[9]);
      u32 p1=cat(e[1],e[2]),d0=cat(e[3],e[4]),d1=cat(e[5],e[6]),d2=cat(e[7],e[8]),d3=cat(e[9],e[10]);
      ldsw128(wo+3072,u4{d0,d1,d2,d3}); ldsw128(wo+3072+4160,u4{o1,o2,o3,o4}); ldsw128(wo+3072+8320,u4{p1,d0,d1,d2}); ldsw128(wo+3072+12480,u4{o0,o1,o2,o3}); }
    if(lane<4){ if(lane){k01=0;k23=0;}
      u32 d0=k01&0xffffu, o1=__builtin_amdgcn_alignbit(k01,k01,16), o0=__builtin_amdgcn_alignbit(k23,k23,16), p1=(k23&0xffffu)|(k01&0xffff0000u);
      ldsw128(wo+4096,u4{d0,0,0,0}); ldsw128(wo+4096+4160,u4{o1,0,0,0}); ldsw128(wo+4096+8320,u4{p1,d0,0,0}); ldsw128(wo+4096+12480,u4{o0,o1,0,0}); }
  }
}
DI f4 mfma16h(u4 a,u4 b,f4 c){ return __builtin_amdgcn_mfma_f32_16x16x32_f16(__builtin_bit_cast(hf8,a),__builtin_bit_cast(hf8,b),c,0,0,0); }
#define RDA(S,i,m) asm volatile("ds_read_b64 %0, %2 offset:%3\n\tds_read_b64 %1, %2 offset:%4" : "=&v"(AL[S][i][m]),"=&v"(AH[S][i][m]) : "v"(ka), "n"((3-(i))*1024+(1-(m))*32), "n"((3-(i))*1024+(1-(m))*32+8))
#define RDB(S,i,ad,o) asm volatile("ds_read_b128 %0, %1 offset:%2" : "=v"(BF[S][i]) : "v"(ad), "n"(o))
#define TIEA(S,i) "+v"(AL[S][i][0]),"+v"(AH[S][i][0]),"+v"(AL[S][i][1]),"+v"(AH[S][i][1])
#define CV_A(S,i,m) u4{AL[S][i][m][0],AL[S][i][m][1],AH[S][i][m][0],AH[S][i][m][1]}
#define CV_M(S,i,n) do{ y[0][n]=mfma16h(CV_A(S,i,0),BF[S][(n)-(i)],y[0][n]); y[1][n]=mfma16h(CV_A(S,i,1),BF[S][(n)-(i)],y[1][n]); SCHED(); }while(0)
#define CV_U0() u32 u0=u1-256; u0=((int)u0<(int)uz)?uz:u0
#define CV_SETUP() const int r=lane&15,q=lane>>4; u32 ka=cb+(r&3)*4160+16*q-8*(r>>2)+992; const u32 uz=cb+UOF+q*1040; u32 u1=uz+(r+17)*16; u2 AL[2][4][2],AH[2][4][2]; u4 BF[2][4]
DI void conv_help(u32 cb,int lane){
  CV_SETUP(); f4 y[2][4]={};
  #define HP_WT(S) asm volatile("s_waitcnt lgkmcnt(0)" : TIEA(S,2),TIEA(S,3),"+v"(BF[S][0]),"+v"(BF[S][1]))
  #define HP_STEP(C,N) do{ CV_U0(); RDA(N,2,0); RDA(N,2,1); RDB(N,1,u1,0); SCHED(); CV_M(C,2,3); RDA(N,3,0); RDA(N,3,1); RDB(N,0,u0,0); ka-=64; u1-=16; SCHED(); CV_M(C,3,3); HP_WT(N); }while(0)
  { CV_U0(); RDA(0,2,0); RDA(0,2,1); RDA(0,3,0); RDA(0,3,1); RDB(0,0,u0,0); RDB(0,1,u1,0); ka-=64; u1-=16; HP_WT(0); }
  #pragma unroll 1
  for(int dl=0;dl<8;dl++){ HP_STEP(0,1); if(dl==7){ ka+=64; u1+=16; } HP_STEP(1,0); }
  *(LAS f4*)(cb+q*4160+r*16)=y[0][3]; *(LAS f4*)(cb+q*4160+256+r*16)=y[1][3];
  WAITL0();
}
DI void conv_wave(const Params& p,rsrc_t rs,int d,u32 cb,u32 flag,int lane,float ksc,float dsk){
  CV_SETUP(); f4 y[2][4]={};
  #define CV_WT(S) asm volatile("s_waitcnt lgkmcnt(0)" : TIEA(S,0),TIEA(S,1),TIEA(S,2),"+v"(BF[S][0]),"+v"(BF[S][1]),"+v"(BF[S][2]),"+v"(BF[S][3]))
  #define CV_STEP(C,N) do{ CV_U0(); CV_M(C,0,0); RDA(N,0,0); RDB(N,0,u0,0); SCHED(); CV_M(C,0,1); RDA(N,0,1); RDB(N,1,u1,0); SCHED(); CV_M(C,0,2); RDA(N,1,0); RDB(N,2,u1,256); SCHED(); \
      CV_M(C,0,3); RDA(N,1,1); RDB(N,3,u1,512); SCHED(); CV_M(C,1,1); RDA(N,2,0); SCHED(); CV_M(C,1,2); RDA(N,2,1); ka-=64; u1-=16; SCHED(); CV_M(C,1,3); CV_M(C,2,2); CV_WT(N); }while(0)
  { CV_U0(); RDA(0,0,0); RDA(0,0,1); RDA(0,1,0); RDA(0,1,1); RDA(0,2,0); RDA(0,2,1); RDB(0,0,u0,0); RDB(0,1,u1,0); RDB(0,2,u1,256); RDB(0,3,u1,512); ka-=64; u1-=16; CV_WT(0); }
  #pragma unroll 1
  for(int dl=0;dl<8;dl++){ CV_STEP(0,1); if(dl==7){ ka+=64; u1+=16; } CV_STEP(1,0); }
  ldswait(flag,2u);
  y[0][3]+=*(LAS f4*)(cb+q*4160+r*16); y[1][3]+=*(LAS f4*)(cb+q*4160+256+r*16);
  #pragma unroll
  for(int n=0;n<4;n++){ u2 o[2];
    #pragma unroll
    for(int m=0;m<2;m++){ u2 uv=ldsr64(cb+UOF+(2*m+(q>>1))*1040+(16*n+r+1)*16+8*(q&1));
      #pragma unroll
      for(int e=0;e<2;e++) o[m][e]=pkbf2(fma2(f2{hlo(uv[e]),hhi(uv[e])},f2{dsk,dsk},f2{y[m][n][2*e],y[m][n][2*e+1]}*ksc)); }
    auto sx=__builtin_amdgcn_permlane16_swap(o[0][0],o[1][0],false,false); auto sy=__builtin_amdgcn_permlane16_swap(o[0][1],o[1][1],false,false);
    st16(rs,OFF_AT+d*4096+(16*n+r)*64+(q&1)*32+(q>>1)*16,u4{sx[0],sy[0],sx[1],sy[1]}); }
  WAITV(0);
}

struct Frag { u4 x[4], w[4][2]; };
__global__ __launch_bounds__(512,2) void fused(Params p){
  extern __shared__ __attribute__((aligned(16))) char sm[];
  const u32 smb=(u32)(size_t)(LAS char*)sm;
  u64 pc0; asm volatile("s_getpc_b64 %0" : "=s"(pc0));
  const int b=blockIdx.x, xc=b&7, idx=b>>3;
  const int tid=threadIdx.x, w=uni(tid>>6); int lane=tid&63;
  const rsrc_t rs=__builtin_amdgcn_make_buffer_rsrc(p.ws,0,WS_END,0x00020000);
  const u4 srdS=mksrd(p.ws);
  const int rtB=8*(xc>>1)+(idx&7), ct=4*(xc&1)+(idx>>3);
  const bool hasc=idx<28 && idx+28*(w&3)<80 && w>=4;
  const int dch=80*xc+(hasc? idx+28*(w&3) : 0);
  const u32 cb=smb+CONV+(w&3)*CVS;
  const int rt=4*(xc>>1)+(idx&3), j=7*(xc&1)+(idx>>2), mi=w&3, g1=w>>2;
  u32 tag; float ksc=0.f, dsk=0.f;
  const u64 dg0=__builtin_amdgcn_s_memrealtime(); u64 dg1=dg0;
  auto warm=[&](int wq){
    const u4 sp=mksrd((const char*)p.wsp+(size_t)(rtB*64+ct*8+2*wq)*4096); setm0<JNK>(smb);
    asm volatile("buffer_load_dword %0, %1, 0 offen" WNT " lds" :: "v"(lane*128), "s"(sp) : "memory");
  };
  if(idx>=28){
    const int a=xc*4+idx-28, r=lane&15, q=lane>>4;
    { u4 sc=mksrd((const void*)(pc0&~127ull)); sc[2]=CODE_BYTES; setm0<JNK>(smb); touch(sc,lane*128,w*8192); }
    const u32 ep=ld4(rs,OFF_EP);
    u4 xr[10]; const char* xs=(const char*)p.x+(size_t)(64*a+8*w+(lane>>3))*1280+(lane&7)*16;
    #pragma unroll
    for(int i=0;i<10;i++) xr[i]=*(const u4*)(xs+i*128);
    if(tid<32) *(LAS u32*)(smb+PM+tid*4)=0u;
    tag=uni(ep)+1u;
    float s0=0.f,s1=0.f,q0=0.f,q1=0.f;
    #pragma unroll
    for(int i=0;i<10;i++){ s0=dot2(xr[i][0],0x3f803f80u,s0); q0=dot2(xr[i][0],xr[i][0],q0); s1=dot2(xr[i][1],0x3f803f80u,s1); q1=dot2(xr[i][1],xr[i][1],q1);
      s0=dot2(xr[i][2],0x3f803f80u,s0); q0=dot2(xr[i][2],xr[i][2],q0); s1=dot2(xr[i][3],0x3f803f80u,s1); q1=dot2(xr[i][3],xr[i][3],q1); }
    s0+=s1; q0+=q1;
    #define DPPADD(v,c) v+=__int_as_float(__builtin_amdgcn_update_dpp(0,__float_as_int(v),c,0xf,0xf,false))
    DPPADD(s0,0xB1); DPPADD(q0,0xB1); DPPADD(s0,0x4E); DPPADD(q0,0x4E); DPPADD(s0,0x141); DPPADD(q0,0x141);
    float m1,ri; { float mean=s0*(1.f/640.f); ln_fin(mean,q0*(1.f/640.f)-mean*mean,m1,ri); }
    if((lane&7)==0) st16(rs,OFF_RS+(64*a+8*w+(lane>>3))*16,u4{__float_as_uint(m1),tag,__float_as_uint(ri),tag});
    STAMP(5,0);
    #pragma unroll
    for(int i=0;i<10;i++){ u4 o;
      #pragma unroll
      for(int e=0;e<4;e++) o[e]=pkhf2(fma2(unpk(xr[i][e]),f2{ri,ri},f2{-m1*ri,-m1*ri}));
      ldsw128(smb+RB+(8*w+(lane>>3))*1296+(lane&7)*16+i*128,o); }
    bar();
    #pragma unroll
    for(int i=0;i<10;i++){ const int c=w*10+i; const u32 ad=smb+RB+(32*((r>>1)&1)+8*q+(r>>2))*1296+c*16+(r&1)*8; u2 lo=ldstr(ad), hi=ldstr(ad+4*1296);
      st16(rs,OFF_XNT+(8*c+(r&7))*4096+(64*a+32*(r>>3)+8*q)*2,u4{lo[0],lo[1],hi[0],hi[1]}); }
    WAITV(0);
    if(arrive(smb+C_Z1,lane)==7u && lane==0) st4(rs,OFF_FX+a*128,tag);
    STAMP(6,0);
    if(g1) warm(mi);
    if(a<27){ const u4 srdO=mksrd((const char*)p.wout+a*(32*1280)+w*5120); const u32 mo=smb+w*5120; setm0<0>(mo);
      dma<0>(srdO,lane*16,0); dma<1024>(srdO,lane*16,0); dma<2048>(srdO,lane*16,0); dma<3072>(srdO,lane*16,0); setm0<4096>(mo); dma<0>(srdO,lane*16,4096); }
    WAITV(0); bar();
    if(a<27){
      #pragma unroll
      for(int i=0;i<5;i++){ u32 ad=smb+(8*q+(r>>2))*1280+(w*5+i)*32+(r&3)*8; u2 lo=ldstr(ad), hi=ldstr(ad+4*1280);
        st16(rs,OFF_WOF+(u32)((w*27+a)*5+i)*1024+lane*16,u4{lo[0],lo[1],hi[0],hi[1]}); } }
    WAITV(0); bar();
    if(tid==0) st4(rs,OFF_FW+a*128,tag);
  } else {
    const int r=lane&31, h=lane>>5;
    const u4 srdX=mksrd((const char*)p.x+(size_t)rt*128*1280);
    const u4 srdW=mksrd((const char*)p.win+j*128-3072);
    const u32 mw=smb+w*4096;
    u32 vx0,vx1,vw1,vw2,epv;
    { u32 lr=lane>>3,c=lane&7; vx0=(16*w+lr)*1280+((c^(lr>>1))<<4); vx1=(vx0^64u)+9216;
      vw1=(8*w+lr)*3456+((c^((lr&2)<<1))<<4); vw2=vw1+1024+(j==13?0:14*128); }
    u32 kx[10]; { u32 k=KROT;
      #pragma unroll
      for(int i=0;i<10;i++){ kx[i]=k*128; k=k==9?0:k+1; } }
    auto issue1=[&](auto sc,int k){ constexpr int s=sc.value;
      if(k==0){ setm0<(s&3)*STG>(mw); dma<0>(srdX,vx0,kx[s]); } if(k==1) dma<1024>(srdX,vx1,kx[s]); if(k==2) dma<2048>(srdW,vw2,kx[s]*1728); if(k==3) dma<3072>(srdW,vw1,kx[s]*1728); };
    auto issue=[&](auto sc){ issue1(sc,0); issue1(sc,1); issue1(sc,2); issue1(sc,3); };
    issue(std::integral_constant<int,0>{});
    u32 pr[6];
    gload4<0>(epv,0u,p.ws+OFF_EP);
    gload4<0>(pr[0],lane*4,(const char*)(p.lar+dch*128)); gload4<0>(pr[1],lane*4,(const char*)(p.aim+dch*128)); gload4<0>(pr[2],lane*4,(const char*)(p.cre+dch*128)); gload4<0>(pr[3],lane*4,(const char*)(p.cim+dch*128));
    gload2(pr[4],0u,(const char*)(p.ldt+dch)); gload2(pr[5],0u,(const char*)(p.dsk+dch));
    asm volatile("s_barrier" ::: "memory");
    issue(std::integral_constant<int,1>{});
    if(tid<32) *(LAS u32*)(smb+PM+tid*4)=0u;
    u32 ax[2][4],aw[2][4][2];
    { u32 row=32*mi+r, a0=smb+(row>>4)*4096+(row&15)*128+((h^((r>>1)&7))<<4);
      u32 qq=(lane&15)>>2, w0=smb+2048+g1*1024+h*4096+qq*128+8*(lane&1)+(((2*((lane>>4)&1)+((lane>>1)&1))^((qq&2)<<1))<<4);
      #pragma unroll
      for(int i=0;i<2;i++)
        #pragma unroll
        for(int k=0;k<4;k++){ const u32 kk=(k+mi)&3, wo=i*2*STG; ax[i][k]=opq((a0^(kk*32u))+wo); aw[i][k][0]=opq(w0+kk*8192+wo); aw[i][k][1]=opq((w0^64u)+kk*8192+wo); } }
    u4 sel; { const u32 o=((lane&15)==((lane>>4)&1))?0x3f803f80u:0u; sel=u4{o,o,o,o}; asm volatile("" : "+v"(sel)); }
    Frag F[2];
    asm volatile("s_waitcnt vmcnt(4)" : "+v"(epv),"+v"(pr[0]),"+v"(pr[1]),"+v"(pr[2]),"+v"(pr[3]),"+v"(pr[4]),"+v"(pr[5]) :: "memory");
    bar();
    tag=uni(epv)+1u; dsk=bflo(pr[5]);
    #pragma unroll
    for(int k=0;k<4;k++){
      #pragma unroll
      for(int n=0;n<2;n++){ u2 lo=ldstr(aw[0][k][n]), hi=ldstr(aw[0][k][n]+512); F[0].w[k][n]=u4{lo[0],lo[1],hi[0],hi[1]}; }
      F[0].x[k]=ldsr128(ax[0][k]); }
    issue(std::integral_constant<int,2>{});
    STAMP(1,0);
    f16v acc[2]={}; f4 cacc[2]={}; u4 G;
    const u32 go=OFF_RS+rt*2048+mi*512, cp=smb+MISC+g1*1024;
    sfor<10>([&](auto ic){
      constexpr int s=ic.value; Frag& cur=F[s&1]; Frag& nxt=F[(s&1)^1];
      if constexpr(s<9){ WAITV(s==8? 2 : s==7? 6 : 4); asm volatile("s_barrier" ::: "memory"); }
      auto rw=[&](int k,int n){ if constexpr(s<9){ const int i=((s+1)>>1)&1; const u32 so=((s+1)&1)*STG; u2 lo=ldstr(aw[i][k][n]+so), hi=ldstr(aw[i][k][n]+so+512); nxt.w[k][n]=u4{lo[0],lo[1],hi[0],hi[1]}; } };
      auto rx=[&](int k){ if constexpr(s<9){ const int i=((s+1)>>1)&1; const u32 so=((s+1)&1)*STG; nxt.x[k]=ldsr128(ax[i][k]+so); } };
      #pragma unroll
      for(int k=0;k<4;k++){
        acc[0]=mfma32b(cur.w[k][0],cur.x[k],acc[0]); SCHED();
        if constexpr(s+3<=9) issue1(std::integral_constant<int,s+3>{},k);
        rw(k,0); rx(k);
        SCHED();
        acc[1]=mfma32b(cur.w[k][1],cur.x[k],acc[1]); SCHED();
        rw(k,1);
        if(k==0){ cacc[0]=mfma16(cur.w[0][0],sel,cacc[0]); cacc[1]=mfma16(cur.w[0][1],sel,cacc[1]); }
        if constexpr(s==6){ if(k==3){ gload<0>(G,r*16,p.ws+go); if(g1) warm(mi); else { setm0<JNK>(smb); touch(srdS,0u,go); } } }
        if constexpr(s==8){ if(k==3) signal(smb+C_DONE,lane); }
        if constexpr(s==9){ if(k==1){
          if((lane&14)==0){ *(LAS f4*)(cp+mi*256+(lane&1)*64+(lane>>4)*16)=cacc[0]; *(LAS f4*)(cp+mi*256+128+(lane&1)*64+(lane>>4)*16)=cacc[1]; }
          signal(smb+C_CS+g1*4,lane); } }
        SCHED();
      }
    });
    RST(0);
    if(!g1) __builtin_amdgcn_s_setprio(2);
    asm volatile("s_waitcnt vmcnt(1)" : "+v"(G) :: "memory");
    RST(1);
    ldswait(smb+C_CS+g1*4,4u);
    const float cs0=*(LAS float*)(cp+h*512+r*4)+*(LAS float*)(cp+h*512+256+r*4), cs1=*(LAS float*)(cp+h*512+128+r*4)+*(LAS float*)(cp+h*512+384+r*4);
    RST(2);
    #pragma unroll 1
    for(int sp=0;sp<SPIN && !__all(G[1]==tag&&G[3]==tag);sp++)asm volatile("global_load_dwordx4 %0, %1, %2 sc1\n\ts_waitcnt vmcnt(0)" : "=&v"(G) : "v"(r*16), "s"(p.ws+go) : "memory");
    RST(3);
    const float nm=-__uint_as_float(G[0]), ri1=__uint_as_float(G[2]);
    acc[0]=__builtin_amdgcn_mfma_f32_32x32x2f32(cs0,nm,acc[0],0,0,0); acc[1]=__builtin_amdgcn_mfma_f32_32x32x2f32(cs1,nm,acc[1],0,0,0);
    if(g1){
      u4 zq[4][4];
      #pragma unroll
      for(int e=0;e<16;e++) zq[3][e>>2][e&3]=pkbf2(f2{acc[e>>3][2*(e&7)],acc[e>>3][2*(e&7)+1]}*ri1);
      if(w<7){ ldswait(smb+C_DONE,8u);
        #pragma unroll
        for(int i=0;i<4;i++) ldsw128(cb+UOF+i*1024+lane*16,zq[3][i]);
        signal(smb+C_ZQ,lane); }
      if(w==7){
        ldswait(smb+C_ZQ,3u);
        #pragma unroll
        for(int c=0;c<3;c++)
          #pragma unroll
          for(int i=0;i<4;i++) zq[c][i]=ldsr128(smb+c*CVS+UOF+i*1024+lane*16);
        WAITL0(); signal(smb+C_FULL,lane);
        #pragma unroll 1
        for(int c=0;c<4;c++){ u4 o[4];
          #pragma unroll
          for(int e=0;e<16;e++) o[e>>2][e&3]=pkbf2(gelu2(unpk(zq[0][e>>2][e&3])));
          #pragma unroll
          for(int n=0;n<2;n++) if(2*j+n<27){ const u32 oo=OFF_Z1T+((rt*8+2*c+(r>>4))*27+2*j+n)*1024+(h*16+(r&15))*16;
            st16(rs,oo,u4{o[2*n][0],o[2*n][1],o[2*n+1][0],o[2*n+1][1]}); st16(rs,oo+512,u4{o[2*n][2],o[2*n][3],o[2*n+1][2],o[2*n+1][3]}); }
          #pragma unroll
          for(int i=0;i<4;i++){ zq[0][i]=zq[1][i]; zq[1][i]=zq[2][i]; zq[2][i]=zq[3][i]; } }
        WAITV(0); if(lane==0) st4(rs,OFF_F1+(rt*16+j)*128,tag);
      } else {
        u4 uu[4]; float kp[11]; u32 fxv=0;
        auto mid=[&](){ fxv=ld4(rs,OFF_FX+r*128); asm volatile("" ::: "memory"); };
        if(hasc){ ksc=kgen_tab(cb,lane,pr,kp,mid);
          if(!__all(fxv==tag)) pollw<16>(rs,OFF_FX+r*128,tag);
          #pragma unroll
          for(int i=0;i<4;i++) uu[i]=ld16(rs,OFF_XNT+dch*4096+(lane+64*i)*16);
          asm volatile("" ::: "memory");
          kgen_exp(cb,lane,kp);
          ldswait(smb+C_FULL,1u);
          #pragma unroll
          for(int i=0;i<4;i++){ const int x=lane+64*i; ldsw128(cb+UOF+(x&3)*1040+((x>>2)+1)*16,uu[i]); }
          if(lane<4) ldsw128(cb+UOF+lane*1040,u4{0,0,0,0});
          WAITL0(); signal(smb+C_KP+mi*4,lane);
        }
        STAMP(13,4);
      }
    } else {
      u32 zp[2][8];
      #pragma unroll
      for(int e=0;e<16;e++) zp[e>>3][e&7]=pkbf2(gelu2(f2{acc[e>>3][2*(e&7)],acc[e>>3][2*(e&7)+1]}*ri1));
      float S=0.f,Q=0.f;
      if(j<13){
        #pragma unroll
        for(int e=0;e<8;e++){ S=dot2(zp[0][e],0x3f803f80u,S); Q=dot2(zp[0][e],zp[0][e],Q); } }
      #pragma unroll
      for(int e=0;e<8;e++){ S=dot2(zp[1][e],0x3f803f80u,S); Q=dot2(zp[1][e],zp[1][e],Q); }
      #define XSUM32(v) do{ auto t_=__builtin_amdgcn_permlane32_swap(__float_as_uint(v),__float_as_uint(v),false,false); v=__uint_as_float(t_[0])+__uint_as_float(t_[1]); }while(0)
      XSUM32(S); XSUM32(Q);
      const u32 sto=OFF_ST+rt*(14*2048)+(32*mi+r)*16;
      if(h==0) st16(rs,sto+j*2048,u4{__float_as_uint(S),tag,__float_as_uint(Q),tag});
      RST(6);
      u4 g[7];
      __builtin_amdgcn_s_sleep(PDLY);
      #pragma unroll 1
      for(int sp=0;sp<SPIN;sp++){ bool ok=true; asm volatile("" ::: "memory");
        #pragma unroll
        for(int i=0;i<7;i++) g[i]=ld16(rs,sto+(h*7+i)*2048);
        #pragma unroll
        for(int i=0;i<7;i++) ok=ok&&g[i][1]==tag&&g[i][3]==tag;
        if(__all(ok)) break;
        __builtin_amdgcn_s_sleep(4); }
      RST(7);
      S=0.f; Q=0.f;
      #pragma unroll
      for(int i=0;i<7;i++){ S+=__uint_as_float(g[i][0]); Q+=__uint_as_float(g[i][2]); }
      XSUM32(S); XSUM32(Q);
      float mean=S*(1.f/864.f), m2,r2; ln_fin(mean,Q*(1.f/864.f)-mean*mean,m2,r2); m2*=-r2;
      ldswait(smb+C_DONE,8u);
      const u32 tb=smb+RB+mi*RING, ad=tb+(8*(lane>>4)+((lane&15)>>2))*80+(lane&3)*8;
      const u32 zo=OFF_Z2F+((rt*4+mi)*54+(j==13?-2:2+4*j))*1024+lane*16;
      #pragma unroll
      for(int n=0;n<2;n++){
        #pragma unroll
        for(int g2=0;g2<4;g2++){ u2 o;
          #pragma unroll
          for(int e=0;e<2;e++) o[e]=pkbf2(fma2(unpk(zp[n][2*g2+e]),f2{r2,r2},f2{m2,m2}));
          *(LAS u2*)(tb+r*80+g2*16+h*8)=o; }
        u2 l0=ldstr(ad), h0=ldstr(ad+320), l1=ldstr(ad+32), h1=ldstr(ad+32+320);
        if(j<13||n){ st16(rs,zo+n*2048,u4{l0[0],l0[1],h0[0],h0[1]}); st16(rs,zo+n*2048+1024,u4{l1[0],l1[1],h1[0],h1[1]}); } }
      RST(8);
      WAITV(0);
      if(arrive(smb+C_Z2,lane)==3u && lane==0) st4(rs,OFF_F2+(rt*16+j)*128,tag);
      RST(9);
    }
  }
  const int ft0=ct<7?7*ct:47;
  if(w>=4){
    STAMP(15,4);
    if(hasc){
      conv_wave(p,rs,dch,cb,smb+C_KP+mi*4,lane,ksc,dsk);
      if(arrive(smb+C_CV,lane)==(idx<24?2u:1u) && lane==0) st4(rs,OFF_FA+b*128,tag);
    }
    if(w==7){
      const u32 fo=lane<27? OFF_FW+lane*128 : OFF_F1+((rtB>>1)*16+(((ft0>>1)+(lane&3))>>1))*128; bool seen=false;
      if(idx<28){
        #pragma unroll 1
        for(int c=0;c<(idx<24?3:2);c++){ ldswait(smb+C_KP+c*4,1u); const u32 fv=ld4(rs,fo); asm volatile("" ::: "memory");
          conv_help(smb+c*CVS,lane); signal(smb+C_KP+c*4,lane);
          seen=__all(fv==tag); if(seen && lane==0) *(LAS u32*)(smb+C_Z1R)=1u; } }
      if(!seen){ pollw<16>(rs,fo,tag); if(lane==0) *(LAS u32*)(smb+C_Z1R)=1u; }
    }
    STAMP(14,4);
    signal(smb+C_FREE,lane);
    ldswait(smb+C_FREE,8u);
    { const u32 so=OFF_WOF+ct*(135*1024)+(w-4)*4096;
      #pragma unroll 1
      for(u32 i=(w-4)*4096;i<135*1024;i+=16384){ setm0<0>(smb+i); const u32 o=OFF_WOF+ct*(135*1024)+i; dma<0>(srdS,lane*16,o); dma<1024>(srdS,lane*16,o); dma<2048>(srdS,lane*16,o); dma<3072>(srdS,lane*16,o); } (void)so; }
    if(w==7){ const u32 vo=(lane>>3)*4096+(lane&7)*16;
      pollw(rs,OFF_FA+((lane<28?lane:0)*8+ct)*128,tag);
      #pragma unroll 1
      for(int i=0;i<10;i++){ setm0<ATL>(smb+i*1024); dma<0>(srdS,vo,OFF_AT+(u32)(80*ct+8*i)*4096+rtB*128); } }
    if(w==6 && b==255){
      #pragma unroll 1
      for(int sp=0;sp<SPIN;sp++){ const u32 o=OFF_FA+lane*128; u32 v0=ld4(rs,o),v1=ld4(rs,o+8192),v2=ld4(rs,o+16384),v3=ld4(rs,lane<32? o+24576 : OFF_FW+(lane-32)*128); asm volatile("" ::: "memory");
        if(__all(v0==tag&&v1==tag&&v2==tag&&v3==tag)) break; __builtin_amdgcn_s_sleep(4); }
      if(lane==0) st4(rs,OFF_EP,tag); }
    WAITV(0);
  }
  int w2=w; asm volatile("" : "+s"(w2));
  if(w2<4){
    __builtin_amdgcn_s_setprio(G2PRIO);
    int r=lane&15, q=lane>>4;
    const u32 rb=smb+RB+w*RING;
    u4 srdA=mksrd((const char*)p.wsp+(size_t)rtB*64*4096+w*1024-4096);
    const char* zb=p.ws+OFF_Z2F+(u32)(16*w*54+ft0+3)*1024;
    u32 voA[2]; { u32 lr=lane>>3,c=lane&7; voA[0]=lr*4096+((c^(lr>>1))<<4); voA[1]=voA[0]^64u; }
    u32 sofa[8];
    #pragma unroll
    for(int g=0;g<8;g++) sofa[g]=((g+2*w)&7)*32768+4096-(g&3)*1024;
    const u32 voB=lane*16;
    u32 la[2]; la[0]=rb+r*128+((q^(r>>1))<<4); la[1]=la[0]^64u;
    f4 acc[4][7]={}; u4 B[3][7]; u4 A[2][4]; u2 z1[7];
    auto issueA=[&](auto sc,auto gc){ constexpr int s=sc.value,g=gc.value; if constexpr((g&3)==0) setm0<((s+1)%3)*8192+g*1024>(rb); dma<(g&3)*1024>(srdA,voA[g&1],sofa[g]);
      if constexpr(g==7){ u64 a=((u64)srdA[1]<<32|srdA[0])+128; srdA[0]=(u32)a; srdA[1]=(u32)(a>>32); } };

    u32 bsr;
    sfor<3>([&](auto s){ sfor<8>([&](auto g){ issueA(s,g); }); });
    #pragma unroll
    for(int m=0;m<4;m++)
      #pragma unroll
      for(int n=0;n<7;n++) asm volatile("" : "+v"(acc[m][n]));
    { const int hf=(ft0>>1)+q; pollw(rs,OFF_F2+((4*w+(lane&3))*16+(hf==0?13:(hf-1)>>1))*128,tag); }
    RST(10);
    sfor<3>([&](auto hh){ const char* s=zb+(u32)hh.value*(54*1024); sfor<7>([&](auto n){ gload<(n.value-3)*1024>(B[hh][n],voB,s); }); });
    #pragma unroll
    for(int m=0;m<4;m++) A[0][m]=ldsr128(la[0]+m*2048+8192);
    const char* z1b=p.ws+OFF_Z1T+(u32)((rtB*4+w)*27+(ft0>>1))*1024;
    sfor<16>([&](auto hc){
      constexpr int h=hc.value, S=h%3, P=h&1;
      constexpr int cnt = h<2? 14 : ((h+1<16?7:0)+(h+2<16?7:0) + ((((h-2)&1)&&((h-2)/2+3<8))?8:0) + ((((h-1)&1)&&((h-1)/2+3<8))?8:0) + (h>=14?8:0));
      waitB<cnt>(B[S]); waitA(A[P]);
      if constexpr(h==13) ldswait(smb+C_Z1R,1u);
      if constexpr(h+1<16){
        #pragma unroll
        for(int m=0;m<4;m++) A[P^1][m]=ldsr128(la[(h+1)&1]+m*2048+((((h+1)>>1)+1)%3)*8192);
      }
      const char* s=zb+(u32)(h+3)*(54*1024);
      sfor<7>([&](auto n){
        mfma4(acc[0][n],acc[1][n],acc[2][n],acc[3][n],B[S][n],A[P]);
        if constexpr((h&1)&&(h/2+3<8)){ issueA(std::integral_constant<int,h/2+3>{},n); if constexpr(n.value==6) issueA(std::integral_constant<int,h/2+3>{},std::integral_constant<int,7>{}); }
        if constexpr(h+3<16) gload_t<(n.value-3)*1024>(B[S][n],voB,s);
        if constexpr(h==13){ gload8<(n.value>>1)*1024>(z1[n],voB,z1b+((n.value&1)? ((ft0&1)?1024:8) : (ft0&1)*8));
          if constexpr(n.value==6) gload2(bsr,(voB>>3)&30u,(const char*)(p.bsp+(rtB*4+w)*16)); }
      });
    });
    asm volatile("s_nop 15\n\ts_nop 3" ::: "memory");
    SCHED();
    STAMP(10,0);
    lane=__builtin_amdgcn_mbcnt_hi(~0u,__builtin_amdgcn_mbcnt_lo(~0u,0u)); asm volatile("" : "+v"(lane)); r=lane&15; q=lane>>4;
    #pragma unroll
    for(int m=1;m<4;m++)
      #pragma unroll
      for(int n=0;n<7;n++) *(LAS f4*)(rb+((m-1)*7+n)*1024+lane*16)=acc[m][n];
    WAITL0();
    signal(smb+C_RED,lane);
    ldswait(smb+C_RED,4u);
    #pragma unroll
    for(int m=1;m<4;m++){ u32 src=smb+RB+((w-m)&3)*RING+(m-1)*7168+lane*16;
      #pragma unroll
      for(int n=0;n<7;n++) acc[0][n]+=*(LAS f4*)(src+n*1024); }
    asm volatile("s_waitcnt vmcnt(0) lgkmcnt(0)" : "+v"(z1[0]),"+v"(z1[1]),"+v"(z1[2]),"+v"(z1[3]),"+v"(z1[4]),"+v"(z1[5]),"+v"(z1[6]),"+v"(bsr) :: "memory");
    const float bs=bflo(bsr);
    signal(smb+C_FREE,lane);
    u2 o[7];
    #pragma unroll
    for(int n=0;n<7;n++)
      #pragma unroll
      for(int e=0;e<2;e++)o[n][e]=pkbf2(unpk(z1[n][e])*(f2{acc[0][n][2*e],acc[0][n][2*e+1]}+bs));
    const u32 ho=OFF_HF+(u32)((rtB*4+w)*54+ft0)*512+(q>>1)*256+r*16;
    #pragma unroll
    for(int n=0;n<6;n+=2){ auto sx=__builtin_amdgcn_permlane16_swap(o[n][0],o[n+1][0],false,false); auto sy=__builtin_amdgcn_permlane16_swap(o[n][1],o[n+1][1],false,false);
      if(ct<7||n>=2) st16(rs,ho+(n+(q&1))*512,u4{sx[0],sy[0],sx[1],sy[1]}); }
    st8(rs,ho+6*512+(q&1)*8,o[6]);
    STAMP(11,0);
    WAITV(0);
    if(arrive(smb+C_H,lane)==3u && lane==0) st4(rs,OFF_FH+(rtB*8+ct)*128,tag);
    STAMP(12,0);
    __builtin_amdgcn_s_setprio(0);
  }
  { const int r=lane&15, q=lane>>4, m=w&3, kh=w>>2, t=rtB*64+m*16+r;
    u2 xv[5];
    #pragma unroll
    for(int n=0;n<5;n++) xv[n]=*(const u2*)(p.x+t*640+80*ct+16*n+4*q);
    if(w==0) pollw(rs,OFF_FH+(rtB*8+(lane&7))*128,tag);
    STAMP(16,0);
    bar();
    STAMP(17,0);
    u4 hf[14];
    #pragma unroll
    for(int k=0;k<14;k++) hf[k]=ld16(rs,OFF_HF+(u32)((rtB*4+m)*27+kh*13+k)*1024+lane*16);
    f4 acc[5]={}; u4 W[2][5];
    const u32 wl=smb+kh*13*5120+lane*16;
    #define WAD(k,n) (wl+((k)*5+(n))*1024)
    #pragma unroll
    for(int n=0;n<5;n++) W[0][n]=ldsr128(WAD(0,n));
    f2 xa[5][2];
    #pragma unroll
    for(int n=0;n<5;n++){ u2 av=ldstr(smb+ATL+(16*n+4*q+(r>>2))*128+(16*m+4*(r&3))*2); xa[n][0]=unpk(xv[n][0])+unpk(av[0]); xa[n][1]=unpk(xv[n][1])+unpk(av[1]); }
    if(kh) hf[0]=u4{0,0,0,0};
    #pragma unroll
    for(int k=0;k<14;k++){
      if(k<13){
        #pragma unroll
        for(int n=0;n<5;n++) W[(k+1)&1][n]=ldsr128(WAD(k+1,n));
      }
      SCHED();
      #pragma unroll
      for(int n=0;n<5;n++) acc[n]=mfma16(W[k&1][n],hf[k],acc[n]);
      SCHED();
    }
    STAMP(18,0);
    bar();
    if(kh){
      #pragma unroll
      for(int n=0;n<5;n++) *(LAS f4*)(smb+(m*5+n)*1024+lane*16)=acc[n];
    }
    bar();
    if(!kh){
      #pragma unroll
      for(int n=0;n<5;n++){ f4 o=acc[n]+*(LAS f4*)(smb+(m*5+n)*1024+lane*16); u2 res;
        #pragma unroll
        for(int e=0;e<2;e++)res[e]=pkbf2(xa[n][e]+f2{o[2*e],o[2*e+1]});
        *(u2*)(p.out+t*640+80*ct+16*n+4*q)=res; }
    }
    STAMP(19,0);
    if(w==0){ const u64 e=__builtin_amdgcn_s_memrealtime()+2*(dg1-dg0); while(__builtin_amdgcn_s_memrealtime()<e) __builtin_amdgcn_s_sleep(1); }
  }
}
extern "C" void kernel_launch(void* const* d_in,const int* in_sizes,int n_in,void* d_out,int out_size,void* d_ws,size_t ws_size,hipStream_t stream){
  Params p;
  p.x=(const u16*)d_in[0]; p.lar=(const u16*)d_in[1]; p.aim=(const u16*)d_in[2]; p.cre=(const u16*)d_in[3]; p.cim=(const u16*)d_in[4];
  p.ldt=(const u16*)d_in[5]; p.dsk=(const u16*)d_in[6]; p.win=(const u16*)d_in[8]; p.wsp=(const u16*)d_in[9]; p.bsp=(const u16*)d_in[10]; p.wout=(const u16*)d_in[11];
  p.out=(u16*)d_out; p.ws=(char*)d_ws;
  (void)hipFuncSetAttribute((const void*)fused,hipFuncAttributeMaxDynamicSharedMemorySize,LDS_TOTAL);
  fused<<<256,512,LDS_TOTAL,stream>>>(p);
}
```

```cpp
#include <hip/hip_runtime.h>
#include <utility>
#pragma clang diagnostic ignored "-Wint-to-pointer-cast"
typedef unsigned short u16;
typedef unsigned int u32;
typedef unsigned long long u64;
typedef __attribute__((ext_vector_type(8))) __bf16 bf8;
typedef __attribute__((ext_vector_type(8))) _Float16 hf8;
typedef __attribute__((ext_vector_type(2))) __bf16 bf2;
typedef __attribute__((ext_vector_type(2))) _Float16 hf2;
typedef __attribute__((ext_vector_type(2))) float f2;
typedef __attribute__((ext_vector_type(4))) float f4;
typedef __attribute__((ext_vector_type(16))) float f16v;
typedef __attribute__((ext_vector_type(4))) u32 u4;
typedef __attribute__((ext_vector_type(2))) u32 u2;
typedef __attribute__((ext_vector_type(4))) short s4;
#define LAS __attribute__((address_space(3)))
#define GAS __attribute__((address_space(1)))
#define DI __device__ __forceinline__

constexpr u32 OFF_Z2F = 0;
constexpr u32 OFF_Z1T = OFF_Z2F + 64*54*1024;
constexpr u32 OFF_XNT = OFF_Z1T + 128*27*1024;
constexpr u32 OFF_WOF = OFF_XNT + 640*4096;
constexpr u32 OFF_HF  = OFF_WOF + 8*27*5*1024;
constexpr u32 OFF_AT  = OFF_HF + 128*27*1024;
constexpr u32 OFF_ST  = OFF_AT + 640*4096;
constexpr u32 OFF_RS  = OFF_ST + 16*14*2048+8192;
constexpr u32 OFF_EP  = OFF_RS + 2048*16;
constexpr u32 OFF_F2  = OFF_EP + 256;
constexpr u32 OFF_F1  = OFF_F2 + 256*128;
constexpr u32 OFF_FX  = OFF_F1 + 256*128;
constexpr u32 OFF_FW  = OFF_FX + 256*128;
constexpr u32 OFF_FH  = OFF_FW + 32*128;
constexpr u32 OFF_FA  = OFF_FH + 256*128;
constexpr u32 WS_END  = OFF_FA + 256*128;

struct Params {
  const u16 *x, *lar, *aim, *cre, *cim, *ldt, *dsk, *win, *wsp, *bsp, *wout;
  u16* out; char* ws;
};
#define STAMP(id,wv) do{}while(0)
#define RST(i) do{}while(0)
template<class F,int... I> DI void sfor_impl(F&& f, std::integer_sequence<int,I...>){ (f(std::integral_constant<int,I>{}),...); }
template<int N,class F> DI void sfor(F&& f){ sfor_impl(f, std::make_integer_sequence<int,N>{}); }

DI float bflo(u32 v){ return __uint_as_float(v<<16); }
DI float bfhi(u32 v){ return __uint_as_float(v&0xffff0000u); }
DI float bf1(u16 v){ return __uint_as_float(((u32)v)<<16); }
DI u32 pkbf(float a,float b){ f2 v={a,b}; return __builtin_bit_cast(u32,__builtin_convertvector(v,bf2)); }
DI u32 pkhf(float a,float b){ f2 v={a,b}; return __builtin_bit_cast(u32,__builtin_convertvector(v,hf2)); }
DI float rbf(float a){ return bflo(pkbf(a,a)); }
DI float hlo(u32 v){ return (float)__builtin_bit_cast(hf2,v)[0]; }
DI float hhi(u32 v){ return (float)__builtin_bit_cast(hf2,v)[1]; }
DI float dot2(u32 a,u32 b,float c){ return __builtin_amdgcn_fdot2_f32_bf16(__builtin_bit_cast(bf2,a),__builtin_bit_cast(bf2,b),c,false); }
DI f4 mfma16(u4 a,u4 b,f4 c){ return __builtin_amdgcn_mfma_f32_16x16x32_bf16(__builtin_bit_cast(bf8,a),__builtin_bit_cast(bf8,b),c,0,0,0); }
DI f16v mfma32h(u4 a,u4 b,f16v c){ return __builtin_amdgcn_mfma_f32_32x32x16_f16(__builtin_bit_cast(hf8,a),__builtin_bit_cast(hf8,b),c,0,0,0); }
DI u4 ldsr128(u32 a){ return *(LAS u4*)a; }
DI u2 ldsr64(u32 a){ return *(LAS u2*)a; }
DI void ldsw128(u32 a,u4 v){ *(LAS u4*)a=v; }
DI u2 ldstr(u32 a){ return __builtin_bit_cast(u2,__builtin_amdgcn_ds_read_tr16_b64_v4i16((LAS s4*)a)); }
DI u32 uni(u32 v){ return __builtin_amdgcn_readfirstlane(v); }

template<int OFF> DI void gload(u4& dst,u32 voff,const char* base){ asm volatile("global_load_dwordx4 %0, %1, %2 offset:%3 sc1" : "=v"(dst) : "v"(voff), "s"(base), "n"(OFF)); }
template<int OFF> DI void gload_t(u4& dst,u32 voff,const char* base){ asm volatile("global_load_dwordx4 %0, %1, %2 offset:%3 sc1" : "+v"(dst) : "v"(voff), "s"(base), "n"(OFF)); }
DI void mfma4(f4& c0,f4& c1,f4& c2,f4& c3,const u4& a,const u4 (&b)[4]){
  asm volatile("v_mfma_f32_16x16x32_bf16 %0, %4, %5, %0\n\tv_mfma_f32_16x16x32_bf16 %1, %4, %6, %1\n\tv_mfma_f32_16x16x32_bf16 %2, %4, %7, %2\n\tv_mfma_f32_16x16x32_bf16 %3, %4, %8, %3"
    : "+v"(c0),"+v"(c1),"+v"(c2),"+v"(c3) : "v"(a),"v"(b[0]),"v"(b[1]),"v"(b[2]),"v"(b[3])); }
template<int CNT> DI void waitB(u4 (&B)[7]){ asm volatile("s_waitcnt vmcnt(%7)" : "+v"(B[0]),"+v"(B[1]),"+v"(B[2]),"+v"(B[3]),"+v"(B[4]),"+v"(B[5]),"+v"(B[6]) : "n"(CNT) : "memory"); }
DI void waitA(u4 (&A)[4]){ asm volatile("s_waitcnt lgkmcnt(0)" : "+v"(A[0]),"+v"(A[1]),"+v"(A[2]),"+v"(A[3]) :: "memory"); }
#define WAITV(n) asm volatile("s_waitcnt vmcnt(%0)" :: "n"(n) : "memory")
#define WAITL0() asm volatile("s_waitcnt lgkmcnt(0)" ::: "memory")
#define SCHED() __builtin_amdgcn_sched_barrier(0)
DI void bar(){ asm volatile("s_waitcnt lgkmcnt(0)\n\ts_barrier" ::: "memory"); }
DI f2 unpk(u32 v){ return f2{bflo(v),bfhi(v)}; }
DI u32 pkbf2(f2 v){ return __builtin_bit_cast(u32,__builtin_convertvector(v,bf2)); }
DI u32 pkhf2(f2 v){ return __builtin_bit_cast(u32,__builtin_convertvector(v,hf2)); }
DI f2 fma2(f2 a,f2 b,f2 c){ return __builtin_elementwise_fma(a,b,c); }
DI f2 gelu2(f2 x){
  f2 u=x*fma2(x*x,f2{-0.10294324f,-0.10294324f},f2{-2.30220820f,-2.30220820f});
  f2 d=f2{__builtin_amdgcn_exp2f(u[0]),__builtin_amdgcn_exp2f(u[1])}+f2{1.f,1.f};
  return x*f2{__builtin_amdgcn_rcpf(d[0]),__builtin_amdgcn_rcpf(d[1])};
}
DI void ln_fin(float mean,float var,float& m,float& rinv){
  m=rbf(mean); float v=rbf(__builtin_fmaxf(var,0.f));
  float s=rbf(__builtin_amdgcn_sqrtf(rbf(v+1e-5f))); float r=__builtin_amdgcn_rcpf(s); rinv=__builtin_fmaf(__builtin_fmaf(-s,r,1.f),r,r);
}
typedef __amdgpu_buffer_rsrc_t rsrc_t;
DI void st16(rsrc_t rs,u32 off,u4 v){ __builtin_amdgcn_raw_buffer_store_b128(v,rs,off,0,16); }
DI void st8(rsrc_t rs,u32 off,u2 v){ __builtin_amdgcn_raw_buffer_store_b64(v,rs,off,0,16); }
DI void st4(rsrc_t rs,u32 off,u32 v){ __builtin_amdgcn_raw_buffer_store_b32(v,rs,off,0,16); }
DI u4 ld16(rsrc_t rs,u32 off){ return __builtin_amdgcn_raw_buffer_load_b128(rs,off,0,16); }
DI u2 ld8(rsrc_t rs,u32 off){ return __builtin_amdgcn_raw_buffer_load_b64(rs,off,0,16); }
DI u32 ld4(rsrc_t rs,u32 off){ return __builtin_amdgcn_raw_buffer_load_b32(rs,off,0,16); }
constexpr int SPIN=1<<17;
template<int SL=2> DI void pollw(rsrc_t rs,u32 off,u32 tag){
  #pragma unroll 1
  for(int s=0;s<SPIN;s++){ u32 v=ld4(rs,off); asm volatile("" ::: "memory"); if(__all(v==tag)) break; __builtin_amdgcn_s_sleep(SL); }
}

DI f16v mfma32b(u4 a,u4 b,f16v c){ return __builtin_amdgcn_mfma_f32_32x32x16_bf16(__builtin_bit_cast(bf8,a),__builtin_bit_cast(bf8,b),c,0,0,0); }
DI u4 mksrd(const void* b){ u64 a=(u64)b; return u4{(u32)a,(u32)(a>>32)&0xffffu,0xffffffffu,0x00020000u}; }
template<int IMM> DI void setm0(u32 base){ asm volatile("s_add_u32 m0, %0, %1\n\ts_nop 0" :: "s"(base), "n"(IMM) : "scc","memory"); }
template<int OFF> DI void dma(u4 srd,u32 voff,u32 soff){ asm volatile("buffer_load_dwordx4 %0, %1, %2 offen offset:%3 sc1 lds" :: "v"(voff), "s"(srd), "s"(soff), "n"(OFF) : "memory"); }
DI void touch(u4 srd,u32 voff,u32 soff){ asm volatile("buffer_load_dword %0, %1, %2 offen sc1 lds" :: "v"(voff), "s"(srd), "s"(soff) : "memory"); }
template<int OFF> DI void gload8(u2& dst,u32 voff,const char* base){ asm volatile("global_load_dwordx2 %0, %1, %2 offset:%3 sc1" : "=v"(dst) : "v"(voff), "s"(base), "n"(OFF)); }
DI void gload2(u32& dst,u32 voff,const char* base){ asm volatile("global_load_ushort %0, %1, %2" : "=v"(dst) : "v"(voff), "s"(base)); }
DI u32 arrive(u32 a,int){ u32 o; asm volatile("s_mov_b64 exec, 1\n\tds_add_rtn_u32 %0, %1, %2\n\ts_mov_b64 exec, -1\n\ts_waitcnt lgkmcnt(0)" : "=&v"(o) : "v"(a), "v"(1u) : "memory"); return uni(o); }
DI void ldswait(u32 a,u32 n){ while(__hip_atomic_load((LAS u32*)a,__ATOMIC_RELAXED,__HIP_MEMORY_SCOPE_WORKGROUP)<n) __builtin_amdgcn_s_sleep(1); asm volatile("" ::: "memory"); }
DI float addf(float a,float b){ float o; asm("v_add_f32 %0, %1, %2" : "=v"(o) : "v"(a), "v"(b)); return o; }
DI void tie1(u32& g){ asm volatile("" : "+v"(g) :: "memory"); }
DI u32 opq(u32 v){ asm volatile("" : "+v"(v)); return v; }

constexpr u32 STG=32768;
constexpr u32 RING=24576, RB=62848, CONV=0, KP=4192, CVS=4*KP+4160, UOF=4*KP, MISC=RB+4*RING, JNK=MISC+2048, PM=163840-128, LDS_TOTAL=163840;
constexpr u32 ATL=2*CVS;
constexpr u32 C_FULL=PM, C_G1=PM+4, C_Z2=PM+8, C_Z1=PM+12, C_RED=PM+16, C_H=PM+20, C_FREE=PM+24, C_CV=PM+28, C_Z1R=PM+32, C_TAG=PM+40, C_KP=PM+48, C_DONE=PM+64, C_ZQ=PM+68, C_CS=PM+72;
constexpr u32 CODE_BYTES=32768;
#define LA 3
#define TC_S 7
#define G2PRIO 2
#define XMAX 9
#define GS_K 35
#define WNT ""
#define TS1 6
#define HPRIO 3
#define CPRIO 3
#define PDLY 8
#define RS_S 7
#define KROT ((j+3*(rt&3))%10)
DI void signal(u32 a,int){ asm volatile("s_mov_b64 exec, 1\n\tds_add_u32 %0, %1\n\ts_mov_b64 exec, -1" :: "v"(a), "v"(1u) : "memory"); }
template<int OFF> DI void gload4(u32& dst,u32 voff,const char* base){ asm volatile("global_load_dword %0, %1, %2 offset:%3 sc1" : "=v"(dst) : "v"(voff), "s"(base), "n"(OFF)); }
template<int CNT> DI void waitG(u4& g){ asm volatile("s_waitcnt vmcnt(%1)" : "+v"(g) : "n"(CNT) : "memory"); }

DI f2 cmulp(f2 a,f2 b,f2 bs){ return fma2(f2{a[1],a[1]},bs,f2{a[0],a[0]}*b); }
DI float fmaf1(float a,float b,float c){ float o; asm("v_fma_f32 %0, %1, %2, %3" : "=v"(o) : "v"(a),"v"(b),"v"(c)); return o; }
DI u32 ldsv(u32 a){ return __hip_atomic_load((LAS u32*)a,__ATOMIC_RELAXED,__HIP_MEMORY_SCOPE_WORKGROUP); }
DI void ldswait4(u32 a,int lane,u32 n){ while(__any(ldsv(a+(lane&3)*4)<n)) __builtin_amdgcn_s_sleep(1); asm volatile("" ::: "memory"); }
DI u32 gettag(u32 a){ ldswait(a+4,1u); return uni(ldsv(a)); }
DI f2 cpow_dir(float lr,float rv,float pw){ float e=__builtin_amdgcn_exp2f(lr*pw); float f=__builtin_amdgcn_fractf(rv*pw);
  return f2{e*__builtin_amdgcn_cosf(f),e*__builtin_amdgcn_sinf(f)}; }
DI u32 dup(u32 a){ return __builtin_amdgcn_perm(a,a,0x01000100u); }
DI u32 cat(u32 lo,u32 hi){ return __builtin_amdgcn_perm(hi,lo,0x05040100u); }
DI u32 lds16(u32 a){ return *(LAS u16*)a; }

template<class CB> DI float kgen_tab(u32 cb,int lane,const u32 (&pr)[6],float (&kp)[11],CB&& mid){
  const float dt=rbf(__expf(bflo(pr[4])));
  const float cr[2]={bflo(pr[2]),bfhi(pr[2])}, cm[2]={bflo(pr[3]),bfhi(pr[3])};
  float mx=fmaxf(fmaxf(fabsf(cr[0]),fabsf(cm[0])),fmaxf(fabsf(cr[1]),fabsf(cm[1])));
  u32 eb=__float_as_uint(mx)&0x7f800000u;
  #define DPPMAX(c) eb=max(eb,(u32)__builtin_amdgcn_update_dpp(0,(int)eb,c,0xf,0xf,false))
  DPPMAX(0x128); DPPMAX(0x124); DPPMAX(0x122); DPPMAX(0x121);
  eb=max(max((u32)__builtin_amdgcn_readlane(eb,0),(u32)__builtin_amdgcn_readlane(eb,16)),max((u32)__builtin_amdgcn_readlane(eb,32),(u32)__builtin_amdgcn_readlane(eb,48))); eb=eb<0x01000000u?0x3f800000u:(eb>0x7e000000u?0x7e000000u:eb);
  const float ksc=2.f*dt*__uint_as_float(eb), isc=__uint_as_float(0x7f000000u-eb);
  const int r=lane&31,h=lane>>5;
  const u32 TP=272, TA=cb, TB=cb+22*TP;
  f16v acc={};
  #pragma unroll 1
  for(int c=0;c<2;c++){
    if(c) mid();
    float l_a=c?bfhi(pr[0]):bflo(pr[0]), i_m=c?bfhi(pr[1]):bflo(pr[1]), c_r=c?cr[1]:cr[0], c_i=c?cm[1]:cm[0];
    const float lr=(-rbf(__expf(l_a))*dt)*1.44269504f, rv=(i_m*dt)*0.15915494f;
    const f2 z1=cpow_dir(lr,rv,1.f), z8=cpow_dir(lr,-rv,8.f), zs=cpow_dir(lr,-rv,16.f), zk=cpow_dir(lr,rv,256.f);
    const f2 z1s={-z1[1],z1[0]}, zss={-zs[1],zs[0]}, zks={-zk[1],zk[0]};
    const u32 wa=TA+lane*4, wb=TB+lane*4;
    #define PUT(base,row,v) do{ *(LAS u32*)((base)+(row)*TP)=pkhf2(v); }while(0)
    f2 e={1.f,0.f}, o=z8;
    #pragma unroll
    for(int k=0;k<16;k++){ PUT(wb,2*k,e); PUT(wb,2*k+1,o); if(k<15){ e=k?cmulp(e,zs,zss):zs; o=cmulp(o,zs,zss);} }
    f2 b0={c_r*isc,c_i*isc}; PUT(wa,0,b0);
    f2 t=cmulp(b0,zk,zks); PUT(wa,8,t);
    f2 b1=cmulp(b0,z1,z1s); PUT(wa,1,b1); t=cmulp(t,zk,zks); PUT(wa,10,t);
    f2 b2=cmulp(b1,z1,z1s); PUT(wa,2,b2); t=cmulp(t,zk,zks); PUT(wa,11,t);
    f2 b3=cmulp(b2,z1,z1s); PUT(wa,3,b3); t=cmulp(t,zk,zks); PUT(wa,16,t);
    f2 b4=cmulp(b3,z1,z1s); PUT(wa,4,b4); t=cmulp(t,zk,zks); PUT(wa,17,t);
    f2 b5=cmulp(b4,z1,z1s); PUT(wa,5,b5); t=cmulp(t,zk,zks); PUT(wa,20,t);
    f2 b6=cmulp(b5,z1,z1s); PUT(wa,6,b6); t=cmulp(t,zk,zks); PUT(wa,21,t);
    f2 b7=cmulp(b6,z1,z1s); PUT(wa,7,b7); t=cmulp(t,zk,zks); PUT(wa,18,t);
    f2 s=cmulp(b4,zk,zks); PUT(wa,12,s); t=cmulp(b2,zk,zks); PUT(wa,9,t);
    s=cmulp(s,zk,zks); PUT(wa,14,s); t=cmulp(b6,zk,zks); PUT(wa,13,t);
    s=cmulp(s,zk,zks); PUT(wa,15,s);
    #undef PUT
    WAITL0();
    const int ra=(r>21||r==19)?0:r;
    #pragma unroll
    for(int ks=0;ks<8;ks++){ u4 fa=ldsr128(TA+ra*TP+ks*32+h*16), fb=ldsr128(TB+r*TP+ks*32+h*16); acc=mfma32h(fa,fb,acc); }
    WAITL0();
  }
  #pragma unroll
  for(int i=0;i<11;i++) kp[i]=acc[i];
  return ksc;
}
DI void kgen_exp(u32 cb,int lane,const float (&acc)[11]){
  const int r=lane&31,h=lane>>5;
  { const u32 a4=cb+r*16+h*8, a5=cb+r*16+h*1024;
    #define PK(addr,base,reg) do{ *(LAS _Float16*)((addr)+2*(base))=(_Float16)acc[reg]; }while(0)
    PK(a4,0,0);PK(a4,1,1);PK(a4,2,2);PK(a4,3,3); PK(a4,256,4);PK(a4,258,5);PK(a4,512,6);PK(a4,768,7);
    PK(a5,1024,8);PK(a5,1280,9);PK(a5,2048,10);
    #undef PK
  }
  WAITL0();
  { const u32 g0=cb+2*(2048-8*lane);
    u32 a0=lds16(g0), b0=lds16(g0-16), a1=lds16(g0-1024), b1=lds16(g0-1024-16);
    u32 f2_=lds16(g0-2048+(lane?8:0)), a2=lds16(g0-2048), m2=lds16(g0-2048-8), b2=lds16(g0-2048-16);
    u32 e[11];
    #pragma unroll
    for(int j=0;j<11;j++) e[j]=lds16(cb+2u*(u32)rbf((float)(512-8*lane+3-j)));
    u32 k01=*(LAS u32*)cb, k23=*(LAS u32*)(cb+4);
    WAITL0();
    const u32 wo=cb+lane*16;
    { u32 aa=dup(a0),bb=dup(b0),c=(lane&1)?b0:a0, cbv=cat(c,b0), ac=cat(a0,c);
      ldsw128(wo,u4{aa,aa,cbv,bb}); ldsw128(wo+KP,u4{aa,aa,ac,bb}); ldsw128(wo+2*KP,u4{aa,aa,aa,cbv}); ldsw128(wo+3*KP,u4{aa,aa,aa,ac}); }
    { u32 aa=dup(a1),bb=dup(b1),c=(lane&1)?b1:a1, cbv=cat(c,b1), ac=cat(a1,c);
      ldsw128(wo+1024,u4{aa,aa,cbv,bb}); ldsw128(wo+1024+KP,u4{aa,aa,ac,bb}); ldsw128(wo+1024+2*KP,u4{aa,aa,aa,cbv}); ldsw128(wo+1024+3*KP,u4{aa,aa,aa,ac}); }
    { u32 aa=dup(a2),mm=dup(m2),bb=dup(b2),am=cat(a2,m2),fa=cat(f2_,a2),mb=cat(m2,b2);
      ldsw128(wo+2048,u4{aa,am,mm,bb}); ldsw128(wo+2048+KP,u4{aa,aa,mm,mb}); ldsw128(wo+2048+2*KP,u4{aa,aa,am,mm}); ldsw128(wo+2048+3*KP,u4{fa,aa,aa,mm}); }
    { u32 o0=cat(e[0],e[1]),o1=cat(e[2],e[3]),o2=cat(e[4],e[5]),o3=cat(e[6],e[7]),o4=cat(e[8],e[9]);
      u32 p1=cat(e[1],e[2]),d0=cat(e[3],e[4]),d1=cat(e[5],e[6]),d2=cat(e[7],e[8]),d3=cat(e[9],e[10]);
      ldsw128(wo+3072,u4{d0,d1,d2,d3}); ldsw128(wo+3072+KP,u4{o1,o2,o3,o4}); ldsw128(wo+3072+2*KP,u4{p1,d0,d1,d2}); ldsw128(wo+3072+3*KP,u4{o0,o1,o2,o3}); }
    if(lane<4){ if(lane){k01=0;k23=0;}
      u32 d0=k01&0xffffu, o1=__builtin_amdgcn_alignbit(k01,k01,16), o0=__builtin_amdgcn_alignbit(k23,k23,16), p1=(k23&0xffffu)|(k01&0xffff0000u);
      ldsw128(wo+4096,u4{d0,0,0,0}); ldsw128(wo+4096+KP,u4{o1,0,0,0}); ldsw128(wo+4096+2*KP,u4{p1,d0,0,0}); ldsw128(wo+4096+3*KP,u4{o0,o1,0,0}); }
  }
}
DI f4 mfma16h(u4 a,u4 b,f4 c){ return __builtin_amdgcn_mfma_f32_16x16x32_f16(__builtin_bit_cast(hf8,a),__builtin_bit_cast(hf8,b),c,0,0,0); }
#define RDA(S,i,m) asm volatile("ds_read2_b64 %0, %1 offset0:%2 offset1:%3" : "=v"(AF[S][i][m]) : "v"((i)<2?kb:ka), "n"((1-((i)&1))*128+(1-(m))*4), "n"((1-((i)&1))*128+(1-(m))*4+1))
#define RDB(S,i,ad,o) asm volatile("ds_read_b128 %0, %1 offset:%2" : "=v"(BF[S][i]) : "v"(ad), "n"(o))
#define TIEA(S,i) "+v"(AF[S][i][0]),"+v"(AF[S][i][1])
#define CV_M(S,i,n) do{ y[0][n]=mfma16h(AF[S][i][0],BF[S][(n)-(i)],y[0][n]); y[1][n]=mfma16h(AF[S][i][1],BF[S][(n)-(i)],y[1][n]); SCHED(); }while(0)
#define CV_U0() u32 u0=u1-256; u0=((int)u0<(int)uz)?uz:u0; const u32 kb=ka+2048; (void)kb
#define CV_SETUP() const int r=lane&15,q=lane>>4; u32 ka=cb+(r&3)*KP+16*q-8*(r>>2)+992; const u32 uz=cb+UOF+q*1040; u32 u1=uz+(r+17)*16; u4 AF[2][4][2]; u4 BF[2][4]
DI void conv_help(u32 cb,int lane){
  CV_SETUP(); f4 y[2][4]={};
  #define HP_WT(S) asm volatile("s_waitcnt lgkmcnt(0)" : TIEA(S,0),TIEA(S,1),TIEA(S,2),TIEA(S,3),"+v"(BF[S][0]),"+v"(BF[S][1]),"+v"(BF[S][2]),"+v"(BF[S][3]))
  #define HP_AD() const u32 kb=ka-64, u3=u1-16; u32 u0=u1-256, u2_=u1-272; u0=((int)u0<(int)uz)?uz:u0; u2_=((int)u2_<(int)uz)?uz:u2_
  #define HP_M(S,i,bi,n) do{ y[0][n]=mfma16h(AF[S][i][0],BF[S][bi],y[0][n]); y[1][n]=mfma16h(AF[S][i][1],BF[S][bi],y[1][n]); SCHED(); }while(0)
  #define HP_STEP(C,N) do{ HP_AD(); RDA(N,2,0); RDA(N,2,1); RDB(N,1,u1,0); RDA(N,3,0); SCHED(); HP_M(C,2,1,3); RDA(N,3,1); RDB(N,0,u0,0); RDA(N,0,0); RDA(N,0,1); SCHED(); HP_M(C,3,0,2); \
      RDB(N,3,u3,0); RDA(N,1,0); RDA(N,1,1); RDB(N,2,u2_,0); ka-=128; u1-=32; SCHED(); HP_M(C,0,3,3); HP_M(C,1,2,2); HP_WT(N); }while(0)
  { HP_AD(); RDA(0,2,0); RDA(0,2,1); RDB(0,1,u1,0); RDA(0,3,0); RDA(0,3,1); RDB(0,0,u0,0); RDA(0,0,0); RDA(0,0,1); RDB(0,3,u3,0); RDA(0,1,0); RDA(0,1,1); RDB(0,2,u2_,0); ka-=128; u1-=32; HP_WT(0); }
  #pragma unroll 1
  for(int dl=0;dl<4;dl++){ HP_STEP(0,1); if(dl==3){ ka+=128; u1+=32; } HP_STEP(1,0); }
  y[0][3]+=y[0][2]; y[1][3]+=y[1][2];
  *(LAS f4*)(cb+q*KP+r*16)=y[0][3]; *(LAS f4*)(cb+q*KP+256+r*16)=y[1][3];
  WAITL0();
}
DI void conv_wave(const Params& p,rsrc_t rs,int d,u32 cb,u32 flag,int lane,float ksc,float dsk){
  CV_SETUP(); f4 y[2][4]={};
  #define CV_WT(S) asm volatile("s_waitcnt lgkmcnt(0)" : TIEA(S,0),TIEA(S,1),TIEA(S,2),"+v"(BF[S][0]),"+v"(BF[S][1]),"+v"(BF[S][2]),"+v"(BF[S][3]))
  #define CV_STEP(C,N) do{ CV_U0(); CV_M(C,0,0); RDA(N,0,0); RDB(N,0,u0,0); SCHED(); CV_M(C,0,1); RDA(N,0,1); RDB(N,1,u1,0); SCHED(); CV_M(C,0,2); RDA(N,1,0); RDB(N,2,u1,256); SCHED(); \
      CV_M(C,0,3); RDA(N,1,1); RDB(N,3,u1,512); SCHED(); CV_M(C,1,1); RDA(N,2,0); SCHED(); CV_M(C,1,2); RDA(N,2,1); ka-=64; u1-=16; SCHED(); CV_M(C,1,3); CV_M(C,2,2); CV_WT(N); }while(0)
  { CV_U0(); RDA(0,0,0); RDA(0,0,1); RDA(0,1,0); RDA(0,1,1); RDA(0,2,0); RDA(0,2,1); RDB(0,0,u0,0); RDB(0,1,u1,0); RDB(0,2,u1,256); RDB(0,3,u1,512); ka-=64; u1-=16; CV_WT(0); }
  #pragma unroll 1
  for(int dl=0;dl<8;dl++){ CV_STEP(0,1); if(dl==7){ ka+=64; u1+=16; } CV_STEP(1,0); }
  ldswait(flag,2u);
  y[0][3]+=*(LAS f4*)(cb+q*KP+r*16); y[1][3]+=*(LAS f4*)(cb+q*KP+256+r*16);
  #pragma unroll
  for(int n=0;n<4;n++){ u2 o[2];
    #pragma unroll
    for(int m=0;m<2;m++){ u2 uv=ldsr64(cb+UOF+(2*m+(q>>1))*1040+(16*n+r+1)*16+8*(q&1));
      #pragma unroll
      for(int e=0;e<2;e++) o[m][e]=pkbf2(fma2(f2{hlo(uv[e]),hhi(uv[e])},f2{dsk,dsk},f2{y[m][n][2*e],y[m][n][2*e+1]}*ksc)); }
    auto sx=__builtin_amdgcn_permlane16_swap(o[0][0],o[1][0],false,false); auto sy=__builtin_amdgcn_permlane16_swap(o[0][1],o[1][1],false,false);
    const u32 sc=cb+1024+n*1280; ldsw128(sc+r*80+(q&1)*32+(q>>1)*16,u4{sx[0],sy[0],sx[1],sy[1]});
    st16(rs,OFF_AT+d*4096+n*1024+lane*16,ldsr128(sc+(lane>>2)*80+(lane&3)*16)); }
  WAITV(0);
}

struct Frag { u4 x[4], w[4][2]; };
__global__ __launch_bounds__(512,2) void fused(Params p){
  extern __shared__ __attribute__((aligned(16))) char sm[];
  const u32 smb=(u32)(size_t)(LAS char*)sm;
  u64 pc0; asm volatile("s_getpc_b64 %0" : "=s"(pc0));
  const int b=blockIdx.x, xc=b&7, idx=b>>3;
  const int tid=threadIdx.x, w=uni(tid>>6); int lane=tid&63;
  const rsrc_t rs=__builtin_amdgcn_make_buffer_rsrc(p.ws,0,WS_END,0x00020000);
  const u4 srdS=mksrd(p.ws);
  const int rtB=8*(xc>>1)+(idx&7), ct=4*(xc&1)+(idx>>3);
  const bool hasc=idx<28 && idx+28*(w&3)<80 && w>=4;
  const int dch=80*xc+(hasc? idx+28*(w&3) : 0);
  const u32 cb=smb+CONV+(w&3)*CVS;
  const int rt=4*(xc>>1)+(idx&3), j=7*(xc&1)+(idx>>2), mi=w&3, g1=w>>2;
  u32 tag; float ksc=0.f, dsk=0.f;
  STAMP(0,0);
  auto warm=[&](int wq,int part){
    const u4 sp=mksrd((const char*)p.wsp+(size_t)(rtB*64+ct*8+2*wq)*4096); setm0<JNK>(smb);
    const u32 vo= part==2? lane*128 : ((lane>>2)&7)*1024+(lane&3)*128+part*512;
    asm volatile("buffer_load_dword %0, %1, 0 offen lds" :: "v"(vo), "s"(sp) : "memory");
  };
  if(idx>=28){
    const int a=xc*4+idx-28, r=lane&15, q=lane>>4;
    { u4 sc=mksrd((const void*)(pc0&~127ull)); sc[2]=CODE_BYTES; setm0<JNK>(smb); touch(sc,lane*128,w*8192); }
    const u32 ep=ld4(rs,OFF_EP);
    u4 xr[10]; const char* xs=(const char*)p.x+(size_t)(64*a+8*w+(lane>>3))*1280+(lane&7)*16;
    #pragma unroll
    for(int i=0;i<10;i++) xr[i]=*(const u4*)(xs+i*128);
    if(tid<32) *(LAS u32*)(smb+PM+tid*4)=0u;
    tag=uni(ep)+1u;
    float s0=0.f,s1=0.f,q0=0.f,q1=0.f;
    #pragma unroll
    for(int i=0;i<10;i++){ s0=dot2(xr[i][0],0x3f803f80u,s0); q0=dot2(xr[i][0],xr[i][0],q0); s1=dot2(xr[i][1],0x3f803f80u,s1); q1=dot2(xr[i][1],xr[i][1],q1);
      s0=dot2(xr[i][2],0x3f803f80u,s0); q0=dot2(xr[i][2],xr[i][2],q0); s1=dot2(xr[i][3],0x3f803f80u,s1); q1=dot2(xr[i][3],xr[i][3],q1); }
    s0+=s1; q0+=q1;
    #define DPPADD(v,c) v+=__int_as_float(__builtin_amdgcn_update_dpp(0,__float_as_int(v),c,0xf,0xf,false))
    DPPADD(s0,0xB1); DPPADD(q0,0xB1); DPPADD(s0,0x4E); DPPADD(q0,0x4E); DPPADD(s0,0x141); DPPADD(q0,0x141);
    float m1,ri; { float mean=s0*(1.f/640.f); ln_fin(mean,q0*(1.f/640.f)-mean*mean,m1,ri); }
    if((lane&7)==0) st16(rs,OFF_RS+(64*a+8*w+(lane>>3))*16,u4{__float_as_uint(m1),tag,__float_as_uint(ri),tag});
    STAMP(5,0);
    #pragma unroll
    for(int i=0;i<10;i++){ u4 o;
      #pragma unroll
      for(int e=0;e<4;e++) o[e]=pkhf2(fma2(unpk(xr[i][e]),f2{ri,ri},f2{-m1*ri,-m1*ri}));
      ldsw128(smb+RB+(8*w+(lane>>3))*1296+(lane&7)*16+i*128,o); }
    bar();
    #pragma unroll
    for(int i=0;i<10;i++){ const int c=w*10+i; const u32 ad=smb+RB+(32*((r>>1)&1)+8*q+(r>>2))*1296+c*16+(r&1)*8; u2 lo=ldstr(ad), hi=ldstr(ad+4*1296);
      const u32 sc=smb+w*5120+(i%5)*1024; ldsw128(sc+(r&7)*128+((((r>>3)*4+q)^(r&7))<<4),u4{lo[0],lo[1],hi[0],hi[1]});
      st16(rs,OFF_XNT+a*81920+c*1024+lane*16,ldsr128(sc+(lane>>3)*128+(((lane&7)^(lane>>3))<<4))); }
    WAITV(0);
    if(arrive(smb+C_Z1,lane)==7u && lane==0) st4(rs,OFF_FX+a*128,tag);
    STAMP(6,7);
    if(a<27){ const u4 srdO=mksrd((const char*)p.wout+a*(32*1280)+w*5120); const u32 mo=smb+w*5120; setm0<0>(mo);
      dma<0>(srdO,lane*16,0); dma<1024>(srdO,lane*16,0); dma<2048>(srdO,lane*16,0); dma<3072>(srdO,lane*16,0); setm0<4096>(mo); dma<0>(srdO,lane*16,4096); }
    WAITV(0); bar();
    if(a<27){
      #pragma unroll
      for(int i=0;i<5;i++){ u32 ad=smb+(8*q+(r>>2))*1280+(w*5+i)*32+(r&3)*8; u2 lo=ldstr(ad), hi=ldstr(ad+4*1280);
        st16(rs,OFF_WOF+(u32)((w*27+a)*5+i)*1024+lane*16,u4{lo[0],lo[1],hi[0],hi[1]}); } }
    WAITV(0); bar();
    if(tid==0) st4(rs,OFF_FW+a*128,tag);
    if(g1) warm(mi,2);
    STAMP(31,0);
  } else {
    const int r=lane&31, h=lane>>5;
    const u4 srdX=mksrd((const char*)p.x+(size_t)rt*128*1280);
    const u4 srdW=mksrd((const char*)p.win+j*128-3072);
    const u32 mw=smb+w*4096;
    u32 vx0,vx1,vw1,vw2,epv;
    { u32 lr=lane>>3,c=lane&7; vx0=(16*w+lr)*1280+((c^(lr>>1))<<4); vx1=(vx0^64u)+9216;
      vw1=(8*w+lr)*3456+((c^((lr&2)<<1))<<4); vw2=vw1+1024+(j==13?0:14*128); }
    u32 kx[10]; { u32 k=KROT;
      #pragma unroll
      for(int i=0;i<10;i++){ kx[i]=k*128; k=k==9?0:k+1; } }
    auto issue1=[&](auto sc,int k){ constexpr int s=sc.value;
      if(k==0){ setm0<(s&3)*STG>(mw); dma<0>(srdX,vx0,kx[s]); } if(k==1) dma<1024>(srdX,vx1,kx[s]); if(k==2) dma<2048>(srdW,vw2,kx[s]*1728); if(k==3) dma<3072>(srdW,vw1,kx[s]*1728); };
    auto issue=[&](auto sc){ issue1(sc,0); issue1(sc,1); issue1(sc,2); issue1(sc,3); };
    issue(std::integral_constant<int,0>{});
    u32 pr[6];
    gload4<0>(epv,0u,p.ws+OFF_EP);
    gload4<0>(pr[0],lane*4,(const char*)(p.lar+dch*128)); gload4<0>(pr[1],lane*4,(const char*)(p.aim+dch*128)); gload4<0>(pr[2],lane*4,(const char*)(p.cre+dch*128)); gload4<0>(pr[3],lane*4,(const char*)(p.cim+dch*128));
    gload2(pr[4],0u,(const char*)(p.ldt+dch)); gload2(pr[5],0u,(const char*)(p.dsk+dch));
    asm volatile("s_barrier" ::: "memory");
    issue(std::integral_constant<int,1>{});
    if(tid<32) *(LAS u32*)(smb+PM+tid*4)=0u;
    u32 ax[2][4],aw[2][4][2];
    { u32 row=32*mi+r, a0=smb+(row>>4)*4096+(row&15)*128+((h^((r>>1)&7))<<4);
      u32 qq=(lane&15)>>2, w0=smb+2048+g1*1024+h*4096+qq*128+8*(lane&1)+(((2*((lane>>4)&1)+((lane>>1)&1))^((qq&2)<<1))<<4);
      #pragma unroll
      for(int i=0;i<2;i++)
        #pragma unroll
        for(int k=0;k<4;k++){ const u32 kk=(k+mi)&3, wo=i*2*STG; ax[i][k]=opq((a0^(kk*32u))+wo); aw[i][k][0]=opq(w0+kk*8192+wo); aw[i][k][1]=opq((w0^64u)+kk*8192+wo); } }
    u4 sel; { const u32 o=((lane&15)==((lane>>4)&1))?0x3f803f80u:0u; sel=u4{o,o,o,o}; asm volatile("" : "+v"(sel)); }
    Frag F[2];
    asm volatile("s_waitcnt vmcnt(4)" : "+v"(epv),"+v"(pr[0]),"+v"(pr[1]),"+v"(pr[2]),"+v"(pr[3]),"+v"(pr[4]),"+v"(pr[5]) :: "memory");
    bar();
    tag=uni(epv)+1u; dsk=bflo(pr[5]);
    #pragma unroll
    for(int k=0;k<4;k++){
      #pragma unroll
      for(int n=0;n<2;n++){ u2 lo=ldstr(aw[0][k][n]), hi=ldstr(aw[0][k][n]+512); F[0].w[k][n]=u4{lo[0],lo[1],hi[0],hi[1]}; }
      F[0].x[k]=ldsr128(ax[0][k]); }
    issue(std::integral_constant<int,2>{});
    STAMP(1,0);
    f16v acc[2]={}; f4 cacc[2]={}; u4 G;
    const u32 go=OFF_RS+rt*2048+mi*512, cp=smb+MISC+g1*1024;
    sfor<10>([&](auto ic){
      constexpr int s=ic.value; Frag& cur=F[s&1]; Frag& nxt=F[(s&1)^1];
      if constexpr(s<9){ WAITV((s==8? 2 : s==7? 6 : 4)+((TS1<6&&(s==TS1+1||s==TS1+2))?1:0)); asm volatile("s_barrier" ::: "memory"); }
      auto rw=[&](int k,int n){ if constexpr(s<9){ const int i=((s+1)>>1)&1; const u32 so=((s+1)&1)*STG; u2 lo=ldstr(aw[i][k][n]+so), hi=ldstr(aw[i][k][n]+so+512); nxt.w[k][n]=u4{lo[0],lo[1],hi[0],hi[1]}; } };
      auto rx=[&](int k){ if constexpr(s<9){ const int i=((s+1)>>1)&1; const u32 so=((s+1)&1)*STG; nxt.x[k]=ldsr128(ax[i][k]+so); } };
      #pragma unroll
      for(int k=0;k<4;k++){
        acc[0]=mfma32b(cur.w[k][0],cur.x[k],acc[0]); SCHED();
        if constexpr(s+3<=9) issue1(std::integral_constant<int,s+3>{},k);
        rw(k,0); rx(k);
        SCHED();
        acc[1]=mfma32b(cur.w[k][1],cur.x[k],acc[1]); SCHED();
        rw(k,1);
        if(k==0){ cacc[0]=mfma16(cur.w[0][0],sel,cacc[0]); cacc[1]=mfma16(cur.w[0][1],sel,cacc[1]); }
        if constexpr(s==TS1 && TS1<6){ if(k==3){ if(g1) warm(mi,0); else { setm0<JNK>(smb); touch(srdS,0u,OFF_EP); } } }
        if constexpr(s==6){ if(k==3){ gload<0>(G,r*16,p.ws+go); if(g1) warm(mi,TS1<6?1:2); else { setm0<JNK>(smb); touch(srdS,0u,go); } } }
        if constexpr(s==8){ if(k==3) signal(smb+C_DONE,lane); }
        if constexpr(s==9){ if(k==1){
          if((lane&14)==0){ *(LAS f4*)(cp+mi*256+(lane&1)*64+(lane>>4)*16)=cacc[0]; *(LAS f4*)(cp+mi*256+128+(lane&1)*64+(lane>>4)*16)=cacc[1]; }
          signal(smb+C_CS+g1*4,lane); } }
        SCHED();
      }
    });
    RST(0);
    if(!g1) __builtin_amdgcn_s_setprio(2);
    asm volatile("s_waitcnt vmcnt(1)" : "+v"(G) :: "memory");
    RST(1);
    ldswait(smb+C_CS+g1*4,4u);
    const float cs0=*(LAS float*)(cp+h*512+r*4)+*(LAS float*)(cp+h*512+256+r*4), cs1=*(LAS float*)(cp+h*512+128+r*4)+*(LAS float*)(cp+h*512+384+r*4);
    RST(2);
    #pragma unroll 1
    for(int sp=0;sp<SPIN && !__all(G[1]==tag&&G[3]==tag);sp++)asm volatile("global_load_dwordx4 %0, %1, %2 sc1\n\ts_waitcnt vmcnt(0)" : "=&v"(G) : "v"(r*16), "s"(p.ws+go) : "memory");
    RST(3);
    const float nm=-__uint_as_float(G[0]), ri1=__uint_as_float(G[2]);
    acc[0]=__builtin_amdgcn_mfma_f32_32x32x2f32(cs0,nm,acc[0],0,0,0); acc[1]=__builtin_amdgcn_mfma_f32_32x32x2f32(cs1,nm,acc[1],0,0,0);
    if(g1){
      u4 zq[4][4];
      #pragma unroll
      for(int e=0;e<16;e++) zq[3][e>>2][e&3]=pkbf2(f2{acc[e>>3][2*(e&7)],acc[e>>3][2*(e&7)+1]}*ri1);
      if(w<7){ ldswait(smb+C_DONE,8u);
        #pragma unroll
        for(int i=0;i<4;i++) ldsw128(cb+UOF+i*1024+lane*16,zq[3][i]);
        signal(smb+C_ZQ,lane); RST(4); }
      if(w==7){
        ldswait(smb+C_ZQ,3u);
        #pragma unroll
        for(int c=0;c<3;c++)
          #pragma unroll
          for(int i=0;i<4;i++) zq[c][i]=ldsr128(smb+c*CVS+UOF+i*1024+lane*16);
        WAITL0(); signal(smb+C_FULL,lane);
        #pragma unroll 1
        for(int c=0;c<4;c++){ u4 o[4];
          #pragma unroll
          for(int e=0;e<16;e++) o[e>>2][e&3]=pkbf2(gelu2(unpk(zq[0][e>>2][e&3])));
          #pragma unroll
          for(int i=0;i<4;i++){ zq[0][i]=zq[1][i]; zq[1][i]=zq[2][i]; zq[2][i]=zq[3][i]; zq[3][i]=o[i]; } }
        RST(4);
        #pragma unroll
        for(int c=0;c<4;c++)
          #pragma unroll
          for(int n=0;n<2;n++) if(2*j+n<27){ const u32 oo=OFF_Z1T+((rt*8+2*c+(r>>4))*27+2*j+n)*1024+(h*16+(r&15))*16;
            st16(rs,oo,u4{zq[c][2*n][0],zq[c][2*n][1],zq[c][2*n+1][0],zq[c][2*n+1][1]}); st16(rs,oo+512,u4{zq[c][2*n][2],zq[c][2*n][3],zq[c][2*n+1][2],zq[c][2*n+1][3]}); }
        RST(5);
      } else {
        u4 uu[4]; float kp[11]; u32 fxv=0;
        auto mid=[&](){ RST(5); fxv=ld4(rs,OFF_FX+r*128); asm volatile("" ::: "memory"); };
        if(hasc){ ksc=kgen_tab(cb,lane,pr,kp,mid);
          RST(6);
          if(!__all(fxv==tag)) pollw<16>(rs,OFF_FX+r*128,tag);
          RST(7);
          #pragma unroll
          for(int i=0;i<4;i++) uu[i]=ld16(rs,OFF_XNT+((lane>>3)+8*i)*81920+dch*128+(lane&7)*16);
          asm volatile("" ::: "memory");
          kgen_exp(cb,lane,kp);
          RST(8);
          ldswait(smb+C_FULL,1u);
          #pragma unroll
          for(int i=0;i<4;i++){ const int x=lane+64*i; ldsw128(cb+UOF+(x&3)*1040+((x>>2)+1)*16,uu[i]); }
          if(lane<4) ldsw128(cb+UOF+lane*1040,u4{0,0,0,0});
          WAITL0(); signal(smb+C_KP+mi*4,lane);
        }
        RST(9);
      }
    } else {
      u32 zp[2][8];
      #pragma unroll
      for(int e=0;e<16;e++) zp[e>>3][e&7]=pkbf2(gelu2(f2{acc[e>>3][2*(e&7)],acc[e>>3][2*(e&7)+1]}*ri1));
      float S=0.f,Q=0.f;
      if(j<13){
        #pragma unroll
        for(int e=0;e<8;e++){ S=dot2(zp[0][e],0x3f803f80u,S); Q=dot2(zp[0][e],zp[0][e],Q); } }
      #pragma unroll
      for(int e=0;e<8;e++){ S=dot2(zp[1][e],0x3f803f80u,S); Q=dot2(zp[1][e],zp[1][e],Q); }
      #define XSUM32(v) do{ auto t_=__builtin_amdgcn_permlane32_swap(__float_as_uint(v),__float_as_uint(v),false,false); v=__uint_as_float(t_[0])+__uint_as_float(t_[1]); }while(0)
      XSUM32(S); XSUM32(Q);
      const u32 sto=OFF_ST+rt*(14*2048)+(32*mi+r)*16;
      if(h==0) st16(rs,sto+j*2048,u4{__float_as_uint(S),tag,__float_as_uint(Q),tag});
      RST(6);
      u4 g[7];
      __builtin_amdgcn_s_sleep(PDLY);
      #pragma unroll 1
      for(int sp=0;sp<SPIN;sp++){ bool ok=true; asm volatile("" ::: "memory");
        #pragma unroll
        for(int i=0;i<7;i++) g[i]=ld16(rs,sto+(h*7+i)*2048);
        #pragma unroll
        for(int i=0;i<7;i++) ok=ok&&g[i][1]==tag&&g[i][3]==tag;
        if(__all(ok)) break;
        __builtin_amdgcn_s_sleep(4); }
      RST(7);
      S=0.f; Q=0.f;
      #pragma unroll
      for(int i=0;i<7;i++){ S+=__uint_as_float(g[i][0]); Q+=__uint_as_float(g[i][2]); }
      XSUM32(S); XSUM32(Q);
      float mean=S*(1.f/864.f), m2,r2; ln_fin(mean,Q*(1.f/864.f)-mean*mean,m2,r2); m2*=-r2;
      ldswait(smb+C_DONE,8u);
      const u32 tb=smb+RB+mi*RING, ad=tb+(8*(lane>>4)+((lane&15)>>2))*80+(lane&3)*8;
      const u32 zo=OFF_Z2F+((rt*4+mi)*54+(j==13?-2:2+4*j))*1024+lane*16;
      #pragma unroll
      for(int n=0;n<2;n++){
        #pragma unroll
        for(int g2=0;g2<4;g2++){ u2 o;
          #pragma unroll
          for(int e=0;e<2;e++) o[e]=pkbf2(fma2(unpk(zp[n][2*g2+e]),f2{r2,r2},f2{m2,m2}));
          *(LAS u2*)(tb+r*80+g2*16+h*8)=o; }
        u2 l0=ldstr(ad), h0=ldstr(ad+320), l1=ldstr(ad+32), h1=ldstr(ad+32+320);
        if(j<13||n){ st16(rs,zo+n*2048,u4{l0[0],l0[1],h0[0],h0[1]}); st16(rs,zo+n*2048+1024,u4{l1[0],l1[1],h1[0],h1[1]}); } }
      RST(8);
      WAITV(0);
      if(arrive(smb+C_Z2,lane)==3u && lane==0) st4(rs,OFF_F2+(rt*16+j)*128,tag);
      RST(9);
    }
  }
  const int ft0=ct<7?7*ct:47;
  bool adone=true; const u32 fao=OFF_FA+((lane<28?lane:0)*8+ct)*128;
  auto aload=[&](){ const u32 vo=(lane>>3)*4096+(lane&7)*16;
    #pragma unroll 1
    for(int i=0;i<10;i++){ setm0<ATL>(smb+i*1024); dma<0>(srdS,vo,OFF_AT+(u32)(80*ct+8*i)*4096+rtB*128); } };
  if(w>=4){
    if(hasc){
      __builtin_amdgcn_s_setprio(CPRIO);
      conv_wave(p,rs,dch,cb,smb+C_KP+mi*4,lane,ksc,dsk);
      __builtin_amdgcn_s_setprio(0);
      if(arrive(smb+C_CV,lane)==(idx<24?2u:1u) && lane==0) st4(rs,OFF_FA+b*128,tag);
    }
    if(w==7){
      __builtin_amdgcn_s_setprio(HPRIO);
      if(idx<28){
        #pragma unroll 1
        for(int c=0;c<(idx<24?3:2);c++){ ldswait(smb+C_KP+c*4,1u); conv_help(smb+c*CVS,lane); signal(smb+C_KP+c*4,lane);
          if(c==0){ WAITV(0); if(lane==0) st4(rs,OFF_F1+(rt*16+j)*128,tag); } } }
      __builtin_amdgcn_s_setprio(0);
      RST(6);
    }
    RST(10);
    if(w<6){ ldswait(smb+C_Z1R,1u);
      #pragma unroll 1
      for(u32 i=0;i<20480;i+=4096){ setm0<0>(cb+i); const u32 o=OFF_WOF+ct*(135*1024)+mi*20480+i; dma<0>(srdS,lane*16,o); dma<1024>(srdS,lane*16,o); dma<2048>(srdS,lane*16,o); dma<3072>(srdS,lane*16,o); } }
    if(w==6){ ldswait(smb+C_Z1R,1u);
      #pragma unroll 1
      for(;;){ adone=__all(ld4(rs,fao)==tag); asm volatile("" ::: "memory"); if(adone||ldsv(smb+C_FREE)) break; __builtin_amdgcn_s_sleep(24); }
      if(adone) aload(); }
    if(w==7 && b==255){
      #pragma unroll 1
      for(int sp=0;sp<SPIN;sp++){ const u32 o=OFF_FA+lane*128; u32 v0=ld4(rs,o),v1=ld4(rs,o+8192),v2=ld4(rs,o+16384),v3=ld4(rs,lane<32? o+24576 : OFF_FW+(lane-32)*128); asm volatile("" ::: "memory");
        if(__all(v0==tag&&v1==tag&&v2==tag&&v3==tag)) break; __builtin_amdgcn_s_sleep(4); }
      if(lane==0) st4(rs,OFF_EP,tag); }
    WAITV(0);
  }
  int w2=w; asm volatile("" : "+s"(w2));
  if(w2<4){
    __builtin_amdgcn_s_setprio(G2PRIO);
    int r=lane&15, q=lane>>4;
    const u32 rb=smb+RB+w*RING;
    u4 srdA=mksrd((const char*)p.wsp+(size_t)rtB*64*4096+w*1024-4096);
    const char* zb=p.ws+OFF_Z2F+(u32)(16*w*54+ft0+3)*1024;
    u32 voA[2]; { u32 lr=lane>>3,c=lane&7; voA[0]=lr*4096+((c^(lr>>1))<<4); voA[1]=voA[0]^64u; }
    u32 sofa[8];
    #pragma unroll
    for(int g=0;g<8;g++) sofa[g]=((g+2*w)&7)*32768+4096-(g&3)*1024;
    const u32 voB=lane*16;
    u32 la[2]; la[0]=rb+r*128+((q^(r>>1))<<4); la[1]=la[0]^64u;
    f4 acc[4][7]={}; u4 B[3][7]; u4 A[2][4]; u2 z1[7];
    auto issueA=[&](auto sc,auto gc){ constexpr int s=sc.value,g=gc.value; if constexpr((g&3)==0) setm0<((s+1)%3)*8192+g*1024>(rb); dma<(g&3)*1024>(srdA,voA[g&1],sofa[g]);
      if constexpr(g==7){ u64 a=((u64)srdA[1]<<32|srdA[0])+128; srdA[0]=(u32)a; srdA[1]=(u32)(a>>32); } };

    u32 bsr,fv;
    auto fof=[&](){ const u32 l=voB>>4; return l<27? OFF_FW+l*128 : OFF_F1+((rtB>>1)*16+(((ft0>>1)+(l&3))>>1))*128; };
    sfor<3>([&](auto s){ sfor<8>([&](auto g){ issueA(s,g); }); });
    #pragma unroll
    for(int m=0;m<4;m++)
      #pragma unroll
      for(int n=0;n<7;n++) asm volatile("" : "+v"(acc[m][n]));
    { const int hf=(ft0>>1)+q; pollw(rs,OFF_F2+((4*w+(lane&3))*16+(hf==0?13:(hf-1)>>1))*128,tag); }
    RST(10);
    sfor<3>([&](auto hh){ const char* s=zb+(u32)hh.value*(54*1024); sfor<7>([&](auto n){ gload<(n.value-3)*1024>(B[hh][n],voB,s); }); });
    #pragma unroll
    for(int m=0;m<4;m++) A[0][m]=ldsr128(la[0]+m*2048+8192);
    const char* z1b=p.ws+OFF_Z1T+(u32)((rtB*4+w)*27+(ft0>>1))*1024;
    sfor<16>([&](auto hc){
      constexpr int h=hc.value, S=h%3, P=h&1;
      constexpr int cnt = h<2? 14 : ((h+1<16?7:0)+(h+2<16?7:0) + ((((h-2)&1)&&((h-2)/2+3<8))?8:0) + ((((h-1)&1)&&((h-1)/2+3<8))?8:0) + (h>=14?8:0) + ((h==11||h==12)?1:0));
      waitB<cnt>(B[S]); waitA(A[P]);
      if constexpr(h==13){ tie1(fv); if(!__all(fv==tag)) pollw(rs,fof(),tag); signal(smb+C_Z1R,lane); }
      if constexpr(h+1<16){
        #pragma unroll
        for(int m=0;m<4;m++) A[P^1][m]=ldsr128(la[(h+1)&1]+m*2048+((((h+1)>>1)+1)%3)*8192);
      }
      if constexpr(h==10) gload4<0>(fv,fof(),p.ws);
      const char* s=zb+(u32)(h+3)*(54*1024);
      sfor<7>([&](auto n){
        mfma4(acc[0][n],acc[1][n],acc[2][n],acc[3][n],B[S][n],A[P]);
        if constexpr((h&1)&&(h/2+3<8)){ issueA(std::integral_constant<int,h/2+3>{},n); if constexpr(n.value==6) issueA(std::integral_constant<int,h/2+3>{},std::integral_constant<int,7>{}); }
        if constexpr(h+3<16) gload_t<(n.value-3)*1024>(B[S][n],voB,s);
        if constexpr(h==13){ gload8<(n.value>>1)*1024>(z1[n],voB,z1b+((n.value&1)? ((ft0&1)?1024:8) : (ft0&1)*8));
          if constexpr(n.value==6) gload2(bsr,(voB>>3)&30u,(const char*)(p.bsp+(rtB*4+w)*16)); }
      });
    });
    asm volatile("s_nop 15\n\ts_nop 3" ::: "memory");
    SCHED();
    STAMP(10,0);
    lane=__builtin_amdgcn_mbcnt_hi(~0u,__builtin_amdgcn_mbcnt_lo(~0u,0u)); asm volatile("" : "+v"(lane)); r=lane&15; q=lane>>4;
    #pragma unroll
    for(int m=1;m<4;m++)
      #pragma unroll
      for(int n=0;n<7;n++) *(LAS f4*)(rb+((m-1)*7+n)*1024+lane*16)=acc[m][n];
    WAITL0();
    signal(smb+C_RED,lane);
    ldswait(smb+C_RED,4u);
    #pragma unroll
    for(int m=1;m<4;m++){ u32 src=smb+RB+((w-m)&3)*RING+(m-1)*7168+lane*16;
      #pragma unroll
      for(int n=0;n<7;n++) acc[0][n]+=*(LAS f4*)(src+n*1024); }
    asm volatile("s_waitcnt vmcnt(0) lgkmcnt(0)" : "+v"(z1[0]),"+v"(z1[1]),"+v"(z1[2]),"+v"(z1[3]),"+v"(z1[4]),"+v"(z1[5]),"+v"(z1[6]),"+v"(bsr) :: "memory");
    const float bs=bflo(bsr);
    u2 o[7];
    #pragma unroll
    for(int n=0;n<7;n++)
      #pragma unroll
      for(int e=0;e<2;e++)o[n][e]=pkbf2(unpk(z1[n][e])*(f2{acc[0][n][2*e],acc[0][n][2*e+1]}+bs));
    const u32 ho=OFF_HF+(u32)((rtB*4+w)*54+ft0)*512+(q>>1)*256+r*16;
    #pragma unroll
    for(int n=0;n<6;n+=2){ auto sx=__builtin_amdgcn_permlane16_swap(o[n][0],o[n+1][0],false,false); auto sy=__builtin_amdgcn_permlane16_swap(o[n][1],o[n+1][1],false,false);
      if(ct<7||n>=2) st16(rs,ho+(n+(q&1))*512,u4{sx[0],sy[0],sx[1],sy[1]}); }
    st8(rs,ho+6*512+(q&1)*8,o[6]);
    STAMP(11,0);
    WAITV(0);
    if(arrive(smb+C_H,lane)==3u && lane==0) st4(rs,OFF_FH+(rtB*8+ct)*128,tag);
    STAMP(12,0);
    ldswait(smb+C_H,4u);
    #pragma unroll 1
    for(u32 i=0;i<RING;i+=4096){ setm0<0>(rb+i); const u32 o=OFF_WOF+ct*(135*1024)+40960+w*RING+i; dma<0>(srdS,lane*16,o); dma<1024>(srdS,lane*16,o); dma<2048>(srdS,lane*16,o); dma<3072>(srdS,lane*16,o); }
    __builtin_amdgcn_s_setprio(0);
  }
  { const int r=lane&15, q=lane>>4, m=w&3, kh=w>>2;
    const int tid2=w*64+lane, r0=(tid2*205)>>11, c0=tid2-r0*10, r1=((tid2+512)*205)>>11, c1=tid2+512-r1*10;
    const u32 o0=(rtB*64+r0)*1280+ct*160+c0*16, o1=(rtB*64+(w<2?r1:0))*1280+ct*160+(w<2?c1:0)*16;
    const u4 xv0=*(const u4*)((const char*)p.x+o0), xv1=*(const u4*)((const char*)p.x+o1);
    if(w==0){ pollw(rs,OFF_FH+(rtB*8+(lane&7))*128,tag); signal(smb+C_FREE,lane); }
    WAITV(0);
    STAMP(16,0);
    bar();
    STAMP(17,0);
    u4 hf[14];
    #pragma unroll
    for(int k=0;k<14;k++) hf[k]=ld16(rs,OFF_HF+(u32)((rtB*4+m)*27+kh*13+k)*1024+lane*16);
    f4 acc[5]={}; u4 W[2][5];
    const u32 wl2=smb+lane*16+RB-40960+kh*13*5120, wl0=kh? wl2 : smb+lane*16, wl1=kh? wl2 : smb+lane*16+CVS-20480;
    #define WAD(k,n) (((k)<4? wl0 : (k)<8? wl1 : wl2)+((k)*5+(n))*1024)
    #pragma unroll
    for(int n=0;n<5;n++) W[0][n]=ldsr128(WAD(0,n));
    if(kh) hf[0]=u4{0,0,0,0};
    #pragma unroll
    for(int k=0;k<14;k++){
      if(k<13){
        #pragma unroll
        for(int n=0;n<5;n++) W[(k+1)&1][n]=ldsr128(WAD(k+1,n));
      }
      SCHED();
      #pragma unroll
      for(int n=0;n<5;n++) acc[n]=mfma16(W[k&1][n],hf[k],acc[n]);
      SCHED();
    }
    STAMP(18,0);
    if(!adone){ pollw(rs,fao,tag); aload(); WAITV(0); }
    bar();
    { const u32 pb=smb+RB+kh*(64*336)+(16*m+r)*336+q*16;
      #pragma unroll
      for(int n=0;n<5;n++){ f4 v=acc[n];
        if(kh){ u2 av=ldstr(smb+ATL+(16*n+4*q+(r>>2))*128+(16*m+4*(r&3))*2); v+=f4{bflo(av[0]),bfhi(av[0]),bflo(av[1]),bfhi(av[1])}; }
        *(LAS f4*)(pb+n*64)=v; } }
    bar();
    auto fin=[&](int rr,int cc,u4 xv,u32 oo){ const u32 ad=smb+RB+rr*336+cc*32; const f4 lo=*(LAS f4*)ad+*(LAS f4*)(ad+64*336), hi=*(LAS f4*)(ad+16)+*(LAS f4*)(ad+16+64*336);
      *(u4*)((char*)p.out+oo)=u4{pkbf2(unpk(xv[0])+f2{lo[0],lo[1]}),pkbf2(unpk(xv[1])+f2{lo[2],lo[3]}),pkbf2(unpk(xv[2])+f2{hi[0],hi[1]}),pkbf2(unpk(xv[3])+f2{hi[2],hi[3]})}; };
    fin(r0,c0,xv0,o0);
    if(w<2) fin(r1,c1,xv1,o1);
    STAMP(19,0);
  }
}
extern "C" void kernel_launch(void* const* d_in,const int* in_sizes,int n_in,void* d_out,int out_size,void* d_ws,size_t ws_size,hipStream_t stream){
  Params p;
  p.x=(const u16*)d_in[0]; p.lar=(const u16*)d_in[1]; p.aim=(const u16*)d_in[2]; p.cre=(const u16*)d_in[3]; p.cim=(const u16*)d_in[4];
  p.ldt=(const u16*)d_in[5]; p.dsk=(const u16*)d_in[6]; p.win=(const u16*)d_in[8]; p.wsp=(const u16*)d_in[9]; p.bsp=(const u16*)d_in[10]; p.wout=(const u16*)d_in[11];
  p.out=(u16*)d_out; p.ws=(char*)d_ws;
  (void)hipFuncSetAttribute((const void*)fused,hipFuncAttributeMaxDynamicSharedMemorySize,LDS_TOTAL);
  fused<<<256,512,LDS_TOTAL,stream>>>(p);
}
```

```cpp
#include <hip/hip_runtime.h>
#include <utility>
#pragma clang diagnostic ignored "-Wint-to-pointer-cast"
typedef unsigned short u16;
typedef unsigned int u32;
typedef unsigned long long u64;
typedef __attribute__((ext_vector_type(8))) __bf16 bf8;
typedef __attribute__((ext_vector_type(8))) _Float16 hf8;
typedef __attribute__((ext_vector_type(2))) __bf16 bf2;
typedef __attribute__((ext_vector_type(2))) _Float16 hf2;
typedef __attribute__((ext_vector_type(2))) float f2;
typedef __attribute__((ext_vector_type(4))) float f4;
typedef __attribute__((ext_vector_type(16))) float f16v;
typedef __attribute__((ext_vector_type(4))) u32 u4;
typedef __attribute__((ext_vector_type(2))) u32 u2;
typedef __attribute__((ext_vector_type(4))) short s4;
#define LAS __attribute__((address_space(3)))
#define GAS __attribute__((address_space(1)))
#define DI __device__ __forceinline__

constexpr u32 OFF_Z2F = 0;
constexpr u32 OFF_Z1T = OFF_Z2F + 64*54*1024;
constexpr u32 OFF_XNT = OFF_Z1T + 128*27*1024;
constexpr u32 OFF_WOF = OFF_XNT + 640*4096;
constexpr u32 OFF_HF  = OFF_WOF + 8*27*5*1024;
constexpr u32 OFF_AT  = OFF_HF + 128*27*1024;
constexpr u32 OFF_ST  = OFF_AT + 640*4096;
constexpr u32 OFF_RS  = OFF_ST + 16*14*2048+8192;
constexpr u32 OFF_EP  = OFF_RS + 2048*16;
constexpr u32 OFF_F2  = OFF_EP + 256;
constexpr u32 OFF_F1  = OFF_F2 + 256*128;
constexpr u32 OFF_FX  = OFF_F1 + 256*128;
constexpr u32 OFF_FW  = OFF_FX + 256*128;
constexpr u32 OFF_FH  = OFF_FW + 32*128;
constexpr u32 OFF_FA  = OFF_FH + 256*128;
constexpr u32 WS_END  = OFF_FA + 256*128;

struct Params {
  const u16 *x, *lar, *aim, *cre, *cim, *ldt, *dsk, *win, *wsp, *bsp, *wout;
  u16* out; char* ws;
};
#define STAMP(id,wv) do{}while(0)
#define RST(i) do{}while(0)
template<class F,int... I> DI void sfor_impl(F&& f, std::integer_sequence<int,I...>){ (f(std::integral_constant<int,I>{}),...); }
template<int N,class F> DI void sfor(F&& f){ sfor_impl(f, std::make_integer_sequence<int,N>{}); }

DI float bflo(u32 v){ return __uint_as_float(v<<16); }
DI float bfhi(u32 v){ return __uint_as_float(v&0xffff0000u); }
DI float bf1(u16 v){ return __uint_as_float(((u32)v)<<16); }
DI u32 pkbf(float a,float b){ f2 v={a,b}; return __builtin_bit_cast(u32,__builtin_convertvector(v,bf2)); }
DI u32 pkhf(float a,float b){ f2 v={a,b}; return __builtin_bit_cast(u32,__builtin_convertvector(v,hf2)); }
DI float rbf(float a){ return bflo(pkbf(a,a)); }
DI float hlo(u32 v){ return (float)__builtin_bit_cast(hf2,v)[0]; }
DI float hhi(u32 v){ return (float)__builtin_bit_cast(hf2,v)[1]; }
DI float dot2(u32 a,u32 b,float c){ return __builtin_amdgcn_fdot2_f32_bf16(__builtin_bit_cast(bf2,a),__builtin_bit_cast(bf2,b),c,false); }
DI f4 mfma16(u4 a,u4 b,f4 c){ return __builtin_amdgcn_mfma_f32_16x16x32_bf16(__builtin_bit_cast(bf8,a),__builtin_bit_cast(bf8,b),c,0,0,0); }
DI f16v mfma32h(u4 a,u4 b,f16v c){ return __builtin_amdgcn_mfma_f32_32x32x16_f16(__builtin_bit_cast(hf8,a),__builtin_bit_cast(hf8,b),c,0,0,0); }
DI u4 ldsr128(u32 a){ return *(LAS u4*)a; }
DI u2 ldsr64(u32 a){ return *(LAS u2*)a; }
DI void ldsw128(u32 a,u4 v){ *(LAS u4*)a=v; }
DI u2 ldstr(u32 a){ return __builtin_bit_cast(u2,__builtin_amdgcn_ds_read_tr16_b64_v4i16((LAS s4*)a)); }
DI u32 uni(u32 v){ return __builtin_amdgcn_readfirstlane(v); }

template<int OFF> DI void gload(u4& dst,u32 voff,const char* base){ asm volatile("global_load_dwordx4 %0, %1, %2 offset:%3 sc1" : "=v"(dst) : "v"(voff), "s"(base), "n"(OFF)); }
template<int OFF> DI void gload_t(u4& dst,u32 voff,const char* base){ asm volatile("global_load_dwordx4 %0, %1, %2 offset:%3 sc1" : "+v"(dst) : "v"(voff), "s"(base), "n"(OFF)); }
DI void mfma4(f4& c0,f4& c1,f4& c2,f4& c3,const u4& a,const u4 (&b)[4]){
  asm volatile("v_mfma_f32_16x16x32_bf16 %0, %4, %5, %0\n\tv_mfma_f32_16x16x32_bf16 %1, %4, %6, %1\n\tv_mfma_f32_16x16x32_bf16 %2, %4, %7, %2\n\tv_mfma_f32_16x16x32_bf16 %3, %4, %8, %3"
    : "+v"(c0),"+v"(c1),"+v"(c2),"+v"(c3) : "v"(a),"v"(b[0]),"v"(b[1]),"v"(b[2]),"v"(b[3])); }
template<int CNT> DI void waitB(u4 (&B)[7]){ asm volatile("s_waitcnt vmcnt(%7)" : "+v"(B[0]),"+v"(B[1]),"+v"(B[2]),"+v"(B[3]),"+v"(B[4]),"+v"(B[5]),"+v"(B[6]) : "n"(CNT) : "memory"); }
DI void waitA(u4 (&A)[4]){ asm volatile("s_waitcnt lgkmcnt(0)" : "+v"(A[0]),"+v"(A[1]),"+v"(A[2]),"+v"(A[3]) :: "memory"); }
#define WAITV(n) asm volatile("s_waitcnt vmcnt(%0)" :: "n"(n) : "memory")
#define WAITL0() asm volatile("s_waitcnt lgkmcnt(0)" ::: "memory")
#define SCHED() __builtin_amdgcn_sched_barrier(0)
DI void bar(){ asm volatile("s_waitcnt lgkmcnt(0)\n\ts_barrier" ::: "memory"); }
DI f2 unpk(u32 v){ return f2{bflo(v),bfhi(v)}; }
DI u32 pkbf2(f2 v){ return __builtin_bit_cast(u32,__builtin_convertvector(v,bf2)); }
DI u32 pkhf2(f2 v){ return __builtin_bit_cast(u32,__builtin_convertvector(v,hf2)); }
DI f2 fma2(f2 a,f2 b,f2 c){ return __builtin_elementwise_fma(a,b,c); }
DI f2 gelu2(f2 x){
  f2 u=x*fma2(x*x,f2{-0.10294324f,-0.10294324f},f2{-2.30220820f,-2.30220820f});
  f2 d=f2{__builtin_amdgcn_exp2f(u[0]),__builtin_amdgcn_exp2f(u[1])}+f2{1.f,1.f};
  return x*f2{__builtin_amdgcn_rcpf(d[0]),__builtin_amdgcn_rcpf(d[1])};
}
DI void ln_fin(float mean,float var,float& m,float& rinv){
  m=rbf(mean); float v=rbf(__builtin_fmaxf(var,0.f));
  float s=rbf(__builtin_amdgcn_sqrtf(rbf(v+1e-5f))); float r=__builtin_amdgcn_rcpf(s); rinv=__builtin_fmaf(__builtin_fmaf(-s,r,1.f),r,r);
}
typedef __amdgpu_buffer_rsrc_t rsrc_t;
DI void st16(rsrc_t rs,u32 off,u4 v){ __builtin_amdgcn_raw_buffer_store_b128(v,rs,off,0,16); }
DI void st8(rsrc_t rs,u32 off,u2 v){ __builtin_amdgcn_raw_buffer_store_b64(v,rs,off,0,16); }
DI void st4(rsrc_t rs,u32 off,u32 v){ __builtin_amdgcn_raw_buffer_store_b32(v,rs,off,0,16); }
DI u4 ld16(rsrc_t rs,u32 off){ return __builtin_amdgcn_raw_buffer_load_b128(rs,off,0,16); }
DI u2 ld8(rsrc_t rs,u32 off){ return __builtin_amdgcn_raw_buffer_load_b64(rs,off,0,16); }
DI u32 ld4(rsrc_t rs,u32 off){ return __builtin_amdgcn_raw_buffer_load_b32(rs,off,0,16); }
constexpr int SPIN=1<<17;
template<int SL=2> DI void pollw(rsrc_t rs,u32 off,u32 tag){
  #pragma unroll 1
  for(int s=0;s<SPIN;s++){ u32 v=ld4(rs,off); asm volatile("" ::: "memory"); if(__all(v==tag)) break; __builtin_amdgcn_s_sleep(SL); }
}

DI f16v mfma32b(u4 a,u4 b,f16v c){ return __builtin_amdgcn_mfma_f32_32x32x16_bf16(__builtin_bit_cast(bf8,a),__builtin_bit_cast(bf8,b),c,0,0,0); }
DI u4 mksrd(const void* b){ u64 a=(u64)b; return u4{(u32)a,(u32)(a>>32)&0xffffu,0xffffffffu,0x00020000u}; }
template<int IMM> DI void setm0(u32 base){ asm volatile("s_add_u32 m0, %0, %1\n\ts_nop 0" :: "s"(base), "n"(IMM) : "scc","memory"); }
template<int OFF> DI void dma(u4 srd,u32 voff,u32 soff){ asm volatile("buffer_load_dwordx4 %0, %1, %2 offen offset:%3 sc1 lds" :: "v"(voff), "s"(srd), "s"(soff), "n"(OFF) : "memory"); }
DI void touch(u4 srd,u32 voff,u32 soff){ asm volatile("buffer_load_dword %0, %1, %2 offen sc1 lds" :: "v"(voff), "s"(srd), "s"(soff) : "memory"); }
template<int OFF> DI void gload8(u2& dst,u32 voff,const char* base){ asm volatile("global_load_dwordx2 %0, %1, %2 offset:%3 sc1" : "=v"(dst) : "v"(voff), "s"(base), "n"(OFF)); }
DI void gload2(u32& dst,u32 voff,const char* base){ asm volatile("global_load_ushort %0, %1, %2" : "=v"(dst) : "v"(voff), "s"(base)); }
DI u32 arrive(u32 a,int){ u32 o; asm volatile("s_mov_b64 exec, 1\n\tds_add_rtn_u32 %0, %1, %2\n\ts_mov_b64 exec, -1\n\ts_waitcnt lgkmcnt(0)" : "=&v"(o) : "v"(a), "v"(1u) : "memory"); return uni(o); }
DI void ldswait(u32 a,u32 n){ while(__hip_atomic_load((LAS u32*)a,__ATOMIC_RELAXED,__HIP_MEMORY_SCOPE_WORKGROUP)<n) __builtin_amdgcn_s_sleep(1); asm volatile("" ::: "memory"); }
DI float addf(float a,float b){ float o; asm("v_add_f32 %0, %1, %2" : "=v"(o) : "v"(a), "v"(b)); return o; }
DI void tie1(u32& g){ asm volatile("" : "+v"(g) :: "memory"); }
DI u32 opq(u32 v){ asm volatile("" : "+v"(v)); return v; }

constexpr u32 STG=32768;
constexpr u32 RING=24576, RB=62848, CONV=0, KP=4192, CVS=4*KP+4160, UOF=4*KP, MISC=RB+4*RING, JNK=MISC+2048, PM=163840-128, LDS_TOTAL=163840;
constexpr u32 ATL=2*CVS;
constexpr u32 C_FULL=PM, C_G1=PM+4, C_Z2=PM+8, C_Z1=PM+12, C_RED=PM+16, C_H=PM+20, C_FREE=PM+24, C_CV=PM+28, C_Z1R=PM+32, C_TAG=PM+40, C_KP=PM+48, C_DONE=PM+64, C_ZQ=PM+68, C_CS=PM+72, C_K2=PM+80, C_P=PM+96, C_AT=PM+112, C_WR=PM+116;
constexpr u32 CODE_BYTES=36864;
#define LA 3
#define TC_S 7
#define G2PRIO 2
#define XMAX 9
#define GS_K 35
#define WNT ""
#define TS1 6
#define HPRIO 3
#define CPRIO 3
#define POLLM 1
#define HPF 5
#define CVSKIP 0
#define BROT 0
#define PDLY 8
#define RS_S 7
#define KROT ((j+3*(rt&3))%10)
DI void signal(u32 a,int){ asm volatile("s_mov_b64 exec, 1\n\tds_add_u32 %0, %1\n\ts_mov_b64 exec, -1" :: "v"(a), "v"(1u) : "memory"); }
template<int OFF> DI void gload4(u32& dst,u32 voff,const char* base){ asm volatile("global_load_dword %0, %1, %2 offset:%3 sc1" : "=v"(dst) : "v"(voff), "s"(base), "n"(OFF)); }
template<int CNT> DI void waitG(u4& g){ asm volatile("s_waitcnt vmcnt(%1)" : "+v"(g) : "n"(CNT) : "memory"); }

DI f2 cmulp(f2 a,f2 b,f2 bs){ return fma2(f2{a[1],a[1]},bs,f2{a[0],a[0]}*b); }
DI float fmaf1(float a,float b,float c){ float o; asm("v_fma_f32 %0, %1, %2, %3" : "=v"(o) : "v"(a),"v"(b),"v"(c)); return o; }
DI u32 ldsv(u32 a){ return __hip_atomic_load((LAS u32*)a,__ATOMIC_RELAXED,__HIP_MEMORY_SCOPE_WORKGROUP); }
DI void ldswait4(u32 a,int lane,u32 n){ while(__any(ldsv(a+(lane&3)*4)<n)) __builtin_amdgcn_s_sleep(1); asm volatile("" ::: "memory"); }
DI u32 gettag(u32 a){ ldswait(a+4,1u); return uni(ldsv(a)); }
DI f2 cpow_dir(float lr,float rv,float pw){ float e=__builtin_amdgcn_exp2f(lr*pw); float f=__builtin_amdgcn_fractf(rv*pw);
  return f2{e*__builtin_amdgcn_cosf(f),e*__builtin_amdgcn_sinf(f)}; }
DI u32 dup(u32 a){ return __builtin_amdgcn_perm(a,a,0x01000100u); }
DI u32 cat(u32 lo,u32 hi){ return __builtin_amdgcn_perm(hi,lo,0x05040100u); }
DI u32 lds16(u32 a){ return *(LAS u16*)a; }

template<class CB> DI float kgen_tab(u32 cb,int lane,const u32 (&pr)[6],float (&kp)[11],CB&& mid){
  const float dt=rbf(__expf(bflo(pr[4])));
  const float cr[2]={bflo(pr[2]),bfhi(pr[2])}, cm[2]={bflo(pr[3]),bfhi(pr[3])};
  float mx=fmaxf(fmaxf(fabsf(cr[0]),fabsf(cm[0])),fmaxf(fabsf(cr[1]),fabsf(cm[1])));
  u32 eb=__float_as_uint(mx)&0x7f800000u;
  #define DPPMAX(c) eb=max(eb,(u32)__builtin_amdgcn_update_dpp(0,(int)eb,c,0xf,0xf,false))
  DPPMAX(0x128); DPPMAX(0x124); DPPMAX(0x122); DPPMAX(0x121);
  eb=max(max((u32)__builtin_amdgcn_readlane(eb,0),(u32)__builtin_amdgcn_readlane(eb,16)),max((u32)__builtin_amdgcn_readlane(eb,32),(u32)__builtin_amdgcn_readlane(eb,48))); eb=eb<0x01000000u?0x3f800000u:(eb>0x7e000000u?0x7e000000u:eb);
  const float ksc=2.f*dt*__uint_as_float(eb), isc=__uint_as_float(0x7f000000u-eb);
  const int r=lane&31,h=lane>>5;
  const u32 TP=272, TA=cb, TB=cb+22*TP;
  f16v acc={};
  #pragma unroll 1
  for(int c=0;c<2;c++){
    if(c) mid();
    float l_a=c?bfhi(pr[0]):bflo(pr[0]), i_m=c?bfhi(pr[1]):bflo(pr[1]), c_r=c?cr[1]:cr[0], c_i=c?cm[1]:cm[0];
    const float lr=(-rbf(__expf(l_a))*dt)*1.44269504f, rv=(i_m*dt)*0.15915494f;
    const f2 z1=cpow_dir(lr,rv,1.f), z8=cpow_dir(lr,-rv,8.f), zs=cpow_dir(lr,-rv,16.f), zk=cpow_dir(lr,rv,256.f);
    const f2 z1s={-z1[1],z1[0]}, zss={-zs[1],zs[0]}, zks={-zk[1],zk[0]};
    const u32 wa=TA+lane*4, wb=TB+lane*4;
    #define PUT(base,row,v) do{ *(LAS u32*)((base)+(row)*TP)=pkhf2(v); }while(0)
    f2 e={1.f,0.f}, o=z8;
    #pragma unroll
    for(int k=0;k<16;k++){ PUT(wb,2*k,e); PUT(wb,2*k+1,o); if(k<15){ e=k?cmulp(e,zs,zss):zs; o=cmulp(o,zs,zss);} }
    f2 b0={c_r*isc,c_i*isc}; PUT(wa,0,b0);
    f2 t=cmulp(b0,zk,zks); PUT(wa,8,t);
    f2 b1=cmulp(b0,z1,z1s); PUT(wa,1,b1); t=cmulp(t,zk,zks); PUT(wa,10,t);
    f2 b2=cmulp(b1,z1,z1s); PUT(wa,2,b2); t=cmulp(t,zk,zks); PUT(wa,11,t);
    f2 b3=cmulp(b2,z1,z1s); PUT(wa,3,b3); t=cmulp(t,zk,zks); PUT(wa,16,t);
    f2 b4=cmulp(b3,z1,z1s); PUT(wa,4,b4); t=cmulp(t,zk,zks); PUT(wa,17,t);
    f2 b5=cmulp(b4,z1,z1s); PUT(wa,5,b5); t=cmulp(t,zk,zks); PUT(wa,20,t);
    f2 b6=cmulp(b5,z1,z1s); PUT(wa,6,b6); t=cmulp(t,zk,zks); PUT(wa,21,t);
    f2 b7=cmulp(b6,z1,z1s); PUT(wa,7,b7); t=cmulp(t,zk,zks); PUT(wa,18,t);
    f2 s=cmulp(b4,zk,zks); PUT(wa,12,s); t=cmulp(b2,zk,zks); PUT(wa,9,t);
    s=cmulp(s,zk,zks); PUT(wa,14,s); t=cmulp(b6,zk,zks); PUT(wa,13,t);
    s=cmulp(s,zk,zks); PUT(wa,15,s);
    #undef PUT
    WAITL0();
    const int ra=(r>21||r==19)?0:r;
    #pragma unroll
    for(int ks=0;ks<8;ks++){ u4 fa=ldsr128(TA+ra*TP+ks*32+h*16), fb=ldsr128(TB+r*TP+ks*32+h*16); acc=mfma32h(fa,fb,acc); }
    WAITL0();
  }
  #pragma unroll
  for(int i=0;i<11;i++) kp[i]=acc[i];
  return ksc;
}
DI void kgen_exp(u32 cb,int lane,const float (&acc)[11],const u32 (&ga)[11]){
  const int r=lane&31,h=lane>>5;
  { const u32 a4=cb+r*16+h*8, a5=cb+r*16+h*1024;
    #define PK(addr,base,reg) do{ *(LAS _Float16*)((addr)+2*(base))=(_Float16)acc[reg]; }while(0)
    PK(a4,0,0);PK(a4,1,1);PK(a4,2,2);PK(a4,3,3); PK(a4,256,4);PK(a4,258,5);PK(a4,512,6);PK(a4,768,7);
    PK(a5,1024,8);PK(a5,1280,9);PK(a5,2048,10);
    #undef PK
  }
  WAITL0();
  { const u32 g0=cb+2*(2048-8*lane);
    u32 a0=lds16(g0), b0=lds16(g0-16), a1=lds16(g0-1024), b1=lds16(g0-1024-16);
    u32 f2_=lds16(g0-2048+(lane?8:0)), a2=lds16(g0-2048), m2=lds16(g0-2048-8), b2=lds16(g0-2048-16);
    u32 e[11];
    #pragma unroll
    for(int j=0;j<11;j++) e[j]=lds16(ga[j]);
    u32 k01=*(LAS u32*)cb, k23=*(LAS u32*)(cb+4);
    WAITL0();
    const u32 wo=cb+lane*16;
    { u32 aa=dup(a0),bb=dup(b0),c=(lane&1)?b0:a0, cbv=cat(c,b0), ac=cat(a0,c);
      ldsw128(wo,u4{aa,aa,cbv,bb}); ldsw128(wo+KP,u4{aa,aa,ac,bb}); ldsw128(wo+2*KP,u4{aa,aa,aa,cbv}); ldsw128(wo+3*KP,u4{aa,aa,aa,ac}); }
    { u32 aa=dup(a1),bb=dup(b1),c=(lane&1)?b1:a1, cbv=cat(c,b1), ac=cat(a1,c);
      ldsw128(wo+1024,u4{aa,aa,cbv,bb}); ldsw128(wo+1024+KP,u4{aa,aa,ac,bb}); ldsw128(wo+1024+2*KP,u4{aa,aa,aa,cbv}); ldsw128(wo+1024+3*KP,u4{aa,aa,aa,ac}); }
    { u32 aa=dup(a2),mm=dup(m2),bb=dup(b2),am=cat(a2,m2),fa=cat(f2_,a2),mb=cat(m2,b2);
      ldsw128(wo+2048,u4{aa,am,mm,bb}); ldsw128(wo+2048+KP,u4{aa,aa,mm,mb}); ldsw128(wo+2048+2*KP,u4{aa,aa,am,mm}); ldsw128(wo+2048+3*KP,u4{fa,aa,aa,mm}); }
    { u32 o0=cat(e[0],e[1]),o1=cat(e[2],e[3]),o2=cat(e[4],e[5]),o3=cat(e[6],e[7]),o4=cat(e[8],e[9]);
      u32 p1=cat(e[1],e[2]),d0=cat(e[3],e[4]),d1=cat(e[5],e[6]),d2=cat(e[7],e[8]),d3=cat(e[9],e[10]);
      ldsw128(wo+3072,u4{d0,d1,d2,d3}); ldsw128(wo+3072+KP,u4{o1,o2,o3,o4}); ldsw128(wo+3072+2*KP,u4{p1,d0,d1,d2}); ldsw128(wo+3072+3*KP,u4{o0,o1,o2,o3}); }
    if(lane<4){ if(lane){k01=0;k23=0;}
      u32 d0=k01&0xffffu, o1=__builtin_amdgcn_alignbit(k01,k01,16), o0=__builtin_amdgcn_alignbit(k23,k23,16), p1=(k23&0xffffu)|(k01&0xffff0000u);
      ldsw128(wo+4096,u4{d0,0,0,0}); ldsw128(wo+4096+KP,u4{o1,0,0,0}); ldsw128(wo+4096+2*KP,u4{p1,d0,0,0}); ldsw128(wo+4096+3*KP,u4{o0,o1,0,0}); }
  }
}
DI f4 mfma16h(u4 a,u4 b,f4 c){ return __builtin_amdgcn_mfma_f32_16x16x32_f16(__builtin_bit_cast(hf8,a),__builtin_bit_cast(hf8,b),c,0,0,0); }
#define RDA(S,i,m) asm volatile("ds_read2_b64 %0, %1 offset0:%2 offset1:%3" : "=v"(AF[S][i][m]) : "v"((i)<2?kb:ka), "n"((1-((i)&1))*128+(1-(m))*4), "n"((1-((i)&1))*128+(1-(m))*4+1))
#define RDB(S,i,ad,o) asm volatile("ds_read_b128 %0, %1 offset:%2" : "=v"(BF[S][i]) : "v"(ad), "n"(o))
#define TIEA(S,i) "+v"(AF[S][i][0]),"+v"(AF[S][i][1])
#define CV_M(S,i,n) do{ y[0][n]=mfma16h(AF[S][i][0],BF[S][(n)-(i)],y[0][n]); y[1][n]=mfma16h(AF[S][i][1],BF[S][(n)-(i)],y[1][n]); SCHED(); }while(0)
#define CV_U0() u32 u0=u1-256; u0=((int)u0<(int)uz)?uz:u0; const u32 kb=ka+2048; (void)kb
#define CV_SETUP() const int r=lane&15,q=lane>>4; u32 ka=cb+(r&3)*KP+16*q-8*(r>>2)+992; const u32 uz=cb+UOF+q*1040; u32 u1=uz+(r+17)*16; u4 AF[2][4][2]; u4 BF[2][4]
DI void conv_help(u32 cb,int lane){
  CV_SETUP(); f4 y[2][4]={};
  #define HP_WT(S) asm volatile("s_waitcnt lgkmcnt(0)" : TIEA(S,0),TIEA(S,1),TIEA(S,2),TIEA(S,3),"+v"(BF[S][0]),"+v"(BF[S][1]),"+v"(BF[S][2]),"+v"(BF[S][3]))
  #define HP_AD() const u32 kb=ka-64, u3=u1-16; u32 u0=u1-256, u2_=u1-272; u0=((int)u0<(int)uz)?uz:u0; u2_=((int)u2_<(int)uz)?uz:u2_
  #define HP_M(S,i,bi,n) do{ y[0][n]=mfma16h(AF[S][i][0],BF[S][bi],y[0][n]); y[1][n]=mfma16h(AF[S][i][1],BF[S][bi],y[1][n]); SCHED(); }while(0)
  #define HP_STEP(C,N) do{ HP_AD(); RDA(N,2,0); RDA(N,2,1); RDB(N,1,u1,0); RDA(N,3,0); SCHED(); HP_M(C,2,1,3); RDA(N,3,1); RDB(N,0,u0,0); RDA(N,0,0); RDA(N,0,1); SCHED(); HP_M(C,3,0,2); \
      RDB(N,3,u3,0); RDA(N,1,0); RDA(N,1,1); RDB(N,2,u2_,0); ka-=128; u1-=32; SCHED(); HP_M(C,0,3,3); HP_M(C,1,2,2); HP_WT(N); }while(0)
  { HP_AD(); RDA(0,2,0); RDA(0,2,1); RDB(0,1,u1,0); RDA(0,3,0); RDA(0,3,1); RDB(0,0,u0,0); RDA(0,0,0); RDA(0,0,1); RDB(0,3,u3,0); RDA(0,1,0); RDA(0,1,1); RDB(0,2,u2_,0); ka-=128; u1-=32; HP_WT(0); }
  #pragma unroll 1
  for(int dl=CVSKIP*3;dl<4;dl++){ HP_STEP(0,1); if(dl==3){ ka+=128; u1+=32; } HP_STEP(1,0); }
  y[0][3]+=y[0][2]; y[1][3]+=y[1][2];
  *(LAS f4*)(cb+q*KP+r*16)=y[0][3]; *(LAS f4*)(cb+q*KP+256+r*16)=y[1][3];
  WAITL0();
}
DI void conv_wave(const Params& p,rsrc_t rs,int d,u32 cb,u32 flag,int lane,float ksc,float dsk){
  CV_SETUP(); f4 y[2][4]={};
  #define CV_WT(S) asm volatile("s_waitcnt lgkmcnt(0)" : TIEA(S,0),TIEA(S,1),TIEA(S,2),"+v"(BF[S][0]),"+v"(BF[S][1]),"+v"(BF[S][2]),"+v"(BF[S][3]))
  #define CV_STEP(C,N) do{ CV_U0(); CV_M(C,0,0); RDA(N,0,0); RDB(N,0,u0,0); SCHED(); CV_M(C,0,1); RDA(N,0,1); RDB(N,1,u1,0); SCHED(); CV_M(C,0,2); RDA(N,1,0); RDB(N,2,u1,256); SCHED(); \
      CV_M(C,0,3); RDA(N,1,1); RDB(N,3,u1,512); SCHED(); CV_M(C,1,1); RDA(N,2,0); SCHED(); CV_M(C,1,2); RDA(N,2,1); ka-=64; u1-=16; SCHED(); CV_M(C,1,3); CV_M(C,2,2); CV_WT(N); }while(0)
  { CV_U0(); RDA(0,0,0); RDA(0,0,1); RDA(0,1,0); RDA(0,1,1); RDA(0,2,0); RDA(0,2,1); RDB(0,0,u0,0); RDB(0,1,u1,0); RDB(0,2,u1,256); RDB(0,3,u1,512); ka-=64; u1-=16; CV_WT(0); }
  #pragma unroll 1
  for(int dl=CVSKIP*7;dl<8;dl++){ CV_STEP(0,1); if(dl==7){ ka+=64; u1+=16; } CV_STEP(1,0); }
  ldswait(flag,2u);
  y[0][3]+=*(LAS f4*)(cb+q*KP+r*16); y[1][3]+=*(LAS f4*)(cb+q*KP+256+r*16);
  #pragma unroll
  for(int n=0;n<4;n++){ u2 o[2];
    #pragma unroll
    for(int m=0;m<2;m++){ u2 uv=ldsr64(cb+UOF+(2*m+(q>>1))*1040+(16*n+r+1)*16+8*(q&1));
      #pragma unroll
      for(int e=0;e<2;e++) o[m][e]=pkbf2(fma2(f2{hlo(uv[e]),hhi(uv[e])},f2{dsk,dsk},f2{y[m][n][2*e],y[m][n][2*e+1]}*ksc)); }
    auto sx=__builtin_amdgcn_permlane16_swap(o[0][0],o[1][0],false,false); auto sy=__builtin_amdgcn_permlane16_swap(o[0][1],o[1][1],false,false);
    const u32 sc=cb+1024+n*1280; ldsw128(sc+r*80+(q&1)*32+(q>>1)*16,u4{sx[0],sy[0],sx[1],sy[1]});
    st16(rs,OFF_AT+d*4096+n*1024+lane*16,ldsr128(sc+(lane>>2)*80+(lane&3)*16)); }
  WAITV(0);
}

struct Frag { u4 x[4], w[4][2]; };
__global__ __launch_bounds__(512,2) void fused(Params p){
  extern __shared__ __attribute__((aligned(16))) char sm[];
  const u32 smb=(u32)(size_t)(LAS char*)sm;
  u64 pc0; asm volatile("s_getpc_b64 %0" : "=s"(pc0));
  const int b=blockIdx.x, xc=b&7, idx=b>>3;
  const int tid=threadIdx.x, w=uni(tid>>6); int lane=tid&63;
  const rsrc_t rs=__builtin_amdgcn_make_buffer_rsrc(p.ws,0,WS_END,0x00020000);
  const u4 srdS=mksrd(p.ws);
  const int rtB=4*xc+(idx&3), ct=idx>>2;
  const bool hask=idx<28 && idx+28*(w&3)<80, hasc=hask && w>=4;
  const int dch=80*xc+(hask? idx+28*(w&3) : 0);
  const u32 cb=smb+CONV+(w&3)*CVS;
  const int rt=4*(xc>>1)+(idx&3), j=7*(xc&1)+(idx>>2), mi=w&3, g1=w>>2;
  u32 tag; float ksc=0.f, dsk=0.f;
  STAMP(0,0);
  auto pre01=[&](){
    const u4 sa=mksrd((const char*)p.wsp+(size_t)rtB*64*4096+mi*1024-4096); const u32 v0=(lane>>3)*4096+(((lane&7)^(lane>>4))<<4);
    #pragma unroll 1
    for(int i=0;i<4;i++){ setm0<RB+8192>(smb+mi*RING+i*4096); const u32 g=(i&1)*4+2*mi, k=(i>>1)*128+4096;
      dma<0>(sa,v0,(g&7)*32768+k); dma<1024>(sa,v0^64u,(g&7)*32768+32768-1024+k); dma<2048>(sa,v0,((g+2)&7)*32768-2048+k); dma<3072>(sa,v0^64u,((g+2)&7)*32768+32768-3072+k); } };
  auto warm=[&](int wq,int part){
    const u4 sp=mksrd((const char*)p.wsp+(size_t)(rtB*64+ct*8+2*wq)*4096); setm0<JNK>(smb);
    const u32 vo= part==2? lane*128 : ((lane>>2)&7)*1024+(lane&3)*128+part*512;
    asm volatile("buffer_load_dword %0, %1, 0 offen lds" :: "v"(vo), "s"(sp) : "memory");
  };
  if(idx>=28){
    const int a=xc*4+idx-28, r=lane&15, q=lane>>4;
    { u4 sc=mksrd((const void*)(pc0&~127ull)); sc[2]=CODE_BYTES; setm0<JNK>(smb); touch(sc,lane*128,w*8192); }
    const u32 ep=ld4(rs,OFF_EP);
    u4 xr[10]; const char* xs=(const char*)p.x+(size_t)(64*a+8*w+(lane>>3))*1280+(lane&7)*16;
    #pragma unroll
    for(int i=0;i<10;i++) xr[i]=*(const u4*)(xs+i*128);
    if(tid<32) *(LAS u32*)(smb+PM+tid*4)=0u;
    tag=uni(ep)+1u;
    float s0=0.f,s1=0.f,q0=0.f,q1=0.f;
    #pragma unroll
    for(int i=0;i<10;i++){ s0=dot2(xr[i][0],0x3f803f80u,s0); q0=dot2(xr[i][0],xr[i][0],q0); s1=dot2(xr[i][1],0x3f803f80u,s1); q1=dot2(xr[i][1],xr[i][1],q1);
      s0=dot2(xr[i][2],0x3f803f80u,s0); q0=dot2(xr[i][2],xr[i][2],q0); s1=dot2(xr[i][3],0x3f803f80u,s1); q1=dot2(xr[i][3],xr[i][3],q1); }
    s0+=s1; q0+=q1;
    #define DPPADD(v,c) v+=__int_as_float(__builtin_amdgcn_update_dpp(0,__float_as_int(v),c,0xf,0xf,false))
    DPPADD(s0,0xB1); DPPADD(q0,0xB1); DPPADD(s0,0x4E); DPPADD(q0,0x4E); DPPADD(s0,0x141); DPPADD(q0,0x141);
    float m1,ri; { float mean=s0*(1.f/640.f); ln_fin(mean,q0*(1.f/640.f)-mean*mean,m1,ri); }
    if((lane&7)==0) st16(rs,OFF_RS+(64*a+8*w+(lane>>3))*16,u4{__float_as_uint(m1),tag,__float_as_uint(ri),tag});
    STAMP(5,0);
    #pragma unroll
    for(int i=0;i<10;i++){ u4 o;
      #pragma unroll
      for(int e=0;e<4;e++) o[e]=pkhf2(fma2(unpk(xr[i][e]),f2{ri,ri},f2{-m1*ri,-m1*ri}));
      ldsw128(smb+RB+(8*w+(lane>>3))*1296+(lane&7)*16+i*128,o); }
    bar();
    #pragma unroll
    for(int i=0;i<10;i++){ const int c=w*10+i; const u32 ad=smb+RB+(32*((r>>1)&1)+8*q+(r>>2))*1296+c*16+(r&1)*8; u2 lo=ldstr(ad), hi=ldstr(ad+4*1296);
      const u32 sc=smb+w*5120+(i%5)*1024; ldsw128(sc+(r&7)*128+((((r>>3)*4+q)^(r&7))<<4),u4{lo[0],lo[1],hi[0],hi[1]});
      st16(rs,OFF_XNT+a*81920+c*1024+lane*16,ldsr128(sc+(lane>>3)*128+(((lane&7)^(lane>>3))<<4))); }
    WAITV(0);
    if(arrive(smb+C_Z1,lane)==7u && lane==0) st4(rs,OFF_FX+a*128,tag);
    STAMP(6,7);
    if(a<27){ const u4 srdO=mksrd((const char*)p.wout+a*(32*1280)+w*5120); const u32 mo=smb+w*5120; setm0<0>(mo);
      dma<0>(srdO,lane*16,0); dma<1024>(srdO,lane*16,0); dma<2048>(srdO,lane*16,0); dma<3072>(srdO,lane*16,0); setm0<4096>(mo); dma<0>(srdO,lane*16,4096); }
    WAITV(0); bar();
    if(a<27){
      #pragma unroll
      for(int i=0;i<5;i++){ u32 ad=smb+(8*q+(r>>2))*1280+(w*5+i)*32+(r&3)*8; u2 lo=ldstr(ad), hi=ldstr(ad+4*1280);
        st16(rs,OFF_WOF+(u32)((w*27+a)*5+i)*1024+lane*16,u4{lo[0],lo[1],hi[0],hi[1]}); } }
    WAITV(0); bar();
    if(tid==0) st4(rs,OFF_FW+a*128,tag);
    if(g1) warm(mi,2); else { pre01(); WAITV(0); }
    STAMP(31,0);
  } else {
    const int r=lane&31, h=lane>>5;
    const u4 srdX=mksrd((const char*)p.x+(size_t)rt*128*1280);
    const u4 srdW=mksrd((const char*)p.win+j*128-3072);
    const u32 mw=smb+w*4096;
    u32 vx0,vx1,vw1,vw2,epv;
    { u32 lr=lane>>3,c=lane&7; vx0=(16*w+lr)*1280+((c^(lr>>1))<<4); vx1=(vx0^64u)+9216;
      vw1=(8*w+lr)*3456+((c^((lr&2)<<1))<<4); vw2=vw1+1024+(j==13?0:14*128); }
    u32 kx[10]; { u32 k=KROT;
      #pragma unroll
      for(int i=0;i<10;i++){ kx[i]=k*128; k=k==9?0:k+1; } }
    auto issue1=[&](auto sc,int k){ constexpr int s=sc.value;
      if(k==0){ setm0<(s&3)*STG>(mw); dma<0>(srdX,vx0,kx[s]); } if(k==1) dma<1024>(srdX,vx1,kx[s]); if(k==2) dma<2048>(srdW,vw2,kx[s]*1728); if(k==3) dma<3072>(srdW,vw1,kx[s]*1728); };
    auto issue=[&](auto sc){ issue1(sc,0); issue1(sc,1); issue1(sc,2); issue1(sc,3); };
    issue(std::integral_constant<int,0>{});
    u32 pr[6];
    gload4<0>(epv,0u,p.ws+OFF_EP);
    gload4<0>(pr[0],lane*4,(const char*)(p.lar+dch*128)); gload4<0>(pr[1],lane*4,(const char*)(p.aim+dch*128)); gload4<0>(pr[2],lane*4,(const char*)(p.cre+dch*128)); gload4<0>(pr[3],lane*4,(const char*)(p.cim+dch*128));
    gload2(pr[4],0u,(const char*)(p.ldt+dch)); gload2(pr[5],0u,(const char*)(p.dsk+dch));
    asm volatile("s_barrier" ::: "memory");
    issue(std::integral_constant<int,1>{});
    if(tid<32) *(LAS u32*)(smb+PM+tid*4)=0u;
    u32 ax[2][4],aw[2][4][2];
    { u32 row=32*mi+r, a0=smb+(row>>4)*4096+(row&15)*128+((h^((r>>1)&7))<<4);
      u32 qq=(lane&15)>>2, w0=smb+2048+g1*1024+h*4096+qq*128+8*(lane&1)+(((2*((lane>>4)&1)+((lane>>1)&1))^((qq&2)<<1))<<4);
      #pragma unroll
      for(int i=0;i<2;i++)
        #pragma unroll
        for(int k=0;k<4;k++){ const u32 kk=(k+mi)&3, wo=i*2*STG; ax[i][k]=opq((a0^(kk*32u))+wo); aw[i][k][0]=opq(w0+kk*8192+wo); aw[i][k][1]=opq((w0^64u)+kk*8192+wo); } }
    u4 sel; { const u32 o=((lane&15)==((lane>>4)&1))?0x3f803f80u:0u; sel=u4{o,o,o,o}; asm volatile("" : "+v"(sel)); }
    u32 ga[11];
    #pragma unroll
    for(int i=0;i<11;i++) ga[i]=opq(cb+2u*(u32)rbf((float)(512-8*lane+3-i)));
    Frag F[2];
    asm volatile("s_waitcnt vmcnt(4)" : "+v"(epv),"+v"(pr[0]),"+v"(pr[1]),"+v"(pr[2]),"+v"(pr[3]),"+v"(pr[4]),"+v"(pr[5]) :: "memory");
    bar();
    tag=uni(epv)+1u; dsk=bflo(pr[5]);
    #pragma unroll
    for(int k=0;k<4;k++){
      #pragma unroll
      for(int n=0;n<2;n++){ u2 lo=ldstr(aw[0][k][n]), hi=ldstr(aw[0][k][n]+512); F[0].w[k][n]=u4{lo[0],lo[1],hi[0],hi[1]}; }
      F[0].x[k]=ldsr128(ax[0][k]); }
    issue(std::integral_constant<int,2>{});
    STAMP(1,0);
    f16v acc[2]={}; f4 cacc[2]={}; u4 G;
    const u32 go=OFF_RS+rt*2048+mi*512, cp=smb+MISC+g1*1024;
    sfor<10>([&](auto ic){
      constexpr int s=ic.value; Frag& cur=F[s&1]; Frag& nxt=F[(s&1)^1];
      if constexpr(s<9){ WAITV((s==8? 2 : s==7? 6 : 4)+((TS1<6&&(s==TS1+1||s==TS1+2))?1:0)); asm volatile("s_barrier" ::: "memory"); }
      auto rw=[&](int k,int n){ if constexpr(s<9){ const int i=((s+1)>>1)&1; const u32 so=((s+1)&1)*STG; u2 lo=ldstr(aw[i][k][n]+so), hi=ldstr(aw[i][k][n]+so+512); nxt.w[k][n]=u4{lo[0],lo[1],hi[0],hi[1]}; } };
      auto rx=[&](int k){ if constexpr(s<9){ const int i=((s+1)>>1)&1; const u32 so=((s+1)&1)*STG; nxt.x[k]=ldsr128(ax[i][k]+so); } };
      #pragma unroll
      for(int k=0;k<4;k++){
        acc[0]=mfma32b(cur.w[k][0],cur.x[k],acc[0]); SCHED();
        if constexpr(s+3<=9) issue1(std::integral_constant<int,s+3>{},k);
        rw(k,0); rx(k);
        SCHED();
        acc[1]=mfma32b(cur.w[k][1],cur.x[k],acc[1]); SCHED();
        rw(k,1);
        if(k==0){ cacc[0]=mfma16(cur.w[0][0],sel,cacc[0]); cacc[1]=mfma16(cur.w[0][1],sel,cacc[1]); }
        if constexpr(s==TS1 && TS1<6){ if(k==3){ if(g1) warm(mi,0); else { setm0<JNK>(smb); touch(srdS,0u,OFF_EP); } } }
        if constexpr(s==6){ if(k==3){ gload<0>(G,r*16,p.ws+go); if(g1) warm(mi,TS1<6?1:2); else { setm0<JNK>(smb); touch(srdS,0u,go); } } }
        if constexpr(s==8){ if(k==3) signal(smb+C_DONE,lane); }
        if constexpr(s==9){ if(k==1){
          if((lane&14)==0){ *(LAS f4*)(cp+mi*256+(lane&1)*64+(lane>>4)*16)=cacc[0]; *(LAS f4*)(cp+mi*256+128+(lane&1)*64+(lane>>4)*16)=cacc[1]; }
          signal(smb+C_CS+g1*4,lane); } }
        SCHED();
      }
    });
    RST(0);
    if(!g1) __builtin_amdgcn_s_setprio(2);
    asm volatile("s_waitcnt vmcnt(1)" : "+v"(G) :: "memory");
    ldswait(smb+C_CS+g1*4,4u);
    const float cs0=*(LAS float*)(cp+h*512+r*4)+*(LAS float*)(cp+h*512+256+r*4), cs1=*(LAS float*)(cp+h*512+128+r*4)+*(LAS float*)(cp+h*512+384+r*4);
    RST(2);
    #pragma unroll 1
    for(int sp=0;sp<SPIN && !__all(G[1]==tag&&G[3]==tag);sp++)asm volatile("global_load_dwordx4 %0, %1, %2 sc1\n\ts_waitcnt vmcnt(0)" : "=&v"(G) : "v"(r*16), "s"(p.ws+go) : "memory");
    RST(3);
    const float nm=-__uint_as_float(G[0]), ri1=__uint_as_float(G[2]);
    acc[0]=__builtin_amdgcn_mfma_f32_32x32x2f32(cs0,nm,acc[0],0,0,0); acc[1]=__builtin_amdgcn_mfma_f32_32x32x2f32(cs1,nm,acc[1],0,0,0);
    u32 zp[2][8]; const u32 zqa=smb+RB+mi*RING+4096+lane*16;
    if(g1){
      ldswait(smb+C_DONE,8u);
      #pragma unroll
      for(int i=0;i<4;i++){ u4 o;
        #pragma unroll
        for(int e=0;e<4;e++) o[e]=pkbf2(f2{acc[i>>1][8*(i&1)+2*e],acc[i>>1][8*(i&1)+2*e+1]}*ri1);
        ldsw128(zqa+i*1024,o); }
      signal(smb+C_K2+mi*4,lane); RST(4);
    } else {

      #pragma unroll
      for(int e=0;e<16;e++) zp[e>>3][e&7]=pkbf2(gelu2(f2{acc[e>>3][2*(e&7)],acc[e>>3][2*(e&7)+1]}*ri1));
      float S=0.f,Q=0.f;
      if(j<13){
        #pragma unroll
        for(int e=0;e<8;e++){ S=dot2(zp[0][e],0x3f803f80u,S); Q=dot2(zp[0][e],zp[0][e],Q); } }
      #pragma unroll
      for(int e=0;e<8;e++){ S=dot2(zp[1][e],0x3f803f80u,S); Q=dot2(zp[1][e],zp[1][e],Q); }
      #define XSUM32(v) do{ auto t_=__builtin_amdgcn_permlane32_swap(__float_as_uint(v),__float_as_uint(v),false,false); v=__uint_as_float(t_[0])+__uint_as_float(t_[1]); }while(0)
      XSUM32(S); XSUM32(Q);
      const u32 sto=OFF_ST+rt*(14*2048)+(32*mi+r)*16;
      if(h==0) st16(rs,sto+j*2048,u4{__float_as_uint(S),tag,__float_as_uint(Q),tag});
      RST(6);
    }
    float kp[11]; u4 g[7]={};
    const u32 gof=g1? OFF_FX+r*128 : OFF_ST+rt*(14*2048)+(32*mi+r)*16+h*(7*2048);
    auto samp=[&](){ asm volatile("" ::: "memory"); g[0]=ld16(rs,gof);
      if(!g1){
        #pragma unroll
        for(int i=1;i<7;i++) g[i]=ld16(rs,gof+i*2048); }
      asm volatile("" ::: "memory"); };
    if(hasc) ksc=kgen_tab(cb,lane,pr,kp,samp); else if(!g1){
      pre01(); ldswait(smb+C_K2+mi*4,1u); u4 zq[4];
      #pragma unroll
      for(int i=0;i<4;i++){ const u4 v=ldsr128(zqa+i*1024);
        #pragma unroll
        for(int e=0;e<4;e++) zq[i][e]=pkbf2(gelu2(unpk(v[e]))); }
      SCHED(); samp();
      #pragma unroll
      for(int n=0;n<2;n++) if(2*j+n<27){ const u32 oo=OFF_Z1T+((rt*8+2*mi+(r>>4))*27+2*j+n)*1024+(h*16+(r&15))*16;
        st16(rs,oo,u4{zq[2*n][0],zq[2*n][1],zq[2*n+1][0],zq[2*n+1][1]}); st16(rs,oo+512,u4{zq[2*n][2],zq[2*n][3],zq[2*n+1][2],zq[2*n+1][3]}); }
    }
    if(g1){
      if(w<7){ u4 uu[4]; const u32 fxv=g[0][0];
        if(hasc){
          RST(6);
          if(!__all(fxv==tag)) pollw<16>(rs,OFF_FX+r*128,tag);
          RST(7);
          #pragma unroll
          for(int i=0;i<4;i++) uu[i]=ld16(rs,OFF_XNT+((lane>>3)+8*i)*81920+dch*128+(lane&7)*16);
          asm volatile("" ::: "memory");
          kgen_exp(cb,lane,kp,ga);
          RST(8);
          #pragma unroll
          for(int i=0;i<4;i++){ const int x=lane+64*i; ldsw128(cb+UOF+(x&3)*1040+((x>>2)+1)*16,uu[i]); }
          if(lane<4) ldsw128(cb+UOF+lane*1040,u4{0,0,0,0});
          WAITL0(); signal(smb+C_KP+mi*4,lane);
        }
        RST(9);
      }
    } else {
      RST(1);
      float S,Q;
      #pragma unroll 1
      for(int sp=0;sp<SPIN;sp++){ bool ok=true;
        #pragma unroll
        for(int i=0;i<7;i++) ok=ok&&g[i][1]==tag&&g[i][3]==tag;
        if(__all(ok)) break;
        __builtin_amdgcn_s_sleep(4); samp(); }
      RST(7);
      S=0.f; Q=0.f;
      #pragma unroll
      for(int i=0;i<7;i++){ S+=__uint_as_float(g[i][0]); Q+=__uint_as_float(g[i][2]); }
      XSUM32(S); XSUM32(Q);
      float mean=S*(1.f/864.f), m2,r2; ln_fin(mean,Q*(1.f/864.f)-mean*mean,m2,r2); m2*=-r2;
      ldswait(smb+C_DONE,8u);
      const u32 tb=smb+RB+mi*RING, ad=tb+(8*(lane>>4)+((lane&15)>>2))*80+(lane&3)*8;
      const u32 zo=OFF_Z2F+((rt*4+mi)*54+(j==13?-2:2+4*j))*1024+lane*16;
      #pragma unroll
      for(int n=0;n<2;n++){
        #pragma unroll
        for(int g2=0;g2<4;g2++){ u2 o;
          #pragma unroll
          for(int e=0;e<2;e++) o[e]=pkbf2(fma2(unpk(zp[n][2*g2+e]),f2{r2,r2},f2{m2,m2}));
          *(LAS u2*)(tb+r*80+g2*16+h*8)=o; }
        u2 l0=ldstr(ad), h0=ldstr(ad+320), l1=ldstr(ad+32), h1=ldstr(ad+32+320);
        if(j<13||n){ st16(rs,zo+n*2048,u4{l0[0],l0[1],h0[0],h0[1]}); st16(rs,zo+n*2048+1024,u4{l1[0],l1[1],h1[0],h1[1]}); } }
      RST(8);
      WAITV(0);
      if(arrive(smb+C_Z2,lane)==3u && lane==0) st4(rs,OFF_F2+(rt*16+j)*128,tag);
      RST(9);
    }
  }
  const int ft0=ct<7?7*ct:47;
  bool adone=true; const u32 fao=OFF_FA+((lane<28?lane:0)*8+ct)*128;
  auto aload=[&](){ const u32 vo=(lane>>3)*4096+(lane&7)*16;
    #pragma unroll 1
    for(int i=0;i<10;i++){ setm0<ATL>(smb+i*1024); dma<0>(srdS,vo,OFF_AT+(u32)(80*ct+8*i)*4096+rtB*128); } };
  if(w>=4){
    if(hasc){
      __builtin_amdgcn_s_setprio(CPRIO);
      conv_wave(p,rs,dch,cb,smb+C_KP+mi*4,lane,ksc,dsk);
      __builtin_amdgcn_s_setprio(0);
      if(arrive(smb+C_CV,lane)==(idx<24?2u:1u) && lane==0) st4(rs,OFF_FA+b*128,tag);
    }
    if(w==7){
      __builtin_amdgcn_s_setprio(HPRIO);
      if(idx<28){
        #pragma unroll 1
        for(int c=0;c<(idx<24?3:2);c++){ ldswait(smb+C_KP+c*4,1u); conv_help(smb+c*CVS,lane); signal(smb+C_KP+c*4,lane); } }
      __builtin_amdgcn_s_setprio(0);
      RST(6);
    }
    RST(10);
    ldswait(smb+C_Z1R,4u);
    u4 wq[24];
    #pragma unroll
    for(int i=0;i<24;i++) wq[i]=ld16(rs,OFF_WOF+ct*(135*1024)+40960+mi*RING+i*1024+lane*16);
    if(w<6){
      #pragma unroll 1
      for(u32 i=0;i<20480;i+=4096){ setm0<0>(cb+i); const u32 o=OFF_WOF+ct*(135*1024)+mi*20480+i; dma<0>(srdS,lane*16,o); dma<1024>(srdS,lane*16,o); dma<2048>(srdS,lane*16,o); dma<3072>(srdS,lane*16,o); } }
    if(w==6){ adone=__all(ld4(rs,fao)==tag); if(adone) aload(); }
    ldswait(smb+C_FREE,4u);
    #pragma unroll
    for(int i=0;i<24;i++) ldsw128(smb+RB+mi*RING+i*1024+lane*16,wq[i]);
    if(w<6) WAITV(0);
    signal(smb+C_WR,lane);
    if(!adone){ adone=__all(ld4(rs,fao)==tag); if(adone) aload(); }
    if(w==7 && b==255){
      #pragma unroll 1
      for(int sp=0;sp<SPIN;sp++){ const u32 o=OFF_FA+lane*128; u32 v0=ld4(rs,o),v1=ld4(rs,o+8192),v2=ld4(rs,o+16384),v3=ld4(rs,lane<32? o+24576 : OFF_FW+(lane-32)*128); asm volatile("" ::: "memory");
        if(__all(v0==tag&&v1==tag&&v2==tag&&v3==tag)) break; __builtin_amdgcn_s_sleep(4); }
      if(lane==0) st4(rs,OFF_EP,tag); }
  }
  int w2=w; asm volatile("" : "+s"(w2));
  if(w2<4){
    __builtin_amdgcn_s_setprio(G2PRIO);
    int r=lane&15, q=lane>>4;
    const u32 rb=smb+RB+w*RING;
    u4 srdA=mksrd((const char*)p.wsp+(size_t)rtB*64*4096+w*1024-4096+256);
    const char* zb=p.ws+OFF_Z2F+(u32)(16*w*54+ft0+3)*1024;
    u32 voA[2]; { u32 lr=lane>>3,c=lane&7; voA[0]=lr*4096+((c^(lr>>1))<<4); voA[1]=voA[0]^64u; }
    u32 sofa[8];
    #pragma unroll
    for(int g=0;g<8;g++) sofa[g]=((g+2*w)&7)*32768+4096-(g&3)*1024;
    const u32 voB=lane*16;
    u32 la[2]; la[0]=rb+r*128+((q^(r>>1))<<4); la[1]=la[0]^64u;
    f4 acc[4][7]={}; u4 B[3][7]; u4 A[2][4]; u2 z1[7];
    auto issueA=[&](auto sc,auto gc){ constexpr int s=sc.value,g=gc.value; if constexpr((g&3)==0) setm0<((s+1)%3)*8192+g*1024>(rb); dma<(g&3)*1024>(srdA,voA[g&1],sofa[g]);
      if constexpr(g==7){ u64 a=((u64)srdA[1]<<32|srdA[0])+128; srdA[0]=(u32)a; srdA[1]=(u32)(a>>32); } };

    u32 bsr,fv;
    auto fof=[&](){ const u32 l=voB>>4; return l<27? OFF_FW+l*128 : OFF_F2+((rtB>>1)*16+(((ft0>>1)+(l&3))>>1))*128; };
    #pragma unroll
    for(int m=0;m<4;m++){ A[0][m]=ldsr128(la[0]+m*2048+8192); A[1][m]=ldsr128(la[1]+m*2048+8192); }
    sfor<8>([&](auto g){ issueA(std::integral_constant<int,2>{},g); });
    waitA(A[0]); waitA(A[1]);
    sfor<8>([&](auto g){ issueA(std::integral_constant<int,3>{},g); });
    #pragma unroll
    for(int m=0;m<4;m++)
      #pragma unroll
      for(int n=0;n<7;n++) asm volatile("" : "+v"(acc[m][n]));
    RST(2);
    { const int hf=(ft0>>1)+q; pollw(rs,OFF_F2+((4*w+(lane&3))*16+(hf==0?13:(hf-1)>>1))*128,tag); }
    RST(10);
    sfor<3>([&](auto hh){ const char* s=zb+(u32)hh.value*(54*1024); sfor<7>([&](auto n){ gload<(n.value-3)*1024>(B[hh][n],voB,s); }); });
    auto redw=[&](auto nc){ constexpr int n=nc.value; *(LAS f4*)(rb+n*1024+voB)=acc[1][n]; *(LAS f4*)(rb+(7+n)*1024+voB)=acc[2][n]; *(LAS f4*)(rb+(14+n)*1024+voB)=acc[3][n]; };
    const char* z1b=p.ws+OFF_Z1T+(u32)((rtB*4+w)*27+(ft0>>1))*1024;
    sfor<16>([&](auto hc){
      constexpr int h=hc.value, S=h%3, P=h&1;
      constexpr int cnt = h<4? 14 : ((h+1<16?7:0)+(h+2<16?7:0) + ((((h-2)&1)&&((h-2)/2+3<8))?8:0) + ((((h-1)&1)&&((h-1)/2+3<8))?8:0) + (h>=14?8:0) + ((h==11||h==12)?1:0));
      waitB<cnt>(B[S]); waitA(A[P]);
      if constexpr(h==13){ tie1(fv); if(!__all(fv==tag)) pollw(rs,fof(),tag); }
      if constexpr(h+1<16 && h>0){
        #pragma unroll
        for(int m=0;m<4;m++) A[P^1][m]=ldsr128(la[(h+1)&1]+m*2048+((((h+1)>>1)+1)%3)*8192);
      }
      if constexpr(h==10) gload4<0>(fv,fof(),p.ws);
      const char* s=zb+(u32)(h+3)*(54*1024);
      sfor<7>([&](auto n){
        mfma4(acc[0][n],acc[1][n],acc[2][n],acc[3][n],B[S][n],A[P]);
        if constexpr((h&1)&&(h/2+3<8)&&h>=3){ issueA(std::integral_constant<int,h/2+3>{},n); if constexpr(n.value==6) issueA(std::integral_constant<int,h/2+3>{},std::integral_constant<int,7>{}); }
        if constexpr(h+3<16) gload_t<(n.value-3)*1024>(B[S][n],voB,s);
        if constexpr(h==13){ gload8<(n.value>>1)*1024>(z1[n],voB,z1b+((n.value&1)? ((ft0&1)?1024:8) : (ft0&1)*8));
          if constexpr(n.value==6) { gload2(bsr,(voB>>3)&30u,(const char*)(p.bsp+(rtB*4+w)*16)); signal(smb+C_Z1R,lane); } }
        if constexpr(h==15){ SCHED(); if constexpr(n.value>=2) redw(std::integral_constant<int,n.value-2>{}); SCHED(); }
      });
    });
    asm volatile("s_nop 15\n\ts_nop 3" ::: "memory");
    SCHED();
    STAMP(10,0);
    lane=__builtin_amdgcn_mbcnt_hi(~0u,__builtin_amdgcn_mbcnt_lo(~0u,0u)); asm volatile("" : "+v"(lane)); r=lane&15; q=lane>>4;
    redw(std::integral_constant<int,5>{}); redw(std::integral_constant<int,6>{});
    signal(smb+C_RED,lane);
    ldswait(smb+C_RED,4u);
    #pragma unroll
    for(int m=1;m<4;m++){ u32 src=smb+RB+((w-m)&3)*RING+(m-1)*7168+lane*16;
      #pragma unroll
      for(int n=0;n<7;n++) acc[0][n]+=*(LAS f4*)(src+n*1024); }
    asm volatile("s_waitcnt vmcnt(0) lgkmcnt(0)" : "+v"(z1[0]),"+v"(z1[1]),"+v"(z1[2]),"+v"(z1[3]),"+v"(z1[4]),"+v"(z1[5]),"+v"(z1[6]),"+v"(bsr) :: "memory");
    const float bs=bflo(bsr);
    signal(smb+C_FREE,lane);
    u2 o[7];
    #pragma unroll
    for(int n=0;n<7;n++)
      #pragma unroll
      for(int e=0;e<2;e++)o[n][e]=pkbf2(unpk(z1[n][e])*(f2{acc[0][n][2*e],acc[0][n][2*e+1]}+bs));
    const u32 ho=OFF_HF+(u32)((rtB*4+w)*54+ft0)*512+(q>>1)*256+r*16;
    #pragma unroll
    for(int n=0;n<6;n+=2){ auto sx=__builtin_amdgcn_permlane16_swap(o[n][0],o[n+1][0],false,false); auto sy=__builtin_amdgcn_permlane16_swap(o[n][1],o[n+1][1],false,false);
      if(ct<7||n>=2) st16(rs,ho+(n+(q&1))*512,u4{sx[0],sy[0],sx[1],sy[1]}); }
    st8(rs,ho+6*512+(q&1)*8,o[6]);
    STAMP(11,0);
    WAITV(0);
    if(arrive(smb+C_H,lane)==3u && lane==0) st4(rs,OFF_FH+(rtB*8+ct)*128,tag);
    STAMP(12,0);
    __builtin_amdgcn_s_setprio(0);
  }
  { const int r=lane&15, q=lane>>4, m=w&3, kh=w>>2;
    u2 xv[5]={};
    if(kh){
      #pragma unroll
      for(int n=0;n<5;n++) xv[n]=*(const u2*)(p.x+(rtB*64+m*16+r)*640+80*ct+16*n+4*q);
    }
    f4 acc[5]={}; u4 W[3][5];
    const u32 wl2=smb+lane*16+RB-40960+kh*14*5120, wl0=kh? wl2 : smb+lane*16, wl1=kh? wl2 : smb+lane*16+CVS-20480;
    #define WAD(k,n) (((k)<4? wl0 : (k)<8? wl1 : wl2)+((k)*5+(n))*1024)
    auto addax=[&](){
      #pragma unroll
      for(int n=0;n<5;n++){ u2 av=ldstr(smb+ATL+(16*n+4*q+(r>>2))*128+(16*m+4*(r&3))*2); acc[n]+=f4{bflo(av[0]),bfhi(av[0]),bflo(av[1]),bfhi(av[1])}+f4{bflo(xv[n][0]),bfhi(xv[n][0]),bflo(xv[n][1]),bfhi(xv[n][1])}; } };
    if(w==6 && adone){ WAITV(0); signal(smb+C_AT,lane); }
    u32 oo[3],to[3]; const u32 tb=smb+CVS+m*3072;
    #pragma unroll
    for(int i=0;i<3;i++){ const int it=lane+64*i, rr=(it*205)>>11, cc=it-rr*10; oo[i]=opq((rtB*64+m*16+rr)*1280+ct*160+cc*16); to[i]=opq(tb+rr*176+cc*16); }
    if(m==0){ ldswait(smb+C_H,4u); pollw(rs,OFF_FH+(rtB*8+kh*4+(lane&3))*128,tag); signal(smb+C_TAG+kh*4,lane); } else ldswait(smb+C_TAG+kh*4,1u);
    STAMP(16,0);
    u4 hf[14];
    const u32 ho=OFF_HF+(u32)((rtB*4+m)*27+kh*14)*1024+lane*16;
    #pragma unroll
    for(int k=0;k<HPF;k++) hf[k]=ld16(rs,ho+k*1024);
    const bool early=kh && uni(ldsv(smb+C_AT))!=0u;
    if(early) addax();
    ldswait(smb+C_WR,4u);
    STAMP(17,0);
    const u32 wl3=kh? wl2-5120 : wl2;
    #pragma unroll
    for(int n=0;n<10;n++) W[n/5][n%5]=ldsr128(WAD(n/5,n%5));
    #pragma unroll
    for(int k=0;k<13;k++){
      if(k+HPF<14) hf[k+HPF]=ld16(rs,ho+((k+HPF==13&&kh)?12:k+HPF)*1024);
      SCHED();
      #pragma unroll
      for(int n=0;n<5;n++){ acc[n]=mfma16(W[k%3][n],hf[k],acc[n]); SCHED();
        if(k<11) W[(k+2)%3][n]=ldsr128(WAD(k+2,n)); else if(k==11) W[1][n]=ldsr128(wl3+(65+n)*1024);
        SCHED(); }
      if(k==5 && !kh) signal(smb+C_G1,lane);
    }
    const u32 pb=smb+m*5120+lane*16;
    if(!kh){
      #pragma unroll
      for(int n=0;n<5;n++) acc[n]=mfma16(W[1][n],hf[13],acc[n]);
      STAMP(18,0);
      ldswait(smb+C_G1,4u);
      #pragma unroll
      for(int n=0;n<5;n++) *(LAS f4*)(pb+n*1024)=acc[n];
      signal(smb+C_P+m*4,lane);
    } else {
      if(w==6 && !adone){ pollw(rs,fao,tag); aload(); WAITV(0); signal(smb+C_AT,lane); }
      if(!early){ ldswait(smb+C_AT,1u); addax(); }
      RST(7);
      ldswait(smb+C_P+m*4,1u);
      #pragma unroll
      for(int n=0;n<5;n++){ const f4 o=acc[n]+*(LAS f4*)(pb+n*1024); *(LAS u2*)(tb+r*176+n*32+q*8)=u2{pkbf(o[0],o[1]),pkbf(o[2],o[3])}; }
      #pragma unroll
      for(int i=0;i<3;i++) if(i<2||lane<32) *(u4*)((char*)p.out+oo[i])=ldsr128(to[i]);
      STAMP(19,4);
    }
  }
}
extern "C" void kernel_launch(void* const* d_in,const int* in_sizes,int n_in,void* d_out,int out_size,void* d_ws,size_t ws_size,hipStream_t stream){
  Params p;
  p.x=(const u16*)d_in[0]; p.lar=(const u16*)d_in[1]; p.aim=(const u16*)d_in[2]; p.cre=(const u16*)d_in[3]; p.cim=(const u16*)d_in[4];
  p.ldt=(const u16*)d_in[5]; p.dsk=(const u16*)d_in[6]; p.win=(const u16*)d_in[8]; p.wsp=(const u16*)d_in[9]; p.bsp=(const u16*)d_in[10]; p.wout=(const u16*)d_in[11];
  p.out=(u16*)d_out; p.ws=(char*)d_ws;
  (void)hipFuncSetAttribute((const void*)fused,hipFuncAttributeMaxDynamicSharedMemorySize,LDS_TOTAL);
  fused<<<256,512,LDS_TOTAL,stream>>>(p);
}
```

```cpp
#define DIAG 99
#include <hip/hip_runtime.h>
#include <utility>
#pragma clang diagnostic ignored "-Wint-to-pointer-cast"
typedef unsigned short u16;
typedef unsigned int u32;
typedef unsigned long long u64;
typedef __attribute__((ext_vector_type(8))) __bf16 bf8;
typedef __attribute__((ext_vector_type(8))) _Float16 hf8;
typedef __attribute__((ext_vector_type(2))) __bf16 bf2;
typedef __attribute__((ext_vector_type(2))) _Float16 hf2;
typedef __attribute__((ext_vector_type(2))) float f2;
typedef __attribute__((ext_vector_type(4))) float f4;
typedef __attribute__((ext_vector_type(16))) float f16v;
typedef __attribute__((ext_vector_type(4))) u32 u4;
typedef __attribute__((ext_vector_type(2))) u32 u2;
typedef __attribute__((ext_vector_type(4))) short s4;
#define LAS __attribute__((address_space(3)))
#define GAS __attribute__((address_space(1)))
#define DI __device__ __forceinline__

constexpr u32 OFF_Z2F = 0;
constexpr u32 OFF_Z1T = OFF_Z2F + 64*54*1024;
constexpr u32 OFF_XNT = OFF_Z1T + 128*27*1024;
constexpr u32 OFF_WOF = OFF_XNT + 640*4096;
constexpr u32 OFF_HF  = OFF_WOF + 8*27*5*1024;
constexpr u32 OFF_AT  = OFF_HF + 128*27*1024;
constexpr u32 OFF_ST  = OFF_AT + 640*4096;
constexpr u32 OFF_RS  = OFF_ST + 16*14*2048+8192;
constexpr u32 OFF_EP  = OFF_RS + 2048*16;
constexpr u32 OFF_F2  = OFF_EP + 256;
constexpr u32 OFF_F1  = OFF_F2 + 256*128;
constexpr u32 OFF_FX  = OFF_F1 + 256*128;
constexpr u32 OFF_FW  = OFF_FX + 256*128;
constexpr u32 OFF_FH  = OFF_FW + 32*128;
constexpr u32 OFF_FA  = OFF_FH + 256*128;
constexpr u32 WS_END  = OFF_FA + 256*128;

struct Params {
  const u16 *x, *lar, *aim, *cre, *cim, *ldt, *dsk, *win, *wsp, *bsp, *wout;
  u16* out; char* ws;
};
#define STAMP(id,wv) do{ if((id)==DIAG) dg1=__builtin_amdgcn_s_memrealtime(); }while(0)
#define RST(i) do{ if((i)+20==DIAG) dg1=__builtin_amdgcn_s_memrealtime(); }while(0)
template<class F,int... I> DI void sfor_impl(F&& f, std::integer_sequence<int,I...>){ (f(std::integral_constant<int,I>{}),...); }
template<int N,class F> DI void sfor(F&& f){ sfor_impl(f, std::make_integer_sequence<int,N>{}); }

DI float bflo(u32 v){ return __uint_as_float(v<<16); }
DI float bfhi(u32 v){ return __uint_as_float(v&0xffff0000u); }
DI float bf1(u16 v){ return __uint_as_float(((u32)v)<<16); }
DI u32 pkbf(float a,float b){ f2 v={a,b}; return __builtin_bit_cast(u32,__builtin_convertvector(v,bf2)); }
DI u32 pkhf(float a,float b){ f2 v={a,b}; return __builtin_bit_cast(u32,__builtin_convertvector(v,hf2)); }
DI float rbf(float a){ return bflo(pkbf(a,a)); }
DI float hlo(u32 v){ return (float)__builtin_bit_cast(hf2,v)[0]; }
DI float hhi(u32 v){ return (float)__builtin_bit_cast(hf2,v)[1]; }
DI float dot2(u32 a,u32 b,float c){ return __builtin_amdgcn_fdot2_f32_bf16(__builtin_bit_cast(bf2,a),__builtin_bit_cast(bf2,b),c,false); }
DI f4 mfma16(u4 a,u4 b,f4 c){ return __builtin_amdgcn_mfma_f32_16x16x32_bf16(__builtin_bit_cast(bf8,a),__builtin_bit_cast(bf8,b),c,0,0,0); }
DI f16v mfma32h(u4 a,u4 b,f16v c){ return __builtin_amdgcn_mfma_f32_32x32x16_f16(__builtin_bit_cast(hf8,a),__builtin_bit_cast(hf8,b),c,0,0,0); }
DI u4 ldsr128(u32 a){ return *(LAS u4*)a; }
DI u2 ldsr64(u32 a){ return *(LAS u2*)a; }
DI void ldsw128(u32 a,u4 v){ *(LAS u4*)a=v; }
DI u2 ldstr(u32 a){ return __builtin_bit_cast(u2,__builtin_amdgcn_ds_read_tr16_b64_v4i16((LAS s4*)a)); }
DI u32 uni(u32 v){ return __builtin_amdgcn_readfirstlane(v); }

template<int OFF> DI void gload(u4& dst,u32 voff,const char* base){ asm volatile("global_load_dwordx4 %0, %1, %2 offset:%3 sc1" : "=v"(dst) : "v"(voff), "s"(base), "n"(OFF)); }
template<int OFF> DI void gload_t(u4& dst,u32 voff,const char* base){ asm volatile("global_load_dwordx4 %0, %1, %2 offset:%3 sc1" : "+v"(dst) : "v"(voff), "s"(base), "n"(OFF)); }
DI void mfma4(f4& c0,f4& c1,f4& c2,f4& c3,const u4& a,const u4 (&b)[4]){
  asm volatile("v_mfma_f32_16x16x32_bf16 %0, %4, %5, %0\n\tv_mfma_f32_16x16x32_bf16 %1, %4, %6, %1\n\tv_mfma_f32_16x16x32_bf16 %2, %4, %7, %2\n\tv_mfma_f32_16x16x32_bf16 %3, %4, %8, %3"
    : "+v"(c0),"+v"(c1),"+v"(c2),"+v"(c3) : "v"(a),"v"(b[0]),"v"(b[1]),"v"(b[2]),"v"(b[3])); }
template<int CNT> DI void waitB(u4 (&B)[7]){ asm volatile("s_waitcnt vmcnt(%7)" : "+v"(B[0]),"+v"(B[1]),"+v"(B[2]),"+v"(B[3]),"+v"(B[4]),"+v"(B[5]),"+v"(B[6]) : "n"(CNT) : "memory"); }
DI void waitA(u4 (&A)[4]){ asm volatile("s_waitcnt lgkmcnt(0)" : "+v"(A[0]),"+v"(A[1]),"+v"(A[2]),"+v"(A[3]) :: "memory"); }
#define WAITV(n) asm volatile("s_waitcnt vmcnt(%0)" :: "n"(n) : "memory")
#define WAITL0() asm volatile("s_waitcnt lgkmcnt(0)" ::: "memory")
#define SCHED() __builtin_amdgcn_sched_barrier(0)
DI void bar(){ asm volatile("s_waitcnt lgkmcnt(0)\n\ts_barrier" ::: "memory"); }
DI f2 unpk(u32 v){ return f2{bflo(v),bfhi(v)}; }
DI u32 pkbf2(f2 v){ return __builtin_bit_cast(u32,__builtin_convertvector(v,bf2)); }
DI u32 pkhf2(f2 v){ return __builtin_bit_cast(u32,__builtin_convertvector(v,hf2)); }
DI f2 fma2(f2 a,f2 b,f2 c){ return __builtin_elementwise_fma(a,b,c); }
DI f2 gelu2(f2 x){
  f2 u=x*fma2(x*x,f2{-0.10294324f,-0.10294324f},f2{-2.30220820f,-2.30220820f});
  f2 d=f2{__builtin_amdgcn_exp2f(u[0]),__builtin_amdgcn_exp2f(u[1])}+f2{1.f,1.f};
  return x*f2{__builtin_amdgcn_rcpf(d[0]),__builtin_amdgcn_rcpf(d[1])};
}
DI void ln_fin(float mean,float var,float& m,float& rinv){
  m=rbf(mean); float v=rbf(__builtin_fmaxf(var,0.f));
  float s=rbf(__builtin_amdgcn_sqrtf(rbf(v+1e-5f))); float r=__builtin_amdgcn_rcpf(s); rinv=__builtin_fmaf(__builtin_fmaf(-s,r,1.f),r,r);
}
typedef __amdgpu_buffer_rsrc_t rsrc_t;
DI void st16(rsrc_t rs,u32 off,u4 v){ __builtin_amdgcn_raw_buffer_store_b128(v,rs,off,0,16); }
DI void st8(rsrc_t rs,u32 off,u2 v){ __builtin_amdgcn_raw_buffer_store_b64(v,rs,off,0,16); }
DI void st4(rsrc_t rs,u32 off,u32 v){ __builtin_amdgcn_raw_buffer_store_b32(v,rs,off,0,16); }
DI u4 ld16(rsrc_t rs,u32 off){ return __builtin_amdgcn_raw_buffer_load_b128(rs,off,0,16); }
DI u2 ld8(rsrc_t rs,u32 off){ return __builtin_amdgcn_raw_buffer_load_b64(rs,off,0,16); }
DI u32 ld4(rsrc_t rs,u32 off){ return __builtin_amdgcn_raw_buffer_load_b32(rs,off,0,16); }
constexpr int SPIN=1<<17;
template<int SL=2> DI void pollw(rsrc_t rs,u32 off,u32 tag){
  #pragma unroll 1
  for(int s=0;s<SPIN;s++){ u32 v=ld4(rs,off); asm volatile("" ::: "memory"); if(__all(v==tag)) break; __builtin_amdgcn_s_sleep(SL); }
}

DI f16v mfma32b(u4 a,u4 b,f16v c){ return __builtin_amdgcn_mfma_f32_32x32x16_bf16(__builtin_bit_cast(bf8,a),__builtin_bit_cast(bf8,b),c,0,0,0); }
DI u4 mksrd(const void* b){ u64 a=(u64)b; return u4{(u32)a,(u32)(a>>32)&0xffffu,0xffffffffu,0x00020000u}; }
template<int IMM> DI void setm0(u32 base){ asm volatile("s_add_u32 m0, %0, %1\n\ts_nop 0" :: "s"(base), "n"(IMM) : "scc","memory"); }
template<int OFF> DI void dma(u4 srd,u32 voff,u32 soff){ asm volatile("buffer_load_dwordx4 %0, %1, %2 offen offset:%3 sc1 lds" :: "v"(voff), "s"(srd), "s"(soff), "n"(OFF) : "memory"); }
DI void touch(u4 srd,u32 voff,u32 soff){ asm volatile("buffer_load_dword %0, %1, %2 offen sc1 lds" :: "v"(voff), "s"(srd), "s"(soff) : "memory"); }
template<int OFF> DI void gload8(u2& dst,u32 voff,const char* base){ asm volatile("global_load_dwordx2 %0, %1, %2 offset:%3 sc1" : "=v"(dst) : "v"(voff), "s"(base), "n"(OFF)); }
DI void gload2(u32& dst,u32 voff,const char* base){ asm volatile("global_load_ushort %0, %1, %2" : "=v"(dst) : "v"(voff), "s"(base)); }
DI u32 arrive(u32 a,int){ u32 o; asm volatile("s_mov_b64 exec, 1\n\tds_add_rtn_u32 %0, %1, %2\n\ts_mov_b64 exec, -1\n\ts_waitcnt lgkmcnt(0)" : "=&v"(o) : "v"(a), "v"(1u) : "memory"); return uni(o); }
DI void ldswait(u32 a,u32 n){ while(__hip_atomic_load((LAS u32*)a,__ATOMIC_RELAXED,__HIP_MEMORY_SCOPE_WORKGROUP)<n) __builtin_amdgcn_s_sleep(1); asm volatile("" ::: "memory"); }
DI float addf(float a,float b){ float o; asm("v_add_f32 %0, %1, %2" : "=v"(o) : "v"(a), "v"(b)); return o; }
DI void tie1(u32& g){ asm volatile("" : "+v"(g) :: "memory"); }
DI u32 opq(u32 v){ asm volatile("" : "+v"(v)); return v; }

constexpr u32 STG=32768;
constexpr u32 RING=24576, RB=62848, CONV=0, KP=4192, CVS=4*KP+4160, UOF=4*KP, MISC=RB+4*RING, JNK=MISC+2048, PM=163840-128, LDS_TOTAL=163840;
constexpr u32 ATL=2*CVS;
constexpr u32 C_FULL=PM, C_G1=PM+4, C_Z2=PM+8, C_Z1=PM+12, C_RED=PM+16, C_H=PM+20, C_FREE=PM+24, C_CV=PM+28, C_Z1R=PM+32, C_TAG=PM+40, C_KP=PM+48, C_DONE=PM+64, C_ZQ=PM+68, C_CS=PM+72, C_K2=PM+80, C_P=PM+96, C_AT=PM+112, C_WR=PM+116;
constexpr u32 CODE_BYTES=36864;
#define LA 3
#define TC_S 7
#define G2PRIO 2
#define XMAX 9
#define GS_K 35
#define WNT ""
#define TS1 6
#define HPRIO 3
#define CPRIO 3
#define POLLM 1
#define HPF 5
#define CVSKIP 0
#define BROT 0
#define PDLY 8
#define RS_S 7
#define KROT ((j+3*(rt&3))%10)
DI void signal(u32 a,int){ asm volatile("s_mov_b64 exec, 1\n\tds_add_u32 %0, %1\n\ts_mov_b64 exec, -1" :: "v"(a), "v"(1u) : "memory"); }
template<int OFF> DI void gload4(u32& dst,u32 voff,const char* base){ asm volatile("global_load_dword %0, %1, %2 offset:%3 sc1" : "=v"(dst) : "v"(voff), "s"(base), "n"(OFF)); }
template<int CNT> DI void waitG(u4& g){ asm volatile("s_waitcnt vmcnt(%1)" : "+v"(g) : "n"(CNT) : "memory"); }

DI f2 cmulp(f2 a,f2 b,f2 bs){ return fma2(f2{a[1],a[1]},bs,f2{a[0],a[0]}*b); }
DI float fmaf1(float a,float b,float c){ float o; asm("v_fma_f32 %0, %1, %2, %3" : "=v"(o) : "v"(a),"v"(b),"v"(c)); return o; }
DI u32 ldsv(u32 a){ return __hip_atomic_load((LAS u32*)a,__ATOMIC_RELAXED,__HIP_MEMORY_SCOPE_WORKGROUP); }
DI void ldswait4(u32 a,int lane,u32 n){ while(__any(ldsv(a+(lane&3)*4)<n)) __builtin_amdgcn_s_sleep(1); asm volatile("" ::: "memory"); }
DI u32 gettag(u32 a){ ldswait(a+4,1u); return uni(ldsv(a)); }
DI f2 cpow_dir(float lr,float rv,float pw){ float e=__builtin_amdgcn_exp2f(lr*pw); float f=__builtin_amdgcn_fractf(rv*pw);
  return f2{e*__builtin_amdgcn_cosf(f),e*__builtin_amdgcn_sinf(f)}; }
DI u32 dup(u32 a){ return __builtin_amdgcn_perm(a,a,0x01000100u); }
DI u32 cat(u32 lo,u32 hi){ return __builtin_amdgcn_perm(hi,lo,0x05040100u); }
DI u32 lds16(u32 a){ return *(LAS u16*)a; }

template<class CB> DI float kgen_tab(u32 cb,int lane,const u32 (&pr)[6],float (&kp)[11],CB&& mid){
  const float dt=rbf(__expf(bflo(pr[4])));
  const float cr[2]={bflo(pr[2]),bfhi(pr[2])}, cm[2]={bflo(pr[3]),bfhi(pr[3])};
  float mx=fmaxf(fmaxf(fabsf(cr[0]),fabsf(cm[0])),fmaxf(fabsf(cr[1]),fabsf(cm[1])));
  u32 eb=__float_as_uint(mx)&0x7f800000u;
  #define DPPMAX(c) eb=max(eb,(u32)__builtin_amdgcn_update_dpp(0,(int)eb,c,0xf,0xf,false))
  DPPMAX(0x128); DPPMAX(0x124); DPPMAX(0x122); DPPMAX(0x121);
  eb=max(max((u32)__builtin_amdgcn_readlane(eb,0),(u32)__builtin_amdgcn_readlane(eb,16)),max((u32)__builtin_amdgcn_readlane(eb,32),(u32)__builtin_amdgcn_readlane(eb,48))); eb=eb<0x01000000u?0x3f800000u:(eb>0x7e000000u?0x7e000000u:eb);
  const float ksc=2.f*dt*__uint_as_float(eb), isc=__uint_as_float(0x7f000000u-eb);
  const int r=lane&31,h=lane>>5;
  const u32 TP=272, TA=cb, TB=cb+22*TP;
  f16v acc={};
  #pragma unroll 1
  for(int c=0;c<2;c++){
    if(c) mid();
    float l_a=c?bfhi(pr[0]):bflo(pr[0]), i_m=c?bfhi(pr[1]):bflo(pr[1]), c_r=c?cr[1]:cr[0], c_i=c?cm[1]:cm[0];
    const float lr=(-rbf(__expf(l_a))*dt)*1.44269504f, rv=(i_m*dt)*0.15915494f;
    const f2 z1=cpow_dir(lr,rv,1.f), z8=cpow_dir(lr,-rv,8.f), zs=cpow_dir(lr,-rv,16.f), zk=cpow_dir(lr,rv,256.f);
    const f2 z1s={-z1[1],z1[0]}, zss={-zs[1],zs[0]}, zks={-zk[1],zk[0]};
    const u32 wa=TA+lane*4, wb=TB+lane*4;
    #define PUT(base,row,v) do{ *(LAS u32*)((base)+(row)*TP)=pkhf2(v); }while(0)
    f2 e={1.f,0.f}, o=z8;
    #pragma unroll
    for(int k=0;k<16;k++){ PUT(wb,2*k,e); PUT(wb,2*k+1,o); if(k<15){ e=k?cmulp(e,zs,zss):zs; o=cmulp(o,zs,zss);} }
    f2 b0={c_r*isc,c_i*isc}; PUT(wa,0,b0);
    f2 t=cmulp(b0,zk,zks); PUT(wa,8,t);
    f2 b1=cmulp(b0,z1,z1s); PUT(wa,1,b1); t=cmulp(t,zk,zks); PUT(wa,10,t);
    f2 b2=cmulp(b1,z1,z1s); PUT(wa,2,b2); t=cmulp(t,zk,zks); PUT(wa,11,t);
    f2 b3=cmulp(b2,z1,z1s); PUT(wa,3,b3); t=cmulp(t,zk,zks); PUT(wa,16,t);
    f2 b4=cmulp(b3,z1,z1s); PUT(wa,4,b4); t=cmulp(t,zk,zks); PUT(wa,17,t);
    f2 b5=cmulp(b4,z1,z1s); PUT(wa,5,b5); t=cmulp(t,zk,zks); PUT(wa,20,t);
    f2 b6=cmulp(b5,z1,z1s); PUT(wa,6,b6); t=cmulp(t,zk,zks); PUT(wa,21,t);
    f2 b7=cmulp(b6,z1,z1s); PUT(wa,7,b7); t=cmulp(t,zk,zks); PUT(wa,18,t);
    f2 s=cmulp(b4,zk,zks); PUT(wa,12,s); t=cmulp(b2,zk,zks); PUT(wa,9,t);
    s=cmulp(s,zk,zks); PUT(wa,14,s); t=cmulp(b6,zk,zks); PUT(wa,13,t);
    s=cmulp(s,zk,zks); PUT(wa,15,s);
    #undef PUT
    WAITL0();
    const int ra=(r>21||r==19)?0:r;
    #pragma unroll
    for(int ks=0;ks<8;ks++){ u4 fa=ldsr128(TA+ra*TP+ks*32+h*16), fb=ldsr128(TB+r*TP+ks*32+h*16); acc=mfma32h(fa,fb,acc); }
    WAITL0();
  }
  #pragma unroll
  for(int i=0;i<11;i++) kp[i]=acc[i];
  return ksc;
}
DI void kgen_exp(u32 cb,int lane,const float (&acc)[11],const u32 (&ga)[11]){
  const int r=lane&31,h=lane>>5;
  { const u32 a4=cb+r*16+h*8, a5=cb+r*16+h*1024;
    #define PK(addr,base,reg) do{ *(LAS _Float16*)((addr)+2*(base))=(_Float16)acc[reg]; }while(0)
    PK(a4,0,0);PK(a4,1,1);PK(a4,2,2);PK(a4,3,3); PK(a4,256,4);PK(a4,258,5);PK(a4,512,6);PK(a4,768,7);
    PK(a5,1024,8);PK(a5,1280,9);PK(a5,2048,10);
    #undef PK
  }
  WAITL0();
  { const u32 g0=cb+2*(2048-8*lane);
    u32 a0=lds16(g0), b0=lds16(g0-16), a1=lds16(g0-1024), b1=lds16(g0-1024-16);
    u32 f2_=lds16(g0-2048+(lane?8:0)), a2=lds16(g0-2048), m2=lds16(g0-2048-8), b2=lds16(g0-2048-16);
    u32 e[11];
    #pragma unroll
    for(int j=0;j<11;j++) e[j]=lds16(ga[j]);
    u32 k01=*(LAS u32*)cb, k23=*(LAS u32*)(cb+4);
    WAITL0();
    const u32 wo=cb+lane*16;
    { u32 aa=dup(a0),bb=dup(b0),c=(lane&1)?b0:a0, cbv=cat(c,b0), ac=cat(a0,c);
      ldsw128(wo,u4{aa,aa,cbv,bb}); ldsw128(wo+KP,u4{aa,aa,ac,bb}); ldsw128(wo+2*KP,u4{aa,aa,aa,cbv}); ldsw128(wo+3*KP,u4{aa,aa,aa,ac}); }
    { u32 aa=dup(a1),bb=dup(b1),c=(lane&1)?b1:a1, cbv=cat(c,b1), ac=cat(a1,c);
      ldsw128(wo+1024,u4{aa,aa,cbv,bb}); ldsw128(wo+1024+KP,u4{aa,aa,ac,bb}); ldsw128(wo+1024+2*KP,u4{aa,aa,aa,cbv}); ldsw128(wo+1024+3*KP,u4{aa,aa,aa,ac}); }
    { u32 aa=dup(a2),mm=dup(m2),bb=dup(b2),am=cat(a2,m2),fa=cat(f2_,a2),mb=cat(m2,b2);
      ldsw128(wo+2048,u4{aa,am,mm,bb}); ldsw128(wo+2048+KP,u4{aa,aa,mm,mb}); ldsw128(wo+2048+2*KP,u4{aa,aa,am,mm}); ldsw128(wo+2048+3*KP,u4{fa,aa,aa,mm}); }
    { u32 o0=cat(e[0],e[1]),o1=cat(e[2],e[3]),o2=cat(e[4],e[5]),o3=cat(e[6],e[7]),o4=cat(e[8],e[9]);
      u32 p1=cat(e[1],e[2]),d0=cat(e[3],e[4]),d1=cat(e[5],e[6]),d2=cat(e[7],e[8]),d3=cat(e[9],e[10]);
      ldsw128(wo+3072,u4{d0,d1,d2,d3}); ldsw128(wo+3072+KP,u4{o1,o2,o3,o4}); ldsw128(wo+3072+2*KP,u4{p1,d0,d1,d2}); ldsw128(wo+3072+3*KP,u4{o0,o1,o2,o3}); }
    if(lane<4){ if(lane){k01=0;k23=0;}
      u32 d0=k01&0xffffu, o1=__builtin_amdgcn_alignbit(k01,k01,16), o0=__builtin_amdgcn_alignbit(k23,k23,16), p1=(k23&0xffffu)|(k01&0xffff0000u);
      ldsw128(wo+4096,u4{d0,0,0,0}); ldsw128(wo+4096+KP,u4{o1,0,0,0}); ldsw128(wo+4096+2*KP,u4{p1,d0,0,0}); ldsw128(wo+4096+3*KP,u4{o0,o1,0,0}); }
  }
}
DI f4 mfma16h(u4 a,u4 b,f4 c){ return __builtin_amdgcn_mfma_f32_16x16x32_f16(__builtin_bit_cast(hf8,a),__builtin_bit_cast(hf8,b),c,0,0,0); }
#define RDA(S,i,m) asm volatile("ds_read2_b64 %0, %1 offset0:%2 offset1:%3" : "=v"(AF[S][i][m]) : "v"((i)<2?kb:ka), "n"((1-((i)&1))*128+(1-(m))*4), "n"((1-((i)&1))*128+(1-(m))*4+1))
#define RDB(S,i,ad,o) asm volatile("ds_read_b128 %0, %1 offset:%2" : "=v"(BF[S][i]) : "v"(ad), "n"(o))
#define TIEA(S,i) "+v"(AF[S][i][0]),"+v"(AF[S][i][1])
#define CV_M(S,i,n) do{ y[0][n]=mfma16h(AF[S][i][0],BF[S][(n)-(i)],y[0][n]); y[1][n]=mfma16h(AF[S][i][1],BF[S][(n)-(i)],y[1][n]); SCHED(); }while(0)
#define CV_U0() u32 u0=u1-256; u0=((int)u0<(int)uz)?uz:u0; const u32 kb=ka+2048; (void)kb
#define CV_SETUP() const int r=lane&15,q=lane>>4; u32 ka=cb+(r&3)*KP+16*q-8*(r>>2)+992; const u32 uz=cb+UOF+q*1040; u32 u1=uz+(r+17)*16; u4 AF[2][4][2]; u4 BF[2][4]
DI void conv_help(u32 cb,int lane){
  CV_SETUP(); f4 y[2][4]={};
  #define HP_WT(S) asm volatile("s_waitcnt lgkmcnt(0)" : TIEA(S,0),TIEA(S,1),TIEA(S,2),TIEA(S,3),"+v"(BF[S][0]),"+v"(BF[S][1]),"+v"(BF[S][2]),"+v"(BF[S][3]))
  #define HP_AD() const u32 kb=ka-64, u3=u1-16; u32 u0=u1-256, u2_=u1-272; u0=((int)u0<(int)uz)?uz:u0; u2_=((int)u2_<(int)uz)?uz:u2_
  #define HP_M(S,i,bi,n) do{ y[0][n]=mfma16h(AF[S][i][0],BF[S][bi],y[0][n]); y[1][n]=mfma16h(AF[S][i][1],BF[S][bi],y[1][n]); SCHED(); }while(0)
  #define HP_STEP(C,N) do{ HP_AD(); RDA(N,2,0); RDA(N,2,1); RDB(N,1,u1,0); RDA(N,3,0); SCHED(); HP_M(C,2,1,3); RDA(N,3,1); RDB(N,0,u0,0); RDA(N,0,0); RDA(N,0,1); SCHED(); HP_M(C,3,0,2); \
      RDB(N,3,u3,0); RDA(N,1,0); RDA(N,1,1); RDB(N,2,u2_,0); ka-=128; u1-=32; SCHED(); HP_M(C,0,3,3); HP_M(C,1,2,2); HP_WT(N); }while(0)
  { HP_AD(); RDA(0,2,0); RDA(0,2,1); RDB(0,1,u1,0); RDA(0,3,0); RDA(0,3,1); RDB(0,0,u0,0); RDA(0,0,0); RDA(0,0,1); RDB(0,3,u3,0); RDA(0,1,0); RDA(0,1,1); RDB(0,2,u2_,0); ka-=128; u1-=32; HP_WT(0); }
  #pragma unroll 1
  for(int dl=CVSKIP*3;dl<4;dl++){ HP_STEP(0,1); if(dl==3){ ka+=128; u1+=32; } HP_STEP(1,0); }
  y[0][3]+=y[0][2]; y[1][3]+=y[1][2];
  *(LAS f4*)(cb+q*KP+r*16)=y[0][3]; *(LAS f4*)(cb+q*KP+256+r*16)=y[1][3];
  WAITL0();
}
DI void conv_wave(const Params& p,rsrc_t rs,int d,u32 cb,u32 flag,int lane,float ksc,float dsk){
  CV_SETUP(); f4 y[2][4]={};
  #define CV_WT(S) asm volatile("s_waitcnt lgkmcnt(0)" : TIEA(S,0),TIEA(S,1),TIEA(S,2),"+v"(BF[S][0]),"+v"(BF[S][1]),"+v"(BF[S][2]),"+v"(BF[S][3]))
  #define CV_STEP(C,N) do{ CV_U0(); CV_M(C,0,0); RDA(N,0,0); RDB(N,0,u0,0); SCHED(); CV_M(C,0,1); RDA(N,0,1); RDB(N,1,u1,0); SCHED(); CV_M(C,0,2); RDA(N,1,0); RDB(N,2,u1,256); SCHED(); \
      CV_M(C,0,3); RDA(N,1,1); RDB(N,3,u1,512); SCHED(); CV_M(C,1,1); RDA(N,2,0); SCHED(); CV_M(C,1,2); RDA(N,2,1); ka-=64; u1-=16; SCHED(); CV_M(C,1,3); CV_M(C,2,2); CV_WT(N); }while(0)
  { CV_U0(); RDA(0,0,0); RDA(0,0,1); RDA(0,1,0); RDA(0,1,1); RDA(0,2,0); RDA(0,2,1); RDB(0,0,u0,0); RDB(0,1,u1,0); RDB(0,2,u1,256); RDB(0,3,u1,512); ka-=64; u1-=16; CV_WT(0); }
  #pragma unroll 1
  for(int dl=CVSKIP*7;dl<8;dl++){ CV_STEP(0,1); if(dl==7){ ka+=64; u1+=16; } CV_STEP(1,0); }
  ldswait(flag,2u);
  y[0][3]+=*(LAS f4*)(cb+q*KP+r*16); y[1][3]+=*(LAS f4*)(cb+q*KP+256+r*16);
  #pragma unroll
  for(int n=0;n<4;n++){ u2 o[2];
    #pragma unroll
    for(int m=0;m<2;m++){ u2 uv=ldsr64(cb+UOF+(2*m+(q>>1))*1040+(16*n+r+1)*16+8*(q&1));
      #pragma unroll
      for(int e=0;e<2;e++) o[m][e]=pkbf2(fma2(f2{hlo(uv[e]),hhi(uv[e])},f2{dsk,dsk},f2{y[m][n][2*e],y[m][n][2*e+1]}*ksc)); }
    auto sx=__builtin_amdgcn_permlane16_swap(o[0][0],o[1][0],false,false); auto sy=__builtin_amdgcn_permlane16_swap(o[0][1],o[1][1],false,false);
    const u32 sc=cb+1024+n*1280; ldsw128(sc+r*80+(q&1)*32+(q>>1)*16,u4{sx[0],sy[0],sx[1],sy[1]});
    st16(rs,OFF_AT+d*4096+n*1024+lane*16,ldsr128(sc+(lane>>2)*80+(lane&3)*16)); }
  WAITV(0);
}

struct Frag { u4 x[4], w[4][2]; };
__global__ __launch_bounds__(512,2) void fused(Params p){
  extern __shared__ __attribute__((aligned(16))) char sm[];
  const u32 smb=(u32)(size_t)(LAS char*)sm;
  u64 pc0; asm volatile("s_getpc_b64 %0" : "=s"(pc0));
  const int b=blockIdx.x, xc=b&7, idx=b>>3;
  const int tid=threadIdx.x, w=uni(tid>>6); int lane=tid&63;
  const rsrc_t rs=__builtin_amdgcn_make_buffer_rsrc(p.ws,0,WS_END,0x00020000);
  const u4 srdS=mksrd(p.ws);
  const int rtB=4*xc+(idx&3), ct=idx>>2;
  const bool hask=idx<28 && idx+28*(w&3)<80, hasc=hask && w>=4;
  const int dch=80*xc+(hask? idx+28*(w&3) : 0);
  const u32 cb=smb+CONV+(w&3)*CVS;
  const int rt=4*(xc>>1)+(idx&3), j=7*(xc&1)+(idx>>2), mi=w&3, g1=w>>2;
  u32 tag; float ksc=0.f, dsk=0.f;
  const u64 dg0=__builtin_amdgcn_s_memrealtime(); u64 dg1=dg0;
  auto pre01=[&](){
    const u4 sa=mksrd((const char*)p.wsp+(size_t)rtB*64*4096+mi*1024-4096); const u32 v0=(lane>>3)*4096+(((lane&7)^(lane>>4))<<4);
    #pragma unroll 1
    for(int i=0;i<4;i++){ setm0<RB+8192>(smb+mi*RING+i*4096); const u32 g=(i&1)*4+2*mi, k=(i>>1)*128+4096;
      dma<0>(sa,v0,(g&7)*32768+k); dma<1024>(sa,v0^64u,(g&7)*32768+32768-1024+k); dma<2048>(sa,v0,((g+2)&7)*32768-2048+k); dma<3072>(sa,v0^64u,((g+2)&7)*32768+32768-3072+k); } };
  auto warm=[&](int wq,int part){
    const u4 sp=mksrd((const char*)p.wsp+(size_t)(rtB*64+ct*8+2*wq)*4096); setm0<JNK>(smb);
    const u32 vo= part==2? lane*128 : ((lane>>2)&7)*1024+(lane&3)*128+part*512;
    asm volatile("buffer_load_dword %0, %1, 0 offen lds" :: "v"(vo), "s"(sp) : "memory");
  };
  if(idx>=28){
    const int a=xc*4+idx-28, r=lane&15, q=lane>>4;
    { u4 sc=mksrd((const void*)(pc0&~127ull)); sc[2]=CODE_BYTES; setm0<JNK>(smb); touch(sc,lane*128,w*8192); }
    const u32 ep=ld4(rs,OFF_EP);
    u4 xr[10]; const char* xs=(const char*)p.x+(size_t)(64*a+8*w+(lane>>3))*1280+(lane&7)*16;
    #pragma unroll
    for(int i=0;i<10;i++) xr[i]=*(const u4*)(xs+i*128);
    if(tid<32) *(LAS u32*)(smb+PM+tid*4)=0u;
    tag=uni(ep)+1u;
    float s0=0.f,s1=0.f,q0=0.f,q1=0.f;
    #pragma unroll
    for(int i=0;i<10;i++){ s0=dot2(xr[i][0],0x3f803f80u,s0); q0=dot2(xr[i][0],xr[i][0],q0); s1=dot2(xr[i][1],0x3f803f80u,s1); q1=dot2(xr[i][1],xr[i][1],q1);
      s0=dot2(xr[i][2],0x3f803f80u,s0); q0=dot2(xr[i][2],xr[i][2],q0); s1=dot2(xr[i][3],0x3f803f80u,s1); q1=dot2(xr[i][3],xr[i][3],q1); }
    s0+=s1; q0+=q1;
    #define DPPADD(v,c) v+=__int_as_float(__builtin_amdgcn_update_dpp(0,__float_as_int(v),c,0xf,0xf,false))
    DPPADD(s0,0xB1); DPPADD(q0,0xB1); DPPADD(s0,0x4E); DPPADD(q0,0x4E); DPPADD(s0,0x141); DPPADD(q0,0x141);
    float m1,ri; { float mean=s0*(1.f/640.f); ln_fin(mean,q0*(1.f/640.f)-mean*mean,m1,ri); }
    if((lane&7)==0) st16(rs,OFF_RS+(64*a+8*w+(lane>>3))*16,u4{__float_as_uint(m1),tag,__float_as_uint(ri),tag});
    STAMP(5,0);
    #pragma unroll
    for(int i=0;i<10;i++){ u4 o;
      #pragma unroll
      for(int e=0;e<4;e++) o[e]=pkhf2(fma2(unpk(xr[i][e]),f2{ri,ri},f2{-m1*ri,-m1*ri}));
      ldsw128(smb+RB+(8*w+(lane>>3))*1296+(lane&7)*16+i*128,o); }
    bar();
    #pragma unroll
    for(int i=0;i<10;i++){ const int c=w*10+i; const u32 ad=smb+RB+(32*((r>>1)&1)+8*q+(r>>2))*1296+c*16+(r&1)*8; u2 lo=ldstr(ad), hi=ldstr(ad+4*1296);
      const u32 sc=smb+w*5120+(i%5)*1024; ldsw128(sc+(r&7)*128+((((r>>3)*4+q)^(r&7))<<4),u4{lo[0],lo[1],hi[0],hi[1]});
      st16(rs,OFF_XNT+a*81920+c*1024+lane*16,ldsr128(sc+(lane>>3)*128+(((lane&7)^(lane>>3))<<4))); }
    WAITV(0);
    if(arrive(smb+C_Z1,lane)==7u && lane==0) st4(rs,OFF_FX+a*128,tag);
    STAMP(6,7);
    if(a<27){ const u4 srdO=mksrd((const char*)p.wout+a*(32*1280)+w*5120); const u32 mo=smb+w*5120; setm0<0>(mo);
      dma<0>(srdO,lane*16,0); dma<1024>(srdO,lane*16,0); dma<2048>(srdO,lane*16,0); dma<3072>(srdO,lane*16,0); setm0<4096>(mo); dma<0>(srdO,lane*16,4096); }
    WAITV(0); bar();
    if(a<27){
      #pragma unroll
      for(int i=0;i<5;i++){ u32 ad=smb+(8*q+(r>>2))*1280+(w*5+i)*32+(r&3)*8; u2 lo=ldstr(ad), hi=ldstr(ad+4*1280);
        st16(rs,OFF_WOF+(u32)((w*27+a)*5+i)*1024+lane*16,u4{lo[0],lo[1],hi[0],hi[1]}); } }
    WAITV(0); bar();
    if(tid==0) st4(rs,OFF_FW+a*128,tag);
    if(g1) warm(mi,2); else { pre01(); WAITV(0); }
    STAMP(31,0);
  } else {
    const int r=lane&31, h=lane>>5;
    const u4 srdX=mksrd((const char*)p.x+(size_t)rt*128*1280);
    const u4 srdW=mksrd((const char*)p.win+j*128-3072);
    const u32 mw=smb+w*4096;
    u32 vx0,vx1,vw1,vw2,epv;
    { u32 lr=lane>>3,c=lane&7; vx0=(16*w+lr)*1280+((c^(lr>>1))<<4); vx1=(vx0^64u)+9216;
      vw1=(8*w+lr)*3456+((c^((lr&2)<<1))<<4); vw2=vw1+1024+(j==13?0:14*128); }
    u32 kx[10]; { u32 k=KROT;
      #pragma unroll
      for(int i=0;i<10;i++){ kx[i]=k*128; k=k==9?0:k+1; } }
    auto issue1=[&](auto sc,int k){ constexpr int s=sc.value;
      if(k==0){ setm0<(s&3)*STG>(mw); dma<0>(srdX,vx0,kx[s]); } if(k==1) dma<1024>(srdX,vx1,kx[s]); if(k==2) dma<2048>(srdW,vw2,kx[s]*1728); if(k==3) dma<3072>(srdW,vw1,kx[s]*1728); };
    auto issue=[&](auto sc){ issue1(sc,0); issue1(sc,1); issue1(sc,2); issue1(sc,3); };
    issue(std::integral_constant<int,0>{});
    u32 pr[6];
    gload4<0>(epv,0u,p.ws+OFF_EP);
    gload4<0>(pr[0],lane*4,(const char*)(p.lar+dch*128)); gload4<0>(pr[1],lane*4,(const char*)(p.aim+dch*128)); gload4<0>(pr[2],lane*4,(const char*)(p.cre+dch*128)); gload4<0>(pr[3],lane*4,(const char*)(p.cim+dch*128));
    gload2(pr[4],0u,(const char*)(p.ldt+dch)); gload2(pr[5],0u,(const char*)(p.dsk+dch));
    asm volatile("s_barrier" ::: "memory");
    issue(std::integral_constant<int,1>{});
    if(tid<32) *(LAS u32*)(smb+PM+tid*4)=0u;
    u32 ax[2][4],aw[2][4][2];
    { u32 row=32*mi+r, a0=smb+(row>>4)*4096+(row&15)*128+((h^((r>>1)&7))<<4);
      u32 qq=(lane&15)>>2, w0=smb+2048+g1*1024+h*4096+qq*128+8*(lane&1)+(((2*((lane>>4)&1)+((lane>>1)&1))^((qq&2)<<1))<<4);
      #pragma unroll
      for(int i=0;i<2;i++)
        #pragma unroll
        for(int k=0;k<4;k++){ const u32 kk=(k+mi)&3, wo=i*2*STG; ax[i][k]=opq((a0^(kk*32u))+wo); aw[i][k][0]=opq(w0+kk*8192+wo); aw[i][k][1]=opq((w0^64u)+kk*8192+wo); } }
    u4 sel; { const u32 o=((lane&15)==((lane>>4)&1))?0x3f803f80u:0u; sel=u4{o,o,o,o}; asm volatile("" : "+v"(sel)); }
    u32 ga[11];
    #pragma unroll
    for(int i=0;i<11;i++) ga[i]=opq(cb+2u*(u32)rbf((float)(512-8*lane+3-i)));
    Frag F[2];
    asm volatile("s_waitcnt vmcnt(4)" : "+v"(epv),"+v"(pr[0]),"+v"(pr[1]),"+v"(pr[2]),"+v"(pr[3]),"+v"(pr[4]),"+v"(pr[5]) :: "memory");
    bar();
    tag=uni(epv)+1u; dsk=bflo(pr[5]);
    #pragma unroll
    for(int k=0;k<4;k++){
      #pragma unroll
      for(int n=0;n<2;n++){ u2 lo=ldstr(aw[0][k][n]), hi=ldstr(aw[0][k][n]+512); F[0].w[k][n]=u4{lo[0],lo[1],hi[0],hi[1]}; }
      F[0].x[k]=ldsr128(ax[0][k]); }
    issue(std::integral_constant<int,2>{});
    STAMP(1,0);
    f16v acc[2]={}; f4 cacc[2]={}; u4 G;
    const u32 go=OFF_RS+rt*2048+mi*512, cp=smb+MISC+g1*1024;
    sfor<10>([&](auto ic){
      constexpr int s=ic.value; Frag& cur=F[s&1]; Frag& nxt=F[(s&1)^1];
      if constexpr(s<9){ WAITV((s==8? 2 : s==7? 6 : 4)+((TS1<6&&(s==TS1+1||s==TS1+2))?1:0)); asm volatile("s_barrier" ::: "memory"); }
      auto rw=[&](int k,int n){ if constexpr(s<9){ const int i=((s+1)>>1)&1; const u32 so=((s+1)&1)*STG; u2 lo=ldstr(aw[i][k][n]+so), hi=ldstr(aw[i][k][n]+so+512); nxt.w[k][n]=u4{lo[0],lo[1],hi[0],hi[1]}; } };
      auto rx=[&](int k){ if constexpr(s<9){ const int i=((s+1)>>1)&1; const u32 so=((s+1)&1)*STG; nxt.x[k]=ldsr128(ax[i][k]+so); } };
      #pragma unroll
      for(int k=0;k<4;k++){
        acc[0]=mfma32b(cur.w[k][0],cur.x[k],acc[0]); SCHED();
        if constexpr(s+3<=9) issue1(std::integral_constant<int,s+3>{},k);
        rw(k,0); rx(k);
        SCHED();
        acc[1]=mfma32b(cur.w[k][1],cur.x[k],acc[1]); SCHED();
        rw(k,1);
        if(k==0){ cacc[0]=mfma16(cur.w[0][0],sel,cacc[0]); cacc[1]=mfma16(cur.w[0][1],sel,cacc[1]); }
        if constexpr(s==TS1 && TS1<6){ if(k==3){ if(g1) warm(mi,0); else { setm0<JNK>(smb); touch(srdS,0u,OFF_EP); } } }
        if constexpr(s==6){ if(k==3){ gload<0>(G,r*16,p.ws+go); if(g1) warm(mi,TS1<6?1:2); else { setm0<JNK>(smb); touch(srdS,0u,go); } } }
        if constexpr(s==8){ if(k==3) signal(smb+C_DONE,lane); }
        if constexpr(s==9){ if(k==1){
          if((lane&14)==0){ *(LAS f4*)(cp+mi*256+(lane&1)*64+(lane>>4)*16)=cacc[0]; *(LAS f4*)(cp+mi*256+128+(lane&1)*64+(lane>>4)*16)=cacc[1]; }
          signal(smb+C_CS+g1*4,lane); } }
        SCHED();
      }
    });
    RST(0);
    if(!g1) __builtin_amdgcn_s_setprio(2);
    asm volatile("s_waitcnt vmcnt(1)" : "+v"(G) :: "memory");
    ldswait(smb+C_CS+g1*4,4u);
    const float cs0=*(LAS float*)(cp+h*512+r*4)+*(LAS float*)(cp+h*512+256+r*4), cs1=*(LAS float*)(cp+h*512+128+r*4)+*(LAS float*)(cp+h*512+384+r*4);
    RST(2);
    #pragma unroll 1
    for(int sp=0;sp<SPIN && !__all(G[1]==tag&&G[3]==tag);sp++)asm volatile("global_load_dwordx4 %0, %1, %2 sc1\n\ts_waitcnt vmcnt(0)" : "=&v"(G) : "v"(r*16), "s"(p.ws+go) : "memory");
    RST(3);
    const float nm=-__uint_as_float(G[0]), ri1=__uint_as_float(G[2]);
    acc[0]=__builtin_amdgcn_mfma_f32_32x32x2f32(cs0,nm,acc[0],0,0,0); acc[1]=__builtin_amdgcn_mfma_f32_32x32x2f32(cs1,nm,acc[1],0,0,0);
    u32 zp[2][8]; const u32 zqa=smb+RB+mi*RING+4096+lane*16;
    if(g1){
      ldswait(smb+C_DONE,8u);
      #pragma unroll
      for(int i=0;i<4;i++){ u4 o;
        #pragma unroll
        for(int e=0;e<4;e++) o[e]=pkbf2(f2{acc[i>>1][8*(i&1)+2*e],acc[i>>1][8*(i&1)+2*e+1]}*ri1);
        ldsw128(zqa+i*1024,o); }
      signal(smb+C_K2+mi*4,lane); RST(4);
    } else {

      #pragma unroll
      for(int e=0;e<16;e++) zp[e>>3][e&7]=pkbf2(gelu2(f2{acc[e>>3][2*(e&7)],acc[e>>3][2*(e&7)+1]}*ri1));
      float S=0.f,Q=0.f;
      if(j<13){
        #pragma unroll
        for(int e=0;e<8;e++){ S=dot2(zp[0][e],0x3f803f80u,S); Q=dot2(zp[0][e],zp[0][e],Q); } }
      #pragma unroll
      for(int e=0;e<8;e++){ S=dot2(zp[1][e],0x3f803f80u,S); Q=dot2(zp[1][e],zp[1][e],Q); }
      #define XSUM32(v) do{ auto t_=__builtin_amdgcn_permlane32_swap(__float_as_uint(v),__float_as_uint(v),false,false); v=__uint_as_float(t_[0])+__uint_as_float(t_[1]); }while(0)
      XSUM32(S); XSUM32(Q);
      const u32 sto=OFF_ST+rt*(14*2048)+(32*mi+r)*16;
      if(h==0) st16(rs,sto+j*2048,u4{__float_as_uint(S),tag,__float_as_uint(Q),tag});
      RST(6);
    }
    float kp[11]; u4 g[7]={};
    const u32 gof=g1? OFF_FX+r*128 : OFF_ST+rt*(14*2048)+(32*mi+r)*16+h*(7*2048);
    auto samp=[&](){ asm volatile("" ::: "memory"); g[0]=ld16(rs,gof);
      if(!g1){
        #pragma unroll
        for(int i=1;i<7;i++) g[i]=ld16(rs,gof+i*2048); }
      asm volatile("" ::: "memory"); };
    if(hasc) ksc=kgen_tab(cb,lane,pr,kp,samp); else if(!g1){
      pre01(); ldswait(smb+C_K2+mi*4,1u); u4 zq[4];
      #pragma unroll
      for(int i=0;i<4;i++){ const u4 v=ldsr128(zqa+i*1024);
        #pragma unroll
        for(int e=0;e<4;e++) zq[i][e]=pkbf2(gelu2(unpk(v[e]))); }
      SCHED(); samp();
      #pragma unroll
      for(int n=0;n<2;n++) if(2*j+n<27){ const u32 oo=OFF_Z1T+((rt*8+2*mi+(r>>4))*27+2*j+n)*1024+(h*16+(r&15))*16;
        st16(rs,oo,u4{zq[2*n][0],zq[2*n][1],zq[2*n+1][0],zq[2*n+1][1]}); st16(rs,oo+512,u4{zq[2*n][2],zq[2*n][3],zq[2*n+1][2],zq[2*n+1][3]}); }
    }
    if(g1){
      if(w<7){ u4 uu[4]; const u32 fxv=g[0][0];
        if(hasc){
          RST(6);
          if(!__all(fxv==tag)) pollw<16>(rs,OFF_FX+r*128,tag);
          RST(7);
          #pragma unroll
          for(int i=0;i<4;i++) uu[i]=ld16(rs,OFF_XNT+((lane>>3)+8*i)*81920+dch*128+(lane&7)*16);
          asm volatile("" ::: "memory");
          kgen_exp(cb,lane,kp,ga);
          RST(8);
          #pragma unroll
          for(int i=0;i<4;i++){ const int x=lane+64*i; ldsw128(cb+UOF+(x&3)*1040+((x>>2)+1)*16,uu[i]); }
          if(lane<4) ldsw128(cb+UOF+lane*1040,u4{0,0,0,0});
          WAITL0(); signal(smb+C_KP+mi*4,lane);
        }
        RST(9);
      }
    } else {
      RST(1);
      float S,Q;
      #pragma unroll 1
      for(int sp=0;sp<SPIN;sp++){ bool ok=true;
        #pragma unroll
        for(int i=0;i<7;i++) ok=ok&&g[i][1]==tag&&g[i][3]==tag;
        if(__all(ok)) break;
        __builtin_amdgcn_s_sleep(4); samp(); }
      RST(7);
      S=0.f; Q=0.f;
      #pragma unroll
      for(int i=0;i<7;i++){ S+=__uint_as_float(g[i][0]); Q+=__uint_as_float(g[i][2]); }
      XSUM32(S); XSUM32(Q);
      float mean=S*(1.f/864.f), m2,r2; ln_fin(mean,Q*(1.f/864.f)-mean*mean,m2,r2); m2*=-r2;
      ldswait(smb+C_DONE,8u);
      const u32 tb=smb+RB+mi*RING, ad=tb+(8*(lane>>4)+((lane&15)>>2))*80+(lane&3)*8;
      const u32 zo=OFF_Z2F+((rt*4+mi)*54+(j==13?-2:2+4*j))*1024+lane*16;
      #pragma unroll
      for(int n=0;n<2;n++){
        #pragma unroll
        for(int g2=0;g2<4;g2++){ u2 o;
          #pragma unroll
          for(int e=0;e<2;e++) o[e]=pkbf2(fma2(unpk(zp[n][2*g2+e]),f2{r2,r2},f2{m2,m2}));
          *(LAS u2*)(tb+r*80+g2*16+h*8)=o; }
        u2 l0=ldstr(ad), h0=ldstr(ad+320), l1=ldstr(ad+32), h1=ldstr(ad+32+320);
        if(j<13||n){ st16(rs,zo+n*2048,u4{l0[0],l0[1],h0[0],h0[1]}); st16(rs,zo+n*2048+1024,u4{l1[0],l1[1],h1[0],h1[1]}); } }
      RST(8);
      WAITV(0);
      if(arrive(smb+C_Z2,lane)==3u && lane==0) st4(rs,OFF_F2+(rt*16+j)*128,tag);
      RST(9);
    }
  }
  const int ft0=ct<7?7*ct:47;
  bool adone=true; const u32 fao=OFF_FA+((lane<28?lane:0)*8+ct)*128;
  auto aload=[&](){ const u32 vo=(lane>>3)*4096+(lane&7)*16;
    #pragma unroll 1
    for(int i=0;i<10;i++){ setm0<ATL>(smb+i*1024); dma<0>(srdS,vo,OFF_AT+(u32)(80*ct+8*i)*4096+rtB*128); } };
  if(w>=4){
    if(hasc){
      __builtin_amdgcn_s_setprio(CPRIO);
      conv_wave(p,rs,dch,cb,smb+C_KP+mi*4,lane,ksc,dsk);
      __builtin_amdgcn_s_setprio(0);
      if(arrive(smb+C_CV,lane)==(idx<24?2u:1u) && lane==0) st4(rs,OFF_FA+b*128,tag);
    }
    if(w==7){
      __builtin_amdgcn_s_setprio(HPRIO);
      if(idx<28){
        #pragma unroll 1
        for(int c=0;c<(idx<24?3:2);c++){ ldswait(smb+C_KP+c*4,1u); conv_help(smb+c*CVS,lane); signal(smb+C_KP+c*4,lane); } }
      __builtin_amdgcn_s_setprio(0);
      RST(6);
    }
    RST(10);
    ldswait(smb+C_Z1R,4u);
    u4 wq[24];
    #pragma unroll
    for(int i=0;i<24;i++) wq[i]=ld16(rs,OFF_WOF+ct*(135*1024)+40960+mi*RING+i*1024+lane*16);
    if(w<6){
      #pragma unroll 1
      for(u32 i=0;i<20480;i+=4096){ setm0<0>(cb+i); const u32 o=OFF_WOF+ct*(135*1024)+mi*20480+i; dma<0>(srdS,lane*16,o); dma<1024>(srdS,lane*16,o); dma<2048>(srdS,lane*16,o); dma<3072>(srdS,lane*16,o); } }
    if(w==6){ adone=__all(ld4(rs,fao)==tag); if(adone) aload(); }
    ldswait(smb+C_FREE,4u);
    #pragma unroll
    for(int i=0;i<24;i++) ldsw128(smb+RB+mi*RING+i*1024+lane*16,wq[i]);
    if(w<6) WAITV(0);
    signal(smb+C_WR,lane);
    if(!adone){ adone=__all(ld4(rs,fao)==tag); if(adone) aload(); }
    if(w==7 && b==255){
      #pragma unroll 1
      for(int sp=0;sp<SPIN;sp++){ const u32 o=OFF_FA+lane*128; u32 v0=ld4(rs,o),v1=ld4(rs,o+8192),v2=ld4(rs,o+16384),v3=ld4(rs,lane<32? o+24576 : OFF_FW+(lane-32)*128); asm volatile("" ::: "memory");
        if(__all(v0==tag&&v1==tag&&v2==tag&&v3==tag)) break; __builtin_amdgcn_s_sleep(4); }
      if(lane==0) st4(rs,OFF_EP,tag); }
  }
  int w2=w; asm volatile("" : "+s"(w2));
  if(w2<4){
    __builtin_amdgcn_s_setprio(G2PRIO);
    int r=lane&15, q=lane>>4;
    const u32 rb=smb+RB+w*RING;
    u4 srdA=mksrd((const char*)p.wsp+(size_t)rtB*64*4096+w*1024-4096+256);
    const char* zb=p.ws+OFF_Z2F+(u32)(16*w*54+ft0+3)*1024;
    u32 voA[2]; { u32 lr=lane>>3,c=lane&7; voA[0]=lr*4096+((c^(lr>>1))<<4); voA[1]=voA[0]^64u; }
    u32 sofa[8];
    #pragma unroll
    for(int g=0;g<8;g++) sofa[g]=((g+2*w)&7)*32768+4096-(g&3)*1024;
    const u32 voB=lane*16;
    u32 la[2]; la[0]=rb+r*128+((q^(r>>1))<<4); la[1]=la[0]^64u;
    f4 acc[4][7]={}; u4 B[3][7]; u4 A[2][4]; u2 z1[7];
    auto issueA=[&](auto sc,auto gc){ constexpr int s=sc.value,g=gc.value; if constexpr((g&3)==0) setm0<((s+1)%3)*8192+g*1024>(rb); dma<(g&3)*1024>(srdA,voA[g&1],sofa[g]);
      if constexpr(g==7){ u64 a=((u64)srdA[1]<<32|srdA[0])+128; srdA[0]=(u32)a; srdA[1]=(u32)(a>>32); } };

    u32 bsr,fv;
    auto fof=[&](){ const u32 l=voB>>4; return l<27? OFF_FW+l*128 : OFF_F2+((rtB>>1)*16+(((ft0>>1)+(l&3))>>1))*128; };
    #pragma unroll
    for(int m=0;m<4;m++){ A[0][m]=ldsr128(la[0]+m*2048+8192); A[1][m]=ldsr128(la[1]+m*2048+8192); }
    sfor<8>([&](auto g){ issueA(std::integral_constant<int,2>{},g); });
    waitA(A[0]); waitA(A[1]);
    sfor<8>([&](auto g){ issueA(std::integral_constant<int,3>{},g); });
    #pragma unroll
    for(int m=0;m<4;m++)
      #pragma unroll
      for(int n=0;n<7;n++) asm volatile("" : "+v"(acc[m][n]));
    RST(2);
    { const int hf=(ft0>>1)+q; pollw(rs,OFF_F2+((4*w+(lane&3))*16+(hf==0?13:(hf-1)>>1))*128,tag); }
    RST(10);
    sfor<3>([&](auto hh){ const char* s=zb+(u32)hh.value*(54*1024); sfor<7>([&](auto n){ gload<(n.value-3)*1024>(B[hh][n],voB,s); }); });
    auto redw=[&](auto nc){ constexpr int n=nc.value; *(LAS f4*)(rb+n*1024+voB)=acc[1][n]; *(LAS f4*)(rb+(7+n)*1024+voB)=acc[2][n]; *(LAS f4*)(rb+(14+n)*1024+voB)=acc[3][n]; };
    const char* z1b=p.ws+OFF_Z1T+(u32)((rtB*4+w)*27+(ft0>>1))*1024;
    sfor<16>([&](auto hc){
      constexpr int h=hc.value, S=h%3, P=h&1;
      constexpr int cnt = h<4? 14 : ((h+1<16?7:0)+(h+2<16?7:0) + ((((h-2)&1)&&((h-2)/2+3<8))?8:0) + ((((h-1)&1)&&((h-1)/2+3<8))?8:0) + (h>=14?8:0) + ((h==11||h==12)?1:0));
      waitB<cnt>(B[S]); waitA(A[P]);
      if constexpr(h==13){ tie1(fv); if(!__all(fv==tag)) pollw(rs,fof(),tag); }
      if constexpr(h+1<16 && h>0){
        #pragma unroll
        for(int m=0;m<4;m++) A[P^1][m]=ldsr128(la[(h+1)&1]+m*2048+((((h+1)>>1)+1)%3)*8192);
      }
      if constexpr(h==10) gload4<0>(fv,fof(),p.ws);
      const char* s=zb+(u32)(h+3)*(54*1024);
      sfor<7>([&](auto n){
        mfma4(acc[0][n],acc[1][n],acc[2][n],acc[3][n],B[S][n],A[P]);
        if constexpr((h&1)&&(h/2+3<8)&&h>=3){ issueA(std::integral_constant<int,h/2+3>{},n); if constexpr(n.value==6) issueA(std::integral_constant<int,h/2+3>{},std::integral_constant<int,7>{}); }
        if constexpr(h+3<16) gload_t<(n.value-3)*1024>(B[S][n],voB,s);
        if constexpr(h==13){ gload8<(n.value>>1)*1024>(z1[n],voB,z1b+((n.value&1)? ((ft0&1)?1024:8) : (ft0&1)*8));
          if constexpr(n.value==6) { gload2(bsr,(voB>>3)&30u,(const char*)(p.bsp+(rtB*4+w)*16)); signal(smb+C_Z1R,lane); } }
        if constexpr(h==15){ SCHED(); if constexpr(n.value>=2) redw(std::integral_constant<int,n.value-2>{}); SCHED(); }
      });
    });
    asm volatile("s_nop 15\n\ts_nop 3" ::: "memory");
    SCHED();
    STAMP(10,0);
    lane=__builtin_amdgcn_mbcnt_hi(~0u,__builtin_amdgcn_mbcnt_lo(~0u,0u)); asm volatile("" : "+v"(lane)); r=lane&15; q=lane>>4;
    redw(std::integral_constant<int,5>{}); redw(std::integral_constant<int,6>{});
    signal(smb+C_RED,lane);
    ldswait(smb+C_RED,4u);
    #pragma unroll
    for(int m=1;m<4;m++){ u32 src=smb+RB+((w-m)&3)*RING+(m-1)*7168+lane*16;
      #pragma unroll
      for(int n=0;n<7;n++) acc[0][n]+=*(LAS f4*)(src+n*1024); }
    asm volatile("s_waitcnt vmcnt(0) lgkmcnt(0)" : "+v"(z1[0]),"+v"(z1[1]),"+v"(z1[2]),"+v"(z1[3]),"+v"(z1[4]),"+v"(z1[5]),"+v"(z1[6]),"+v"(bsr) :: "memory");
    const float bs=bflo(bsr);
    signal(smb+C_FREE,lane);
    u2 o[7];
    #pragma unroll
    for(int n=0;n<7;n++)
      #pragma unroll
      for(int e=0;e<2;e++)o[n][e]=pkbf2(unpk(z1[n][e])*(f2{acc[0][n][2*e],acc[0][n][2*e+1]}+bs));
    const u32 ho=OFF_HF+(u32)((rtB*4+w)*54+ft0)*512+(q>>1)*256+r*16;
    #pragma unroll
    for(int n=0;n<6;n+=2){ auto sx=__builtin_amdgcn_permlane16_swap(o[n][0],o[n+1][0],false,false); auto sy=__builtin_amdgcn_permlane16_swap(o[n][1],o[n+1][1],false,false);
      if(ct<7||n>=2) st16(rs,ho+(n+(q&1))*512,u4{sx[0],sy[0],sx[1],sy[1]}); }
    st8(rs,ho+6*512+(q&1)*8,o[6]);
    STAMP(11,0);
    WAITV(0);
    if(arrive(smb+C_H,lane)==3u && lane==0) st4(rs,OFF_FH+(rtB*8+ct)*128,tag);
    STAMP(12,0);
    __builtin_amdgcn_s_setprio(0);
  }
  { const int r=lane&15, q=lane>>4, m=w&3, kh=w>>2;
    u2 xv[5]={};
    if(kh){
      #pragma unroll
      for(int n=0;n<5;n++) xv[n]=*(const u2*)(p.x+(rtB*64+m*16+r)*640+80*ct+16*n+4*q);
    }
    f4 acc[5]={}; u4 W[3][5];
    const u32 wl2=smb+lane*16+RB-40960+kh*14*5120, wl0=kh? wl2 : smb+lane*16, wl1=kh? wl2 : smb+lane*16+CVS-20480;
    #define WAD(k,n) (((k)<4? wl0 : (k)<8? wl1 : wl2)+((k)*5+(n))*1024)
    f4 ax[5]={};
    auto addax=[&](){
      #pragma unroll
      for(int n=0;n<5;n++){ u2 av=ldstr(smb+ATL+(16*n+4*q+(r>>2))*128+(16*m+4*(r&3))*2); ax[n]=f4{bflo(av[0]),bfhi(av[0]),bflo(av[1]),bfhi(av[1])}+f4{bflo(xv[n][0]),bfhi(xv[n][0]),bflo(xv[n][1]),bfhi(xv[n][1])}; } };
    if(w==6 && adone){ WAITV(0); signal(smb+C_AT,lane); }
    u32 oo[3],to[3]; const u32 tb=smb+CVS+m*3072;
    #pragma unroll
    for(int i=0;i<3;i++){ const int it=lane+64*i, rr=(it*205)>>11, cc=it-rr*10; oo[i]=opq((rtB*64+m*16+rr)*1280+ct*160+cc*16); to[i]=opq(tb+rr*176+cc*16); }
    if(m==0){ ldswait(smb+C_H,4u); pollw(rs,OFF_FH+(rtB*8+kh*4+(lane&3))*128,tag); signal(smb+C_TAG+kh*4,lane); } else ldswait(smb+C_TAG+kh*4,1u);
    STAMP(16,0);
    u4 hf[14];
    const u32 ho=OFF_HF+(u32)((rtB*4+m)*27+kh*14)*1024+lane*16;
    #pragma unroll
    for(int k=0;k<HPF;k++) hf[k]=ld16(rs,ho+k*1024);
    const bool early=kh && uni(ldsv(smb+C_AT))!=0u;
    if(early) addax();
    ldswait(smb+C_WR,4u);
    STAMP(17,0);
    const u32 wl3=kh? wl2-5120 : wl2;
    #pragma unroll
    for(int n=0;n<10;n++) W[n/5][n%5]=ldsr128(WAD(n/5,n%5));
    #pragma unroll
    for(int k=0;k<13;k++){
      if(k+HPF<14) hf[k+HPF]=ld16(rs,ho+((k+HPF==13&&kh)?12:k+HPF)*1024);
      SCHED();
      #pragma unroll
      for(int n=0;n<5;n++){ acc[n]=mfma16(W[k%3][n],hf[k],acc[n]); SCHED();
        if(k<11) W[(k+2)%3][n]=ldsr128(WAD(k+2,n)); else if(k==11) W[1][n]=ldsr128(wl3+(65+n)*1024);
        SCHED(); }
      if(k==5 && !kh) signal(smb+C_G1,lane);
    }
    const u32 pb=smb+m*5120+lane*16;
    if(!kh){
      #pragma unroll
      for(int n=0;n<5;n++) acc[n]=mfma16(W[1][n],hf[13],acc[n]);
      STAMP(18,0);
      ldswait(smb+C_G1,4u);
      #pragma unroll
      for(int n=0;n<5;n++) *(LAS f4*)(pb+n*1024)=acc[n];
      signal(smb+C_P+m*4,lane);
    } else {
      if(w==6 && !adone){ pollw(rs,fao,tag); aload(); WAITV(0); signal(smb+C_AT,lane); }
      if(!early){ ldswait(smb+C_AT,1u); addax(); }
      RST(7);
      ldswait(smb+C_P+m*4,1u);
      #pragma unroll
      for(int n=0;n<5;n++){ const f4 o=(acc[n]+ax[n])+*(LAS f4*)(pb+n*1024); *(LAS u2*)(tb+r*176+n*32+q*8)=u2{pkbf(o[0],o[1]),pkbf(o[2],o[3])}; }
      #pragma unroll
      for(int i=0;i<3;i++) if(i<2||lane<32) *(u4*)((char*)p.out+oo[i])=ldsr128(to[i]);
    }
    bar(); STAMP(19,0);
    if(w==0){ const u64 e=__builtin_amdgcn_s_memrealtime()+2*(dg1-dg0); while(__builtin_amdgcn_s_memrealtime()<e) __builtin_amdgcn_s_sleep(1); }
  }
}
extern "C" void kernel_launch(void* const* d_in,const int* in_sizes,int n_in,void* d_out,int out_size,void* d_ws,size_t ws_size,hipStream_t stream){
  Params p;
  p.x=(const u16*)d_in[0]; p.lar=(const u16*)d_in[1]; p.aim=(const u16*)d_in[2]; p.cre=(const u16*)d_in[3]; p.cim=(const u16*)d_in[4];
  p.ldt=(const u16*)d_in[5]; p.dsk=(const u16*)d_in[6]; p.win=(const u16*)d_in[8]; p.wsp=(const u16*)d_in[9]; p.bsp=(const u16*)d_in[10]; p.wout=(const u16*)d_in[11];
  p.out=(u16*)d_out; p.ws=(char*)d_ws;
  (void)hipFuncSetAttribute((const void*)fused,hipFuncAttributeMaxDynamicSharedMemorySize,LDS_TOTAL);
  fused<<<256,512,LDS_TOTAL,stream>>>(p);
}
```

```cpp
#include <hip/hip_runtime.h>
#include <utility>
#pragma clang diagnostic ignored "-Wint-to-pointer-cast"
typedef unsigned short u16;
typedef unsigned int u32;
typedef unsigned long long u64;
typedef __attribute__((ext_vector_type(8))) __bf16 bf8;
typedef __attribute__((ext_vector_type(8))) _Float16 hf8;
typedef __attribute__((ext_vector_type(2))) __bf16 bf2;
typedef __attribute__((ext_vector_type(2))) _Float16 hf2;
typedef __attribute__((ext_vector_type(2))) float f2;
typedef __attribute__((ext_vector_type(4))) float f4;
typedef __attribute__((ext_vector_type(16))) float f16v;
typedef __attribute__((ext_vector_type(4))) u32 u4;
typedef __attribute__((ext_vector_type(2))) u32 u2;
typedef __attribute__((ext_vector_type(4))) short s4;
#define LAS __attribute__((address_space(3)))
#define GAS __attribute__((address_space(1)))
#define DI __device__ __forceinline__

constexpr u32 OFF_Z2F = 0;
constexpr u32 OFF_Z1T = OFF_Z2F + 64*54*1024;
constexpr u32 OFF_XNT = OFF_Z1T + 128*27*1024;
constexpr u32 OFF_WOF = OFF_XNT + 640*4096;
constexpr u32 OFF_HF  = OFF_WOF + 8*27*5*1024;
constexpr u32 OFF_AT  = OFF_HF + 128*27*1024;
constexpr u32 OFF_ST  = OFF_AT + 640*4096;
constexpr u32 OFF_RS  = OFF_ST + 16*14*2048+8192;
constexpr u32 OFF_EP  = OFF_RS + 2048*16;
constexpr u32 OFF_F2  = OFF_EP + 256;
constexpr u32 OFF_F1  = OFF_F2 + 256*128;
constexpr u32 OFF_FX  = OFF_F1 + 256*128;
constexpr u32 OFF_FW  = OFF_FX + 256*128;
constexpr u32 OFF_FH  = OFF_FW + 32*128;
constexpr u32 OFF_FA  = OFF_FH + 256*128;
constexpr u32 WS_END  = OFF_FA + 256*128;

struct Params {
  const u16 *x, *lar, *aim, *cre, *cim, *ldt, *dsk, *win, *wsp, *bsp, *wout;
  u16* out; char* ws;
};
#define STAMP(id,wv) do{}while(0)
#define RST(i) do{}while(0)
template<class F,int... I> DI void sfor_impl(F&& f, std::integer_sequence<int,I...>){ (f(std::integral_constant<int,I>{}),...); }
template<int N,class F> DI void sfor(F&& f){ sfor_impl(f, std::make_integer_sequence<int,N>{}); }

DI float bflo(u32 v){ return __uint_as_float(v<<16); }
DI float bfhi(u32 v){ return __uint_as_float(v&0xffff0000u); }
DI float bf1(u16 v){ return __uint_as_float(((u32)v)<<16); }
DI u32 pkbf(float a,float b){ f2 v={a,b}; return __builtin_bit_cast(u32,__builtin_convertvector(v,bf2)); }
DI u32 pkhf(float a,float b){ f2 v={a,b}; return __builtin_bit_cast(u32,__builtin_convertvector(v,hf2)); }
DI float rbf(float a){ return bflo(pkbf(a,a)); }
DI float hlo(u32 v){ return (float)__builtin_bit_cast(hf2,v)[0]; }
DI float hhi(u32 v){ return (float)__builtin_bit_cast(hf2,v)[1]; }
DI float dot2(u32 a,u32 b,float c){ return __builtin_amdgcn_fdot2_f32_bf16(__builtin_bit_cast(bf2,a),__builtin_bit_cast(bf2,b),c,false); }
DI f4 mfma16(u4 a,u4 b,f4 c){ return __builtin_amdgcn_mfma_f32_16x16x32_bf16(__builtin_bit_cast(bf8,a),__builtin_bit_cast(bf8,b),c,0,0,0); }
DI f16v mfma32h(u4 a,u4 b,f16v c){ return __builtin_amdgcn_mfma_f32_32x32x16_f16(__builtin_bit_cast(hf8,a),__builtin_bit_cast(hf8,b),c,0,0,0); }
DI u4 ldsr128(u32 a){ return *(LAS u4*)a; }
DI u2 ldsr64(u32 a){ return *(LAS u2*)a; }
DI void ldsw128(u32 a,u4 v){ *(LAS u4*)a=v; }
DI u2 ldstr(u32 a){ return __builtin_bit_cast(u2,__builtin_amdgcn_ds_read_tr16_b64_v4i16((LAS s4*)a)); }
DI u32 uni(u32 v){ return __builtin_amdgcn_readfirstlane(v); }

template<int OFF> DI void gload(u4& dst,u32 voff,const char* base){ asm volatile("global_load_dwordx4 %0, %1, %2 offset:%3 sc1" : "=v"(dst) : "v"(voff), "s"(base), "n"(OFF)); }
template<int OFF> DI void gload_t(u4& dst,u32 voff,const char* base){ asm volatile("global_load_dwordx4 %0, %1, %2 offset:%3 sc1" : "+v"(dst) : "v"(voff), "s"(base), "n"(OFF)); }
DI void mfma4(f4& c0,f4& c1,f4& c2,f4& c3,const u4& a,const u4 (&b)[4]){
  asm volatile("v_mfma_f32_16x16x32_bf16 %0, %4, %5, %0\n\tv_mfma_f32_16x16x32_bf16 %1, %4, %6, %1\n\tv_mfma_f32_16x16x32_bf16 %2, %4, %7, %2\n\tv_mfma_f32_16x16x32_bf16 %3, %4, %8, %3"
    : "+v"(c0),"+v"(c1),"+v"(c2),"+v"(c3) : "v"(a),"v"(b[0]),"v"(b[1]),"v"(b[2]),"v"(b[3])); }
template<int CNT> DI void waitB(u4 (&B)[7]){ asm volatile("s_waitcnt vmcnt(%7)" : "+v"(B[0]),"+v"(B[1]),"+v"(B[2]),"+v"(B[3]),"+v"(B[4]),"+v"(B[5]),"+v"(B[6]) : "n"(CNT) : "memory"); }
DI void waitA(u4 (&A)[4]){ asm volatile("s_waitcnt lgkmcnt(0)" : "+v"(A[0]),"+v"(A[1]),"+v"(A[2]),"+v"(A[3]) :: "memory"); }
#define WAITV(n) asm volatile("s_waitcnt vmcnt(%0)" :: "n"(n) : "memory")
#define WAITL0() asm volatile("s_waitcnt lgkmcnt(0)" ::: "memory")
#define SCHED() __builtin_amdgcn_sched_barrier(0)
DI void bar(){ asm volatile("s_waitcnt lgkmcnt(0)\n\ts_barrier" ::: "memory"); }
DI f2 unpk(u32 v){ return f2{bflo(v),bfhi(v)}; }
DI u32 pkbf2(f2 v){ return __builtin_bit_cast(u32,__builtin_convertvector(v,bf2)); }
DI u32 pkhf2(f2 v){ return __builtin_bit_cast(u32,__builtin_convertvector(v,hf2)); }
DI f2 fma2(f2 a,f2 b,f2 c){ return __builtin_elementwise_fma(a,b,c); }
DI f2 gelu2(f2 x){
  f2 u=x*fma2(x*x,f2{-0.10294324f,-0.10294324f},f2{-2.30220820f,-2.30220820f});
  f2 d=f2{__builtin_amdgcn_exp2f(u[0]),__builtin_amdgcn_exp2f(u[1])}+f2{1.f,1.f};
  return x*f2{__builtin_amdgcn_rcpf(d[0]),__builtin_amdgcn_rcpf(d[1])};
}
DI void ln_fin(float mean,float var,float& m,float& rinv){
  m=rbf(mean); float v=rbf(__builtin_fmaxf(var,0.f));
  float s=rbf(__builtin_amdgcn_sqrtf(rbf(v+1e-5f))); float r=__builtin_amdgcn_rcpf(s); rinv=__builtin_fmaf(__builtin_fmaf(-s,r,1.f),r,r);
}
typedef __amdgpu_buffer_rsrc_t rsrc_t;
DI void st16(rsrc_t rs,u32 off,u4 v){ __builtin_amdgcn_raw_buffer_store_b128(v,rs,off,0,16); }
DI void st8(rsrc_t rs,u32 off,u2 v){ __builtin_amdgcn_raw_buffer_store_b64(v,rs,off,0,16); }
DI void st4(rsrc_t rs,u32 off,u32 v){ __builtin_amdgcn_raw_buffer_store_b32(v,rs,off,0,16); }
DI u4 ld16(rsrc_t rs,u32 off){ return __builtin_amdgcn_raw_buffer_load_b128(rs,off,0,16); }
DI u2 ld8(rsrc_t rs,u32 off){ return __builtin_amdgcn_raw_buffer_load_b64(rs,off,0,16); }
DI u32 ld4(rsrc_t rs,u32 off){ return __builtin_amdgcn_raw_buffer_load_b32(rs,off,0,16); }
constexpr int SPIN=1<<17;
template<int SL=2> DI void pollw(rsrc_t rs,u32 off,u32 tag){
  #pragma unroll 1
  for(int s=0;s<SPIN;s++){ u32 v=ld4(rs,off); asm volatile("" ::: "memory"); if(__all(v==tag)) break; __builtin_amdgcn_s_sleep(SL); }
}

DI f16v mfma32b(u4 a,u4 b,f16v c){ return __builtin_amdgcn_mfma_f32_32x32x16_bf16(__builtin_bit_cast(bf8,a),__builtin_bit_cast(bf8,b),c,0,0,0); }
DI u4 mksrd(const void* b){ u64 a=(u64)b; return u4{(u32)a,(u32)(a>>32)&0xffffu,0xffffffffu,0x00020000u}; }
template<int IMM> DI void setm0(u32 base){ asm volatile("s_add_u32 m0, %0, %1\n\ts_nop 0" :: "s"(base), "n"(IMM) : "scc","memory"); }
template<int OFF> DI void dma(u4 srd,u32 voff,u32 soff){ asm volatile("buffer_load_dwordx4 %0, %1, %2 offen offset:%3 sc1 lds" :: "v"(voff), "s"(srd), "s"(soff), "n"(OFF) : "memory"); }
DI void touch(u4 srd,u32 voff,u32 soff){ asm volatile("buffer_load_dword %0, %1, %2 offen sc1 lds" :: "v"(voff), "s"(srd), "s"(soff) : "memory"); }
template<int OFF> DI void gload8(u2& dst,u32 voff,const char* base){ asm volatile("global_load_dwordx2 %0, %1, %2 offset:%3 sc1" : "=v"(dst) : "v"(voff), "s"(base), "n"(OFF)); }
DI void gload2(u32& dst,u32 voff,const char* base){ asm volatile("global_load_ushort %0, %1, %2" : "=v"(dst) : "v"(voff), "s"(base)); }
DI u32 arrive(u32 a,int){ u32 o; asm volatile("s_mov_b64 exec, 1\n\tds_add_rtn_u32 %0, %1, %2\n\ts_mov_b64 exec, -1\n\ts_waitcnt lgkmcnt(0)" : "=&v"(o) : "v"(a), "v"(1u) : "memory"); return uni(o); }
DI void ldswait(u32 a,u32 n){ while(__hip_atomic_load((LAS u32*)a,__ATOMIC_RELAXED,__HIP_MEMORY_SCOPE_WORKGROUP)<n) __builtin_amdgcn_s_sleep(1); asm volatile("" ::: "memory"); }
DI float addf(float a,float b){ float o; asm("v_add_f32 %0, %1, %2" : "=v"(o) : "v"(a), "v"(b)); return o; }
DI void tie1(u32& g){ asm volatile("" : "+v"(g) :: "memory"); }
DI u32 opq(u32 v){ asm volatile("" : "+v"(v)); return v; }

constexpr u32 STG=32768;
constexpr u32 RING=24576, RB=62848, CONV=0, KP=4192, CVS=4*KP+4160, UOF=4*KP, MISC=RB+4*RING, JNK=MISC+2048, PM=163840-128, LDS_TOTAL=163840;
constexpr u32 ATL=2*CVS;
constexpr u32 C_FULL=PM, C_G1=PM+4, C_Z2=PM+8, C_Z1=PM+12, C_RED=PM+16, C_H=PM+20, C_FREE=PM+24, C_CV=PM+28, C_Z1R=PM+32, C_TAG=PM+40, C_KP=PM+48, C_DONE=PM+64, C_ZQ=PM+68, C_CS=PM+72, C_K2=PM+80, C_P=PM+96, C_AT=PM+112, C_WR=PM+116;
constexpr u32 CODE_BYTES=36864;
#define LA 3
#define TC_S 7
#define G2PRIO 2
#define XMAX 9
#define GS_K 35
#define WNT ""
#define TS1 6
#define HPRIO 3
#define CPRIO 3
#define POLLM 1
#define HPF 5
#define CVSKIP 0
#define BROT 0
#define PDLY 8
#define RS_S 7
#define KROT ((j+3*(rt&3))%10)
DI void signal(u32 a,int){ asm volatile("s_mov_b64 exec, 1\n\tds_add_u32 %0, %1\n\ts_mov_b64 exec, -1" :: "v"(a), "v"(1u) : "memory"); }
template<int OFF> DI void gload4(u32& dst,u32 voff,const char* base){ asm volatile("global_load_dword %0, %1, %2 offset:%3 sc1" : "=v"(dst) : "v"(voff), "s"(base), "n"(OFF)); }
template<int CNT> DI void waitG(u4& g){ asm volatile("s_waitcnt vmcnt(%1)" : "+v"(g) : "n"(CNT) : "memory"); }

DI f2 cmulp(f2 a,f2 b,f2 bs){ return fma2(f2{a[1],a[1]},bs,f2{a[0],a[0]}*b); }
DI float fmaf1(float a,float b,float c){ float o; asm("v_fma_f32 %0, %1, %2, %3" : "=v"(o) : "v"(a),"v"(b),"v"(c)); return o; }
DI u32 ldsv(u32 a){ return __hip_atomic_load((LAS u32*)a,__ATOMIC_RELAXED,__HIP_MEMORY_SCOPE_WORKGROUP); }
DI void ldswait4(u32 a,int lane,u32 n){ while(__any(ldsv(a+(lane&3)*4)<n)) __builtin_amdgcn_s_sleep(1); asm volatile("" ::: "memory"); }
DI u32 gettag(u32 a){ ldswait(a+4,1u); return uni(ldsv(a)); }
DI f2 cpow_dir(float lr,float rv,float pw){ float e=__builtin_amdgcn_exp2f(lr*pw); float f=__builtin_amdgcn_fractf(rv*pw);
  return f2{e*__builtin_amdgcn_cosf(f),e*__builtin_amdgcn_sinf(f)}; }
DI u32 dup(u32 a){ return __builtin_amdgcn_perm(a,a,0x01000100u); }
DI u32 cat(u32 lo,u32 hi){ return __builtin_amdgcn_perm(hi,lo,0x05040100u); }
DI u32 lds16(u32 a){ return *(LAS u16*)a; }

template<class CB> DI float kgen_tab(u32 cb,int lane,const u32 (&pr)[6],float (&kp)[11],CB&& mid){
  const float dt=rbf(__expf(bflo(pr[4])));
  const float cr[2]={bflo(pr[2]),bfhi(pr[2])}, cm[2]={bflo(pr[3]),bfhi(pr[3])};
  float mx=fmaxf(fmaxf(fabsf(cr[0]),fabsf(cm[0])),fmaxf(fabsf(cr[1]),fabsf(cm[1])));
  u32 eb=__float_as_uint(mx)&0x7f800000u;
  #define DPPMAX(c) eb=max(eb,(u32)__builtin_amdgcn_update_dpp(0,(int)eb,c,0xf,0xf,false))
  DPPMAX(0x128); DPPMAX(0x124); DPPMAX(0x122); DPPMAX(0x121);
  eb=max(max((u32)__builtin_amdgcn_readlane(eb,0),(u32)__builtin_amdgcn_readlane(eb,16)),max((u32)__builtin_amdgcn_readlane(eb,32),(u32)__builtin_amdgcn_readlane(eb,48))); eb=eb<0x01000000u?0x3f800000u:(eb>0x7e000000u?0x7e000000u:eb);
  const float ksc=2.f*dt*__uint_as_float(eb), isc=__uint_as_float(0x7f000000u-eb);
  const int r=lane&31,h=lane>>5;
  const u32 TP=272, TA=cb, TB=cb+22*TP;
  f16v acc={};
  #pragma unroll 1
  for(int c=0;c<2;c++){
    if(c) mid();
    float l_a=c?bfhi(pr[0]):bflo(pr[0]), i_m=c?bfhi(pr[1]):bflo(pr[1]), c_r=c?cr[1]:cr[0], c_i=c?cm[1]:cm[0];
    const float lr=(-rbf(__expf(l_a))*dt)*1.44269504f, rv=(i_m*dt)*0.15915494f;
    const f2 z1=cpow_dir(lr,rv,1.f), z8=cpow_dir(lr,-rv,8.f), zs=cpow_dir(lr,-rv,16.f), zk=cpow_dir(lr,rv,256.f);
    const f2 z1s={-z1[1],z1[0]}, zss={-zs[1],zs[0]}, zks={-zk[1],zk[0]};
    const u32 wa=TA+lane*4, wb=TB+lane*4;
    #define PUT(base,row,v) do{ *(LAS u32*)((base)+(row)*TP)=pkhf2(v); }while(0)
    f2 e={1.f,0.f}, o=z8;
    #pragma unroll
    for(int k=0;k<16;k++){ PUT(wb,2*k,e); PUT(wb,2*k+1,o); if(k<15){ e=k?cmulp(e,zs,zss):zs; o=cmulp(o,zs,zss);} }
    f2 b0={c_r*isc,c_i*isc}; PUT(wa,0,b0);
    f2 t=cmulp(b0,zk,zks); PUT(wa,8,t);
    f2 b1=cmulp(b0,z1,z1s); PUT(wa,1,b1); t=cmulp(t,zk,zks); PUT(wa,10,t);
    f2 b2=cmulp(b1,z1,z1s); PUT(wa,2,b2); t=cmulp(t,zk,zks); PUT(wa,11,t);
    f2 b3=cmulp(b2,z1,z1s); PUT(wa,3,b3); t=cmulp(t,zk,zks); PUT(wa,16,t);
    f2 b4=cmulp(b3,z1,z1s); PUT(wa,4,b4); t=cmulp(t,zk,zks); PUT(wa,17,t);
    f2 b5=cmulp(b4,z1,z1s); PUT(wa,5,b5); t=cmulp(t,zk,zks); PUT(wa,20,t);
    f2 b6=cmulp(b5,z1,z1s); PUT(wa,6,b6); t=cmulp(t,zk,zks); PUT(wa,21,t);
    f2 b7=cmulp(b6,z1,z1s); PUT(wa,7,b7); t=cmulp(t,zk,zks); PUT(wa,18,t);
    f2 s=cmulp(b4,zk,zks); PUT(wa,12,s); t=cmulp(b2,zk,zks); PUT(wa,9,t);
    s=cmulp(s,zk,zks); PUT(wa,14,s); t=cmulp(b6,zk,zks); PUT(wa,13,t);
    s=cmulp(s,zk,zks); PUT(wa,15,s);
    #undef PUT
    WAITL0();
    const int ra=(r>21||r==19)?0:r;
    #pragma unroll
    for(int ks=0;ks<8;ks++){ u4 fa=ldsr128(TA+ra*TP+ks*32+h*16), fb=ldsr128(TB+r*TP+ks*32+h*16); acc=mfma32h(fa,fb,acc); }
    WAITL0();
  }
  #pragma unroll
  for(int i=0;i<11;i++) kp[i]=acc[i];
  return ksc;
}
DI void kgen_exp(u32 cb,int lane,const float (&acc)[11],const u32 (&ga)[11]){
  const int r=lane&31,h=lane>>5;
  { const u32 a4=cb+r*16+h*8, a5=cb+r*16+h*1024;
    #define PK(addr,base,reg) do{ *(LAS _Float16*)((addr)+2*(base))=(_Float16)acc[reg]; }while(0)
    PK(a4,0,0);PK(a4,1,1);PK(a4,2,2);PK(a4,3,3); PK(a4,256,4);PK(a4,258,5);PK(a4,512,6);PK(a4,768,7);
    PK(a5,1024,8);PK(a5,1280,9);PK(a5,2048,10);
    #undef PK
  }
  WAITL0();
  { const u32 g0=cb+2*(2048-8*lane);
    u32 a0=lds16(g0), b0=lds16(g0-16), a1=lds16(g0-1024), b1=lds16(g0-1024-16);
    u32 f2_=lds16(g0-2048+(lane?8:0)), a2=lds16(g0-2048), m2=lds16(g0-2048-8), b2=lds16(g0-2048-16);
    u32 e[11];
    #pragma unroll
    for(int j=0;j<11;j++) e[j]=lds16(ga[j]);
    u32 k01=*(LAS u32*)cb, k23=*(LAS u32*)(cb+4);
    WAITL0();
    const u32 wo=cb+lane*16;
    { u32 aa=dup(a0),bb=dup(b0),c=(lane&1)?b0:a0, cbv=cat(c,b0), ac=cat(a0,c);
      ldsw128(wo,u4{aa,aa,cbv,bb}); ldsw128(wo+KP,u4{aa,aa,ac,bb}); ldsw128(wo+2*KP,u4{aa,aa,aa,cbv}); ldsw128(wo+3*KP,u4{aa,aa,aa,ac}); }
    { u32 aa=dup(a1),bb=dup(b1),c=(lane&1)?b1:a1, cbv=cat(c,b1), ac=cat(a1,c);
      ldsw128(wo+1024,u4{aa,aa,cbv,bb}); ldsw128(wo+1024+KP,u4{aa,aa,ac,bb}); ldsw128(wo+1024+2*KP,u4{aa,aa,aa,cbv}); ldsw128(wo+1024+3*KP,u4{aa,aa,aa,ac}); }
    { u32 aa=dup(a2),mm=dup(m2),bb=dup(b2),am=cat(a2,m2),fa=cat(f2_,a2),mb=cat(m2,b2);
      ldsw128(wo+2048,u4{aa,am,mm,bb}); ldsw128(wo+2048+KP,u4{aa,aa,mm,mb}); ldsw128(wo+2048+2*KP,u4{aa,aa,am,mm}); ldsw128(wo+2048+3*KP,u4{fa,aa,aa,mm}); }
    { u32 o0=cat(e[0],e[1]),o1=cat(e[2],e[3]),o2=cat(e[4],e[5]),o3=cat(e[6],e[7]),o4=cat(e[8],e[9]);
      u32 p1=cat(e[1],e[2]),d0=cat(e[3],e[4]),d1=cat(e[5],e[6]),d2=cat(e[7],e[8]),d3=cat(e[9],e[10]);
      ldsw128(wo+3072,u4{d0,d1,d2,d3}); ldsw128(wo+3072+KP,u4{o1,o2,o3,o4}); ldsw128(wo+3072+2*KP,u4{p1,d0,d1,d2}); ldsw128(wo+3072+3*KP,u4{o0,o1,o2,o3}); }
    if(lane<4){ if(lane){k01=0;k23=0;}
      u32 d0=k01&0xffffu, o1=__builtin_amdgcn_alignbit(k01,k01,16), o0=__builtin_amdgcn_alignbit(k23,k23,16), p1=(k23&0xffffu)|(k01&0xffff0000u);
      ldsw128(wo+4096,u4{d0,0,0,0}); ldsw128(wo+4096+KP,u4{o1,0,0,0}); ldsw128(wo+4096+2*KP,u4{p1,d0,0,0}); ldsw128(wo+4096+3*KP,u4{o0,o1,0,0}); }
  }
}
DI f4 mfma16h(u4 a,u4 b,f4 c){ return __builtin_amdgcn_mfma_f32_16x16x32_f16(__builtin_bit_cast(hf8,a),__builtin_bit_cast(hf8,b),c,0,0,0); }
#define RDA(S,i,m) asm volatile("ds_read2_b64 %0, %1 offset0:%2 offset1:%3" : "=v"(AF[S][i][m]) : "v"((i)<2?kb:ka), "n"((1-((i)&1))*128+(1-(m))*4), "n"((1-((i)&1))*128+(1-(m))*4+1))
#define RDB(S,i,ad,o) asm volatile("ds_read_b128 %0, %1 offset:%2" : "=v"(BF[S][i]) : "v"(ad), "n"(o))
#define TIEA(S,i) "+v"(AF[S][i][0]),"+v"(AF[S][i][1])
#define CV_M(S,i,n) do{ y[0][n]=mfma16h(AF[S][i][0],BF[S][(n)-(i)],y[0][n]); y[1][n]=mfma16h(AF[S][i][1],BF[S][(n)-(i)],y[1][n]); SCHED(); }while(0)
#define CV_U0() u32 u0=u1-256; u0=((int)u0<(int)uz)?uz:u0; const u32 kb=ka+2048; (void)kb
#define CV_SETUP() const int r=lane&15,q=lane>>4; u32 ka=cb+(r&3)*KP+16*q-8*(r>>2)+992; const u32 uz=cb+UOF+q*1040; u32 u1=uz+(r+17)*16; u4 AF[2][4][2]; u4 BF[2][4]
DI void conv_help(u32 cb,int lane){
  CV_SETUP(); f4 y[2][4]={};
  #define HP_WT(S) asm volatile("s_waitcnt lgkmcnt(0)" : TIEA(S,0),TIEA(S,1),TIEA(S,2),TIEA(S,3),"+v"(BF[S][0]),"+v"(BF[S][1]),"+v"(BF[S][2]),"+v"(BF[S][3]))
  #define HP_AD() const u32 kb=ka-64, u3=u1-16; u32 u0=u1-256, u2_=u1-272; u0=((int)u0<(int)uz)?uz:u0; u2_=((int)u2_<(int)uz)?uz:u2_
  #define HP_M(S,i,bi,n) do{ y[0][n]=mfma16h(AF[S][i][0],BF[S][bi],y[0][n]); y[1][n]=mfma16h(AF[S][i][1],BF[S][bi],y[1][n]); SCHED(); }while(0)
  #define HP_STEP(C,N) do{ HP_AD(); RDA(N,2,0); RDA(N,2,1); RDB(N,1,u1,0); RDA(N,3,0); SCHED(); HP_M(C,2,1,3); RDA(N,3,1); RDB(N,0,u0,0); RDA(N,0,0); RDA(N,0,1); SCHED(); HP_M(C,3,0,2); \
      RDB(N,3,u3,0); RDA(N,1,0); RDA(N,1,1); RDB(N,2,u2_,0); ka-=128; u1-=32; SCHED(); HP_M(C,0,3,3); HP_M(C,1,2,2); HP_WT(N); }while(0)
  { HP_AD(); RDA(0,2,0); RDA(0,2,1); RDB(0,1,u1,0); RDA(0,3,0); RDA(0,3,1); RDB(0,0,u0,0); RDA(0,0,0); RDA(0,0,1); RDB(0,3,u3,0); RDA(0,1,0); RDA(0,1,1); RDB(0,2,u2_,0); ka-=128; u1-=32; HP_WT(0); }
  #pragma unroll 1
  for(int dl=CVSKIP*3;dl<4;dl++){ HP_STEP(0,1); if(dl==3){ ka+=128; u1+=32; } HP_STEP(1,0); }
  y[0][3]+=y[0][2]; y[1][3]+=y[1][2];
  *(LAS f4*)(cb+q*KP+r*16)=y[0][3]; *(LAS f4*)(cb+q*KP+256+r*16)=y[1][3];
  WAITL0();
}
DI void conv_wave(const Params& p,rsrc_t rs,int d,u32 cb,u32 flag,int lane,float ksc,float dsk){
  CV_SETUP(); f4 y[2][4]={};
  #define CV_WT(S) asm volatile("s_waitcnt lgkmcnt(0)" : TIEA(S,0),TIEA(S,1),TIEA(S,2),"+v"(BF[S][0]),"+v"(BF[S][1]),"+v"(BF[S][2]),"+v"(BF[S][3]))
  #define CV_STEP(C,N) do{ CV_U0(); CV_M(C,0,0); RDA(N,0,0); RDB(N,0,u0,0); SCHED(); CV_M(C,0,1); RDA(N,0,1); RDB(N,1,u1,0); SCHED(); CV_M(C,0,2); RDA(N,1,0); RDB(N,2,u1,256); SCHED(); \
      CV_M(C,0,3); RDA(N,1,1); RDB(N,3,u1,512); SCHED(); CV_M(C,1,1); RDA(N,2,0); SCHED(); CV_M(C,1,2); RDA(N,2,1); ka-=64; u1-=16; SCHED(); CV_M(C,1,3); CV_M(C,2,2); CV_WT(N); }while(0)
  { CV_U0(); RDA(0,0,0); RDA(0,0,1); RDA(0,1,0); RDA(0,1,1); RDA(0,2,0); RDA(0,2,1); RDB(0,0,u0,0); RDB(0,1,u1,0); RDB(0,2,u1,256); RDB(0,3,u1,512); ka-=64; u1-=16; CV_WT(0); }
  #pragma unroll 1
  for(int dl=CVSKIP*7;dl<8;dl++){ CV_STEP(0,1); if(dl==7){ ka+=64; u1+=16; } CV_STEP(1,0); }
  ldswait(flag,2u);
  y[0][3]+=*(LAS f4*)(cb+q*KP+r*16); y[1][3]+=*(LAS f4*)(cb+q*KP+256+r*16);
  #pragma unroll
  for(int n=0;n<4;n++){ u2 o[2];
    #pragma unroll
    for(int m=0;m<2;m++){ u2 uv=ldsr64(cb+UOF+(2*m+(q>>1))*1040+(16*n+r+1)*16+8*(q&1));
      #pragma unroll
      for(int e=0;e<2;e++) o[m][e]=pkbf2(fma2(f2{hlo(uv[e]),hhi(uv[e])},f2{dsk,dsk},f2{y[m][n][2*e],y[m][n][2*e+1]}*ksc)); }
    auto sx=__builtin_amdgcn_permlane16_swap(o[0][0],o[1][0],false,false); auto sy=__builtin_amdgcn_permlane16_swap(o[0][1],o[1][1],false,false);
    const u32 sc=cb+1024+n*1280; ldsw128(sc+r*80+(q&1)*32+(q>>1)*16,u4{sx[0],sy[0],sx[1],sy[1]});
    st16(rs,OFF_AT+d*4096+n*1024+lane*16,ldsr128(sc+(lane>>2)*80+(lane&3)*16)); }
  WAITV(0);
}

struct Frag { u4 x[4], w[4][2]; };
__global__ __launch_bounds__(512,2) void fused(Params p){
  extern __shared__ __attribute__((aligned(16))) char sm[];
  const u32 smb=(u32)(size_t)(LAS char*)sm;
  u64 pc0; asm volatile("s_getpc_b64 %0" : "=s"(pc0));
  const int b=blockIdx.x, xc=b&7, idx=b>>3;
  const int tid=threadIdx.x, w=uni(tid>>6); int lane=tid&63;
  const rsrc_t rs=__builtin_amdgcn_make_buffer_rsrc(p.ws,0,WS_END,0x00020000);
  const u4 srdS=mksrd(p.ws);
  const int rtB=4*xc+(idx&3), ct=idx>>2;
  const bool hask=idx<28 && idx+28*(w&3)<80, hasc=hask && w>=4;
  const int dch=80*xc+(hask? idx+28*(w&3) : 0);
  const u32 cb=smb+CONV+(w&3)*CVS;
  const int rt=4*(xc>>1)+(idx&3), j=7*(xc&1)+(idx>>2), mi=w&3, g1=w>>2;
  u32 tag; float ksc=0.f, dsk=0.f;
  STAMP(0,0);
  auto pre01=[&](){
    const u4 sa=mksrd((const char*)p.wsp+(size_t)rtB*64*4096+mi*1024-4096); const u32 v0=(lane>>3)*4096+(((lane&7)^(lane>>4))<<4);
    #pragma unroll 1
    for(int i=0;i<4;i++){ setm0<RB+8192>(smb+mi*RING+i*4096); const u32 g=(i&1)*4+2*mi, k=(i>>1)*128+4096;
      dma<0>(sa,v0,(g&7)*32768+k); dma<1024>(sa,v0^64u,(g&7)*32768+32768-1024+k); dma<2048>(sa,v0,((g+2)&7)*32768-2048+k); dma<3072>(sa,v0^64u,((g+2)&7)*32768+32768-3072+k); } };
  auto warm=[&](int wq,int part){
    const u4 sp=mksrd((const char*)p.wsp+(size_t)(rtB*64+ct*8+2*wq)*4096); setm0<JNK>(smb);
    const u32 vo= part==2? lane*128 : ((lane>>2)&7)*1024+(lane&3)*128+part*512;
    asm volatile("buffer_load_dword %0, %1, 0 offen lds" :: "v"(vo), "s"(sp) : "memory");
  };
  if(idx>=28){
    const int a=xc*4+idx-28, r=lane&15, q=lane>>4;
    { u4 sc=mksrd((const void*)(pc0&~127ull)); sc[2]=CODE_BYTES; setm0<JNK>(smb); touch(sc,lane*128,w*8192); }
    const u32 ep=ld4(rs,OFF_EP);
    u4 xr[10]; const char* xs=(const char*)p.x+(size_t)(64*a+8*w+(lane>>3))*1280+(lane&7)*16;
    #pragma unroll
    for(int i=0;i<10;i++) xr[i]=*(const u4*)(xs+i*128);
    if(tid<32) *(LAS u32*)(smb+PM+tid*4)=0u;
    tag=uni(ep)+1u;
    float s0=0.f,s1=0.f,q0=0.f,q1=0.f;
    #pragma unroll
    for(int i=0;i<10;i++){ s0=dot2(xr[i][0],0x3f803f80u,s0); q0=dot2(xr[i][0],xr[i][0],q0); s1=dot2(xr[i][1],0x3f803f80u,s1); q1=dot2(xr[i][1],xr[i][1],q1);
      s0=dot2(xr[i][2],0x3f803f80u,s0); q0=dot2(xr[i][2],xr[i][2],q0); s1=dot2(xr[i][3],0x3f803f80u,s1); q1=dot2(xr[i][3],xr[i][3],q1); }
    s0+=s1; q0+=q1;
    #define DPPADD(v,c) v+=__int_as_float(__builtin_amdgcn_update_dpp(0,__float_as_int(v),c,0xf,0xf,false))
    DPPADD(s0,0xB1); DPPADD(q0,0xB1); DPPADD(s0,0x4E); DPPADD(q0,0x4E); DPPADD(s0,0x141); DPPADD(q0,0x141);
    float m1,ri; { float mean=s0*(1.f/640.f); ln_fin(mean,q0*(1.f/640.f)-mean*mean,m1,ri); }
    if((lane&7)==0) st16(rs,OFF_RS+(64*a+8*w+(lane>>3))*16,u4{__float_as_uint(m1),tag,__float_as_uint(ri),tag});
    STAMP(5,0);
    #pragma unroll
    for(int i=0;i<10;i++){ u4 o;
      #pragma unroll
      for(int e=0;e<4;e++) o[e]=pkhf2(fma2(unpk(xr[i][e]),f2{ri,ri},f2{-m1*ri,-m1*ri}));
      ldsw128(smb+RB+(8*w+(lane>>3))*1296+(lane&7)*16+i*128,o); }
    bar();
    #pragma unroll
    for(int i=0;i<10;i++){ const int c=w*10+i; const u32 ad=smb+RB+(32*((r>>1)&1)+8*q+(r>>2))*1296+c*16+(r&1)*8; u2 lo=ldstr(ad), hi=ldstr(ad+4*1296);
      const u32 sc=smb+w*5120+(i%5)*1024; ldsw128(sc+(r&7)*128+((((r>>3)*4+q)^(r&7))<<4),u4{lo[0],lo[1],hi[0],hi[1]});
      st16(rs,OFF_XNT+a*81920+c*1024+lane*16,ldsr128(sc+(lane>>3)*128+(((lane&7)^(lane>>3))<<4))); }
    WAITV(0);
    if(arrive(smb+C_Z1,lane)==7u && lane==0) st4(rs,OFF_FX+a*128,tag);
    STAMP(6,7);
    if(a<27){ const u4 srdO=mksrd((const char*)p.wout+a*(32*1280)+w*5120); const u32 mo=smb+w*5120; setm0<0>(mo);
      dma<0>(srdO,lane*16,0); dma<1024>(srdO,lane*16,0); dma<2048>(srdO,lane*16,0); dma<3072>(srdO,lane*16,0); setm0<4096>(mo); dma<0>(srdO,lane*16,4096); }
    WAITV(0); bar();
    if(a<27){
      #pragma unroll
      for(int i=0;i<5;i++){ u32 ad=smb+(8*q+(r>>2))*1280+(w*5+i)*32+(r&3)*8; u2 lo=ldstr(ad), hi=ldstr(ad+4*1280);
        st16(rs,OFF_WOF+(u32)((w*27+a)*5+i)*1024+lane*16,u4{lo[0],lo[1],hi[0],hi[1]}); } }
    WAITV(0); bar();
    if(tid==0) st4(rs,OFF_FW+a*128,tag);
    if(g1) warm(mi,2); else { pre01(); WAITV(0); }
    STAMP(31,0);
  } else {
    const int r=lane&31, h=lane>>5;
    const u4 srdX=mksrd((const char*)p.x+(size_t)rt*128*1280);
    const u4 srdW=mksrd((const char*)p.win+j*128-3072);
    const u32 mw=smb+w*4096;
    u32 vx0,vx1,vw1,vw2,epv;
    { u32 lr=lane>>3,c=lane&7; vx0=(16*w+lr)*1280+((c^(lr>>1))<<4); vx1=(vx0^64u)+9216;
      vw1=(8*w+lr)*3456+((c^((lr&2)<<1))<<4); vw2=vw1+1024+(j==13?0:14*128); }
    u32 kx[10]; { u32 k=KROT;
      #pragma unroll
      for(int i=0;i<10;i++){ kx[i]=k*128; k=k==9?0:k+1; } }
    auto issue1=[&](auto sc,int k){ constexpr int s=sc.value;
      if(k==0){ setm0<(s&3)*STG>(mw); dma<0>(srdX,vx0,kx[s]); } if(k==1) dma<1024>(srdX,vx1,kx[s]); if(k==2) dma<2048>(srdW,vw2,kx[s]*1728); if(k==3) dma<3072>(srdW,vw1,kx[s]*1728); };
    auto issue=[&](auto sc){ issue1(sc,0); issue1(sc,1); issue1(sc,2); issue1(sc,3); };
    issue(std::integral_constant<int,0>{});
    u32 pr[6];
    gload4<0>(epv,0u,p.ws+OFF_EP);
    gload4<0>(pr[0],lane*4,(const char*)(p.lar+dch*128)); gload4<0>(pr[1],lane*4,(const char*)(p.aim+dch*128)); gload4<0>(pr[2],lane*4,(const char*)(p.cre+dch*128)); gload4<0>(pr[3],lane*4,(const char*)(p.cim+dch*128));
    gload2(pr[4],0u,(const char*)(p.ldt+dch)); gload2(pr[5],0u,(const char*)(p.dsk+dch));
    asm volatile("s_barrier" ::: "memory");
    issue(std::integral_constant<int,1>{});
    if(tid<32) *(LAS u32*)(smb+PM+tid*4)=0u;
    u32 ax[2][4],aw[2][4][2];
    { u32 row=32*mi+r, a0=smb+(row>>4)*4096+(row&15)*128+((h^((r>>1)&7))<<4);
      u32 qq=(lane&15)>>2, w0=smb+2048+g1*1024+h*4096+qq*128+8*(lane&1)+(((2*((lane>>4)&1)+((lane>>1)&1))^((qq&2)<<1))<<4);
      #pragma unroll
      for(int i=0;i<2;i++)
        #pragma unroll
        for(int k=0;k<4;k++){ const u32 kk=(k+mi)&3, wo=i*2*STG; ax[i][k]=opq((a0^(kk*32u))+wo); aw[i][k][0]=opq(w0+kk*8192+wo); aw[i][k][1]=opq((w0^64u)+kk*8192+wo); } }
    u4 sel; { const u32 o=((lane&15)==((lane>>4)&1))?0x3f803f80u:0u; sel=u4{o,o,o,o}; asm volatile("" : "+v"(sel)); }
    u32 ga[11];
    #pragma unroll
    for(int i=0;i<11;i++) ga[i]=opq(cb+2u*(u32)rbf((float)(512-8*lane+3-i)));
    Frag F[2];
    asm volatile("s_waitcnt vmcnt(4)" : "+v"(epv),"+v"(pr[0]),"+v"(pr[1]),"+v"(pr[2]),"+v"(pr[3]),"+v"(pr[4]),"+v"(pr[5]) :: "memory");
    bar();
    tag=uni(epv)+1u; dsk=bflo(pr[5]);
    #pragma unroll
    for(int k=0;k<4;k++){
      #pragma unroll
      for(int n=0;n<2;n++){ u2 lo=ldstr(aw[0][k][n]), hi=ldstr(aw[0][k][n]+512); F[0].w[k][n]=u4{lo[0],lo[1],hi[0],hi[1]}; }
      F[0].x[k]=ldsr128(ax[0][k]); }
    issue(std::integral_constant<int,2>{});
    STAMP(1,0);
    f16v acc[2]={}; f4 cacc[2]={}; u4 G;
    const u32 go=OFF_RS+rt*2048+mi*512, cp=smb+MISC+g1*1024;
    sfor<10>([&](auto ic){
      constexpr int s=ic.value; Frag& cur=F[s&1]; Frag& nxt=F[(s&1)^1];
      if constexpr(s<9){ WAITV((s==8? 2 : s==7? 6 : 4)+((TS1<6&&(s==TS1+1||s==TS1+2))?1:0)); asm volatile("s_barrier" ::: "memory"); }
      auto rw=[&](int k,int n){ if constexpr(s<9){ const int i=((s+1)>>1)&1; const u32 so=((s+1)&1)*STG; u2 lo=ldstr(aw[i][k][n]+so), hi=ldstr(aw[i][k][n]+so+512); nxt.w[k][n]=u4{lo[0],lo[1],hi[0],hi[1]}; } };
      auto rx=[&](int k){ if constexpr(s<9){ const int i=((s+1)>>1)&1; const u32 so=((s+1)&1)*STG; nxt.x[k]=ldsr128(ax[i][k]+so); } };
      #pragma unroll
      for(int k=0;k<4;k++){
        acc[0]=mfma32b(cur.w[k][0],cur.x[k],acc[0]); SCHED();
        if constexpr(s+3<=9) issue1(std::integral_constant<int,s+3>{},k);
        rw(k,0); rx(k);
        SCHED();
        acc[1]=mfma32b(cur.w[k][1],cur.x[k],acc[1]); SCHED();
        rw(k,1);
        if(k==0){ cacc[0]=mfma16(cur.w[0][0],sel,cacc[0]); cacc[1]=mfma16(cur.w[0][1],sel,cacc[1]); }
        if constexpr(s==TS1 && TS1<6){ if(k==3){ if(g1) warm(mi,0); else { setm0<JNK>(smb); touch(srdS,0u,OFF_EP); } } }
        if constexpr(s==6){ if(k==3){ gload<0>(G,r*16,p.ws+go); if(g1) warm(mi,TS1<6?1:2); else { setm0<JNK>(smb); touch(srdS,0u,go); } } }
        if constexpr(s==8){ if(k==3) signal(smb+C_DONE,lane); }
        if constexpr(s==9){ if(k==1){
          if((lane&14)==0){ *(LAS f4*)(cp+mi*256+(lane&1)*64+(lane>>4)*16)=cacc[0]; *(LAS f4*)(cp+mi*256+128+(lane&1)*64+(lane>>4)*16)=cacc[1]; }
          signal(smb+C_CS+g1*4,lane); } }
        SCHED();
      }
    });
    RST(0);
    if(!g1) __builtin_amdgcn_s_setprio(2);
    asm volatile("s_waitcnt vmcnt(1)" : "+v"(G) :: "memory");
    ldswait(smb+C_CS+g1*4,4u);
    const float cs0=*(LAS float*)(cp+h*512+r*4)+*(LAS float*)(cp+h*512+256+r*4), cs1=*(LAS float*)(cp+h*512+128+r*4)+*(LAS float*)(cp+h*512+384+r*4);
    RST(2);
    #pragma unroll 1
    for(int sp=0;sp<SPIN && !__all(G[1]==tag&&G[3]==tag);sp++)asm volatile("global_load_dwordx4 %0, %1, %2 sc1\n\ts_waitcnt vmcnt(0)" : "=&v"(G) : "v"(r*16), "s"(p.ws+go) : "memory");
    RST(3);
    const float nm=-__uint_as_float(G[0]), ri1=__uint_as_float(G[2]);
    acc[0]=__builtin_amdgcn_mfma_f32_32x32x2f32(cs0,nm,acc[0],0,0,0); acc[1]=__builtin_amdgcn_mfma_f32_32x32x2f32(cs1,nm,acc[1],0,0,0);
    u32 zp[2][8]; const u32 zqa=smb+RB+mi*RING+4096+lane*16;
    if(g1){
      ldswait(smb+C_DONE,8u);
      #pragma unroll
      for(int i=0;i<4;i++){ u4 o;
        #pragma unroll
        for(int e=0;e<4;e++) o[e]=pkbf2(f2{acc[i>>1][8*(i&1)+2*e],acc[i>>1][8*(i&1)+2*e+1]}*ri1);
        ldsw128(zqa+i*1024,o); }
      signal(smb+C_K2+mi*4,lane); RST(4);
    } else {

      #pragma unroll
      for(int e=0;e<16;e++) zp[e>>3][e&7]=pkbf2(gelu2(f2{acc[e>>3][2*(e&7)],acc[e>>3][2*(e&7)+1]}*ri1));
      float S=0.f,Q=0.f;
      if(j<13){
        #pragma unroll
        for(int e=0;e<8;e++){ S=dot2(zp[0][e],0x3f803f80u,S); Q=dot2(zp[0][e],zp[0][e],Q); } }
      #pragma unroll
      for(int e=0;e<8;e++){ S=dot2(zp[1][e],0x3f803f80u,S); Q=dot2(zp[1][e],zp[1][e],Q); }
      #define XSUM32(v) do{ auto t_=__builtin_amdgcn_permlane32_swap(__float_as_uint(v),__float_as_uint(v),false,false); v=__uint_as_float(t_[0])+__uint_as_float(t_[1]); }while(0)
      XSUM32(S); XSUM32(Q);
      const u32 sto=OFF_ST+rt*(14*2048)+(32*mi+r)*16;
      if(h==0) st16(rs,sto+j*2048,u4{__float_as_uint(S),tag,__float_as_uint(Q),tag});
      RST(6);
    }
    float kp[11]; u4 g[7]={};
    const u32 gof=g1? OFF_FX+r*128 : OFF_ST+rt*(14*2048)+(32*mi+r)*16+h*(7*2048);
    auto samp=[&](){ asm volatile("" ::: "memory"); g[0]=ld16(rs,gof);
      if(!g1){
        #pragma unroll
        for(int i=1;i<7;i++) g[i]=ld16(rs,gof+i*2048); }
      asm volatile("" ::: "memory"); };
    if(hasc) ksc=kgen_tab(cb,lane,pr,kp,samp); else if(!g1){
      pre01(); ldswait(smb+C_K2+mi*4,1u); u4 zq[4];
      #pragma unroll
      for(int i=0;i<4;i++){ const u4 v=ldsr128(zqa+i*1024);
        #pragma unroll
        for(int e=0;e<4;e++) zq[i][e]=pkbf2(gelu2(unpk(v[e]))); }
      SCHED(); samp();
      #pragma unroll
      for(int n=0;n<2;n++) if(2*j+n<27){ const u32 oo=OFF_Z1T+((rt*8+2*mi+(r>>4))*27+2*j+n)*1024+(h*16+(r&15))*16;
        st16(rs,oo,u4{zq[2*n][0],zq[2*n][1],zq[2*n+1][0],zq[2*n+1][1]}); st16(rs,oo+512,u4{zq[2*n][2],zq[2*n][3],zq[2*n+1][2],zq[2*n+1][3]}); }
    }
    if(g1){
      if(w<7){ u4 uu[4]; const u32 fxv=g[0][0];
        if(hasc){
          RST(6);
          if(!__all(fxv==tag)) pollw<16>(rs,OFF_FX+r*128,tag);
          RST(7);
          #pragma unroll
          for(int i=0;i<4;i++) uu[i]=ld16(rs,OFF_XNT+((lane>>3)+8*i)*81920+dch*128+(lane&7)*16);
          asm volatile("" ::: "memory");
          kgen_exp(cb,lane,kp,ga);
          RST(8);
          #pragma unroll
          for(int i=0;i<4;i++){ const int x=lane+64*i; ldsw128(cb+UOF+(x&3)*1040+((x>>2)+1)*16,uu[i]); }
          if(lane<4) ldsw128(cb+UOF+lane*1040,u4{0,0,0,0});
          WAITL0(); signal(smb+C_KP+mi*4,lane);
        }
        RST(9);
      }
    } else {
      RST(1);
      float S,Q;
      #pragma unroll 1
      for(int sp=0;sp<SPIN;sp++){ bool ok=true;
        #pragma unroll
        for(int i=0;i<7;i++) ok=ok&&g[i][1]==tag&&g[i][3]==tag;
        if(__all(ok)) break;
        __builtin_amdgcn_s_sleep(4); samp(); }
      RST(7);
      S=0.f; Q=0.f;
      #pragma unroll
      for(int i=0;i<7;i++){ S+=__uint_as_float(g[i][0]); Q+=__uint_as_float(g[i][2]); }
      XSUM32(S); XSUM32(Q);
      float mean=S*(1.f/864.f), m2,r2; ln_fin(mean,Q*(1.f/864.f)-mean*mean,m2,r2); m2*=-r2;
      ldswait(smb+C_DONE,8u);
      const u32 tb=smb+RB+mi*RING, ad=tb+(8*(lane>>4)+((lane&15)>>2))*80+(lane&3)*8;
      const u32 zo=OFF_Z2F+((rt*4+mi)*54+(j==13?-2:2+4*j))*1024+lane*16;
      #pragma unroll
      for(int n=0;n<2;n++){
        #pragma unroll
        for(int g2=0;g2<4;g2++){ u2 o;
          #pragma unroll
          for(int e=0;e<2;e++) o[e]=pkbf2(fma2(unpk(zp[n][2*g2+e]),f2{r2,r2},f2{m2,m2}));
          *(LAS u2*)(tb+r*80+g2*16+h*8)=o; }
        u2 l0=ldstr(ad), h0=ldstr(ad+320), l1=ldstr(ad+32), h1=ldstr(ad+32+320);
        if(j<13||n){ st16(rs,zo+n*2048,u4{l0[0],l0[1],h0[0],h0[1]}); st16(rs,zo+n*2048+1024,u4{l1[0],l1[1],h1[0],h1[1]}); } }
      RST(8);
      WAITV(0);
      if(arrive(smb+C_Z2,lane)==3u && lane==0) st4(rs,OFF_F2+(rt*16+j)*128,tag);
      RST(9);
    }
  }
  const int ft0=ct<7?7*ct:47;
  bool adone=true; const u32 fao=OFF_FA+((lane<28?lane:0)*8+ct)*128;
  auto aload=[&](){ const u32 vo=(lane>>3)*4096+(lane&7)*16;
    #pragma unroll 1
    for(int i=0;i<10;i++){ setm0<ATL>(smb+i*1024); dma<0>(srdS,vo,OFF_AT+(u32)(80*ct+8*i)*4096+rtB*128); } };
  if(w>=4){
    if(hasc){
      __builtin_amdgcn_s_setprio(CPRIO);
      conv_wave(p,rs,dch,cb,smb+C_KP+mi*4,lane,ksc,dsk);
      __builtin_amdgcn_s_setprio(0);
      if(arrive(smb+C_CV,lane)==(idx<24?2u:1u) && lane==0) st4(rs,OFF_FA+b*128,tag);
    }
    if(w==7){
      __builtin_amdgcn_s_setprio(HPRIO);
      if(idx<28){
        #pragma unroll 1
        for(int c=0;c<(idx<24?3:2);c++){ ldswait(smb+C_KP+c*4,1u); conv_help(smb+c*CVS,lane); signal(smb+C_KP+c*4,lane); } }
      __builtin_amdgcn_s_setprio(0);
      RST(6);
    }
    RST(10);
    ldswait(smb+C_Z1R,4u);
    u4 wq[24];
    #pragma unroll
    for(int i=0;i<24;i++) wq[i]=ld16(rs,OFF_WOF+ct*(135*1024)+40960+mi*RING+i*1024+lane*16);
    if(w<6){
      #pragma unroll 1
      for(u32 i=0;i<20480;i+=4096){ setm0<0>(cb+i); const u32 o=OFF_WOF+ct*(135*1024)+mi*20480+i; dma<0>(srdS,lane*16,o); dma<1024>(srdS,lane*16,o); dma<2048>(srdS,lane*16,o); dma<3072>(srdS,lane*16,o); } }
    if(w==6){ adone=__all(ld4(rs,fao)==tag); if(adone) aload(); }
    ldswait(smb+C_FREE,4u);
    #pragma unroll
    for(int i=0;i<24;i++) ldsw128(smb+RB+mi*RING+i*1024+lane*16,wq[i]);
    if(w<6) WAITV(0);
    signal(smb+C_WR,lane);
    if(!adone){ adone=__all(ld4(rs,fao)==tag); if(adone) aload(); }
    if(w==7 && b==255){
      #pragma unroll 1
      for(int sp=0;sp<SPIN;sp++){ const u32 o=OFF_FA+lane*128; u32 v0=ld4(rs,o),v1=ld4(rs,o+8192),v2=ld4(rs,o+16384),v3=ld4(rs,lane<32? o+24576 : OFF_FW+(lane-32)*128); asm volatile("" ::: "memory");
        if(__all(v0==tag&&v1==tag&&v2==tag&&v3==tag)) break; __builtin_amdgcn_s_sleep(4); }
      if(lane==0) st4(rs,OFF_EP,tag); }
  }
  int w2=w; asm volatile("" : "+s"(w2));
  if(w2<4){
    __builtin_amdgcn_s_setprio(G2PRIO);
    int r=lane&15, q=lane>>4;
    const u32 rb=smb+RB+w*RING;
    u4 srdA=mksrd((const char*)p.wsp+(size_t)rtB*64*4096+w*1024-4096+256);
    const char* zb=p.ws+OFF_Z2F+(u32)(16*w*54+ft0+3)*1024;
    u32 voA[2]; { u32 lr=lane>>3,c=lane&7; voA[0]=lr*4096+((c^(lr>>1))<<4); voA[1]=voA[0]^64u; }
    u32 sofa[8];
    #pragma unroll
    for(int g=0;g<8;g++) sofa[g]=((g+2*w)&7)*32768+4096-(g&3)*1024;
    const u32 voB=lane*16;
    u32 la[2]; la[0]=rb+r*128+((q^(r>>1))<<4); la[1]=la[0]^64u;
    f4 acc[4][7]={}; u4 B[3][7]; u4 A[2][4]; u2 z1[7];
    auto issueA=[&](auto sc,auto gc){ constexpr int s=sc.value,g=gc.value; if constexpr((g&3)==0) setm0<((s+1)%3)*8192+g*1024>(rb); dma<(g&3)*1024>(srdA,voA[g&1],sofa[g]);
      if constexpr(g==7){ u64 a=((u64)srdA[1]<<32|srdA[0])+128; srdA[0]=(u32)a; srdA[1]=(u32)(a>>32); } };

    u32 bsr,fv;
    auto fof=[&](){ const u32 l=voB>>4; return l<27? OFF_FW+l*128 : OFF_F2+((rtB>>1)*16+(((ft0>>1)+(l&3))>>1))*128; };
    #pragma unroll
    for(int m=0;m<4;m++){ A[0][m]=ldsr128(la[0]+m*2048+8192); A[1][m]=ldsr128(la[1]+m*2048+8192); }
    sfor<8>([&](auto g){ issueA(std::integral_constant<int,2>{},g); });
    waitA(A[0]); waitA(A[1]);
    sfor<8>([&](auto g){ issueA(std::integral_constant<int,3>{},g); });
    #pragma unroll
    for(int m=0;m<4;m++)
      #pragma unroll
      for(int n=0;n<7;n++) asm volatile("" : "+v"(acc[m][n]));
    RST(2);
    { const int hf=(ft0>>1)+q; pollw(rs,OFF_F2+((4*w+(lane&3))*16+(hf==0?13:(hf-1)>>1))*128,tag); }
    RST(10);
    sfor<3>([&](auto hh){ const char* s=zb+(u32)hh.value*(54*1024); sfor<7>([&](auto n){ gload<(n.value-3)*1024>(B[hh][n],voB,s); }); });
    auto redw=[&](auto nc){ constexpr int n=nc.value; *(LAS f4*)(rb+n*1024+voB)=acc[1][n]; *(LAS f4*)(rb+(7+n)*1024+voB)=acc[2][n]; *(LAS f4*)(rb+(14+n)*1024+voB)=acc[3][n]; };
    const char* z1b=p.ws+OFF_Z1T+(u32)((rtB*4+w)*27+(ft0>>1))*1024;
    sfor<16>([&](auto hc){
      constexpr int h=hc.value, S=h%3, P=h&1;
      constexpr int cnt = h<4? 14 : ((h+1<16?7:0)+(h+2<16?7:0) + ((((h-2)&1)&&((h-2)/2+3<8))?8:0) + ((((h-1)&1)&&((h-1)/2+3<8))?8:0) + (h>=14?8:0) + ((h==11||h==12)?1:0));
      waitB<cnt>(B[S]); waitA(A[P]);
      if constexpr(h==13){ tie1(fv); if(!__all(fv==tag)) pollw(rs,fof(),tag); }
      if constexpr(h+1<16 && h>0){
        #pragma unroll
        for(int m=0;m<4;m++) A[P^1][m]=ldsr128(la[(h+1)&1]+m*2048+((((h+1)>>1)+1)%3)*8192);
      }
      if constexpr(h==10) gload4<0>(fv,fof(),p.ws);
      const char* s=zb+(u32)(h+3)*(54*1024);
      sfor<7>([&](auto n){
        mfma4(acc[0][n],acc[1][n],acc[2][n],acc[3][n],B[S][n],A[P]);
        if constexpr((h&1)&&(h/2+3<8)&&h>=3){ issueA(std::integral_constant<int,h/2+3>{},n); if constexpr(n.value==6) issueA(std::integral_constant<int,h/2+3>{},std::integral_constant<int,7>{}); }
        if constexpr(h+3<16) gload_t<(n.value-3)*1024>(B[S][n],voB,s);
        if constexpr(h==13){ gload8<(n.value>>1)*1024>(z1[n],voB,z1b+((n.value&1)? ((ft0&1)?1024:8) : (ft0&1)*8));
          if constexpr(n.value==6) { gload2(bsr,(voB>>3)&30u,(const char*)(p.bsp+(rtB*4+w)*16)); signal(smb+C_Z1R,lane); } }
        if constexpr(h==15){ SCHED(); if constexpr(n.value>=2) redw(std::integral_constant<int,n.value-2>{}); SCHED(); }
      });
    });
    asm volatile("s_nop 15\n\ts_nop 3" ::: "memory");
    SCHED();
    STAMP(10,0);
    lane=__builtin_amdgcn_mbcnt_hi(~0u,__builtin_amdgcn_mbcnt_lo(~0u,0u)); asm volatile("" : "+v"(lane)); r=lane&15; q=lane>>4;
    redw(std::integral_constant<int,5>{}); redw(std::integral_constant<int,6>{});
    signal(smb+C_RED,lane);
    ldswait(smb+C_RED,4u);
    #pragma unroll
    for(int m=1;m<4;m++){ u32 src=smb+RB+((w-m)&3)*RING+(m-1)*7168+lane*16;
      #pragma unroll
      for(int n=0;n<7;n++) acc[0][n]+=*(LAS f4*)(src+n*1024); }
    asm volatile("s_waitcnt vmcnt(0) lgkmcnt(0)" : "+v"(z1[0]),"+v"(z1[1]),"+v"(z1[2]),"+v"(z1[3]),"+v"(z1[4]),"+v"(z1[5]),"+v"(z1[6]),"+v"(bsr) :: "memory");
    const float bs=bflo(bsr);
    signal(smb+C_FREE,lane);
    u2 o[7];
    #pragma unroll
    for(int n=0;n<7;n++)
      #pragma unroll
      for(int e=0;e<2;e++)o[n][e]=pkbf2(unpk(z1[n][e])*(f2{acc[0][n][2*e],acc[0][n][2*e+1]}+bs));
    const u32 ho=OFF_HF+(u32)((rtB*4+w)*54+ft0)*512+(q>>1)*256+r*16;
    #pragma unroll
    for(int n=0;n<6;n+=2){ auto sx=__builtin_amdgcn_permlane16_swap(o[n][0],o[n+1][0],false,false); auto sy=__builtin_amdgcn_permlane16_swap(o[n][1],o[n+1][1],false,false);
      if(ct<7||n>=2) st16(rs,ho+(n+(q&1))*512,u4{sx[0],sy[0],sx[1],sy[1]}); }
    st8(rs,ho+6*512+(q&1)*8,o[6]);
    STAMP(11,0);
    WAITV(0);
    if(arrive(smb+C_H,lane)==3u && lane==0) st4(rs,OFF_FH+(rtB*8+ct)*128,tag);
    STAMP(12,0);
    __builtin_amdgcn_s_setprio(0);
  }
  { const int r=lane&15, q=lane>>4, m=w&3, kh=w>>2;
    u2 xv[5]={};
    if(kh){
      #pragma unroll
      for(int n=0;n<5;n++) xv[n]=*(const u2*)(p.x+(rtB*64+m*16+r)*640+80*ct+16*n+4*q);
    }
    f4 acc[5]={}; u4 W[3][5];
    const u32 wl2=smb+lane*16+RB-40960+kh*14*5120, wl0=kh? wl2 : smb+lane*16, wl1=kh? wl2 : smb+lane*16+CVS-20480;
    #define WAD(k,n) (((k)<4? wl0 : (k)<8? wl1 : wl2)+((k)*5+(n))*1024)
    f4 ax[5]={};
    auto addax=[&](){
      #pragma unroll
      for(int n=0;n<5;n++){ u2 av=ldstr(smb+ATL+(16*n+4*q+(r>>2))*128+(16*m+4*(r&3))*2); ax[n]=f4{bflo(av[0]),bfhi(av[0]),bflo(av[1]),bfhi(av[1])}+f4{bflo(xv[n][0]),bfhi(xv[n][0]),bflo(xv[n][1]),bfhi(xv[n][1])}; } };
    if(w==6 && adone){ WAITV(0); signal(smb+C_AT,lane); }
    u32 oo[3],to[3]; const u32 tb=smb+CVS+m*3072;
    #pragma unroll
    for(int i=0;i<3;i++){ const int it=lane+64*i, rr=(it*205)>>11, cc=it-rr*10; oo[i]=opq((rtB*64+m*16+rr)*1280+ct*160+cc*16); to[i]=opq(tb+rr*176+cc*16); }
    if(m==0){ ldswait(smb+C_H,4u); pollw(rs,OFF_FH+(rtB*8+kh*4+(lane&3))*128,tag); signal(smb+C_TAG+kh*4,lane); } else ldswait(smb+C_TAG+kh*4,1u);
    STAMP(16,0);
    u4 hf[14];
    const u32 ho=OFF_HF+(u32)((rtB*4+m)*27+kh*14)*1024+lane*16;
    #pragma unroll
    for(int k=0;k<HPF;k++) hf[k]=ld16(rs,ho+k*1024);
    const bool early=kh && uni(ldsv(smb+C_AT))!=0u;
    if(early) addax();
    ldswait(smb+C_WR,4u);
    STAMP(17,0);
    const u32 wl3=kh? wl2-5120 : wl2;
    #pragma unroll
    for(int n=0;n<10;n++) W[n/5][n%5]=ldsr128(WAD(n/5,n%5));
    #pragma unroll
    for(int k=0;k<13;k++){
      if(k+HPF<14) hf[k+HPF]=ld16(rs,ho+((k+HPF==13&&kh)?12:k+HPF)*1024);
      SCHED();
      #pragma unroll
      for(int n=0;n<5;n++){ acc[n]=mfma16(W[k%3][n],hf[k],acc[n]); SCHED();
        if(k<11) W[(k+2)%3][n]=ldsr128(WAD(k+2,n)); else if(k==11) W[1][n]=ldsr128(wl3+(65+n)*1024);
        SCHED(); }
      if(k==5 && !kh) signal(smb+C_G1,lane);
    }
    const u32 pb=smb+m*5120+lane*16;
    if(!kh){
      #pragma unroll
      for(int n=0;n<5;n++) acc[n]=mfma16(W[1][n],hf[13],acc[n]);
      STAMP(18,0);
      ldswait(smb+C_G1,4u);
      #pragma unroll
      for(int n=0;n<5;n++) *(LAS f4*)(pb+n*1024)=acc[n];
      signal(smb+C_P+m*4,lane);
    } else {
      if(w==6 && !adone){ pollw(rs,fao,tag); aload(); WAITV(0); signal(smb+C_AT,lane); }
      if(!early){ ldswait(smb+C_AT,1u); addax(); }
      RST(7);
      ldswait(smb+C_P+m*4,1u);
      #pragma unroll
      for(int n=0;n<5;n++){ const f4 o=(acc[n]+ax[n])+*(LAS f4*)(pb+n*1024); *(LAS u2*)(tb+r*176+n*32+q*8)=u2{pkbf(o[0],o[1]),pkbf(o[2],o[3])}; }
      #pragma unroll
      for(int i=0;i<3;i++) if(i<2||lane<32) *(u4*)((char*)p.out+oo[i])=ldsr128(to[i]);
      STAMP(19,4);
    }
  }
}
extern "C" void kernel_launch(void* const* d_in,const int* in_sizes,int n_in,void* d_out,int out_size,void* d_ws,size_t ws_size,hipStream_t stream){
  Params p;
  p.x=(const u16*)d_in[0]; p.lar=(const u16*)d_in[1]; p.aim=(const u16*)d_in[2]; p.cre=(const u16*)d_in[3]; p.cim=(const u16*)d_in[4];
  p.ldt=(const u16*)d_in[5]; p.dsk=(const u16*)d_in[6]; p.win=(const u16*)d_in[8]; p.wsp=(const u16*)d_in[9]; p.bsp=(const u16*)d_in[10]; p.wout=(const u16*)d_in[11];
  p.out=(u16*)d_out; p.ws=(char*)d_ws;
  (void)hipFuncSetAttribute((const void*)fused,hipFuncAttributeMaxDynamicSharedMemorySize,LDS_TOTAL);
  fused<<<256,512,LDS_TOTAL,stream>>>(p);
}
```

```cpp
#include <hip/hip_runtime.h>
#include <utility>
#pragma clang diagnostic ignored "-Wint-to-pointer-cast"
typedef unsigned short u16;
typedef unsigned int u32;
typedef unsigned long long u64;
typedef __attribute__((ext_vector_type(8))) __bf16 bf8;
typedef __attribute__((ext_vector_type(8))) _Float16 hf8;
typedef __attribute__((ext_vector_type(2))) __bf16 bf2;
typedef __attribute__((ext_vector_type(2))) _Float16 hf2;
typedef __attribute__((ext_vector_type(2))) float f2;
typedef __attribute__((ext_vector_type(4))) float f4;
typedef __attribute__((ext_vector_type(16))) float f16v;
typedef __attribute__((ext_vector_type(4))) u32 u4;
typedef __attribute__((ext_vector_type(2))) u32 u2;
typedef __attribute__((ext_vector_type(4))) short s4;
#define LAS __attribute__((address_space(3)))
#define GAS __attribute__((address_space(1)))
#define DI __device__ __forceinline__

constexpr u32 OFF_Z2F = 0;
constexpr u32 OFF_Z1T = OFF_Z2F + 64*54*1024;
constexpr u32 OFF_XNT = OFF_Z1T + 128*27*1024;
constexpr u32 OFF_WOF = OFF_XNT + 640*4096;
constexpr u32 OFF_HF  = OFF_WOF + 8*27*5*1024;
constexpr u32 OFF_AT  = OFF_HF + 128*27*1024;
constexpr u32 OFF_ST  = OFF_AT + 640*4096;
constexpr u32 OFF_RS  = OFF_ST + 16*14*2048+8192;
constexpr u32 OFF_EP  = OFF_RS + 2048*16;
constexpr u32 OFF_F2  = OFF_EP + 256;
constexpr u32 OFF_F1  = OFF_F2 + 256*128;
constexpr u32 OFF_FX  = OFF_F1 + 256*128;
constexpr u32 OFF_FW  = OFF_FX + 256*128;
constexpr u32 OFF_FH  = OFF_FW + 32*128;
constexpr u32 OFF_FA  = OFF_FH + 256*128;
constexpr u32 WS_END  = OFF_FA + 256*128;

struct Params {
  const u16 *x, *lar, *aim, *cre, *cim, *ldt, *dsk, *win, *wsp, *bsp, *wout;
  u16* out; char* ws;
};
#define STAMP(id,wv) do{}while(0)
#define RST(i) do{}while(0)
template<class F,int... I> DI void sfor_impl(F&& f, std::integer_sequence<int,I...>){ (f(std::integral_constant<int,I>{}),...); }
template<int N,class F> DI void sfor(F&& f){ sfor_impl(f, std::make_integer_sequence<int,N>{}); }

DI float bflo(u32 v){ return __uint_as_float(v<<16); }
DI float bfhi(u32 v){ return __uint_as_float(v&0xffff0000u); }
DI float bf1(u16 v){ return __uint_as_float(((u32)v)<<16); }
DI u32 pkbf(float a,float b){ f2 v={a,b}; return __builtin_bit_cast(u32,__builtin_convertvector(v,bf2)); }
DI u32 pkhf(float a,float b){ f2 v={a,b}; return __builtin_bit_cast(u32,__builtin_convertvector(v,hf2)); }
DI float rbf(float a){ return bflo(pkbf(a,a)); }
DI float hlo(u32 v){ return (float)__builtin_bit_cast(hf2,v)[0]; }
DI float hhi(u32 v){ return (float)__builtin_bit_cast(hf2,v)[1]; }
DI float dot2(u32 a,u32 b,float c){ return __builtin_amdgcn_fdot2_f32_bf16(__builtin_bit_cast(bf2,a),__builtin_bit_cast(bf2,b),c,false); }
DI f4 mfma16(u4 a,u4 b,f4 c){ return __builtin_amdgcn_mfma_f32_16x16x32_bf16(__builtin_bit_cast(bf8,a),__builtin_bit_cast(bf8,b),c,0,0,0); }
DI f16v mfma32h(u4 a,u4 b,f16v c){ return __builtin_amdgcn_mfma_f32_32x32x16_f16(__builtin_bit_cast(hf8,a),__builtin_bit_cast(hf8,b),c,0,0,0); }
DI u4 ldsr128(u32 a){ return *(LAS u4*)a; }
DI u2 ldsr64(u32 a){ return *(LAS u2*)a; }
DI void ldsw128(u32 a,u4 v){ *(LAS u4*)a=v; }
DI u2 ldstr(u32 a){ return __builtin_bit_cast(u2,__builtin_amdgcn_ds_read_tr16_b64_v4i16((LAS s4*)a)); }
DI u32 uni(u32 v){ return __builtin_amdgcn_readfirstlane(v); }

template<int OFF> DI void gload(u4& dst,u32 voff,const char* base){ asm volatile("global_load_dwordx4 %0, %1, %2 offset:%3 sc1" : "=v"(dst) : "v"(voff), "s"(base), "n"(OFF)); }
template<int OFF> DI void gload_t(u4& dst,u32 voff,const char* base){ asm volatile("global_load_dwordx4 %0, %1, %2 offset:%3 sc1" : "+v"(dst) : "v"(voff), "s"(base), "n"(OFF)); }
DI void mfma4(f4& c0,f4& c1,f4& c2,f4& c3,const u4& a,const u4 (&b)[4]){
  asm volatile("v_mfma_f32_16x16x32_bf16 %0, %4, %5, %0\n\tv_mfma_f32_16x16x32_bf16 %1, %4, %6, %1\n\tv_mfma_f32_16x16x32_bf16 %2, %4, %7, %2\n\tv_mfma_f32_16x16x32_bf16 %3, %4, %8, %3"
    : "+v"(c0),"+v"(c1),"+v"(c2),"+v"(c3) : "v"(a),"v"(b[0]),"v"(b[1]),"v"(b[2]),"v"(b[3])); }
template<int CNT> DI void waitB(u4 (&B)[7]){ asm volatile("s_waitcnt vmcnt(%7)" : "+v"(B[0]),"+v"(B[1]),"+v"(B[2]),"+v"(B[3]),"+v"(B[4]),"+v"(B[5]),"+v"(B[6]) : "n"(CNT) : "memory"); }
DI void waitA(u4 (&A)[4]){ asm volatile("s_waitcnt lgkmcnt(0)" : "+v"(A[0]),"+v"(A[1]),"+v"(A[2]),"+v"(A[3]) :: "memory"); }
#define WAITV(n) asm volatile("s_waitcnt vmcnt(%0)" :: "n"(n) : "memory")
#define WAITL0() asm volatile("s_waitcnt lgkmcnt(0)" ::: "memory")
#define SCHED() __builtin_amdgcn_sched_barrier(0)
DI void bar(){ asm volatile("s_waitcnt lgkmcnt(0)\n\ts_barrier" ::: "memory"); }
DI f2 unpk(u32 v){ return f2{bflo(v),bfhi(v)}; }
DI u32 pkbf2(f2 v){ return __builtin_bit_cast(u32,__builtin_convertvector(v,bf2)); }
DI u32 pkhf2(f2 v){ return __builtin_bit_cast(u32,__builtin_convertvector(v,hf2)); }
DI f2 fma2(f2 a,f2 b,f2 c){ return __builtin_elementwise_fma(a,b,c); }
DI f2 gelu2(f2 x){
  f2 u=x*fma2(x*x,f2{-0.10294324f,-0.10294324f},f2{-2.30220820f,-2.30220820f});
  f2 d=f2{__builtin_amdgcn_exp2f(u[0]),__builtin_amdgcn_exp2f(u[1])}+f2{1.f,1.f};
  return x*f2{__builtin_amdgcn_rcpf(d[0]),__builtin_amdgcn_rcpf(d[1])};
}
DI void ln_fin(float mean,float var,float& m,float& rinv){
  m=rbf(mean); float v=rbf(__builtin_fmaxf(var,0.f));
  float s=rbf(__builtin_amdgcn_sqrtf(rbf(v+1e-5f))); float r=__builtin_amdgcn_rcpf(s); rinv=__builtin_fmaf(__builtin_fmaf(-s,r,1.f),r,r);
}
typedef __amdgpu_buffer_rsrc_t rsrc_t;
DI void st16(rsrc_t rs,u32 off,u4 v){ __builtin_amdgcn_raw_buffer_store_b128(v,rs,off,0,16); }
DI void st8(rsrc_t rs,u32 off,u2 v){ __builtin_amdgcn_raw_buffer_store_b64(v,rs,off,0,16); }
DI void st4(rsrc_t rs,u32 off,u32 v){ __builtin_amdgcn_raw_buffer_store_b32(v,rs,off,0,16); }
DI u4 ld16(rsrc_t rs,u32 off){ return __builtin_amdgcn_raw_buffer_load_b128(rs,off,0,16); }
DI u2 ld8(rsrc_t rs,u32 off){ return __builtin_amdgcn_raw_buffer_load_b64(rs,off,0,16); }
DI u32 ld4(rsrc_t rs,u32 off){ return __builtin_amdgcn_raw_buffer_load_b32(rs,off,0,16); }
constexpr int SPIN=1<<17;
template<int SL=2> DI void pollw(rsrc_t rs,u32 off,u32 tag){
  #pragma unroll 1
  for(int s=0;s<SPIN;s++){ u32 v=ld4(rs,off); asm volatile("" ::: "memory"); if(__all(v==tag)) break; __builtin_amdgcn_s_sleep(SL); }
}

DI f16v mfma32b(u4 a,u4 b,f16v c){ return __builtin_amdgcn_mfma_f32_32x32x16_bf16(__builtin_bit_cast(bf8,a),__builtin_bit_cast(bf8,b),c,0,0,0); }
DI u4 mksrd(const void* b){ u64 a=(u64)b; return u4{(u32)a,(u32)(a>>32)&0xffffu,0xffffffffu,0x00020000u}; }
template<int IMM> DI void setm0(u32 base){ asm volatile("s_add_u32 m0, %0, %1\n\ts_nop 0" :: "s"(base), "n"(IMM) : "scc","memory"); }
template<int OFF> DI void dma(u4 srd,u32 voff,u32 soff){ asm volatile("buffer_load_dwordx4 %0, %1, %2 offen offset:%3 sc1 lds" :: "v"(voff), "s"(srd), "s"(soff), "n"(OFF) : "memory"); }
DI void touch(u4 srd,u32 voff,u32 soff){ asm volatile("buffer_load_dword %0, %1, %2 offen sc1 lds" :: "v"(voff), "s"(srd), "s"(soff) : "memory"); }
template<int OFF> DI void gload8(u2& dst,u32 voff,const char* base){ asm volatile("global_load_dwordx2 %0, %1, %2 offset:%3 sc1" : "=v"(dst) : "v"(voff), "s"(base), "n"(OFF)); }
DI void gload2(u32& dst,u32 voff,const char* base){ asm volatile("global_load_ushort %0, %1, %2" : "=v"(dst) : "v"(voff), "s"(base)); }
DI u32 arrive(u32 a,int){ u32 o; asm volatile("s_mov_b64 exec, 1\n\tds_add_rtn_u32 %0, %1, %2\n\ts_mov_b64 exec, -1\n\ts_waitcnt lgkmcnt(0)" : "=&v"(o) : "v"(a), "v"(1u) : "memory"); return uni(o); }
DI void ldswait(u32 a,u32 n){ while(__hip_atomic_load((LAS u32*)a,__ATOMIC_RELAXED,__HIP_MEMORY_SCOPE_WORKGROUP)<n) __builtin_amdgcn_s_sleep(1); asm volatile("" ::: "memory"); }
DI float addf(float a,float b){ float o; asm("v_add_f32 %0, %1, %2" : "=v"(o) : "v"(a), "v"(b)); return o; }
DI void tie1(u32& g){ asm volatile("" : "+v"(g) :: "memory"); }
DI u32 opq(u32 v){ asm volatile("" : "+v"(v)); return v; }

constexpr u32 STG=32768;
constexpr u32 RING=24576, RB=62848, CONV=0, KP=4192, CVS=4*KP+4160, UOF=4*KP, MISC=RB+4*RING, JNK=MISC+2048, PM=163840-128, LDS_TOTAL=163840;
constexpr u32 ATL=2*CVS;
constexpr u32 C_FULL=PM, C_G1=PM+4, C_Z2=PM+8, C_Z1=PM+12, C_RED=PM+16, C_H=PM+20, C_FREE=PM+24, C_CV=PM+28, C_Z1R=PM+32, C_TAG=PM+40, C_KP=PM+48, C_DONE=PM+64, C_ZQ=PM+68, C_CS=PM+72, C_K2=PM+80, C_P=PM+96, C_AT=PM+112, C_WR=PM+116;
constexpr u32 CODE_BYTES=38912;
#define LA 3
#define TC_S 7
#define G2PRIO 2
#define XMAX 9
#define GS_K 35
#define WNT ""
#define TS1 6
#define HPRIO 3
#define CPRIO 3
#define POLLM 1
#define HPF 5
#define CVSKIP 0
#define BROT 0
#define PDLY 8
#define RS_S 7
#define KROT ((((j+rt)&3)*5)>>1)
DI void signal(u32 a,int){ asm volatile("s_mov_b64 exec, 1\n\tds_add_u32 %0, %1\n\ts_mov_b64 exec, -1" :: "v"(a), "v"(1u) : "memory"); }
template<int OFF> DI void gload4(u32& dst,u32 voff,const char* base){ asm volatile("global_load_dword %0, %1, %2 offset:%3 sc1" : "=v"(dst) : "v"(voff), "s"(base), "n"(OFF)); }
template<int CNT> DI void waitG(u4& g){ asm volatile("s_waitcnt vmcnt(%1)" : "+v"(g) : "n"(CNT) : "memory"); }

DI f2 cmulp(f2 a,f2 b,f2 bs){ return fma2(f2{a[1],a[1]},bs,f2{a[0],a[0]}*b); }
DI float mulf(float a,float b){ float o; asm("v_mul_f32 %0, %1, %2" : "=v"(o) : "v"(a),"v"(b)); return o; }
DI float fmaf1(float a,float b,float c){ float o; asm("v_fma_f32 %0, %1, %2, %3" : "=v"(o) : "v"(a),"v"(b),"v"(c)); return o; }
DI u32 ldsv(u32 a){ return __hip_atomic_load((LAS u32*)a,__ATOMIC_RELAXED,__HIP_MEMORY_SCOPE_WORKGROUP); }
DI void ldswait4(u32 a,int lane,u32 n){ while(__any(ldsv(a+(lane&3)*4)<n)) __builtin_amdgcn_s_sleep(1); asm volatile("" ::: "memory"); }
DI u32 gettag(u32 a){ ldswait(a+4,1u); return uni(ldsv(a)); }
DI f2 cpow_dir(float lr,float rv,float pw){ float e=__builtin_amdgcn_exp2f(lr*pw); float f=__builtin_amdgcn_fractf(rv*pw);
  return f2{e*__builtin_amdgcn_cosf(f),e*__builtin_amdgcn_sinf(f)}; }
DI u32 dup(u32 a){ return __builtin_amdgcn_perm(a,a,0x01000100u); }
DI u32 cat(u32 lo,u32 hi){ return __builtin_amdgcn_perm(hi,lo,0x05040100u); }
DI u32 lds16(u32 a){ return *(LAS u16*)a; }

template<class CB> DI float kgen_tab(u32 cb,int lane,const u32 (&pr)[6],float (&kp)[11],CB&& mid){
  const float dt=rbf(__expf(bflo(pr[4])));
  const float cr[2]={bflo(pr[2]),bfhi(pr[2])}, cm[2]={bflo(pr[3]),bfhi(pr[3])};
  float mx=fmaxf(fmaxf(fabsf(cr[0]),fabsf(cm[0])),fmaxf(fabsf(cr[1]),fabsf(cm[1])));
  u32 eb=__float_as_uint(mx)&0x7f800000u;
  #define DPPMAX(c) eb=max(eb,(u32)__builtin_amdgcn_update_dpp(0,(int)eb,c,0xf,0xf,false))
  DPPMAX(0x128); DPPMAX(0x124); DPPMAX(0x122); DPPMAX(0x121);
  eb=max(max((u32)__builtin_amdgcn_readlane(eb,0),(u32)__builtin_amdgcn_readlane(eb,16)),max((u32)__builtin_amdgcn_readlane(eb,32),(u32)__builtin_amdgcn_readlane(eb,48))); eb=eb<0x01000000u?0x3f800000u:(eb>0x7e000000u?0x7e000000u:eb);
  const float ksc=2.f*dt*__uint_as_float(eb), isc=__uint_as_float(0x7f000000u-eb);
  const int r=lane&31,h=lane>>5;
  const u32 TP=272, TA=cb, TB=cb+22*TP;
  f16v acc={};
  f2 an[2][4];
  #pragma unroll
  for(int c=0;c<2;c++){ const float l_a=c?bfhi(pr[0]):bflo(pr[0]), i_m=c?bfhi(pr[1]):bflo(pr[1]);
    const float lr=(-rbf(__expf(l_a))*dt)*1.44269504f, rv=(i_m*dt)*0.15915494f;
    an[c][0]=cpow_dir(lr,rv,1.f); an[c][1]=cpow_dir(lr,-rv,8.f); an[c][2]=cpow_dir(lr,-rv,16.f); an[c][3]=cpow_dir(lr,rv,256.f); }
  #pragma unroll
  for(int c=0;c<2;c++){
    const float c_r=cr[c], c_i=cm[c];
    const f2 z1=an[c][0], z8=an[c][1], zs=an[c][2], zk=an[c][3];
    const f2 z1s={-z1[1],z1[0]}, zss={-zs[1],zs[0]}, zks={-zk[1],zk[0]};
    const u32 wa=TA+lane*4, wb=TB+lane*4;
    #define PUT(base,row,v) do{ *(LAS u32*)((base)+(row)*TP)=pkhf2(v); }while(0)
    f2 e={1.f,0.f}, o=z8;
    #pragma unroll
    for(int k=0;k<16;k++){ PUT(wb,2*k,e); PUT(wb,2*k+1,o); if(k<15){ e=k?cmulp(e,zs,zss):zs; o=cmulp(o,zs,zss);} }
    f2 b0={c_r*isc,c_i*isc}; PUT(wa,0,b0);
    f2 t=cmulp(b0,zk,zks); PUT(wa,8,t);
    f2 b1=cmulp(b0,z1,z1s); PUT(wa,1,b1); t=cmulp(t,zk,zks); PUT(wa,10,t);
    f2 b2=cmulp(b1,z1,z1s); PUT(wa,2,b2); t=cmulp(t,zk,zks); PUT(wa,11,t);
    f2 b3=cmulp(b2,z1,z1s); PUT(wa,3,b3); t=cmulp(t,zk,zks); PUT(wa,16,t);
    f2 b4=cmulp(b3,z1,z1s); PUT(wa,4,b4); t=cmulp(t,zk,zks); PUT(wa,17,t);
    f2 b5=cmulp(b4,z1,z1s); PUT(wa,5,b5); t=cmulp(t,zk,zks); PUT(wa,20,t);
    f2 b6=cmulp(b5,z1,z1s); PUT(wa,6,b6); t=cmulp(t,zk,zks); PUT(wa,21,t);
    f2 b7=cmulp(b6,z1,z1s); PUT(wa,7,b7); t=cmulp(t,zk,zks); PUT(wa,18,t);
    f2 s=cmulp(b4,zk,zks); PUT(wa,12,s); t=cmulp(b2,zk,zks); PUT(wa,9,t);
    s=cmulp(s,zk,zks); PUT(wa,14,s); t=cmulp(b6,zk,zks); PUT(wa,13,t);
    s=cmulp(s,zk,zks); PUT(wa,15,s);
    #undef PUT
    if(c==0) mid();
    const int ra=(r>21||r==19)?0:r;
    #pragma unroll
    for(int ks=0;ks<8;ks++){ u4 fa=ldsr128(TA+ra*TP+ks*32+h*16), fb=ldsr128(TB+r*TP+ks*32+h*16); acc=mfma32h(fa,fb,acc); }
  }
  #pragma unroll
  for(int i=0;i<11;i++) kp[i]=acc[i];
  return ksc;
}
DI void kgen_exp(u32 cb,int lane,const float (&acc)[11],const u32 (&ga)[11]){
  const int r=lane&31,h=lane>>5;
  { const u32 a4=cb+r*16+h*8, a5=cb+r*16+h*1024;
    #define PK(addr,base,reg) do{ *(LAS _Float16*)((addr)+2*(base))=(_Float16)acc[reg]; }while(0)
    PK(a4,0,0);PK(a4,1,1);PK(a4,2,2);PK(a4,3,3); PK(a4,256,4);PK(a4,258,5);PK(a4,512,6);PK(a4,768,7);
    PK(a5,1024,8);PK(a5,1280,9);PK(a5,2048,10);
    #undef PK
  }
  WAITL0();
  { const u32 g0=cb+2*(2048-8*lane);
    u32 a0=lds16(g0), b0=lds16(g0-16), a1=lds16(g0-1024), b1=lds16(g0-1024-16);
    u32 f2_=lds16(g0-2048+(lane?8:0)), a2=lds16(g0-2048), m2=lds16(g0-2048-8), b2=lds16(g0-2048-16);
    u32 e[11];
    #pragma unroll
    for(int j=0;j<11;j++) e[j]=lds16(ga[j]);
    u32 k01=*(LAS u32*)cb, k23=*(LAS u32*)(cb+4);
    WAITL0();
    const u32 wo=cb+lane*16;
    { u32 aa=dup(a0),bb=dup(b0),c=(lane&1)?b0:a0, cbv=cat(c,b0), ac=cat(a0,c);
      ldsw128(wo,u4{aa,aa,cbv,bb}); ldsw128(wo+KP,u4{aa,aa,ac,bb}); ldsw128(wo+2*KP,u4{aa,aa,aa,cbv}); ldsw128(wo+3*KP,u4{aa,aa,aa,ac}); }
    { u32 aa=dup(a1),bb=dup(b1),c=(lane&1)?b1:a1, cbv=cat(c,b1), ac=cat(a1,c);
      ldsw128(wo+1024,u4{aa,aa,cbv,bb}); ldsw128(wo+1024+KP,u4{aa,aa,ac,bb}); ldsw128(wo+1024+2*KP,u4{aa,aa,aa,cbv}); ldsw128(wo+1024+3*KP,u4{aa,aa,aa,ac}); }
    { u32 aa=dup(a2),mm=dup(m2),bb=dup(b2),am=cat(a2,m2),fa=cat(f2_,a2),mb=cat(m2,b2);
      ldsw128(wo+2048,u4{aa,am,mm,bb}); ldsw128(wo+2048+KP,u4{aa,aa,mm,mb}); ldsw128(wo+2048+2*KP,u4{aa,aa,am,mm}); ldsw128(wo+2048+3*KP,u4{fa,aa,aa,mm}); }
    { u32 o0=cat(e[0],e[1]),o1=cat(e[2],e[3]),o2=cat(e[4],e[5]),o3=cat(e[6],e[7]),o4=cat(e[8],e[9]);
      u32 p1=cat(e[1],e[2]),d0=cat(e[3],e[4]),d1=cat(e[5],e[6]),d2=cat(e[7],e[8]),d3=cat(e[9],e[10]);
      ldsw128(wo+3072,u4{d0,d1,d2,d3}); ldsw128(wo+3072+KP,u4{o1,o2,o3,o4}); ldsw128(wo+3072+2*KP,u4{p1,d0,d1,d2}); ldsw128(wo+3072+3*KP,u4{o0,o1,o2,o3}); }
    if(lane<4){ if(lane){k01=0;k23=0;}
      u32 d0=k01&0xffffu, o1=__builtin_amdgcn_alignbit(k01,k01,16), o0=__builtin_amdgcn_alignbit(k23,k23,16), p1=(k23&0xffffu)|(k01&0xffff0000u);
      ldsw128(wo+4096,u4{d0,0,0,0}); ldsw128(wo+4096+KP,u4{o1,0,0,0}); ldsw128(wo+4096+2*KP,u4{p1,d0,0,0}); ldsw128(wo+4096+3*KP,u4{o0,o1,0,0}); }
  }
}
DI f4 mfma16h(u4 a,u4 b,f4 c){ return __builtin_amdgcn_mfma_f32_16x16x32_f16(__builtin_bit_cast(hf8,a),__builtin_bit_cast(hf8,b),c,0,0,0); }
#define RDA(S,i,m) asm volatile("ds_read2_b64 %0, %1 offset0:%2 offset1:%3" : "=v"(AF[S][i][m]) : "v"((i)<2?kb:ka), "n"((1-((i)&1))*128+(1-(m))*4), "n"((1-((i)&1))*128+(1-(m))*4+1))
#define RDB(S,i,ad,o) asm volatile("ds_read_b128 %0, %1 offset:%2" : "=v"(BF[S][i]) : "v"(ad), "n"(o))
#define TIEA(S,i) "+v"(AF[S][i][0]),"+v"(AF[S][i][1])
#define CV_M(S,i,n) do{ y[0][n]=mfma16h(AF[S][i][0],BF[S][(n)-(i)],y[0][n]); y[1][n]=mfma16h(AF[S][i][1],BF[S][(n)-(i)],y[1][n]); SCHED(); }while(0)
#define CV_U0() u32 u0=u1-256; u0=((int)u0<(int)uz)?uz:u0; const u32 kb=ka+2048; (void)kb
#define CV_SETUP() const int r=lane&15,q=lane>>4; u32 ka=cb+(r&3)*KP+16*q-8*(r>>2)+992; const u32 uz=cb+UOF+q*1040; u32 u1=uz+(r+17)*16; u4 AF[2][4][2]; u4 BF[2][4]
DI void conv_help(u32 cb,int lane){
  CV_SETUP(); f4 y[2][4]={};
  #define HP_WT(S) asm volatile("s_waitcnt lgkmcnt(0)" : TIEA(S,0),TIEA(S,1),TIEA(S,2),TIEA(S,3),"+v"(BF[S][0]),"+v"(BF[S][1]),"+v"(BF[S][2]),"+v"(BF[S][3]))
  #define HP_AD() const u32 kb=ka-64, u3=u1-16; u32 u0=u1-256, u2_=u1-272; u0=((int)u0<(int)uz)?uz:u0; u2_=((int)u2_<(int)uz)?uz:u2_
  #define HP_M(S,i,bi,n) do{ y[0][n]=mfma16h(AF[S][i][0],BF[S][bi],y[0][n]); y[1][n]=mfma16h(AF[S][i][1],BF[S][bi],y[1][n]); SCHED(); }while(0)
  #define HP_STEP(C,N) do{ HP_AD(); RDA(N,2,0); RDA(N,2,1); RDB(N,1,u1,0); RDA(N,3,0); SCHED(); HP_M(C,2,1,3); RDA(N,3,1); RDB(N,0,u0,0); RDA(N,0,0); RDA(N,0,1); SCHED(); HP_M(C,3,0,2); \
      RDB(N,3,u3,0); RDA(N,1,0); RDA(N,1,1); RDB(N,2,u2_,0); ka-=128; u1-=32; SCHED(); HP_M(C,0,3,3); HP_M(C,1,2,2); HP_WT(N); }while(0)
  { HP_AD(); RDA(0,2,0); RDA(0,2,1); RDB(0,1,u1,0); RDA(0,3,0); RDA(0,3,1); RDB(0,0,u0,0); RDA(0,0,0); RDA(0,0,1); RDB(0,3,u3,0); RDA(0,1,0); RDA(0,1,1); RDB(0,2,u2_,0); ka-=128; u1-=32; HP_WT(0); }
  #pragma unroll 1
  for(int dl=CVSKIP*3;dl<4;dl++){ HP_STEP(0,1); if(dl==3){ ka+=128; u1+=32; } HP_STEP(1,0); }
  y[0][3]+=y[0][2]; y[1][3]+=y[1][2];
  *(LAS f4*)(cb+q*KP+r*16)=y[0][3]; *(LAS f4*)(cb+q*KP+256+r*16)=y[1][3];
  WAITL0();
}
DI void conv_wave(const Params& p,rsrc_t rs,int d,u32 cb,u32 flag,int lane,float ksc,float dsk){
  CV_SETUP(); f4 y[2][4]={};
  #define CV_WT(S) asm volatile("s_waitcnt lgkmcnt(0)" : TIEA(S,0),TIEA(S,1),TIEA(S,2),"+v"(BF[S][0]),"+v"(BF[S][1]),"+v"(BF[S][2]),"+v"(BF[S][3]))
  #define CV_STEP(C,N) do{ CV_U0(); CV_M(C,0,0); RDA(N,0,0); RDB(N,0,u0,0); SCHED(); CV_M(C,0,1); RDA(N,0,1); RDB(N,1,u1,0); SCHED(); CV_M(C,0,2); RDA(N,1,0); RDB(N,2,u1,256); SCHED(); \
      CV_M(C,0,3); RDA(N,1,1); RDB(N,3,u1,512); SCHED(); CV_M(C,1,1); RDA(N,2,0); SCHED(); CV_M(C,1,2); RDA(N,2,1); ka-=64; u1-=16; SCHED(); CV_M(C,1,3); CV_M(C,2,2); CV_WT(N); }while(0)
  { CV_U0(); RDA(0,0,0); RDA(0,0,1); RDA(0,1,0); RDA(0,1,1); RDA(0,2,0); RDA(0,2,1); RDB(0,0,u0,0); RDB(0,1,u1,0); RDB(0,2,u1,256); RDB(0,3,u1,512); ka-=64; u1-=16; CV_WT(0); }
  #pragma unroll 1
  for(int dl=CVSKIP*7;dl<8;dl++){ CV_STEP(0,1); if(dl==7){ ka+=64; u1+=16; } CV_STEP(1,0); }
  __builtin_amdgcn_s_setprio(3);
  u2 uv[2][4];
  #pragma unroll
  for(int n=0;n<4;n++)
    #pragma unroll
    for(int m=0;m<2;m++) uv[m][n]=ldsr64(cb+UOF+(2*m+(q>>1))*1040+(16*n+r+1)*16+8*(q&1));
  const u32 sc=cb+UOF+r*64+q*8;
  auto fin=[&](int n){
    #pragma unroll
    for(int m=0;m<2;m++){ u2 o;
      #pragma unroll
      for(int e=0;e<2;e++) o[e]=pkbf(fmaf1(hlo(uv[m][n][e]),dsk,mulf(y[m][n][2*e],ksc)),fmaf1(hhi(uv[m][n][e]),dsk,mulf(y[m][n][2*e+1],ksc)));
      *(LAS u2*)(sc+n*1024+m*32)=o; } };
  ldswait(flag,2u);
  { const f4 a=*(LAS f4*)(cb+q*KP+r*16), b=*(LAS f4*)(cb+q*KP+256+r*16);
    fin(0); fin(1); fin(2);
    #pragma unroll
    for(int e=0;e<4;e++){ y[0][3][e]=addf(y[0][3][e],a[e]); y[1][3][e]=addf(y[1][3][e],b[e]); }
    fin(3); }
  u4 ov[4];
  #pragma unroll
  for(int n=0;n<4;n++) ov[n]=ldsr128(cb+UOF+n*1024+lane*16);
  #pragma unroll
  for(int n=0;n<4;n++) st16(rs,OFF_AT+d*4096+n*1024+lane*16,ov[n]);
  WAITV(0);
}

#define NB 3
constexpr int g2ops(int h){ return h<0? 7 : (h+NB<16?7:0)+((h>=3&&h<=10)?4:0)+(h==13-NB?1:0)+(h==16-NB?8:0); }
constexpr int g2cnt(int h){ int c=0; for(int i=h-NB+1;i<h;i++) c+=g2ops(i); return c; }
struct Frag { u4 x[4], w[4][2]; };
__global__ __launch_bounds__(512,2) void fused(Params p){
  extern __shared__ __attribute__((aligned(16))) char sm[];
  const u32 smb=(u32)(size_t)(LAS char*)sm;
  u64 pc0; asm volatile("s_getpc_b64 %0" : "=s"(pc0));
  const int b=blockIdx.x, xc=b&7, idx=b>>3;
  const int tid=threadIdx.x, w=uni(tid>>6); int lane=tid&63;
  const rsrc_t rs=__builtin_amdgcn_make_buffer_rsrc(p.ws,0,WS_END,0x00020000);
  const u4 srdS=mksrd(p.ws);
  const int rtB=4*xc+(idx&3), ct=idx>>2;
  const bool hask=idx<28 && idx+28*(w&3)<80, hasc=hask && w>=4;
  const int dch=80*xc+(hask? idx+28*(w&3) : 0);
  const u32 cb=smb+CONV+(w&3)*CVS;
  const int rt=4*(xc>>1)+(idx&3), j=7*(xc&1)+(idx>>2), mi=w&3, g1=w>>2;
  u32 tag; float ksc=0.f, dsk=0.f;
  STAMP(0,0);
  auto pre01=[&](){
    const u4 sa=mksrd((const char*)p.wsp+(size_t)rtB*64*4096+mi*1024-4096); const u32 v0=(lane>>3)*4096+(((lane&7)^(lane>>4))<<4);
    #pragma unroll 1
    for(int i=0;i<4;i++){ setm0<RB+8192>(smb+mi*RING+i*4096); const u32 g=(i&1)*4+2*mi, k=(i>>1)*128+4096;
      dma<0>(sa,v0,(g&7)*32768+k); dma<1024>(sa,v0^64u,(g&7)*32768+32768-1024+k); dma<2048>(sa,v0,((g+2)&7)*32768-2048+k); dma<3072>(sa,v0^64u,((g+2)&7)*32768+32768-3072+k); } };
  auto warm=[&](int wq,int part){
    const u4 sp=mksrd((const char*)p.wsp+(size_t)(rtB*64+ct*8+2*wq)*4096); setm0<JNK>(smb);
    const u32 vo= part==2? lane*128 : ((lane>>2)&7)*1024+(lane&3)*128+part*512;
    asm volatile("buffer_load_dword %0, %1, 0 offen lds" :: "v"(vo), "s"(sp) : "memory");
  };
  if(idx>=28){
    const int a=xc*4+idx-28, r=lane&15, q=lane>>4;
    { u4 sc=mksrd((const void*)(pc0&~127ull)); sc[2]=CODE_BYTES; setm0<JNK>(smb); touch(sc,lane*128,w*8192); }
    const u32 ep=ld4(rs,OFF_EP);
    u4 xr[10]; const char* xs=(const char*)p.x+(size_t)(64*a+8*w+(lane>>3))*1280+(lane&7)*16;
    #pragma unroll
    for(int i=0;i<10;i++) xr[i]=*(const u4*)(xs+i*128);
    if(tid<32) *(LAS u32*)(smb+PM+tid*4)=0u;
    tag=uni(ep)+1u;
    float s0=0.f,s1=0.f,q0=0.f,q1=0.f;
    #pragma unroll
    for(int i=0;i<10;i++){ s0=dot2(xr[i][0],0x3f803f80u,s0); q0=dot2(xr[i][0],xr[i][0],q0); s1=dot2(xr[i][1],0x3f803f80u,s1); q1=dot2(xr[i][1],xr[i][1],q1);
      s0=dot2(xr[i][2],0x3f803f80u,s0); q0=dot2(xr[i][2],xr[i][2],q0); s1=dot2(xr[i][3],0x3f803f80u,s1); q1=dot2(xr[i][3],xr[i][3],q1); }
    s0+=s1; q0+=q1;
    #define DPPADD(v,c) v+=__int_as_float(__builtin_amdgcn_update_dpp(0,__float_as_int(v),c,0xf,0xf,false))
    DPPADD(s0,0xB1); DPPADD(q0,0xB1); DPPADD(s0,0x4E); DPPADD(q0,0x4E); DPPADD(s0,0x141); DPPADD(q0,0x141);
    float m1,ri; { float mean=s0*(1.f/640.f); ln_fin(mean,q0*(1.f/640.f)-mean*mean,m1,ri); }
    if((lane&7)==0) st16(rs,OFF_RS+(64*a+8*w+(lane>>3))*16,u4{__float_as_uint(m1),tag,__float_as_uint(ri),tag});
    STAMP(5,0);
    #pragma unroll
    for(int i=0;i<10;i++){ u4 o;
      #pragma unroll
      for(int e=0;e<4;e++) o[e]=pkhf2(fma2(unpk(xr[i][e]),f2{ri,ri},f2{-m1*ri,-m1*ri}));
      ldsw128(smb+RB+(8*w+(lane>>3))*1296+(lane&7)*16+i*128,o); }
    bar();
    #pragma unroll
    for(int i=0;i<10;i++){ const int c=w*10+i; const u32 ad=smb+RB+(32*((r>>1)&1)+8*q+(r>>2))*1296+c*16+(r&1)*8; u2 lo=ldstr(ad), hi=ldstr(ad+4*1296);
      const u32 sc=smb+w*5120+(i%5)*1024; ldsw128(sc+(r&7)*128+((((r>>3)*4+q)^(r&7))<<4),u4{lo[0],lo[1],hi[0],hi[1]});
      st16(rs,OFF_XNT+a*81920+c*1024+lane*16,ldsr128(sc+(lane>>3)*128+(((lane&7)^(lane>>3))<<4))); }
    WAITV(0);
    if(arrive(smb+C_Z1,lane)==7u && lane==0) st4(rs,OFF_FX+a*128,tag);
    STAMP(6,7);
    if(a<27){ const u4 srdO=mksrd((const char*)p.wout+a*(32*1280)+w*5120); const u32 mo=smb+w*5120; setm0<0>(mo);
      dma<0>(srdO,lane*16,0); dma<1024>(srdO,lane*16,0); dma<2048>(srdO,lane*16,0); dma<3072>(srdO,lane*16,0); setm0<4096>(mo); dma<0>(srdO,lane*16,4096); }
    WAITV(0); bar();
    if(a<27){
      #pragma unroll
      for(int i=0;i<5;i++){ u32 ad=smb+(8*q+(r>>2))*1280+(w*5+i)*32+(r&3)*8; u2 lo=ldstr(ad), hi=ldstr(ad+4*1280);
        st16(rs,OFF_WOF+(u32)((w*27+a)*5+i)*1024+lane*16,u4{lo[0],lo[1],hi[0],hi[1]}); } }
    WAITV(0); bar();
    if(tid==0) st4(rs,OFF_FW+a*128,tag);
    if(g1) warm(mi,2); else { pre01(); WAITV(0); }
    STAMP(31,0);
  } else {
    const int r=lane&31, h=lane>>5;
    const u4 srdX=mksrd((const char*)p.x+(size_t)rt*128*1280);
    const u4 srdW=mksrd((const char*)p.win+j*128-3072);
    const u32 mw=smb+w*4096;
    u32 vx0,vx1,vw1,vw2,epv;
    { u32 lr=lane>>3,c=lane&7; vx0=(16*w+lr)*1280+((c^(lr>>1))<<4); vx1=(vx0^64u)+9216;
      vw1=(8*w+lr)*3456+((c^((lr&2)<<1))<<4); vw2=vw1+1024+(j==13?0:14*128); }
    u32 kx[10]; { u32 k=KROT;
      #pragma unroll
      for(int i=0;i<10;i++){ kx[i]=k*128; k=k==9?0:k+1; } }
    auto issue1=[&](auto sc,int k){ constexpr int s=sc.value;
      if(k==0){ setm0<(s&3)*STG>(mw); dma<0>(srdX,vx0,kx[s]); } if(k==1) dma<1024>(srdX,vx1,kx[s]); if(k==2) dma<2048>(srdW,vw2,kx[s]*1728); if(k==3) dma<3072>(srdW,vw1,kx[s]*1728); };
    auto issue=[&](auto sc){ issue1(sc,0); issue1(sc,1); issue1(sc,2); issue1(sc,3); };
    issue(std::integral_constant<int,0>{});
    u32 pr[6];
    gload4<0>(epv,0u,p.ws+OFF_EP);
    gload4<0>(pr[0],lane*4,(const char*)(p.lar+dch*128)); gload4<0>(pr[1],lane*4,(const char*)(p.aim+dch*128)); gload4<0>(pr[2],lane*4,(const char*)(p.cre+dch*128)); gload4<0>(pr[3],lane*4,(const char*)(p.cim+dch*128));
    gload2(pr[4],0u,(const char*)(p.ldt+dch)); gload2(pr[5],0u,(const char*)(p.dsk+dch));
    asm volatile("s_barrier" ::: "memory");
    issue(std::integral_constant<int,1>{});
    if(tid<32) *(LAS u32*)(smb+PM+tid*4)=0u;
    u32 ax[2][4],aw[2][4][2];
    { u32 row=32*mi+r, a0=smb+(row>>4)*4096+(row&15)*128+((h^((r>>1)&7))<<4);
      u32 qq=(lane&15)>>2, w0=smb+2048+g1*1024+h*4096+qq*128+8*(lane&1)+(((2*((lane>>4)&1)+((lane>>1)&1))^((qq&2)<<1))<<4);
      #pragma unroll
      for(int i=0;i<2;i++)
        #pragma unroll
        for(int k=0;k<4;k++){ const u32 kk=(k+mi)&3, wo=i*2*STG; ax[i][k]=opq((a0^(kk*32u))+wo); aw[i][k][0]=opq(w0+kk*8192+wo); aw[i][k][1]=opq((w0^64u)+kk*8192+wo); } }
    u4 sel; { const u32 o=((lane&15)==((lane>>4)&1))?0x3f803f80u:0u; sel=u4{o,o,o,o}; asm volatile("" : "+v"(sel)); }
    u32 ga[11];
    #pragma unroll
    for(int i=0;i<11;i++) ga[i]=opq(cb+2u*(u32)rbf((float)(512-8*lane+3-i)));
    Frag F[2];
    asm volatile("s_waitcnt vmcnt(4)" : "+v"(epv),"+v"(pr[0]),"+v"(pr[1]),"+v"(pr[2]),"+v"(pr[3]),"+v"(pr[4]),"+v"(pr[5]) :: "memory");
    bar();
    tag=uni(epv)+1u; dsk=bflo(pr[5]);
    #pragma unroll
    for(int k=0;k<4;k++){
      #pragma unroll
      for(int n=0;n<2;n++){ u2 lo=ldstr(aw[0][k][n]), hi=ldstr(aw[0][k][n]+512); F[0].w[k][n]=u4{lo[0],lo[1],hi[0],hi[1]}; }
      F[0].x[k]=ldsr128(ax[0][k]); }
    issue(std::integral_constant<int,2>{});
    STAMP(1,0);
    f16v acc[2]={}; f4 cacc[2]={}; u4 G;
    const u32 go=OFF_RS+rt*2048+mi*512, cp=smb+MISC+g1*1024;
    sfor<10>([&](auto ic){
      constexpr int s=ic.value; Frag& cur=F[s&1]; Frag& nxt=F[(s&1)^1];
      if constexpr(s<9){ WAITV((s==8? 2 : s==7? 6 : 4)+((TS1<6&&(s==TS1+1||s==TS1+2))?1:0)); asm volatile("s_barrier" ::: "memory"); }
      auto rw=[&](int k,int n){ if constexpr(s<9){ const int i=((s+1)>>1)&1; const u32 so=((s+1)&1)*STG; u2 lo=ldstr(aw[i][k][n]+so), hi=ldstr(aw[i][k][n]+so+512); nxt.w[k][n]=u4{lo[0],lo[1],hi[0],hi[1]}; } };
      auto rx=[&](int k){ if constexpr(s<9){ const int i=((s+1)>>1)&1; const u32 so=((s+1)&1)*STG; nxt.x[k]=ldsr128(ax[i][k]+so); } };
      #pragma unroll
      for(int k=0;k<4;k++){
        acc[0]=mfma32b(cur.w[k][0],cur.x[k],acc[0]); SCHED();
        if constexpr(s+3<=9) issue1(std::integral_constant<int,s+3>{},k);
        rw(k,0); rx(k);
        SCHED();
        acc[1]=mfma32b(cur.w[k][1],cur.x[k],acc[1]); SCHED();
        rw(k,1);
        if(k==0){ cacc[0]=mfma16(cur.w[0][0],sel,cacc[0]); cacc[1]=mfma16(cur.w[0][1],sel,cacc[1]); }
        if constexpr(s==TS1 && TS1<6){ if(k==3){ if(g1) warm(mi,0); else { setm0<JNK>(smb); touch(srdS,0u,OFF_EP); } } }
        if constexpr(s==6){ if(k==3){ gload<0>(G,r*16,p.ws+go); if(g1) warm(mi,TS1<6?1:2); else { setm0<JNK>(smb); touch(srdS,0u,go); } } }
        if constexpr(s==8){ if(k==3) signal(smb+C_DONE,lane); }
        if constexpr(s==9){ if(k==1){
          if((lane&14)==0){ *(LAS f4*)(cp+mi*256+(lane&1)*64+(lane>>4)*16)=cacc[0]; *(LAS f4*)(cp+mi*256+128+(lane&1)*64+(lane>>4)*16)=cacc[1]; }
          signal(smb+C_CS+g1*4,lane); } }
        SCHED();
      }
    });
    RST(0);
    if(!g1) __builtin_amdgcn_s_setprio(2);
    asm volatile("s_waitcnt vmcnt(1)" : "+v"(G) :: "memory");
    ldswait(smb+C_CS+g1*4,4u);
    const float cs0=*(LAS float*)(cp+h*512+r*4)+*(LAS float*)(cp+h*512+256+r*4), cs1=*(LAS float*)(cp+h*512+128+r*4)+*(LAS float*)(cp+h*512+384+r*4);
    RST(2);
    #pragma unroll 1
    for(int sp=0;sp<SPIN && !__all(G[1]==tag&&G[3]==tag);sp++)asm volatile("global_load_dwordx4 %0, %1, %2 sc1\n\ts_waitcnt vmcnt(0)" : "=&v"(G) : "v"(r*16), "s"(p.ws+go) : "memory");
    RST(3);
    const float nm=-__uint_as_float(G[0]), ri1=__uint_as_float(G[2]);
    acc[0]=__builtin_amdgcn_mfma_f32_32x32x2f32(cs0,nm,acc[0],0,0,0); acc[1]=__builtin_amdgcn_mfma_f32_32x32x2f32(cs1,nm,acc[1],0,0,0);
    u32 zp[2][8]; const u32 zqa=smb+RB+mi*RING+4096+lane*16;
    if(g1){
      ldswait(smb+C_DONE,8u);
      #pragma unroll
      for(int i=0;i<4;i++){ u4 o;
        #pragma unroll
        for(int e=0;e<4;e++) o[e]=pkbf2(f2{acc[i>>1][8*(i&1)+2*e],acc[i>>1][8*(i&1)+2*e+1]}*ri1);
        ldsw128(zqa+i*1024,o); }
      signal(smb+C_K2+mi*4,lane); RST(4);
    } else {

      #pragma unroll
      for(int e=0;e<16;e++) zp[e>>3][e&7]=pkbf2(gelu2(f2{acc[e>>3][2*(e&7)],acc[e>>3][2*(e&7)+1]}*ri1));
      float S=0.f,Q=0.f;
      if(j<13){
        #pragma unroll
        for(int e=0;e<8;e++){ S=dot2(zp[0][e],0x3f803f80u,S); Q=dot2(zp[0][e],zp[0][e],Q); } }
      #pragma unroll
      for(int e=0;e<8;e++){ S=dot2(zp[1][e],0x3f803f80u,S); Q=dot2(zp[1][e],zp[1][e],Q); }
      #define XSUM32(v) do{ auto t_=__builtin_amdgcn_permlane32_swap(__float_as_uint(v),__float_as_uint(v),false,false); v=__uint_as_float(t_[0])+__uint_as_float(t_[1]); }while(0)
      XSUM32(S); XSUM32(Q);
      const u32 sto=OFF_ST+rt*(14*2048)+(32*mi+r)*16;
      if(h==0) st16(rs,sto+j*2048,u4{__float_as_uint(S),tag,__float_as_uint(Q),tag});
      RST(6);
    }
    float kp[11]; u4 g[7]={};
    const u32 gof=g1? OFF_FX+r*128 : OFF_ST+rt*(14*2048)+(32*mi+r)*16+h*(7*2048);
    auto samp=[&](){ asm volatile("" ::: "memory"); g[0]=ld16(rs,gof);
      if(!g1){
        #pragma unroll
        for(int i=1;i<7;i++) g[i]=ld16(rs,gof+i*2048); }
      asm volatile("" ::: "memory"); };
    if(hasc) ksc=kgen_tab(cb,lane,pr,kp,samp); else if(!g1){
      pre01(); ldswait(smb+C_K2+mi*4,1u); u4 zq[4];
      #pragma unroll
      for(int i=0;i<4;i++){ const u4 v=ldsr128(zqa+i*1024);
        #pragma unroll
        for(int e=0;e<4;e++) zq[i][e]=pkbf2(gelu2(unpk(v[e]))); }
      SCHED(); samp();
      #pragma unroll
      for(int n=0;n<2;n++) if(2*j+n<27){ const u32 oo=OFF_Z1T+((rt*8+2*mi+(r>>4))*27+2*j+n)*1024+(h*16+(r&15))*16;
        st16(rs,oo,u4{zq[2*n][0],zq[2*n][1],zq[2*n+1][0],zq[2*n+1][1]}); st16(rs,oo+512,u4{zq[2*n][2],zq[2*n][3],zq[2*n+1][2],zq[2*n+1][3]}); }
    }
    if(g1){
      if(w<7){ u4 uu[4]; const u32 fxv=g[0][0];
        if(hasc){
          RST(6);
          if(!__all(fxv==tag)) pollw<16>(rs,OFF_FX+r*128,tag);
          RST(7);
          #pragma unroll
          for(int i=0;i<4;i++) uu[i]=ld16(rs,OFF_XNT+((lane>>3)+8*i)*81920+dch*128+(lane&7)*16);
          asm volatile("" ::: "memory");
          kgen_exp(cb,lane,kp,ga);
          RST(8);
          #pragma unroll
          for(int i=0;i<4;i++){ const int x=lane+64*i; ldsw128(cb+UOF+(x&3)*1040+((x>>2)+1)*16,uu[i]); }
          if(lane<4) ldsw128(cb+UOF+lane*1040,u4{0,0,0,0});
          WAITL0(); signal(smb+C_KP+mi*4,lane);
        }
        RST(9);
      }
    } else {
      RST(1);
      float S,Q;
      #pragma unroll 1
      for(int sp=0;sp<SPIN;sp++){ bool ok=true;
        #pragma unroll
        for(int i=0;i<7;i++) ok=ok&&g[i][1]==tag&&g[i][3]==tag;
        if(__all(ok)) break;
        __builtin_amdgcn_s_sleep(4); samp(); }
      RST(7);
      S=0.f; Q=0.f;
      #pragma unroll
      for(int i=0;i<7;i++){ S+=__uint_as_float(g[i][0]); Q+=__uint_as_float(g[i][2]); }
      XSUM32(S); XSUM32(Q);
      float mean=S*(1.f/864.f), m2,r2; ln_fin(mean,Q*(1.f/864.f)-mean*mean,m2,r2); m2*=-r2;
      ldswait(smb+C_DONE,8u);
      const u32 tb=smb+RB+mi*RING, ad=tb+(8*(lane>>4)+((lane&15)>>2))*80+(lane&3)*8;
      const u32 zo=OFF_Z2F+((rt*4+mi)*54+(j==13?-2:2+4*j))*1024+lane*16;
      #pragma unroll
      for(int n=0;n<2;n++){
        #pragma unroll
        for(int g2=0;g2<4;g2++){ u2 o;
          #pragma unroll
          for(int e=0;e<2;e++) o[e]=pkbf2(fma2(unpk(zp[n][2*g2+e]),f2{r2,r2},f2{m2,m2}));
          *(LAS u2*)(tb+r*80+g2*16+h*8)=o; }
        u2 l0=ldstr(ad), h0=ldstr(ad+320), l1=ldstr(ad+32), h1=ldstr(ad+32+320);
        if(j<13||n){ st16(rs,zo+n*2048,u4{l0[0],l0[1],h0[0],h0[1]}); st16(rs,zo+n*2048+1024,u4{l1[0],l1[1],h1[0],h1[1]}); } }
      RST(8);
      WAITV(0);
      if(arrive(smb+C_Z2,lane)==3u && lane==0) st4(rs,OFF_F2+(rt*16+j)*128,tag);
      RST(9);
    }
  }
  const int ft0=ct<7?7*ct:47;
  bool adone=true; const u32 fao=OFF_FA+((lane<28?lane:0)*8+ct)*128;
  auto aload=[&](){ const u32 vo=(lane>>3)*4096+(lane&7)*16;
    #pragma unroll 1
    for(int i=0;i<10;i++){ setm0<ATL>(smb+i*1024); dma<0>(srdS,vo,OFF_AT+(u32)(80*ct+8*i)*4096+rtB*128); } };
  if(w>=4){
    if(hasc){
      __builtin_amdgcn_s_setprio(CPRIO);
      conv_wave(p,rs,dch,cb,smb+C_KP+mi*4,lane,ksc,dsk);
      __builtin_amdgcn_s_setprio(0);
      if(arrive(smb+C_CV,lane)==(idx<24?2u:1u) && lane==0) st4(rs,OFF_FA+b*128,tag);
    }
    if(w==7){
      __builtin_amdgcn_s_setprio(HPRIO);
      if(idx<28){
        #pragma unroll 1
        for(int c=0;c<(idx<24?3:2);c++){ ldswait(smb+C_KP+c*4,1u); conv_help(smb+c*CVS,lane); signal(smb+C_KP+c*4,lane); } }
      __builtin_amdgcn_s_setprio(0);
      RST(6);
    }
    RST(10);
    ldswait(smb+C_Z1R,4u);
    u4 wq[24];
    #pragma unroll
    for(int i=0;i<24;i++) wq[i]=ld16(rs,OFF_WOF+ct*(135*1024)+40960+mi*RING+i*1024+lane*16);
    if(w<6){
      #pragma unroll 1
      for(u32 i=0;i<20480;i+=4096){ setm0<0>(cb+i); const u32 o=OFF_WOF+ct*(135*1024)+mi*20480+i; dma<0>(srdS,lane*16,o); dma<1024>(srdS,lane*16,o); dma<2048>(srdS,lane*16,o); dma<3072>(srdS,lane*16,o); } }
    if(w==6){ adone=__all(ld4(rs,fao)==tag); if(adone) aload(); }
    ldswait(smb+C_FREE,4u);
    #pragma unroll
    for(int i=0;i<24;i++) ldsw128(smb+RB+mi*RING+i*1024+lane*16,wq[i]);
    if(w<6) WAITV(0);
    signal(smb+C_WR,lane);
    if(!adone){ adone=__all(ld4(rs,fao)==tag); if(adone) aload(); }
    if(w==7 && b==255){
      #pragma unroll 1
      for(int sp=0;sp<SPIN;sp++){ const u32 o=OFF_FA+lane*128; u32 v0=ld4(rs,o),v1=ld4(rs,o+8192),v2=ld4(rs,o+16384),v3=ld4(rs,lane<32? o+24576 : OFF_FW+(lane-32)*128); asm volatile("" ::: "memory");
        if(__all(v0==tag&&v1==tag&&v2==tag&&v3==tag)) break; __builtin_amdgcn_s_sleep(4); }
      if(lane==0) st4(rs,OFF_EP,tag); }
  }
  int w2=w; asm volatile("" : "+s"(w2));
  if(w2<4){
    __builtin_amdgcn_s_setprio(G2PRIO);
    int r=lane&15, q=lane>>4;
    const u32 rb=smb+RB+w*RING;
    u4 srdA=mksrd((const char*)p.wsp+(size_t)rtB*64*4096+w*1024-4096+256);
    const char* zb=p.ws+OFF_Z2F+(u32)(16*w*54+ft0+3)*1024;
    u32 voA[2]; { u32 lr=lane>>3,c=lane&7; voA[0]=lr*4096+((c^(lr>>1))<<4); voA[1]=voA[0]^64u; }
    u32 sofa[8];
    #pragma unroll
    for(int g=0;g<8;g++) sofa[g]=((g+2*w)&7)*32768+4096-(g&3)*1024;
    const u32 voB=lane*16;
    u32 la[2]; la[0]=rb+r*128+((q^(r>>1))<<4); la[1]=la[0]^64u;
    f4 acc[4][7]={}; u4 B[NB][7]; u4 A[2][4]; u2 z1[7];
    auto issueA=[&](auto sc,auto gc){ constexpr int s=sc.value,g=gc.value; if constexpr((g&3)==0) setm0<((s+1)%3)*8192+g*1024>(rb); dma<(g&3)*1024>(srdA,voA[g&1],sofa[g]);
      if constexpr(g==7){ u64 a=((u64)srdA[1]<<32|srdA[0])+128; srdA[0]=(u32)a; srdA[1]=(u32)(a>>32); } };

    u32 bsr,fv;
    auto fof=[&](){ const u32 l=voB>>4; return l<27? OFF_FW+l*128 : OFF_F2+((rtB>>1)*16+(((ft0>>1)+(l&3))>>1))*128; };
    #pragma unroll
    for(int m=0;m<4;m++){ A[0][m]=ldsr128(la[0]+m*2048+8192); A[1][m]=ldsr128(la[1]+m*2048+8192); }
    sfor<8>([&](auto g){ issueA(std::integral_constant<int,2>{},g); });
    waitA(A[0]); waitA(A[1]);
    sfor<8>([&](auto g){ issueA(std::integral_constant<int,3>{},g); });
    #pragma unroll
    for(int m=0;m<4;m++)
      #pragma unroll
      for(int n=0;n<7;n++) asm volatile("" : "+v"(acc[m][n]));
    RST(2);
    { const int hf=(ft0>>1)+q; pollw(rs,OFF_F2+((4*w+(lane&3))*16+(hf==0?13:(hf-1)>>1))*128,tag); }
    RST(10);
    sfor<NB>([&](auto hh){ const char* s=zb+(u32)hh.value*(54*1024); sfor<7>([&](auto n){ gload<(n.value-3)*1024>(B[hh][n],voB,s); }); });
    auto redw=[&](auto nc){ constexpr int n=nc.value; *(LAS f4*)(rb+n*1024+voB)=acc[1][n]; *(LAS f4*)(rb+(7+n)*1024+voB)=acc[2][n]; *(LAS f4*)(rb+(14+n)*1024+voB)=acc[3][n]; };
    const char* z1b=p.ws+OFF_Z1T+(u32)((rtB*4+w)*27+(ft0>>1))*1024;
    sfor<16>([&](auto hc){
      constexpr int h=hc.value, S=h%NB, P=h&1, HZ=16-NB, cnt=g2cnt(h);
      waitB<cnt>(B[S]); waitA(A[P]);
      if constexpr(h==HZ){ tie1(fv); if(!__all(fv==tag)) pollw(rs,fof(),tag); }
      if constexpr(h+1<16 && h>0){
        #pragma unroll
        for(int m=0;m<4;m++) A[P^1][m]=ldsr128(la[(h+1)&1]+m*2048+((((h+1)>>1)+1)%3)*8192);
      }
      if constexpr(h==HZ-3) gload4<0>(fv,fof(),p.ws);
      const char* s=zb+(u32)(h+NB)*(54*1024);
      sfor<7>([&](auto n){
        mfma4(acc[0][n],acc[1][n],acc[2][n],acc[3][n],B[S][n],A[P]);
        if constexpr(h>=3 && h<=10 && (n.value&1)==0) issueA(std::integral_constant<int,(h-1)/2+3>{},std::integral_constant<int,((h-1)&1)*4+n.value/2>{});
        if constexpr(h+NB<16) gload_t<(n.value-3)*1024>(B[S][n],voB,s);
        if constexpr(h==HZ){ gload8<(n.value>>1)*1024>(z1[n],voB,z1b+((n.value&1)? ((ft0&1)?1024:8) : (ft0&1)*8));
          if constexpr(n.value==6) { gload2(bsr,(voB>>3)&30u,(const char*)(p.bsp+(rtB*4+w)*16)); signal(smb+C_Z1R,lane); } }
        if constexpr(h==15){ SCHED(); if constexpr(n.value>=2) redw(std::integral_constant<int,n.value-2>{}); SCHED(); }
      });
    });
    asm volatile("s_nop 15\n\ts_nop 3" ::: "memory");
    SCHED();
    STAMP(10,0);
    lane=__builtin_amdgcn_mbcnt_hi(~0u,__builtin_amdgcn_mbcnt_lo(~0u,0u)); asm volatile("" : "+v"(lane)); r=lane&15; q=lane>>4;
    redw(std::integral_constant<int,5>{}); redw(std::integral_constant<int,6>{});
    signal(smb+C_RED,lane);
    ldswait(smb+C_RED,4u);
    #pragma unroll
    for(int m=1;m<4;m++){ u32 src=smb+RB+((w-m)&3)*RING+(m-1)*7168+lane*16;
      #pragma unroll
      for(int n=0;n<7;n++) acc[0][n]+=*(LAS f4*)(src+n*1024); }
    asm volatile("s_waitcnt vmcnt(0) lgkmcnt(0)" : "+v"(z1[0]),"+v"(z1[1]),"+v"(z1[2]),"+v"(z1[3]),"+v"(z1[4]),"+v"(z1[5]),"+v"(z1[6]),"+v"(bsr) :: "memory");
    const float bs=bflo(bsr);
    signal(smb+C_FREE,lane);
    u2 o[7];
    #pragma unroll
    for(int n=0;n<7;n++)
      #pragma unroll
      for(int e=0;e<2;e++)o[n][e]=pkbf2(unpk(z1[n][e])*(f2{acc[0][n][2*e],acc[0][n][2*e+1]}+bs));
    const u32 ho=OFF_HF+(u32)((rtB*4+w)*54+ft0)*512+(q>>1)*256+r*16;
    #pragma unroll
    for(int n=0;n<6;n+=2){ auto sx=__builtin_amdgcn_permlane16_swap(o[n][0],o[n+1][0],false,false); auto sy=__builtin_amdgcn_permlane16_swap(o[n][1],o[n+1][1],false,false);
      if(ct<7||n>=2) st16(rs,ho+(n+(q&1))*512,u4{sx[0],sy[0],sx[1],sy[1]}); }
    st8(rs,ho+6*512+(q&1)*8,o[6]);
    STAMP(11,0);
    WAITV(0);
    if(arrive(smb+C_H,lane)==3u && lane==0) st4(rs,OFF_FH+(rtB*8+ct)*128,tag);
    STAMP(12,0);
    __builtin_amdgcn_s_setprio(0);
  }
  { const int r=lane&15, q=lane>>4, m=w&3, kh=w>>2;
    u2 xv[5]={};
    if(kh){
      #pragma unroll
      for(int n=0;n<5;n++) xv[n]=*(const u2*)(p.x+(rtB*64+m*16+r)*640+80*ct+16*n+4*q);
    }
    f4 acc[5]={}; u4 W[3][5];
    const u32 wl2=smb+lane*16+RB-40960+kh*14*5120, wl0=kh? wl2 : smb+lane*16, wl1=kh? wl2 : smb+lane*16+CVS-20480;
    #define WAD(k,n) (((k)<4? wl0 : (k)<8? wl1 : wl2)+((k)*5+(n))*1024)
    f4 ax[5]={};
    auto addax=[&](){
      #pragma unroll
      for(int n=0;n<5;n++){ u2 av=ldstr(smb+ATL+(16*n+4*q+(r>>2))*128+(16*m+4*(r&3))*2); ax[n]=f4{bflo(av[0]),bfhi(av[0]),bflo(av[1]),bfhi(av[1])}+f4{bflo(xv[n][0]),bfhi(xv[n][0]),bflo(xv[n][1]),bfhi(xv[n][1])}; } };
    if(w==6 && adone){ WAITV(0); signal(smb+C_AT,lane); }
    u32 oo[3],to[3]; const u32 tb=smb+CVS+m*3072;
    #pragma unroll
    for(int i=0;i<3;i++){ const int it=lane+64*i, rr=(it*205)>>11, cc=it-rr*10; oo[i]=opq((rtB*64+m*16+rr)*1280+ct*160+cc*16); to[i]=opq(tb+rr*176+cc*16); }
    if(m==0){ ldswait(smb+C_H,4u); pollw(rs,OFF_FH+(rtB*8+kh*4+(lane&3))*128,tag); signal(smb+C_TAG+kh*4,lane); } else ldswait(smb+C_TAG+kh*4,1u);
    STAMP(16,0);
    u4 hf[14];
    const u32 ho=OFF_HF+(u32)((rtB*4+m)*27+kh*14)*1024+lane*16;
    #pragma unroll
    for(int k=0;k<HPF;k++) hf[k]=ld16(rs,ho+k*1024);
    const bool early=kh && uni(ldsv(smb+C_AT))!=0u;
    if(early) addax();
    ldswait(smb+C_WR,4u);
    STAMP(17,0);
    const u32 wl3=kh? wl2-5120 : wl2;
    #pragma unroll
    for(int n=0;n<10;n++) W[n/5][n%5]=ldsr128(WAD(n/5,n%5));
    #pragma unroll
    for(int k=0;k<13;k++){
      if(k+HPF<14) hf[k+HPF]=ld16(rs,ho+((k+HPF==13&&kh)?12:k+HPF)*1024);
      SCHED();
      #pragma unroll
      for(int n=0;n<5;n++){ acc[n]=mfma16(W[k%3][n],hf[k],acc[n]); SCHED();
        if(k<11) W[(k+2)%3][n]=ldsr128(WAD(k+2,n)); else if(k==11) W[1][n]=ldsr128(wl3+(65+n)*1024);
        SCHED(); }
      if(k==5 && !kh) signal(smb+C_G1,lane);
    }
    const u32 pb=smb+m*5120+lane*16;
    if(!kh){
      #pragma unroll
      for(int n=0;n<5;n++) acc[n]=mfma16(W[1][n],hf[13],acc[n]);
      STAMP(18,0);
      ldswait(smb+C_G1,4u);
      #pragma unroll
      for(int n=0;n<5;n++) *(LAS f4*)(pb+n*1024)=acc[n];
      signal(smb+C_P+m*4,lane);
    } else {
      if(w==6 && !adone){ pollw(rs,fao,tag); aload(); WAITV(0); signal(smb+C_AT,lane); }
      if(!early){ ldswait(smb+C_AT,1u); addax(); }
      RST(7);
      ldswait(smb+C_P+m*4,1u);
      #pragma unroll
      for(int n=0;n<5;n++){ const f4 o=(acc[n]+ax[n])+*(LAS f4*)(pb+n*1024); *(LAS u2*)(tb+r*176+n*32+q*8)=u2{pkbf(o[0],o[1]),pkbf(o[2],o[3])}; }
      #pragma unroll
      for(int i=0;i<3;i++) if(i<2||lane<32) *(u4*)((char*)p.out+oo[i])=ldsr128(to[i]);
      STAMP(19,4);
    }
  }
}
extern "C" void kernel_launch(void* const* d_in,const int* in_sizes,int n_in,void* d_out,int out_size,void* d_ws,size_t ws_size,hipStream_t stream){
  Params p;
  p.x=(const u16*)d_in[0]; p.lar=(const u16*)d_in[1]; p.aim=(const u16*)d_in[2]; p.cre=(const u16*)d_in[3]; p.cim=(const u16*)d_in[4];
  p.ldt=(const u16*)d_in[5]; p.dsk=(const u16*)d_in[6]; p.win=(const u16*)d_in[8]; p.wsp=(const u16*)d_in[9]; p.bsp=(const u16*)d_in[10]; p.wout=(const u16*)d_in[11];
  p.out=(u16*)d_out; p.ws=(char*)d_ws;
  (void)hipFuncSetAttribute((const void*)fused,hipFuncAttributeMaxDynamicSharedMemorySize,LDS_TOTAL);
  fused<<<256,512,LDS_TOTAL,stream>>>(p);
}
```

```cpp
#include <hip/hip_runtime.h>
#include <utility>
#pragma clang diagnostic ignored "-Wint-to-pointer-cast"
typedef unsigned short u16;
typedef unsigned int u32;
typedef unsigned long long u64;
typedef __attribute__((ext_vector_type(8))) __bf16 bf8;
typedef __attribute__((ext_vector_type(8))) _Float16 hf8;
typedef __attribute__((ext_vector_type(2))) __bf16 bf2;
typedef __attribute__((ext_vector_type(2))) _Float16 hf2;
typedef __attribute__((ext_vector_type(2))) float f2;
typedef __attribute__((ext_vector_type(4))) float f4;
typedef __attribute__((ext_vector_type(16))) float f16v;
typedef __attribute__((ext_vector_type(4))) u32 u4;
typedef __attribute__((ext_vector_type(2))) u32 u2;
typedef __attribute__((ext_vector_type(4))) short s4;
#define LAS __attribute__((address_space(3)))
#define GAS __attribute__((address_space(1)))
#define DI __device__ __forceinline__

constexpr u32 OFF_Z2F = 0;
constexpr u32 OFF_Z1T = OFF_Z2F + 64*54*1024;
constexpr u32 OFF_XNT = OFF_Z1T + 128*27*1024;
constexpr u32 OFF_WOF = OFF_XNT + 640*4096;
constexpr u32 OFF_HF  = OFF_WOF + 8*27*5*1024;
constexpr u32 OFF_AT  = OFF_HF + 128*27*1024;
constexpr u32 OFF_ST  = OFF_AT + 640*4096;
constexpr u32 OFF_RS  = OFF_ST + 16*14*2048+8192;
constexpr u32 OFF_EP  = OFF_RS + 2048*16;
constexpr u32 OFF_F2  = OFF_EP + 256;
constexpr u32 OFF_F1  = OFF_F2 + 256*128;
constexpr u32 OFF_FX  = OFF_F1 + 256*128;
constexpr u32 OFF_FW  = OFF_FX + 256*128;
constexpr u32 OFF_FH  = OFF_FW + 32*128;
constexpr u32 OFF_FA  = OFF_FH + 256*128;
constexpr u32 WS_END  = OFF_FA + 256*128;

struct Params {
  const u16 *x, *lar, *aim, *cre, *cim, *ldt, *dsk, *win, *wsp, *bsp, *wout;
  u16* out; char* ws;
};
#define STAMP(id,wv) do{}while(0)
#define RST(i) do{}while(0)
template<class F,int... I> DI void sfor_impl(F&& f, std::integer_sequence<int,I...>){ (f(std::integral_constant<int,I>{}),...); }
template<int N,class F> DI void sfor(F&& f){ sfor_impl(f, std::make_integer_sequence<int,N>{}); }

DI float bflo(u32 v){ return __uint_as_float(v<<16); }
DI float bfhi(u32 v){ return __uint_as_float(v&0xffff0000u); }
DI float bf1(u16 v){ return __uint_as_float(((u32)v)<<16); }
DI u32 pkbf(float a,float b){ f2 v={a,b}; return __builtin_bit_cast(u32,__builtin_convertvector(v,bf2)); }
DI u32 pkhf(float a,float b){ f2 v={a,b}; return __builtin_bit_cast(u32,__builtin_convertvector(v,hf2)); }
DI float rbf(float a){ return bflo(pkbf(a,a)); }
DI float hlo(u32 v){ return (float)__builtin_bit_cast(hf2,v)[0]; }
DI float hhi(u32 v){ return (float)__builtin_bit_cast(hf2,v)[1]; }
DI float dot2(u32 a,u32 b,float c){ return __builtin_amdgcn_fdot2_f32_bf16(__builtin_bit_cast(bf2,a),__builtin_bit_cast(bf2,b),c,false); }
DI f4 mfma16(u4 a,u4 b,f4 c){ return __builtin_amdgcn_mfma_f32_16x16x32_bf16(__builtin_bit_cast(bf8,a),__builtin_bit_cast(bf8,b),c,0,0,0); }
DI f16v mfma32h(u4 a,u4 b,f16v c){ return __builtin_amdgcn_mfma_f32_32x32x16_f16(__builtin_bit_cast(hf8,a),__builtin_bit_cast(hf8,b),c,0,0,0); }
DI u4 ldsr128(u32 a){ return *(LAS u4*)a; }
DI u2 ldsr64(u32 a){ return *(LAS u2*)a; }
DI void ldsw128(u32 a,u4 v){ *(LAS u4*)a=v; }
DI u2 ldstr(u32 a){ return __builtin_bit_cast(u2,__builtin_amdgcn_ds_read_tr16_b64_v4i16((LAS s4*)a)); }
DI u32 uni(u32 v){ return __builtin_amdgcn_readfirstlane(v); }

template<int OFF> DI void gload(u4& dst,u32 voff,const char* base){ asm volatile("global_load_dwordx4 %0, %1, %2 offset:%3 sc1" : "=v"(dst) : "v"(voff), "s"(base), "n"(OFF)); }
template<int OFF> DI void gload_t(u4& dst,u32 voff,const char* base){ asm volatile("global_load_dwordx4 %0, %1, %2 offset:%3 sc1" : "+v"(dst) : "v"(voff), "s"(base), "n"(OFF)); }
DI void mfma4(f4& c0,f4& c1,f4& c2,f4& c3,const u4& a,const u4 (&b)[4]){
  asm volatile("v_mfma_f32_16x16x32_bf16 %0, %4, %5, %0\n\tv_mfma_f32_16x16x32_bf16 %1, %4, %6, %1\n\tv_mfma_f32_16x16x32_bf16 %2, %4, %7, %2\n\tv_mfma_f32_16x16x32_bf16 %3, %4, %8, %3"
    : "+v"(c0),"+v"(c1),"+v"(c2),"+v"(c3) : "v"(a),"v"(b[0]),"v"(b[1]),"v"(b[2]),"v"(b[3])); }
template<int CNT> DI void waitB(u4 (&B)[7]){ asm volatile("s_waitcnt vmcnt(%7)" : "+v"(B[0]),"+v"(B[1]),"+v"(B[2]),"+v"(B[3]),"+v"(B[4]),"+v"(B[5]),"+v"(B[6]) : "n"(CNT) : "memory"); }
DI void waitA(u4 (&A)[4]){ asm volatile("s_waitcnt lgkmcnt(0)" : "+v"(A[0]),"+v"(A[1]),"+v"(A[2]),"+v"(A[3]) :: "memory"); }
#define WAITV(n) asm volatile("s_waitcnt vmcnt(%0)" :: "n"(n) : "memory")
#define WAITL0() asm volatile("s_waitcnt lgkmcnt(0)" ::: "memory")
#define SCHED() __builtin_amdgcn_sched_barrier(0)
DI void bar(){ asm volatile("s_waitcnt lgkmcnt(0)\n\ts_barrier" ::: "memory"); }
DI f2 unpk(u32 v){ return f2{bflo(v),bfhi(v)}; }
DI u32 pkbf2(f2 v){ return __builtin_bit_cast(u32,__builtin_convertvector(v,bf2)); }
DI u32 pkhf2(f2 v){ return __builtin_bit_cast(u32,__builtin_convertvector(v,hf2)); }
DI f2 fma2(f2 a,f2 b,f2 c){ return __builtin_elementwise_fma(a,b,c); }
DI f2 gelu2(f2 x){
  f2 u=x*fma2(x*x,f2{-0.10294324f,-0.10294324f},f2{-2.30220820f,-2.30220820f});
  f2 d=f2{__builtin_amdgcn_exp2f(u[0]),__builtin_amdgcn_exp2f(u[1])}+f2{1.f,1.f};
  return x*f2{__builtin_amdgcn_rcpf(d[0]),__builtin_amdgcn_rcpf(d[1])};
}
DI void ln_fin(float mean,float var,float& m,float& rinv){
  m=rbf(mean); float v=rbf(__builtin_fmaxf(var,0.f));
  float s=rbf(__builtin_amdgcn_sqrtf(rbf(v+1e-5f))); float r=__builtin_amdgcn_rcpf(s); rinv=__builtin_fmaf(__builtin_fmaf(-s,r,1.f),r,r);
}
typedef __amdgpu_buffer_rsrc_t rsrc_t;
DI void st16(rsrc_t rs,u32 off,u4 v){ __builtin_amdgcn_raw_buffer_store_b128(v,rs,off,0,16); }
DI void st8(rsrc_t rs,u32 off,u2 v){ __builtin_amdgcn_raw_buffer_store_b64(v,rs,off,0,16); }
DI void st4(rsrc_t rs,u32 off,u32 v){ __builtin_amdgcn_raw_buffer_store_b32(v,rs,off,0,16); }
DI u4 ld16(rsrc_t rs,u32 off){ return __builtin_amdgcn_raw_buffer_load_b128(rs,off,0,16); }
DI u2 ld8(rsrc_t rs,u32 off){ return __builtin_amdgcn_raw_buffer_load_b64(rs,off,0,16); }
DI u32 ld4(rsrc_t rs,u32 off){ return __builtin_amdgcn_raw_buffer_load_b32(rs,off,0,16); }
constexpr int SPIN=1<<17;
constexpr u32 TAGINC=0x9E3779B9u;
template<int SL=2> DI void pollw(rsrc_t rs,u32 off,u32 tag){
  #pragma unroll 1
  for(int s=0;s<SPIN;s++){ u32 v=ld4(rs,off); asm volatile("" ::: "memory"); if(__all(v==tag)) break; __builtin_amdgcn_s_sleep(SL); }
}

DI f16v mfma32b(u4 a,u4 b,f16v c){ return __builtin_amdgcn_mfma_f32_32x32x16_bf16(__builtin_bit_cast(bf8,a),__builtin_bit_cast(bf8,b),c,0,0,0); }
DI u4 mksrd(const void* b){ u64 a=(u64)b; return u4{(u32)a,(u32)(a>>32)&0xffffu,0xffffffffu,0x00020000u}; }
template<int IMM> DI void setm0(u32 base){ asm volatile("s_add_u32 m0, %0, %1\n\ts_nop 0" :: "s"(base), "n"(IMM) : "scc","memory"); }
template<int OFF> DI void dma(u4 srd,u32 voff,u32 soff){ asm volatile("buffer_load_dwordx4 %0, %1, %2 offen offset:%3 sc1 lds" :: "v"(voff), "s"(srd), "s"(soff), "n"(OFF) : "memory"); }
DI void touch(u4 srd,u32 voff,u32 soff){ asm volatile("buffer_load_dword %0, %1, %2 offen sc1 lds" :: "v"(voff), "s"(srd), "s"(soff) : "memory"); }
template<int OFF> DI void gload8(u2& dst,u32 voff,const char* base){ asm volatile("global_load_dwordx2 %0, %1, %2 offset:%3 sc1" : "=v"(dst) : "v"(voff), "s"(base), "n"(OFF)); }
DI void gload2(u32& dst,u32 voff,const char* base){ asm volatile("global_load_ushort %0, %1, %2" : "=v"(dst) : "v"(voff), "s"(base)); }
DI u32 arrive(u32 a,int){ u32 o; asm volatile("s_mov_b64 exec, 1\n\tds_add_rtn_u32 %0, %1, %2\n\ts_mov_b64 exec, -1\n\ts_waitcnt lgkmcnt(0)" : "=&v"(o) : "v"(a), "v"(1u) : "memory"); return uni(o); }
DI void ldswait(u32 a,u32 n){ while(__hip_atomic_load((LAS u32*)a,__ATOMIC_RELAXED,__HIP_MEMORY_SCOPE_WORKGROUP)<n) __builtin_amdgcn_s_sleep(1); asm volatile("" ::: "memory"); }
DI float addf(float a,float b){ float o; asm("v_add_f32 %0, %1, %2" : "=v"(o) : "v"(a), "v"(b)); return o; }
DI void tie1(u32& g){ asm volatile("" : "+v"(g) :: "memory"); }
DI u32 opq(u32 v){ asm volatile("" : "+v"(v)); return v; }

constexpr u32 STG=32768;
constexpr u32 RING=24576, RB=62848, CONV=0, KP=2208, UOF=16768, CVS=UOF+4160, S7O=4*KP, S9O=S7O+2112, KZO=S9O+2112, MISC=RB+4*RING, JNK=MISC+2048, PM=163840-128, LDS_TOTAL=163840;
constexpr u32 ATL=2*CVS;
constexpr u32 C_FULL=PM, C_G1=PM+4, C_Z2=PM+8, C_Z1=PM+12, C_RED=PM+16, C_H=PM+20, C_FREE=PM+24, C_CV=PM+28, C_Z1R=PM+32, C_TAG=PM+40, C_KP=PM+48, C_DONE=PM+64, C_ZQ=PM+68, C_CS=PM+72, C_K2=PM+80, C_P=PM+96, C_AT=PM+112, C_WR=PM+116;
constexpr u32 CODE_BYTES=39936;
#define LA 3
#define TC_S 7
#define G2PRIO 2
#define XMAX 9
#define GS_K 35
#define WNT ""
#define TS1 6
#define HPRIO 3
#define CPRIO 3
#define POLLM 1
#define HPF 5
#define CVSKIP 0
#define BROT 0
#define STAG 0u
#define FXDLY 24
#define ZC 0
#define PDLY 8
#define RS_S 7
#define KROT ((((j+rt)&3)*5)>>1)
DI void signal(u32 a,int){ asm volatile("s_mov_b64 exec, 1\n\tds_add_u32 %0, %1\n\ts_mov_b64 exec, -1" :: "v"(a), "v"(1u) : "memory"); }
template<int OFF> DI void gload4(u32& dst,u32 voff,const char* base){ asm volatile("global_load_dword %0, %1, %2 offset:%3 sc1" : "=v"(dst) : "v"(voff), "s"(base), "n"(OFF)); }
template<int CNT> DI void waitG(u4& g){ asm volatile("s_waitcnt vmcnt(%1)" : "+v"(g) : "n"(CNT) : "memory"); }

DI f2 cmulp(f2 a,f2 b,f2 bs){ return fma2(f2{a[1],a[1]},bs,f2{a[0],a[0]}*b); }
DI float mulf(float a,float b){ float o; asm("v_mul_f32 %0, %1, %2" : "=v"(o) : "v"(a),"v"(b)); return o; }
DI float fmaf1(float a,float b,float c){ float o; asm("v_fma_f32 %0, %1, %2, %3" : "=v"(o) : "v"(a),"v"(b),"v"(c)); return o; }
DI u32 ldsv(u32 a){ return __hip_atomic_load((LAS u32*)a,__ATOMIC_RELAXED,__HIP_MEMORY_SCOPE_WORKGROUP); }
DI void ldswait4(u32 a,int lane,u32 n){ while(__any(ldsv(a+(lane&3)*4)<n)) __builtin_amdgcn_s_sleep(1); asm volatile("" ::: "memory"); }
DI u32 gettag(u32 a){ ldswait(a+4,1u); return uni(ldsv(a)); }
DI f2 cpow_dir(float lr,float rv,float pw){ float e=__builtin_amdgcn_exp2f(lr*pw); float f=__builtin_amdgcn_fractf(rv*pw);
  return f2{e*__builtin_amdgcn_cosf(f),e*__builtin_amdgcn_sinf(f)}; }
DI u32 dup(u32 a){ return __builtin_amdgcn_perm(a,a,0x01000100u); }
DI u32 cat(u32 lo,u32 hi){ return __builtin_amdgcn_perm(hi,lo,0x05040100u); }
DI u32 lds16(u32 a){ return *(LAS u16*)a; }

template<class CB> DI float kgen_tab(u32 cb,int lane,const u32 (&pr)[6],float (&kp)[11],CB&& mid){
  const float dt=rbf(__expf(bflo(pr[4])));
  const float cr[2]={bflo(pr[2]),bfhi(pr[2])}, cm[2]={bflo(pr[3]),bfhi(pr[3])};
  float mx=fmaxf(fmaxf(fabsf(cr[0]),fabsf(cm[0])),fmaxf(fabsf(cr[1]),fabsf(cm[1])));
  u32 eb=__float_as_uint(mx)&0x7f800000u;
  #define DPPMAX(c) eb=max(eb,(u32)__builtin_amdgcn_update_dpp(0,(int)eb,c,0xf,0xf,false))
  DPPMAX(0x128); DPPMAX(0x124); DPPMAX(0x122); DPPMAX(0x121);
  eb=max(max((u32)__builtin_amdgcn_readlane(eb,0),(u32)__builtin_amdgcn_readlane(eb,16)),max((u32)__builtin_amdgcn_readlane(eb,32),(u32)__builtin_amdgcn_readlane(eb,48))); eb=eb<0x01000000u?0x3f800000u:(eb>0x7e000000u?0x7e000000u:eb);
  const float ksc=2.f*dt*__uint_as_float(eb), isc=__uint_as_float(0x7f000000u-eb);
  const int r=lane&31,h=lane>>5;
  const u32 TP=272, TA=cb, TB=cb+22*TP;
  f16v acc={};
  f2 an[2][4];
  #pragma unroll
  for(int c=0;c<2;c++){ const float l_a=c?bfhi(pr[0]):bflo(pr[0]), i_m=c?bfhi(pr[1]):bflo(pr[1]);
    const float lr=(-rbf(__expf(l_a))*dt)*1.44269504f, rv=(i_m*dt)*0.15915494f;
    an[c][0]=cpow_dir(lr,rv,1.f); an[c][1]=cpow_dir(lr,-rv,8.f); an[c][2]=cpow_dir(lr,-rv,16.f); an[c][3]=cpow_dir(lr,rv,256.f); }
  #pragma unroll
  for(int c=0;c<2;c++){
    const float c_r=cr[c], c_i=cm[c];
    const f2 z1=an[c][0], z8=an[c][1], zs=an[c][2], zk=an[c][3];
    const f2 z1s={-z1[1],z1[0]}, zss={-zs[1],zs[0]}, zks={-zk[1],zk[0]};
    const u32 wa=TA+lane*4, wb=TB+lane*4;
    #define PUT(base,row,v) do{ *(LAS u32*)((base)+(row)*TP)=pkhf2(v); }while(0)
    f2 e={1.f,0.f}, o=z8;
    #pragma unroll
    for(int k=0;k<16;k++){ PUT(wb,2*k,e); PUT(wb,2*k+1,o); if(k<15){ e=k?cmulp(e,zs,zss):zs; o=cmulp(o,zs,zss);} }
    f2 b0={c_r*isc,c_i*isc}; PUT(wa,0,b0);
    f2 t=cmulp(b0,zk,zks); PUT(wa,8,t);
    f2 b1=cmulp(b0,z1,z1s); PUT(wa,1,b1); t=cmulp(t,zk,zks); PUT(wa,10,t);
    f2 b2=cmulp(b1,z1,z1s); PUT(wa,2,b2); t=cmulp(t,zk,zks); PUT(wa,11,t);
    f2 b3=cmulp(b2,z1,z1s); PUT(wa,3,b3); t=cmulp(t,zk,zks); PUT(wa,16,t);
    f2 b4=cmulp(b3,z1,z1s); PUT(wa,4,b4); t=cmulp(t,zk,zks); PUT(wa,17,t);
    f2 b5=cmulp(b4,z1,z1s); PUT(wa,5,b5); t=cmulp(t,zk,zks); PUT(wa,20,t);
    f2 b6=cmulp(b5,z1,z1s); PUT(wa,6,b6); t=cmulp(t,zk,zks); PUT(wa,21,t);
    f2 b7=cmulp(b6,z1,z1s); PUT(wa,7,b7); t=cmulp(t,zk,zks); PUT(wa,18,t);
    f2 s=cmulp(b4,zk,zks); PUT(wa,12,s); t=cmulp(b2,zk,zks); PUT(wa,9,t);
    s=cmulp(s,zk,zks); PUT(wa,14,s); t=cmulp(b6,zk,zks); PUT(wa,13,t);
    s=cmulp(s,zk,zks); PUT(wa,15,s);
    #undef PUT
    if(c==0) mid();
    const int ra=(r>21||r==19)?0:r;
    #pragma unroll
    for(int ks=0;ks<8;ks++){ u4 fa=ldsr128(TA+ra*TP+ks*32+h*16), fb=ldsr128(TB+r*TP+ks*32+h*16); acc=mfma32h(fa,fb,acc); }
  }
  #pragma unroll
  for(int i=0;i<11;i++) kp[i]=acc[i];
  return ksc;
}
DI void kgen_exp(u32 cb,int lane,const float (&acc)[11],const u32 (&ga)[11]){
  const int r=lane&31,h=lane>>5;
  { const u32 a4=cb+r*16+h*8, a5=cb+r*16+h*1024, az=cb+KZO+r*2+h*128;
    #define PK(addr,base,reg) do{ *(LAS _Float16*)((addr)+2*(base))=(_Float16)acc[reg]; }while(0)
    PK(a4,0,0);PK(a4,1,1);PK(a4,2,2);PK(a4,3,3); PK(a4,256,4);PK(a4,258,5);PK(a4,512,6);PK(a4,768,7);
    PK(a5,1024,8); PK(az,255,10); PK(az,127,8); PK(az,159,9);
    #undef PK
    if(lane<16) ldsw128(cb+KZO+lane*16,u4{0,0,0,0});
  }
  { const u32 g0=cb+2*(1024-8*lane);
    u32 f2_=lds16(g0+(lane?8:0)), a2=lds16(g0), m2=lds16(g0-8), b2=lds16(g0-16);
    u32 e[11];
    #pragma unroll
    for(int j=0;j<11;j++) e[j]=lds16(ga[j]);
    u32 k01=*(LAS u32*)cb, k23=*(LAS u32*)(cb+4), kl=lds16(cb+2048);
    WAITL0();
    const u32 wo=cb+96+lane*16;
    { u32 aa=dup(a2),mm=dup(m2),bb=dup(b2),am=cat(a2,m2),fa=cat(f2_,a2),mb=cat(m2,b2);
      ldsw128(wo,u4{aa,am,mm,bb}); ldsw128(wo+KP,u4{aa,aa,mm,mb}); ldsw128(wo+2*KP,u4{aa,aa,am,mm}); ldsw128(wo+3*KP,u4{fa,aa,aa,mm}); }
    { u32 o0=cat(e[0],e[1]),o1=cat(e[2],e[3]),o2=cat(e[4],e[5]),o3=cat(e[6],e[7]),o4=cat(e[8],e[9]);
      u32 p1=cat(e[1],e[2]),d0=cat(e[3],e[4]),d1=cat(e[5],e[6]),d2=cat(e[7],e[8]),d3=cat(e[9],e[10]);
      ldsw128(wo+1024,u4{d0,d1,d2,d3}); ldsw128(wo+1024+KP,u4{o1,o2,o3,o4}); ldsw128(wo+1024+2*KP,u4{p1,d0,d1,d2}); ldsw128(wo+1024+3*KP,u4{o0,o1,o2,o3}); }
    if(lane<10){ if(lane){k01=0;k23=0;} if(lane!=9) kl=0;
      const u32 d0=k01&0xffffu, o1=__builtin_amdgcn_alignbit(k01,k01,16), o0=__builtin_amdgcn_alignbit(k23,k23,16), p1=(k23&0xffffu)|(k01&0xffff0000u), kk=dup(kl), zk=kl<<16;
      const u32 w2=lane<4? wo+2048 : wo-160;
      ldsw128(w2,u4{d0,0,kk,kk}); ldsw128(w2+KP,u4{o1,0,zk,kk}); ldsw128(w2+2*KP,u4{p1,d0,0,kk}); ldsw128(w2+3*KP,u4{o0,o1,0,zk}); }
  }
}
DI f4 mfma16h(u4 a,u4 b,f4 c){ return __builtin_amdgcn_mfma_f32_16x16x32_f16(__builtin_bit_cast(hf8,a),__builtin_bit_cast(hf8,b),c,0,0,0); }
DI u4 lds2x64(u32 a){ const u2 lo=ldsr64(a), hi=ldsr64(a+8); return u4{lo[0],lo[1],hi[0],hi[1]}; }
DI void conv_win(u32 cb,int lane,u4 (&sw)[4]){
  float P[25]; P[0]=0.f;
  #pragma unroll
  for(int c=0;c<4;c++){ const int X=2*lane-2+c; const u4 v=ldsr128(cb+UOF+(X&3)*1040+((X>>2)+1)*16);
    #pragma unroll
    for(int e=0;e<8;e++){ const int i=8*c+e-4; if(i>=0&&i<24) P[i+1]=P[i]+((e&1)?hhi(v[e>>1]):hlo(v[e>>1])); } }
  #pragma unroll
  for(int k=0;k<8;k++){ sw[k>>2][k&3]=pkhf(P[2*k+8]-P[2*k+1],P[2*k+9]-P[2*k+2]); sw[2+(k>>2)][k&3]=pkhf(P[2*k+9]-P[2*k],P[2*k+10]-P[2*k+1]); }
}
DI void conv_help(u32 cb,int lane,const u4 (&sw)[4]){
  const int r=lane&15,q=lane>>4; f4 y[2][2]={};
  ldsw128(cb+S7O+48+lane*32,sw[0]); ldsw128(cb+S7O+64+lane*32,sw[1]); ldsw128(cb+S9O+48+lane*32,sw[2]); ldsw128(cb+S9O+64+lane*32,sw[3]);
  if(lane<3){ ldsw128(cb+S7O+lane*16,u4{0,0,0,0}); ldsw128(cb+S9O+lane*16,u4{0,0,0,0}); }
  const u32 ka=cb+(r&3)*KP+16*q-8*(r>>2)+64, ub=cb+UOF+q*1040+(r+1)*16;
  u4 a[2][4][2],bb[2][4]; const u32 kg=ka+64, ug=ub+272;
  auto rdn=[&](int g){
    #pragma unroll
    for(int i=0;i<4;i++){ a[g&1][i][0]=lds2x64(kg+(3-g)*256+i*64+32); a[g&1][i][1]=lds2x64(kg+(3-g)*256+i*64); bb[g&1][i]=ldsr128(ug+(3-g)*64+i*16); } };
  auto mmn=[&](int g){
    #pragma unroll
    for(int i=0;i<4;i++){ y[0][1]=mfma16h(a[g&1][i][0],bb[g&1][i],y[0][1]); y[1][1]=mfma16h(a[g&1][i][1],bb[g&1][i],y[1][1]); } };
  rdn(0); rdn(1); SCHED(); mmn(0); SCHED(); rdn(2); SCHED(); mmn(1); SCHED(); rdn(3); SCHED(); mmn(2); SCHED();
  const u4 a0=lds2x64(ka+32), a1=lds2x64(ka), b0=ldsr128(ub), b1=ldsr128(ub+256), a2=lds2x64(ka-32), b2=ldsr128(ub-16), b3=ldsr128(ub+240);
  const u32 sa=cb+(((r>>2)&1)?S9O:S7O)+2*(64*q+24-8*(r>>2)+4*(r&3)), kz=cb+KZO+8*(r-2*q+23); u4 bz[4],fa[4][2];
  #pragma unroll
  for(int ks=0;ks<4;ks++) bz[ks]=lds2x64(kz+(3-ks)*64);
  SCHED(); mmn(3); SCHED();
  #pragma unroll
  for(int i=0;i<8;i++){ const u2 lo=ldstr(sa+(i>>1)*512+(i&1)*32+64), hi=ldstr(sa+(i>>1)*512+(i&1)*32); fa[i>>1][i&1]=u4{lo[0],lo[1],hi[0],hi[1]}; }
  SCHED();
  y[0][0]=mfma16h(a2,b2,y[0][0]); y[0][1]=mfma16h(a2,b3,y[0][1]);
  y[0][0]=mfma16h(a0,b0,y[0][0]); y[1][0]=mfma16h(a1,b0,y[1][0]); y[0][1]=mfma16h(a0,b1,y[0][1]); y[1][1]=mfma16h(a1,b1,y[1][1]);
  #pragma unroll
  for(int ks=0;ks<4;ks++)
    #pragma unroll
    for(int m=0;m<2;m++){ y[m][1]=mfma16h(fa[ks][m],bz[ks],y[m][1]); if(ks<2) y[m][0]=mfma16h(fa[ks][m],bz[ks+2],y[m][0]); }
  #pragma unroll
  for(int i=0;i<4;i++) *(LAS f4*)(cb+S7O+i*1024+lane*16)=y[i&1][i>>1];
}
#define RDA(S,i,m) asm volatile("ds_read2_b64 %0, %1 offset0:%2 offset1:%3" : "=v"(AF[S][i][m]) : "v"(ka), "n"((1-(i))*128+(1-(m))*4), "n"((1-(i))*128+(1-(m))*4+1))
#define RDB(S,i,ad,o) asm volatile("ds_read_b128 %0, %1 offset:%2" : "=v"(BF[S][i]) : "v"(ad), "n"(o))
#define TIEA(S,i) "+v"(AF[S][i][0]),"+v"(AF[S][i][1])
#define CV_M(S,i,n) do{ y[0][n]=mfma16h(AF[S][i][0],BF[S][(n)-(i)],y[0][n]); y[1][n]=mfma16h(AF[S][i][1],BF[S][(n)-(i)],y[1][n]); SCHED(); }while(0)
#define CV_U0() u32 u0=u1-256; u0=((int)u0<(int)uz)?uz:u0
DI void conv_wave(const Params& p,rsrc_t rs,int d,u32 cb,u32 flag,u32 turn,u32 need,int lane,float ksc,float dsk){
  const int r=lane&15,q=lane>>4; u32 ka=cb+(r&3)*KP+16*q-8*(r>>2)+1088; const u32 uz=cb+UOF+q*1040; u32 u1=uz+(r+17)*16; u4 AF[2][2][2]; u4 BF[2][4]; f4 y[2][4]={};
  #define CV_WT(S) asm volatile("s_waitcnt lgkmcnt(0)" : TIEA(S,0),TIEA(S,1),"+v"(BF[S][0]),"+v"(BF[S][1]),"+v"(BF[S][2]),"+v"(BF[S][3]))
  #define CV_STEP(C,N) do{ CV_U0(); CV_M(C,0,0); RDA(N,0,0); RDB(N,0,u0,0); RDA(N,0,1); SCHED(); CV_M(C,0,1); RDB(N,1,u1,0); RDA(N,1,0); RDB(N,2,u1,256); SCHED(); CV_M(C,0,2); RDA(N,1,1); RDB(N,3,u1,512); \
      ka-=64; u1-=16; SCHED(); CV_M(C,0,3); CV_M(C,1,1); CV_M(C,1,2); CV_WT(N); }while(0)
  { CV_U0(); RDA(0,0,0); RDA(0,0,1); RDA(0,1,0); RDA(0,1,1); RDB(0,0,u0,0); RDB(0,1,u1,0); RDB(0,2,u1,256); RDB(0,3,u1,512); ka-=64; u1-=16; CV_WT(0); }
  #pragma unroll 1
  for(int dl=0;dl<8;dl++){ const u32 tv=ldsv(turn); CV_STEP(0,1); if(uni(tv)>=need) __builtin_amdgcn_s_setprio(3); if(dl==7){ ka+=64; u1+=16; } CV_STEP(1,0); }
  signal(turn,lane);
  asm volatile("s_nop 15" : "+v"(y[0][0]),"+v"(y[0][1]),"+v"(y[0][2]),"+v"(y[0][3]),"+v"(y[1][0]),"+v"(y[1][1]),"+v"(y[1][2]),"+v"(y[1][3]));
  __builtin_amdgcn_s_setprio(3);
  u2 uv[2][4];
  #pragma unroll
  for(int n=0;n<4;n++)
    #pragma unroll
    for(int m=0;m<2;m++) uv[m][n]=ldsr64(cb+UOF+(2*m+(q>>1))*1040+(16*n+r+1)*16+8*(q&1));
  const u32 sc=cb+UOF+r*64+q*8;
  auto fin=[&](int n){
    #pragma unroll
    for(int m=0;m<2;m++){ u2 o;
      #pragma unroll
      for(int e=0;e<2;e++) o[e]=pkbf(fmaf1(hlo(uv[m][n][e]),dsk,mulf(y[m][n][2*e],ksc)),fmaf1(hhi(uv[m][n][e]),dsk,mulf(y[m][n][2*e+1],ksc)));
      *(LAS u2*)(sc+n*1024+m*32)=o; } };
  ldswait(flag,2u);
  { f4 hp[4];
    #pragma unroll
    for(int i=0;i<4;i++) hp[i]=*(LAS f4*)(cb+S7O+i*1024+lane*16);
    fin(0); fin(1);
    #pragma unroll
    for(int i=0;i<4;i++)
      #pragma unroll
      for(int e=0;e<4;e++) y[i&1][2+(i>>1)][e]=addf(y[i&1][2+(i>>1)][e],hp[i][e]);
    fin(2); fin(3); }
  u4 ov[4];
  #pragma unroll
  for(int n=0;n<4;n++) ov[n]=ldsr128(cb+UOF+n*1024+lane*16);
  #pragma unroll
  for(int n=0;n<4;n++) st16(rs,OFF_AT+d*4096+n*1024+lane*16,ov[n]);
  WAITV(0);
}

#define NB 3
constexpr int g2ops(int h){ return h<0? 7 : (h+NB<16?7:0)+((h>=3&&h<=10)?4:0)+(h==13-NB?1:0)+(h==16-NB?8:0); }
constexpr int g2cnt(int h){ int c=0; for(int i=h-NB+1;i<h;i++) c+=g2ops(i); return c; }
struct Frag { u4 x[4], w[4][2]; };
__global__ __launch_bounds__(512,2) void fused(Params p){
  extern __shared__ __attribute__((aligned(16))) char sm[];
  const u32 smb=(u32)(size_t)(LAS char*)sm;
  u64 pc0; asm volatile("s_getpc_b64 %0" : "=s"(pc0));
  const int b=blockIdx.x, xc=b&7, idx=b>>3;
  const int tid=threadIdx.x, w=uni(tid>>6); int lane=tid&63;
  const rsrc_t rs=__builtin_amdgcn_make_buffer_rsrc(p.ws,0,WS_END,0x00020000);
  const u4 srdS=mksrd(p.ws);
  const int rtB=4*xc+(idx&3), ct=idx>>2;
  const bool hask=idx<28 && idx+28*(w&3)<80, hasc=hask && w>=4;
  const int dch=80*xc+(hask? idx+28*(w&3) : 0);
  const u32 cb=smb+CONV+(w&3)*CVS;
  const int rt=4*(xc>>1)+(idx&3), j=7*(xc&1)+(idx>>2), mi=w&3, g1=w>>2;
  u32 tag; float ksc=0.f, dsk=0.f;
  STAMP(0,0);
  auto pre01=[&](){
    const u4 sa=mksrd((const char*)p.wsp+(size_t)rtB*64*4096+mi*1024-4096); const u32 v0=(lane>>3)*4096+(((lane&7)^(lane>>4))<<4);
    #pragma unroll 1
    for(int i=0;i<4;i++){ setm0<RB+8192>(smb+mi*RING+i*4096); const u32 g=(i&1)*4+2*mi, k=(i>>1)*128+4096;
      dma<0>(sa,v0,(g&7)*32768+k); dma<1024>(sa,v0^64u,(g&7)*32768+32768-1024+k); dma<2048>(sa,v0,((g+2)&7)*32768-2048+k); dma<3072>(sa,v0^64u,((g+2)&7)*32768+32768-3072+k); } };
  if(idx>=28){
    const int a=xc*4+idx-28, r=lane&15, q=lane>>4;
    { u4 sc=mksrd((const void*)(pc0&~127ull)); sc[2]=CODE_BYTES; setm0<JNK>(smb); touch(sc,lane*128,w*8192); }
    const u32 ep=ld4(rs,OFF_EP);
    u4 xr[10]; const char* xs=(const char*)p.x+(size_t)(64*a+8*w+(lane>>3))*1280+(lane&7)*16;
    #pragma unroll
    for(int i=0;i<10;i++) xr[i]=*(const u4*)(xs+i*128);
    if(tid<32) *(LAS u32*)(smb+PM+tid*4)=0u;
    tag=uni(ep)+TAGINC;
    float s0=0.f,s1=0.f,q0=0.f,q1=0.f;
    #pragma unroll
    for(int i=0;i<10;i++){ s0=dot2(xr[i][0],0x3f803f80u,s0); q0=dot2(xr[i][0],xr[i][0],q0); s1=dot2(xr[i][1],0x3f803f80u,s1); q1=dot2(xr[i][1],xr[i][1],q1);
      s0=dot2(xr[i][2],0x3f803f80u,s0); q0=dot2(xr[i][2],xr[i][2],q0); s1=dot2(xr[i][3],0x3f803f80u,s1); q1=dot2(xr[i][3],xr[i][3],q1); }
    s0+=s1; q0+=q1;
    #define DPPADD(v,c) v+=__int_as_float(__builtin_amdgcn_update_dpp(0,__float_as_int(v),c,0xf,0xf,false))
    DPPADD(s0,0xB1); DPPADD(q0,0xB1); DPPADD(s0,0x4E); DPPADD(q0,0x4E); DPPADD(s0,0x141); DPPADD(q0,0x141);
    float m1,ri; { float mean=s0*(1.f/640.f); ln_fin(mean,q0*(1.f/640.f)-mean*mean,m1,ri); }
    if((lane&7)==0) st16(rs,OFF_RS+(64*a+8*w+(lane>>3))*16,u4{__float_as_uint(m1),tag,__float_as_uint(ri),tag});
    STAMP(5,0);
    if(a<27){ const u4 srdO=mksrd((const char*)p.wout+a*(32*1280)+w*5120); const u32 mo=smb+w*5120; setm0<0>(mo);
      dma<0>(srdO,lane*16,0); dma<1024>(srdO,lane*16,0); dma<2048>(srdO,lane*16,0); dma<3072>(srdO,lane*16,0); setm0<4096>(mo); dma<0>(srdO,lane*16,4096); }
    #pragma unroll
    for(int i=0;i<10;i++){ u4 o;
      #pragma unroll
      for(int e=0;e<4;e++) o[e]=pkhf2(fma2(unpk(xr[i][e]),f2{ri,ri},f2{-m1*ri,-m1*ri}));
      ldsw128(smb+RB+(8*w+(lane>>3))*1296+(lane&7)*16+i*128,o); }
    WAITV(0); bar();
    #pragma unroll
    for(int i=0;i<10;i++){ const int c=w*10+i; const u32 ad=smb+RB+(32*((r>>1)&1)+8*q+(r>>2))*1296+c*16+(r&1)*8; u2 lo=ldstr(ad), hi=ldstr(ad+4*1296);
      const u32 sc=smb+40960+w*2048+(i&1)*1024; ldsw128(sc+(r&7)*128+((((r>>3)*4+q)^(r&7))<<4),u4{lo[0],lo[1],hi[0],hi[1]});
      st16(rs,OFF_XNT+a*81920+c*1024+lane*16,ldsr128(sc+(lane>>3)*128+(((lane&7)^(lane>>3))<<4))); }
    WAITV(0);
    if(arrive(smb+C_Z1,lane)==7u && lane==0) st4(rs,OFF_FX+a*128,tag);
    STAMP(6,7);
    if(a<27){
      #pragma unroll
      for(int i=0;i<5;i++){ u32 ad=smb+(8*q+(r>>2))*1280+(w*5+i)*32+(r&3)*8; u2 lo=ldstr(ad), hi=ldstr(ad+4*1280);
        st16(rs,OFF_WOF+(u32)((w*27+a)*5+i)*1024+lane*16,u4{lo[0],lo[1],hi[0],hi[1]}); } }
    WAITV(0); bar();
    if(tid==0) st4(rs,OFF_FW+a*128,tag);
    if(!g1){ pre01(); WAITV(0); }
    STAMP(31,0);
  } else {
    const int r=lane&31, h=lane>>5;
    const u4 srdX=mksrd((const char*)p.x+(size_t)rt*128*1280);
    const u4 srdW=mksrd((const char*)p.win+j*128-3072);
    const u32 mw=smb+w*4096;
    u32 vx0,vx1,vw1,vw2,epv;
    { u32 lr=lane>>3,c=lane&7; vx0=(16*w+lr)*1280+((c^(lr>>1))<<4); vx1=(vx0^64u)+9216;
      vw1=(8*w+lr)*3456+((c^((lr&2)<<1))<<4); vw2=vw1+1024+(j==13?0:14*128); }
    u32 kx[10]; { u32 k=KROT;
      #pragma unroll
      for(int i=0;i<10;i++){ kx[i]=k*128; k=k==9?0:k+1; } }
    auto issue1=[&](auto sc,int k){ constexpr int s=sc.value;
      if(k==0){ setm0<(s&3)*STG>(mw); dma<0>(srdX,vx0,kx[s]); } if(k==1) dma<1024>(srdX,vx1,kx[s]); if(k==2) dma<2048>(srdW,vw2,kx[s]*1728); if(k==3) dma<3072>(srdW,vw1,kx[s]*1728); };
    auto issue=[&](auto sc){ issue1(sc,0); issue1(sc,1); issue1(sc,2); issue1(sc,3); };
    issue(std::integral_constant<int,0>{});
    u32 pr[6];
    gload4<0>(epv,0u,p.ws+OFF_EP);
    gload4<0>(pr[0],lane*4,(const char*)(p.lar+dch*128)); gload4<0>(pr[1],lane*4,(const char*)(p.aim+dch*128)); gload4<0>(pr[2],lane*4,(const char*)(p.cre+dch*128)); gload4<0>(pr[3],lane*4,(const char*)(p.cim+dch*128));
    gload2(pr[4],0u,(const char*)(p.ldt+dch)); gload2(pr[5],0u,(const char*)(p.dsk+dch));
    asm volatile("s_barrier" ::: "memory");
    issue(std::integral_constant<int,1>{});
    if(tid<32) *(LAS u32*)(smb+PM+tid*4)=0u;
    u32 ax[2][4],aw[2][4][2];
    { u32 row=32*mi+r, a0=smb+(row>>4)*4096+(row&15)*128+((h^((r>>1)&7))<<4);
      u32 qq=(lane&15)>>2, w0=smb+2048+g1*1024+h*4096+qq*128+8*(lane&1)+(((2*((lane>>4)&1)+((lane>>1)&1))^((qq&2)<<1))<<4);
      #pragma unroll
      for(int i=0;i<2;i++)
        #pragma unroll
        for(int k=0;k<4;k++){ const u32 kk=(k+mi)&3, wo=i*2*STG; ax[i][k]=opq((a0^(kk*32u))+wo); aw[i][k][0]=opq(w0+kk*8192+wo); aw[i][k][1]=opq((w0^64u)+kk*8192+wo); } }
    u4 sel; { const u32 o=((lane&15)==((lane>>4)&1))?0x3f803f80u:0u; sel=u4{o,o,o,o}; asm volatile("" : "+v"(sel)); }
    u32 ga[11];
    #pragma unroll
    for(int i=0;i<11;i++) ga[i]=opq(cb+2u*(u32)rbf((float)(512-8*lane+3-i)));
    Frag F[2];
    asm volatile("s_waitcnt vmcnt(4)" : "+v"(epv),"+v"(pr[0]),"+v"(pr[1]),"+v"(pr[2]),"+v"(pr[3]),"+v"(pr[4]),"+v"(pr[5]) :: "memory");
    bar();
    tag=uni(epv)+TAGINC; dsk=bflo(pr[5]);
    #pragma unroll
    for(int k=0;k<4;k++){
      #pragma unroll
      for(int n=0;n<2;n++){ u2 lo=ldstr(aw[0][k][n]), hi=ldstr(aw[0][k][n]+512); F[0].w[k][n]=u4{lo[0],lo[1],hi[0],hi[1]}; }
      F[0].x[k]=ldsr128(ax[0][k]); }
    issue(std::integral_constant<int,2>{});
    STAMP(1,0);
    f16v acc[2]={}; f4 cacc[2]={}; u4 G;
    const u32 go=OFF_RS+rt*2048+mi*512, cp=smb+MISC+g1*1024;
    sfor<10>([&](auto ic){
      constexpr int s=ic.value; Frag& cur=F[s&1]; Frag& nxt=F[(s&1)^1];
      if constexpr(s<9){ WAITV(s==8? 3 : s==7? 7 : 4); asm volatile("s_barrier" ::: "memory"); }
      auto rw=[&](int k,int n){ if constexpr(s<9){ const int i=((s+1)>>1)&1; const u32 so=((s+1)&1)*STG; u2 lo=ldstr(aw[i][k][n]+so), hi=ldstr(aw[i][k][n]+so+512); nxt.w[k][n]=u4{lo[0],lo[1],hi[0],hi[1]}; } };
      auto rx=[&](int k){ if constexpr(s<9){ const int i=((s+1)>>1)&1; const u32 so=((s+1)&1)*STG; nxt.x[k]=ldsr128(ax[i][k]+so); } };
      #pragma unroll
      for(int k=0;k<4;k++){
        acc[0]=mfma32b(cur.w[k][0],cur.x[k],acc[0]); SCHED();
        if constexpr(s+3<=9) issue1(std::integral_constant<int,s+3>{},k);
        rw(k,0); rx(k);
        SCHED();
        acc[1]=mfma32b(cur.w[k][1],cur.x[k],acc[1]); SCHED();
        rw(k,1);
        if(k==0){ cacc[0]=mfma16(cur.w[0][0],sel,cacc[0]); cacc[1]=mfma16(cur.w[0][1],sel,cacc[1]); }
        if constexpr(s==6){ if(k==3){ gload<0>(G,r*16,p.ws+go); setm0<JNK>(smb);
            if(g1){ const u4 sp=mksrd((const char*)p.wsp+(size_t)rtB*64*4096); const u32 l0=(ct*4+mi)*74+lane, l1=(ct*4+mi)*74+64+(lane<10?lane:9);
              touch(sp,(l0<2048?l0:2047)*128,0u); touch(sp,(l1<2048?l1:2047)*128,0u); }
            else { touch(srdS,0u,go); touch(srdS,0u,go); } } }
        if constexpr(s==8){ if(k==3) signal(smb+C_DONE,lane); }
        if constexpr(s==9){ if(k==1){
          if((lane&14)==0){ *(LAS f4*)(cp+mi*256+(lane&1)*64+(lane>>4)*16)=cacc[0]; *(LAS f4*)(cp+mi*256+128+(lane&1)*64+(lane>>4)*16)=cacc[1]; }
          signal(smb+C_CS+g1*4,lane); } }
        SCHED();
      }
    });
    RST(0);
    if(!g1) __builtin_amdgcn_s_setprio(2);
    asm volatile("s_waitcnt vmcnt(2)" : "+v"(G) :: "memory");
    ldswait(smb+C_CS+g1*4,4u);
    const float cs0=*(LAS float*)(cp+h*512+r*4)+*(LAS float*)(cp+h*512+256+r*4), cs1=*(LAS float*)(cp+h*512+128+r*4)+*(LAS float*)(cp+h*512+384+r*4);
    RST(2);
    #pragma unroll 1
    for(int sp=0;sp<SPIN && !__all(G[1]==tag&&G[3]==tag);sp++)asm volatile("global_load_dwordx4 %0, %1, %2 sc1\n\ts_waitcnt vmcnt(0)" : "=&v"(G) : "v"(r*16), "s"(p.ws+go) : "memory");
    RST(3);
    const float nm=-__uint_as_float(G[0]), ri1=__uint_as_float(G[2]);
    acc[0]=__builtin_amdgcn_mfma_f32_32x32x2f32(cs0,nm,acc[0],0,0,0); acc[1]=__builtin_amdgcn_mfma_f32_32x32x2f32(cs1,nm,acc[1],0,0,0);
    u32 zp[2][8]; const u32 zqa=smb+RB+mi*RING+4096+lane*16;
    if(g1){
      ldswait(smb+C_DONE,8u);
      #pragma unroll
      for(int i=0;i<4;i++){ u4 o;
        #pragma unroll
        for(int e=0;e<4;e++) o[e]=pkbf2(f2{acc[i>>1][8*(i&1)+2*e],acc[i>>1][8*(i&1)+2*e+1]}*ri1);
        ldsw128(zqa+i*1024,o); }
      signal(smb+C_K2+mi*4,lane); RST(4);
    } else {

      #pragma unroll
      for(int e=0;e<16;e++) zp[e>>3][e&7]=pkbf2(gelu2(f2{acc[e>>3][2*(e&7)],acc[e>>3][2*(e&7)+1]}*ri1));
      float S=0.f,Q=0.f;
      if(j<13){
        #pragma unroll
        for(int e=0;e<8;e++){ S=dot2(zp[0][e],0x3f803f80u,S); Q=dot2(zp[0][e],zp[0][e],Q); } }
      #pragma unroll
      for(int e=0;e<8;e++){ S=dot2(zp[1][e],0x3f803f80u,S); Q=dot2(zp[1][e],zp[1][e],Q); }
      #define XSUM32(v) do{ auto t_=__builtin_amdgcn_permlane32_swap(__float_as_uint(v),__float_as_uint(v),false,false); v=__uint_as_float(t_[0])+__uint_as_float(t_[1]); }while(0)
      XSUM32(S); XSUM32(Q);
      const u32 sto=OFF_ST+rt*(14*2048)+(32*mi+r)*16;
      if(h==0) st16(rs,sto+j*2048,u4{__float_as_uint(S),tag,__float_as_uint(Q),tag});
      RST(6);
    }
    float kp[11]; u4 g[7]={};
    const u32 gof=OFF_ST+rt*(14*2048)+(32*mi+r)*16+h*(7*2048);
    auto samp=[&](){ asm volatile("" ::: "memory");
      #pragma unroll
      for(int i=0;i<7;i++) g[i]=ld16(rs,gof+i*2048);
      asm volatile("" ::: "memory"); };
    if(hasc) ksc=kgen_tab(cb,lane,pr,kp,[](){}); else if(!g1){
      pre01(); ldswait(smb+C_K2+mi*4,1u);
      #pragma unroll
      for(int i=0;i<4;i++){ const u4 v=ldsr128(zqa+i*1024); u4 o;
        #pragma unroll
        for(int e=0;e<4;e++) o[e]=pkbf2(gelu2(unpk(v[e])));
        ldsw128(zqa+i*1024,o); if(i==2){ SCHED(); samp(); SCHED(); } }
      signal(smb+C_Z1,lane);
    }
    if(g1){
      if(hasc){
        RST(6);
        kgen_exp(cb,lane,kp,ga);
        signal(smb+C_KP+mi*4,lane);
        RST(8);
        ldswait(smb+C_FULL,1u);
        RST(9);
      }
    } else {
      RST(1);
      float S,Q;
      #pragma unroll 1
      for(int sp=0;sp<SPIN;sp++){ bool ok=true;
        #pragma unroll
        for(int i=0;i<7;i++) ok=ok&&g[i][1]==tag&&g[i][3]==tag;
        if(__all(ok)) break;
        __builtin_amdgcn_s_sleep(4); samp(); }
      RST(7);
      S=0.f; Q=0.f;
      #pragma unroll
      for(int i=0;i<7;i++){ S+=__uint_as_float(g[i][0]); Q+=__uint_as_float(g[i][2]); }
      XSUM32(S); XSUM32(Q);
      float mean=S*(1.f/864.f), m2,r2; ln_fin(mean,Q*(1.f/864.f)-mean*mean,m2,r2); m2*=-r2;
      ldswait(smb+C_DONE,8u);
      const u32 tb=smb+RB+mi*RING, ad=tb+(8*(lane>>4)+((lane&15)>>2))*80+(lane&3)*8;
      const u32 zo=OFF_Z2F+((rt*4+mi)*54+(j==13?-2:2+4*j))*1024+lane*16;
      #pragma unroll
      for(int n=0;n<2;n++){
        #pragma unroll
        for(int g2=0;g2<4;g2++){ u2 o;
          #pragma unroll
          for(int e=0;e<2;e++) o[e]=pkbf2(fma2(unpk(zp[n][2*g2+e]),f2{r2,r2},f2{m2,m2}));
          *(LAS u2*)(tb+r*80+g2*16+h*8)=o; }
        u2 l0=ldstr(ad), h0=ldstr(ad+320), l1=ldstr(ad+32), h1=ldstr(ad+32+320);
        if(j<13||n){ st16(rs,zo+n*2048,u4{l0[0],l0[1],h0[0],h0[1]}); st16(rs,zo+n*2048+1024,u4{l1[0],l1[1],h1[0],h1[1]}); } }
      RST(8);
      WAITV(0);
      if(arrive(smb+C_Z2,lane)==3u && lane==0) st4(rs,OFF_F2+(rt*16+j)*128,tag);
      RST(9);
      ldswait(smb+C_Z1,5u);
    }
  }
  const int ft0=ct<7?7*ct:47;
  bool adone=true; const u32 fao=OFF_FA+((lane<28?lane:0)*8+ct)*128;
  auto aload=[&](){ const u32 vo=(lane>>3)*4096+(lane&7)*16;
    #pragma unroll 1
    for(int i=0;i<10;i++){ setm0<ATL>(smb+i*1024); dma<0>(srdS,vo,OFF_AT+(u32)(80*ct+8*i)*4096+rtB*128); } };
  if(w>=4){
    if(hasc){
      conv_wave(p,rs,dch,cb,smb+C_KP+mi*4,smb+C_ZQ,STAG,lane,ksc,dsk);
      __builtin_amdgcn_s_setprio(0);
      if(arrive(smb+C_CV,lane)==(idx<24?2u:1u) && lane==0) st4(rs,OFF_FA+b*128,tag);
    }
    if(w==7){
      if(idx<28){
        u4 zq[4][4];
        ldswait(smb+C_Z1,4u);
        #pragma unroll
        for(int c=0;c<4;c++)
          #pragma unroll
          for(int i=0;i<4;i++) zq[c][i]=ldsr128(smb+RB+c*RING+4096+i*1024+lane*16);
        signal(smb+C_Z1,lane);
        pollw<16>(rs,OFF_FX+(lane&31)*128,tag);
        u4 sw[3][4];
        #pragma unroll
        for(int c=0;c<3;c++)
          #pragma unroll
          for(int i=0;i<4;i++) sw[c][i]=ld16(rs,OFF_XNT+((lane>>3)+8*i)*81920+(80*xc+idx+((c<2||idx<24)?28*c:0))*128+(lane&7)*16);
        #pragma unroll
        for(int c=0;c<3;c++){
          #pragma unroll
          for(int i=0;i<4;i++){ const int x=lane+64*i; ldsw128(smb+c*CVS+UOF+(x&3)*1040+((x>>2)+1)*16,sw[c][i]); }
          if(lane<4) ldsw128(smb+c*CVS+UOF+lane*1040,u4{0,0,0,0}); }
        signal(smb+C_FULL,lane);
        RST(4);
        #pragma unroll 1
        for(int c=0;c<3;c++){
          #pragma unroll
          for(int i=0;i<4;i++){ sw[0][i]=sw[1][i]; sw[1][i]=sw[2][i]; }
          conv_win(smb+c*CVS,lane,sw[2]); }
        __builtin_amdgcn_s_setprio(HPRIO);
        #pragma unroll 1
        for(int c=0;c<(idx<24?3:2);c++){ ldswait(smb+C_KP+c*4,1u); conv_help(smb+c*CVS,lane,sw[0]); signal(smb+C_KP+c*4,lane);
          if(c==ZC){ const int r=lane&31,h=lane>>5; ldswait(smb+C_Z1,9u);
            #pragma unroll
            for(int m=0;m<4;m++)
              #pragma unroll
              for(int n=0;n<2;n++) if(2*j+n<27){ const u32 oo=OFF_Z1T+((rt*8+2*m+(r>>4))*27+2*j+n)*1024+(h*16+(r&15))*16;
                st16(rs,oo,u4{zq[m][2*n][0],zq[m][2*n][1],zq[m][2*n+1][0],zq[m][2*n+1][1]}); st16(rs,oo+512,u4{zq[m][2*n][2],zq[m][2*n][3],zq[m][2*n+1][2],zq[m][2*n+1][3]}); } }
          if(c==1){ WAITV(0); if(lane==0) st4(rs,OFF_F1+(rt*16+j)*128,tag); RST(5); }
          #pragma unroll
          for(int i=0;i<4;i++){ sw[0][i]=sw[1][i]; sw[1][i]=sw[2][i]; } } }
      __builtin_amdgcn_s_setprio(0);
      RST(6);
    }
    RST(10);
    ldswait(smb+C_Z1R,4u);
    u4 wq[24];
    #pragma unroll
    for(int i=0;i<24;i++) wq[i]=ld16(rs,OFF_WOF+ct*(135*1024)+40960+mi*RING+((i==23&&mi==3)?22:i)*1024+lane*16);
    if(w<6){
      #pragma unroll 1
      for(u32 i=0;i<20480;i+=4096){ setm0<0>(cb+i); const u32 o=OFF_WOF+ct*(135*1024)+mi*20480+i; dma<0>(srdS,lane*16,o); dma<1024>(srdS,lane*16,o); dma<2048>(srdS,lane*16,o); dma<3072>(srdS,lane*16,o); } }
    if(w==6){ adone=__all(ld4(rs,fao)==tag); if(adone) aload(); }
    ldswait(smb+C_FREE,4u);
    #pragma unroll
    for(int i=0;i<24;i++) ldsw128(smb+RB+mi*RING+i*1024+lane*16,wq[i]);
    if(w<6) WAITV(0);
    signal(smb+C_WR,lane);
    if(!adone){ adone=__all(ld4(rs,fao)==tag); if(adone) aload(); }
    if(w==7 && b==255){
      #pragma unroll 1
      for(int sp=0;sp<SPIN;sp++){ const u32 o=OFF_FA+lane*128; u32 v0=ld4(rs,o),v1=ld4(rs,o+8192),v2=ld4(rs,o+16384),v3=ld4(rs,lane<32? o+24576 : OFF_FW+(lane-32)*128); asm volatile("" ::: "memory");
        if(__all(v0==tag&&v1==tag&&v2==tag&&v3==tag)) break; __builtin_amdgcn_s_sleep(4); }
      if(lane==0) st4(rs,OFF_EP,tag); }
  }
  int w2=w; asm volatile("" : "+s"(w2));
  if(w2<4){
    __builtin_amdgcn_s_setprio(G2PRIO);
    int r=lane&15, q=lane>>4;
    const u32 rb=smb+RB+w*RING;
    u4 srdA=mksrd((const char*)p.wsp+(size_t)rtB*64*4096+w*1024-4096+256);
    const char* zb=p.ws+OFF_Z2F+(u32)(16*w*54+ft0+3)*1024;
    u32 voA[2]; { u32 lr=lane>>3,c=lane&7; voA[0]=lr*4096+((c^(lr>>1))<<4); voA[1]=voA[0]^64u; }
    u32 sofa[8];
    #pragma unroll
    for(int g=0;g<8;g++) sofa[g]=((g+2*w)&7)*32768+4096-(g&3)*1024;
    const u32 voB=lane*16;
    u32 la[2]; la[0]=rb+r*128+((q^(r>>1))<<4); la[1]=la[0]^64u;
    f4 acc[4][7]={}; u4 B[NB][7]; u4 A[2][4]; u2 z1[7];
    auto issueA=[&](auto sc,auto gc){ constexpr int s=sc.value,g=gc.value; if constexpr((g&3)==0) setm0<((s+1)%3)*8192+g*1024>(rb); dma<(g&3)*1024>(srdA,voA[g&1],sofa[g]);
      if constexpr(g==7){ u64 a=((u64)srdA[1]<<32|srdA[0])+128; srdA[0]=(u32)a; srdA[1]=(u32)(a>>32); } };

    u32 bsr,fv;
    auto fof=[&](){ const u32 l=voB>>4; return l<27? OFF_FW+l*128 : OFF_F1+((rtB>>1)*16+(((ft0>>1)+(l&3))>>1))*128; };
    #pragma unroll
    for(int m=0;m<4;m++){ A[0][m]=ldsr128(la[0]+m*2048+8192); A[1][m]=ldsr128(la[1]+m*2048+8192); }
    sfor<8>([&](auto g){ issueA(std::integral_constant<int,2>{},g); });
    waitA(A[0]); waitA(A[1]);
    sfor<8>([&](auto g){ issueA(std::integral_constant<int,3>{},g); });
    #pragma unroll
    for(int m=0;m<4;m++)
      #pragma unroll
      for(int n=0;n<7;n++) asm volatile("" : "+v"(acc[m][n]));
    RST(2);
    { const int hf=(ft0>>1)+q; pollw(rs,OFF_F2+((4*w+(lane&3))*16+(hf==0?13:(hf-1)>>1))*128,tag); }
    RST(10);
    sfor<NB>([&](auto hh){ const char* s=zb+(u32)hh.value*(54*1024); sfor<7>([&](auto n){ gload<(n.value-3)*1024>(B[hh][n],voB,s); }); });
    signal(smb+C_Z1,lane);
    auto redw=[&](auto nc){ constexpr int n=nc.value; *(LAS f4*)(rb+n*1024+voB)=acc[1][n]; *(LAS f4*)(rb+(7+n)*1024+voB)=acc[2][n]; *(LAS f4*)(rb+(14+n)*1024+voB)=acc[3][n]; };
    const char* z1b=p.ws+OFF_Z1T+(u32)((rtB*4+w)*27+(ft0>>1))*1024;
    sfor<16>([&](auto hc){
      constexpr int h=hc.value, S=h%NB, P=h&1, HZ=16-NB, cnt=g2cnt(h);
      waitB<cnt>(B[S]); waitA(A[P]);
      if constexpr(h==HZ){ tie1(fv); if(!__all(fv==tag)) pollw(rs,fof(),tag); }
      if constexpr(h+1<16 && h>0){
        #pragma unroll
        for(int m=0;m<4;m++) A[P^1][m]=ldsr128(la[(h+1)&1]+m*2048+((((h+1)>>1)+1)%3)*8192);
      }
      if constexpr(h==HZ-3) gload4<0>(fv,fof(),p.ws);
      const char* s=zb+(u32)(h+NB)*(54*1024);
      sfor<7>([&](auto n){
        mfma4(acc[0][n],acc[1][n],acc[2][n],acc[3][n],B[S][n],A[P]);
        if constexpr(h>=3 && h<=10 && (n.value&1)==0) issueA(std::integral_constant<int,(h-1)/2+3>{},std::integral_constant<int,((h-1)&1)*4+n.value/2>{});
        if constexpr(h+NB<16) gload_t<(n.value-3)*1024>(B[S][n],voB,s);
        if constexpr(h==HZ){ gload8<(n.value>>1)*1024>(z1[n],voB,z1b+((n.value&1)? ((ft0&1)?1024:8) : (ft0&1)*8));
          if constexpr(n.value==6) { gload2(bsr,(voB>>3)&30u,(const char*)(p.bsp+(rtB*4+w)*16)); signal(smb+C_Z1R,lane); } }
        if constexpr(h==15){ SCHED(); if constexpr(n.value>=2) redw(std::integral_constant<int,n.value-2>{}); SCHED(); }
      });
    });
    asm volatile("s_nop 15\n\ts_nop 3" ::: "memory");
    SCHED();
    STAMP(10,0);
    lane=__builtin_amdgcn_mbcnt_hi(~0u,__builtin_amdgcn_mbcnt_lo(~0u,0u)); asm volatile("" : "+v"(lane)); r=lane&15; q=lane>>4;
    redw(std::integral_constant<int,5>{}); redw(std::integral_constant<int,6>{});
    signal(smb+C_RED,lane);
    ldswait(smb+C_RED,4u);
    #pragma unroll
    for(int m=1;m<4;m++){ u32 src=smb+RB+((w-m)&3)*RING+(m-1)*7168+lane*16;
      #pragma unroll
      for(int n=0;n<7;n++) acc[0][n]+=*(LAS f4*)(src+n*1024); }
    asm volatile("s_waitcnt vmcnt(0) lgkmcnt(0)" : "+v"(z1[0]),"+v"(z1[1]),"+v"(z1[2]),"+v"(z1[3]),"+v"(z1[4]),"+v"(z1[5]),"+v"(z1[6]),"+v"(bsr) :: "memory");
    const float bs=bflo(bsr);
    signal(smb+C_FREE,lane);
    u2 o[7];
    #pragma unroll
    for(int n=0;n<7;n++)
      #pragma unroll
      for(int e=0;e<2;e++)o[n][e]=pkbf2(unpk(z1[n][e])*(f2{acc[0][n][2*e],acc[0][n][2*e+1]}+bs));
    const u32 ho=OFF_HF+(u32)((rtB*4+w)*54+ft0)*512+(q>>1)*256+r*16;
    #pragma unroll
    for(int n=0;n<6;n+=2){ auto sx=__builtin_amdgcn_permlane16_swap(o[n][0],o[n+1][0],false,false); auto sy=__builtin_amdgcn_permlane16_swap(o[n][1],o[n+1][1],false,false);
      if(ct<7||n>=2) st16(rs,ho+(n+(q&1))*512,u4{sx[0],sy[0],sx[1],sy[1]}); }
    st8(rs,ho+6*512+(q&1)*8,o[6]);
    STAMP(11,0);
    WAITV(0);
    if(arrive(smb+C_H,lane)==3u && lane==0) st4(rs,OFF_FH+(rtB*8+ct)*128,tag);
    STAMP(12,0);
    __builtin_amdgcn_s_setprio(0);
  }
  { const int r=lane&15, q=lane>>4, m=w&3, kh=w>>2;
    u2 xv[5]={};
    if(kh){
      #pragma unroll
      for(int n=0;n<5;n++) xv[n]=*(const u2*)(p.x+(rtB*64+m*16+r)*640+80*ct+16*n+4*q);
    }
    f4 acc[5]={}; u4 W[3][5];
    const u32 wl2=smb+lane*16+RB-40960+kh*14*5120, wl0=kh? wl2 : smb+lane*16, wl1=kh? wl2 : smb+lane*16+CVS-20480;
    #define WAD(k,n) (((k)<4? wl0 : (k)<8? wl1 : wl2)+((k)*5+(n))*1024)
    f4 ax[5]={};
    auto addax=[&](){
      #pragma unroll
      for(int n=0;n<5;n++){ u2 av=ldstr(smb+ATL+(16*n+4*q+(r>>2))*128+(16*m+4*(r&3))*2); ax[n]=f4{bflo(av[0]),bfhi(av[0]),bflo(av[1]),bfhi(av[1])}+f4{bflo(xv[n][0]),bfhi(xv[n][0]),bflo(xv[n][1]),bfhi(xv[n][1])}; } };
    if(w==6 && adone){ WAITV(0); signal(smb+C_AT,lane); }
    u32 oo[3],to[3]; const u32 tb=smb+CVS+m*3072;
    #pragma unroll
    for(int i=0;i<3;i++){ const int it=lane+64*i, rr=(it*205)>>11, cc=it-rr*10; oo[i]=opq((rtB*64+m*16+rr)*1280+ct*160+cc*16); to[i]=opq(tb+rr*176+cc*16); }
    if(m==0){ ldswait(smb+C_H,4u); pollw(rs,OFF_FH+(rtB*8+kh*4+(lane&3))*128,tag); signal(smb+C_TAG+kh*4,lane); } else ldswait(smb+C_TAG+kh*4,1u);
    STAMP(16,0);
    u4 hf[14];
    const u32 ho=OFF_HF+(u32)((rtB*4+m)*27+kh*14)*1024+lane*16;
    #pragma unroll
    for(int k=0;k<HPF;k++) hf[k]=ld16(rs,ho+k*1024);
    const bool early=kh && uni(ldsv(smb+C_AT))!=0u;
    if(early) addax();
    ldswait(smb+C_WR,4u);
    STAMP(17,0);
    const u32 wl3=kh? wl2-5120 : wl2;
    #pragma unroll
    for(int n=0;n<10;n++) W[n/5][n%5]=ldsr128(WAD(n/5,n%5));
    #pragma unroll
    for(int k=0;k<13;k++){
      if(k+HPF<14) hf[k+HPF]=ld16(rs,ho+((k+HPF==13&&kh)?12:k+HPF)*1024);
      SCHED();
      #pragma unroll
      for(int n=0;n<5;n++){ acc[n]=mfma16(W[k%3][n],hf[k],acc[n]); SCHED();
        if(k<11) W[(k+2)%3][n]=ldsr128(WAD(k+2,n)); else if(k==11) W[1][n]=ldsr128(wl3+(65+n)*1024);
        SCHED(); }
      if(k==5 && !kh) signal(smb+C_G1,lane);
    }
    const u32 pb=smb+m*5120+lane*16;
    if(!kh){
      #pragma unroll
      for(int n=0;n<5;n++) acc[n]=mfma16(W[1][n],hf[13],acc[n]);
      STAMP(18,0);
      ldswait(smb+C_G1,4u);
      #pragma unroll
      for(int n=0;n<5;n++) *(LAS f4*)(pb+n*1024)=acc[n];
      signal(smb+C_P+m*4,lane);
    } else {
      if(w==6 && !adone){ pollw(rs,fao,tag); aload(); WAITV(0); signal(smb+C_AT,lane); }
      if(!early){ ldswait(smb+C_AT,1u); addax(); }
      RST(7);
      ldswait(smb+C_P+m*4,1u);
      #pragma unroll
      for(int n=0;n<5;n++){ const f4 o=(acc[n]+ax[n])+*(LAS f4*)(pb+n*1024); *(LAS u2*)(tb+r*176+n*32+q*8)=u2{pkbf(o[0],o[1]),pkbf(o[2],o[3])}; }
      #pragma unroll
      for(int i=0;i<3;i++) if(i<2||lane<32) *(u4*)((char*)p.out+oo[i])=ldsr128(to[i]);
      STAMP(19,4);
    }
  }
}
extern "C" void kernel_launch(void* const* d_in,const int* in_sizes,int n_in,void* d_out,int out_size,void* d_ws,size_t ws_size,hipStream_t stream){
  Params p;
  p.x=(const u16*)d_in[0]; p.lar=(const u16*)d_in[1]; p.aim=(const u16*)d_in[2]; p.cre=(const u16*)d_in[3]; p.cim=(const u16*)d_in[4];
  p.ldt=(const u16*)d_in[5]; p.dsk=(const u16*)d_in[6]; p.win=(const u16*)d_in[8]; p.wsp=(const u16*)d_in[9]; p.bsp=(const u16*)d_in[10]; p.wout=(const u16*)d_in[11];
  p.out=(u16*)d_out; p.ws=(char*)d_ws;
  (void)hipFuncSetAttribute((const void*)fused,hipFuncAttributeMaxDynamicSharedMemorySize,LDS_TOTAL);
  fused<<<256,512,LDS_TOTAL,stream>>>(p);
}
```

```cpp
#include <hip/hip_runtime.h>
#include <utility>
#pragma clang diagnostic ignored "-Wint-to-pointer-cast"
typedef unsigned short u16;
typedef unsigned int u32;
typedef unsigned long long u64;
typedef __attribute__((ext_vector_type(8))) __bf16 bf8;
typedef __attribute__((ext_vector_type(8))) _Float16 hf8;
typedef __attribute__((ext_vector_type(2))) __bf16 bf2;
typedef __attribute__((ext_vector_type(2))) _Float16 hf2;
typedef __attribute__((ext_vector_type(2))) float f2;
typedef __attribute__((ext_vector_type(4))) float f4;
typedef __attribute__((ext_vector_type(16))) float f16v;
typedef __attribute__((ext_vector_type(4))) u32 u4;
typedef __attribute__((ext_vector_type(2))) u32 u2;
typedef __attribute__((ext_vector_type(4))) short s4;
#define LAS __attribute__((address_space(3)))
#define GAS __attribute__((address_space(1)))
#define DI __device__ __forceinline__

constexpr u32 OFF_Z2F = 0;
constexpr u32 OFF_Z1T = OFF_Z2F + 64*54*1024;
constexpr u32 OFF_XNT = OFF_Z1T + 128*27*1024;
constexpr u32 OFF_WOF = OFF_XNT + 640*4096;
constexpr u32 OFF_HF  = OFF_WOF + 8*27*5*1024;
constexpr u32 OFF_AT  = OFF_HF + 128*27*1024;
constexpr u32 OFF_ST  = OFF_AT + 640*4096;
constexpr u32 OFF_RS  = OFF_ST + 16*14*2048+8192;
constexpr u32 OFF_EP  = OFF_RS + 2048*16;
constexpr u32 OFF_F2  = OFF_EP + 256;
constexpr u32 OFF_F1  = OFF_F2 + 256*128;
constexpr u32 OFF_FX  = OFF_F1 + 256*128;
constexpr u32 OFF_FW  = OFF_FX + 256*128;
constexpr u32 OFF_FH  = OFF_FW + 32*128;
constexpr u32 OFF_FA  = OFF_FH + 256*128;
constexpr u32 WS_END  = OFF_FA + 256*128;

struct Params {
  const u16 *x, *lar, *aim, *cre, *cim, *ldt, *dsk, *win, *wsp, *bsp, *wout;
  u16* out; char* ws;
};
#define STAMP(id,wv) do{}while(0)
#define RST(i) do{}while(0)
template<class F,int... I> DI void sfor_impl(F&& f, std::integer_sequence<int,I...>){ (f(std::integral_constant<int,I>{}),...); }
template<int N,class F> DI void sfor(F&& f){ sfor_impl(f, std::make_integer_sequence<int,N>{}); }

DI float bflo(u32 v){ return __uint_as_float(v<<16); }
DI float bfhi(u32 v){ return __uint_as_float(v&0xffff0000u); }
DI float bf1(u16 v){ return __uint_as_float(((u32)v)<<16); }
DI u32 pkbf(float a,float b){ f2 v={a,b}; return __builtin_bit_cast(u32,__builtin_convertvector(v,bf2)); }
DI u32 pkhf(float a,float b){ f2 v={a,b}; return __builtin_bit_cast(u32,__builtin_convertvector(v,hf2)); }
DI float rbf(float a){ return bflo(pkbf(a,a)); }
DI float hlo(u32 v){ return (float)__builtin_bit_cast(hf2,v)[0]; }
DI float hhi(u32 v){ return (float)__builtin_bit_cast(hf2,v)[1]; }
DI float dot2(u32 a,u32 b,float c){ return __builtin_amdgcn_fdot2_f32_bf16(__builtin_bit_cast(bf2,a),__builtin_bit_cast(bf2,b),c,false); }
DI f4 mfma16(u4 a,u4 b,f4 c){ return __builtin_amdgcn_mfma_f32_16x16x32_bf16(__builtin_bit_cast(bf8,a),__builtin_bit_cast(bf8,b),c,0,0,0); }
DI f16v mfma32h(u4 a,u4 b,f16v c){ return __builtin_amdgcn_mfma_f32_32x32x16_f16(__builtin_bit_cast(hf8,a),__builtin_bit_cast(hf8,b),c,0,0,0); }
DI u4 ldsr128(u32 a){ return *(LAS u4*)a; }
DI u2 ldsr64(u32 a){ return *(LAS u2*)a; }
DI void ldsw128(u32 a,u4 v){ *(LAS u4*)a=v; }
DI u2 ldstr(u32 a){ return __builtin_bit_cast(u2,__builtin_amdgcn_ds_read_tr16_b64_v4i16((LAS s4*)a)); }
DI u32 uni(u32 v){ return __builtin_amdgcn_readfirstlane(v); }

template<int OFF> DI void gload(u4& dst,u32 voff,const char* base){ asm volatile("global_load_dwordx4 %0, %1, %2 offset:%3 sc1" : "=v"(dst) : "v"(voff), "s"(base), "n"(OFF)); }
template<int OFF> DI void gload_t(u4& dst,u32 voff,const char* base){ asm volatile("global_load_dwordx4 %0, %1, %2 offset:%3 sc1" : "+v"(dst) : "v"(voff), "s"(base), "n"(OFF)); }
DI void mfma4(f4& c0,f4& c1,f4& c2,f4& c3,const u4& a,const u4 (&b)[4]){
  asm volatile("v_mfma_f32_16x16x32_bf16 %0, %4, %5, %0\n\tv_mfma_f32_16x16x32_bf16 %1, %4, %6, %1\n\tv_mfma_f32_16x16x32_bf16 %2, %4, %7, %2\n\tv_mfma_f32_16x16x32_bf16 %3, %4, %8, %3"
    : "+v"(c0),"+v"(c1),"+v"(c2),"+v"(c3) : "v"(a),"v"(b[0]),"v"(b[1]),"v"(b[2]),"v"(b[3])); }
template<int CNT> DI void waitB(u4 (&B)[7]){ asm volatile("s_waitcnt vmcnt(%7)" : "+v"(B[0]),"+v"(B[1]),"+v"(B[2]),"+v"(B[3]),"+v"(B[4]),"+v"(B[5]),"+v"(B[6]) : "n"(CNT) : "memory"); }
DI void waitA(u4 (&A)[4]){ asm volatile("s_waitcnt lgkmcnt(0)" : "+v"(A[0]),"+v"(A[1]),"+v"(A[2]),"+v"(A[3]) :: "memory"); }
#define WAITV(n) asm volatile("s_waitcnt vmcnt(%0)" :: "n"(n) : "memory")
#define WAITL0() asm volatile("s_waitcnt lgkmcnt(0)" ::: "memory")
#define SCHED() __builtin_amdgcn_sched_barrier(0)
DI void bar(){ asm volatile("s_waitcnt lgkmcnt(0)\n\ts_barrier" ::: "memory"); }
DI f2 unpk(u32 v){ return f2{bflo(v),bfhi(v)}; }
DI u32 pkbf2(f2 v){ return __builtin_bit_cast(u32,__builtin_convertvector(v,bf2)); }
DI u32 pkhf2(f2 v){ return __builtin_bit_cast(u32,__builtin_convertvector(v,hf2)); }
DI f2 fma2(f2 a,f2 b,f2 c){ return __builtin_elementwise_fma(a,b,c); }
DI f2 gelu2(f2 x){
  f2 u=x*fma2(x*x,f2{-0.10294324f,-0.10294324f},f2{-2.30220820f,-2.30220820f});
  f2 d=f2{__builtin_amdgcn_exp2f(u[0]),__builtin_amdgcn_exp2f(u[1])}+f2{1.f,1.f};
  return x*f2{__builtin_amdgcn_rcpf(d[0]),__builtin_amdgcn_rcpf(d[1])};
}
DI void ln_fin(float mean,float var,float& m,float& rinv){
  m=rbf(mean); float v=rbf(__builtin_fmaxf(var,0.f));
  float s=rbf(__builtin_amdgcn_sqrtf(rbf(v+1e-5f))); float r=__builtin_amdgcn_rcpf(s); rinv=__builtin_fmaf(__builtin_fmaf(-s,r,1.f),r,r);
}
typedef __amdgpu_buffer_rsrc_t rsrc_t;
DI void st16(rsrc_t rs,u32 off,u4 v){ __builtin_amdgcn_raw_buffer_store_b128(v,rs,off,0,16); }
DI void st8(rsrc_t rs,u32 off,u2 v){ __builtin_amdgcn_raw_buffer_store_b64(v,rs,off,0,16); }
DI void st4(rsrc_t rs,u32 off,u32 v){ __builtin_amdgcn_raw_buffer_store_b32(v,rs,off,0,16); }
DI u4 ld16(rsrc_t rs,u32 off){ return __builtin_amdgcn_raw_buffer_load_b128(rs,off,0,16); }
DI u2 ld8(rsrc_t rs,u32 off){ return __builtin_amdgcn_raw_buffer_load_b64(rs,off,0,16); }
DI u32 ld4(rsrc_t rs,u32 off){ return __builtin_amdgcn_raw_buffer_load_b32(rs,off,0,16); }
constexpr int SPIN=1<<17;
constexpr u32 TAGINC=0x9E3779B9u;
template<int SL=2> DI void pollw(rsrc_t rs,u32 off,u32 tag){
  #pragma unroll 1
  for(int s=0;s<SPIN;s++){ u32 v=ld4(rs,off); asm volatile("" ::: "memory"); if(__all(v==tag)) break; __builtin_amdgcn_s_sleep(SL); }
}

DI f16v mfma32b(u4 a,u4 b,f16v c){ return __builtin_amdgcn_mfma_f32_32x32x16_bf16(__builtin_bit_cast(bf8,a),__builtin_bit_cast(bf8,b),c,0,0,0); }
DI u4 mksrd(const void* b){ u64 a=(u64)b; return u4{(u32)a,(u32)(a>>32)&0xffffu,0xffffffffu,0x00020000u}; }
template<int IMM> DI void setm0(u32 base){ asm volatile("s_add_u32 m0, %0, %1\n\ts_nop 0" :: "s"(base), "n"(IMM) : "scc","memory"); }
template<int OFF> DI void dma(u4 srd,u32 voff,u32 soff){ asm volatile("buffer_load_dwordx4 %0, %1, %2 offen offset:%3 sc1 lds" :: "v"(voff), "s"(srd), "s"(soff), "n"(OFF) : "memory"); }
DI void touch(u4 srd,u32 voff,u32 soff){ asm volatile("buffer_load_dword %0, %1, %2 offen sc1 lds" :: "v"(voff), "s"(srd), "s"(soff) : "memory"); }
template<int OFF> DI void gload8(u2& dst,u32 voff,const char* base){ asm volatile("global_load_dwordx2 %0, %1, %2 offset:%3 sc1" : "=v"(dst) : "v"(voff), "s"(base), "n"(OFF)); }
DI void gload2(u32& dst,u32 voff,const char* base){ asm volatile("global_load_ushort %0, %1, %2" : "=v"(dst) : "v"(voff), "s"(base)); }
DI u32 arrive(u32 a,int){ u32 o; asm volatile("s_mov_b64 exec, 1\n\tds_add_rtn_u32 %0, %1, %2\n\ts_mov_b64 exec, -1\n\ts_waitcnt lgkmcnt(0)" : "=&v"(o) : "v"(a), "v"(1u) : "memory"); return uni(o); }
DI void ldswait(u32 a,u32 n){ while(__hip_atomic_load((LAS u32*)a,__ATOMIC_RELAXED,__HIP_MEMORY_SCOPE_WORKGROUP)<n) __builtin_amdgcn_s_sleep(1); asm volatile("" ::: "memory"); }
DI float addf(float a,float b){ float o; asm("v_add_f32 %0, %1, %2" : "=v"(o) : "v"(a), "v"(b)); return o; }
DI void tie1(u32& g){ asm volatile("" : "+v"(g) :: "memory"); }
DI u32 opq(u32 v){ asm volatile("" : "+v"(v)); return v; }

constexpr u32 STG=32768;
constexpr u32 RING=24576, RB=62848, CONV=0, KP=2208, UOF=16768, CVS=UOF+4160, S7O=4*KP, S9O=S7O+2112, KZO=S9O+2112, MISC=RB+4*RING, JNK=MISC+2048, PM=163840-128, LDS_TOTAL=163840;
constexpr u32 ATL=2*CVS;
constexpr u32 C_FULL=PM, C_G1=PM+4, C_Z2=PM+8, C_Z1=PM+12, C_RED=PM+16, C_H=PM+20, C_FREE=PM+24, C_CV=PM+28, C_Z1R=PM+32, C_TAG=PM+40, C_KP=PM+48, C_DONE=PM+64, C_ZQ=PM+68, C_CS=PM+72, C_K2=PM+80, C_P=PM+96, C_AT=PM+112, C_WR=PM+116;
constexpr u32 CODE_BYTES=39936;
#define LA 3
#define TC_S 7
#define G2PRIO 2
#define XMAX 9
#define GS_K 35
#define WNT ""
#define TS1 6
#define HPRIO 3
#define CPRIO 3
#define POLLM 1
#define HPF 5
#define CVSKIP 0
#define BROT 0
#define STAG 0u
#define FXDLY 24
#define PDLY 8
#define RS_S 7
#define KROT ((((j+rt)&3)*5)>>1)
DI void signal(u32 a,int){ asm volatile("s_mov_b64 exec, 1\n\tds_add_u32 %0, %1\n\ts_mov_b64 exec, -1" :: "v"(a), "v"(1u) : "memory"); }
template<int OFF> DI void gload4(u32& dst,u32 voff,const char* base){ asm volatile("global_load_dword %0, %1, %2 offset:%3 sc1" : "=v"(dst) : "v"(voff), "s"(base), "n"(OFF)); }
template<int CNT> DI void waitG(u4& g){ asm volatile("s_waitcnt vmcnt(%1)" : "+v"(g) : "n"(CNT) : "memory"); }

DI f2 cmulp(f2 a,f2 b,f2 bs){ return fma2(f2{a[1],a[1]},bs,f2{a[0],a[0]}*b); }
DI float mulf(float a,float b){ float o; asm("v_mul_f32 %0, %1, %2" : "=v"(o) : "v"(a),"v"(b)); return o; }
DI float fmaf1(float a,float b,float c){ float o; asm("v_fma_f32 %0, %1, %2, %3" : "=v"(o) : "v"(a),"v"(b),"v"(c)); return o; }
DI u32 ldsv(u32 a){ return __hip_atomic_load((LAS u32*)a,__ATOMIC_RELAXED,__HIP_MEMORY_SCOPE_WORKGROUP); }
DI void ldswait4(u32 a,int lane,u32 n){ while(__any(ldsv(a+(lane&3)*4)<n)) __builtin_amdgcn_s_sleep(1); asm volatile("" ::: "memory"); }
DI u32 gettag(u32 a){ ldswait(a+4,1u); return uni(ldsv(a)); }
DI f2 cpow_dir(float lr,float rv,float pw){ float e=__builtin_amdgcn_exp2f(lr*pw); float f=__builtin_amdgcn_fractf(rv*pw);
  return f2{e*__builtin_amdgcn_cosf(f),e*__builtin_amdgcn_sinf(f)}; }
DI u32 dup(u32 a){ return __builtin_amdgcn_perm(a,a,0x01000100u); }
DI u32 cat(u32 lo,u32 hi){ return __builtin_amdgcn_perm(hi,lo,0x05040100u); }
DI u32 lds16(u32 a){ return *(LAS u16*)a; }

template<class CB> DI float kgen_tab(u32 cb,int lane,const u32 (&pr)[6],float (&kp)[11],CB&& mid){
  const float dt=rbf(__expf(bflo(pr[4])));
  const float cr[2]={bflo(pr[2]),bfhi(pr[2])}, cm[2]={bflo(pr[3]),bfhi(pr[3])};
  float mx=fmaxf(fmaxf(fabsf(cr[0]),fabsf(cm[0])),fmaxf(fabsf(cr[1]),fabsf(cm[1])));
  u32 eb=__float_as_uint(mx)&0x7f800000u;
  #define DPPMAX(c) eb=max(eb,(u32)__builtin_amdgcn_update_dpp(0,(int)eb,c,0xf,0xf,false))
  DPPMAX(0x128); DPPMAX(0x124); DPPMAX(0x122); DPPMAX(0x121);
  eb=max(max((u32)__builtin_amdgcn_readlane(eb,0),(u32)__builtin_amdgcn_readlane(eb,16)),max((u32)__builtin_amdgcn_readlane(eb,32),(u32)__builtin_amdgcn_readlane(eb,48))); eb=eb<0x01000000u?0x3f800000u:(eb>0x7e000000u?0x7e000000u:eb);
  const float ksc=2.f*dt*__uint_as_float(eb), isc=__uint_as_float(0x7f000000u-eb);
  const int r=lane&31,h=lane>>5;
  const u32 TP=272, TA=cb, TB=cb+22*TP;
  f16v acc={};
  f2 an[2][4];
  #pragma unroll
  for(int c=0;c<2;c++){ const float l_a=c?bfhi(pr[0]):bflo(pr[0]), i_m=c?bfhi(pr[1]):bflo(pr[1]);
    const float lr=(-rbf(__expf(l_a))*dt)*1.44269504f, rv=(i_m*dt)*0.15915494f;
    an[c][0]=cpow_dir(lr,rv,1.f); an[c][1]=cpow_dir(lr,-rv,8.f); an[c][2]=cpow_dir(lr,-rv,16.f); an[c][3]=cpow_dir(lr,rv,256.f); }
  #pragma unroll
  for(int c=0;c<2;c++){
    const float c_r=cr[c], c_i=cm[c];
    const f2 z1=an[c][0], z8=an[c][1], zs=an[c][2], zk=an[c][3];
    const f2 z1s={-z1[1],z1[0]}, zss={-zs[1],zs[0]}, zks={-zk[1],zk[0]};
    const u32 wa=TA+lane*4, wb=TB+lane*4;
    #define PUT(base,row,v) do{ *(LAS u32*)((base)+(row)*TP)=pkhf2(v); }while(0)
    f2 e={1.f,0.f}, o=z8;
    #pragma unroll
    for(int k=0;k<16;k++){ PUT(wb,2*k,e); PUT(wb,2*k+1,o); if(k<15){ e=k?cmulp(e,zs,zss):zs; o=cmulp(o,zs,zss);} }
    f2 b0={c_r*isc,c_i*isc}; PUT(wa,0,b0);
    f2 t=cmulp(b0,zk,zks); PUT(wa,8,t);
    f2 b1=cmulp(b0,z1,z1s); PUT(wa,1,b1); t=cmulp(t,zk,zks); PUT(wa,10,t);
    f2 b2=cmulp(b1,z1,z1s); PUT(wa,2,b2); t=cmulp(t,zk,zks); PUT(wa,11,t);
    f2 b3=cmulp(b2,z1,z1s); PUT(wa,3,b3); t=cmulp(t,zk,zks); PUT(wa,16,t);
    f2 b4=cmulp(b3,z1,z1s); PUT(wa,4,b4); t=cmulp(t,zk,zks); PUT(wa,17,t);
    f2 b5=cmulp(b4,z1,z1s); PUT(wa,5,b5); t=cmulp(t,zk,zks); PUT(wa,20,t);
    f2 b6=cmulp(b5,z1,z1s); PUT(wa,6,b6); t=cmulp(t,zk,zks); PUT(wa,21,t);
    f2 b7=cmulp(b6,z1,z1s); PUT(wa,7,b7); t=cmulp(t,zk,zks); PUT(wa,18,t);
    f2 s=cmulp(b4,zk,zks); PUT(wa,12,s); t=cmulp(b2,zk,zks); PUT(wa,9,t);
    s=cmulp(s,zk,zks); PUT(wa,14,s); t=cmulp(b6,zk,zks); PUT(wa,13,t);
    s=cmulp(s,zk,zks); PUT(wa,15,s);
    #undef PUT
    if(c==0) mid();
    const int ra=(r>21||r==19)?0:r;
    #pragma unroll
    for(int ks=0;ks<8;ks++){ u4 fa=ldsr128(TA+ra*TP+ks*32+h*16), fb=ldsr128(TB+r*TP+ks*32+h*16); acc=mfma32h(fa,fb,acc); }
  }
  #pragma unroll
  for(int i=0;i<11;i++) kp[i]=acc[i];
  return ksc;
}
DI void kgen_exp(u32 cb,int lane,const float (&acc)[11],const u32 (&ga)[11]){
  const int r=lane&31,h=lane>>5;
  { const u32 a4=cb+r*16+h*8, a5=cb+r*16+h*1024, az=cb+KZO+r*2+h*128;
    #define PK(addr,base,reg) do{ *(LAS _Float16*)((addr)+2*(base))=(_Float16)acc[reg]; }while(0)
    PK(a4,0,0);PK(a4,1,1);PK(a4,2,2);PK(a4,3,3); PK(a4,256,4);PK(a4,258,5);PK(a4,512,6);PK(a4,768,7);
    PK(a5,1024,8); PK(az,255,10); PK(az,127,8); PK(az,159,9);
    #undef PK
    if(lane<16) ldsw128(cb+KZO+lane*16,u4{0,0,0,0});
  }
  { const u32 g0=cb+2*(1024-8*lane);
    u32 f2_=lds16(g0+(lane?8:0)), a2=lds16(g0), m2=lds16(g0-8), b2=lds16(g0-16);
    u32 e[11];
    #pragma unroll
    for(int j=0;j<11;j++) e[j]=lds16(ga[j]);
    u32 k01=*(LAS u32*)cb, k23=*(LAS u32*)(cb+4), kl=lds16(cb+2048);
    WAITL0();
    const u32 wo=cb+96+lane*16;
    { u32 aa=dup(a2),mm=dup(m2),bb=dup(b2),am=cat(a2,m2),fa=cat(f2_,a2),mb=cat(m2,b2);
      ldsw128(wo,u4{aa,am,mm,bb}); ldsw128(wo+KP,u4{aa,aa,mm,mb}); ldsw128(wo+2*KP,u4{aa,aa,am,mm}); ldsw128(wo+3*KP,u4{fa,aa,aa,mm}); }
    { u32 o0=cat(e[0],e[1]),o1=cat(e[2],e[3]),o2=cat(e[4],e[5]),o3=cat(e[6],e[7]),o4=cat(e[8],e[9]);
      u32 p1=cat(e[1],e[2]),d0=cat(e[3],e[4]),d1=cat(e[5],e[6]),d2=cat(e[7],e[8]),d3=cat(e[9],e[10]);
      ldsw128(wo+1024,u4{d0,d1,d2,d3}); ldsw128(wo+1024+KP,u4{o1,o2,o3,o4}); ldsw128(wo+1024+2*KP,u4{p1,d0,d1,d2}); ldsw128(wo+1024+3*KP,u4{o0,o1,o2,o3}); }
    if(lane<10){ if(lane){k01=0;k23=0;} if(lane!=9) kl=0;
      const u32 d0=k01&0xffffu, o1=__builtin_amdgcn_alignbit(k01,k01,16), o0=__builtin_amdgcn_alignbit(k23,k23,16), p1=(k23&0xffffu)|(k01&0xffff0000u), kk=dup(kl), zk=kl<<16;
      const u32 w2=lane<4? wo+2048 : wo-160;
      ldsw128(w2,u4{d0,0,kk,kk}); ldsw128(w2+KP,u4{o1,0,zk,kk}); ldsw128(w2+2*KP,u4{p1,d0,0,kk}); ldsw128(w2+3*KP,u4{o0,o1,0,zk}); }
  }
}
DI f4 mfma16h(u4 a,u4 b,f4 c){ return __builtin_amdgcn_mfma_f32_16x16x32_f16(__builtin_bit_cast(hf8,a),__builtin_bit_cast(hf8,b),c,0,0,0); }
DI u4 lds2x64(u32 a){ const u2 lo=ldsr64(a), hi=ldsr64(a+8); return u4{lo[0],lo[1],hi[0],hi[1]}; }
DI void conv_win(u32 cb,int lane,u4 (&sw)[4]){
  float P[25]; P[0]=0.f;
  #pragma unroll
  for(int c=0;c<4;c++){ const int X=2*lane-2+c; const u4 v=ldsr128(cb+UOF+(X&3)*1040+((X>>2)+1)*16);
    #pragma unroll
    for(int e=0;e<8;e++){ const int i=8*c+e-4; if(i>=0&&i<24) P[i+1]=P[i]+((e&1)?hhi(v[e>>1]):hlo(v[e>>1])); } }
  #pragma unroll
  for(int k=0;k<8;k++){ sw[k>>2][k&3]=pkhf(P[2*k+8]-P[2*k+1],P[2*k+9]-P[2*k+2]); sw[2+(k>>2)][k&3]=pkhf(P[2*k+9]-P[2*k],P[2*k+10]-P[2*k+1]); }
}
DI void conv_help(u32 cb,int lane,const u4 (&sw)[4]){
  const int r=lane&15,q=lane>>4; f4 y[2][2]={};
  ldsw128(cb+S7O+48+lane*32,sw[0]); ldsw128(cb+S7O+64+lane*32,sw[1]); ldsw128(cb+S9O+48+lane*32,sw[2]); ldsw128(cb+S9O+64+lane*32,sw[3]);
  if(lane<3){ ldsw128(cb+S7O+lane*16,u4{0,0,0,0}); ldsw128(cb+S9O+lane*16,u4{0,0,0,0}); }
  const u32 ka=cb+(r&3)*KP+16*q-8*(r>>2)+64, ub=cb+UOF+q*1040+(r+1)*16;
  u4 a[2][4][2],bb[2][4]; const u32 kg=ka+64, ug=ub+272;
  auto rdn=[&](int g){
    #pragma unroll
    for(int i=0;i<4;i++){ a[g&1][i][0]=lds2x64(kg+(3-g)*256+i*64+32); a[g&1][i][1]=lds2x64(kg+(3-g)*256+i*64); bb[g&1][i]=ldsr128(ug+(3-g)*64+i*16); } };
  auto mmn=[&](int g){
    #pragma unroll
    for(int i=0;i<4;i++){ y[0][1]=mfma16h(a[g&1][i][0],bb[g&1][i],y[0][1]); y[1][1]=mfma16h(a[g&1][i][1],bb[g&1][i],y[1][1]); } };
  rdn(0); rdn(1); SCHED(); mmn(0); SCHED(); rdn(2); SCHED(); mmn(1); SCHED(); rdn(3); SCHED(); mmn(2); SCHED();
  const u4 a0=lds2x64(ka+32), a1=lds2x64(ka), b0=ldsr128(ub), b1=ldsr128(ub+256), a2=lds2x64(ka-32), b2=ldsr128(ub-16), b3=ldsr128(ub+240);
  const u32 sa=cb+(((r>>2)&1)?S9O:S7O)+2*(64*q+24-8*(r>>2)+4*(r&3)), kz=cb+KZO+8*(r-2*q+23); u4 bz[4],fa[4][2];
  #pragma unroll
  for(int ks=0;ks<4;ks++) bz[ks]=lds2x64(kz+(3-ks)*64);
  SCHED(); mmn(3); SCHED();
  #pragma unroll
  for(int i=0;i<8;i++){ const u2 lo=ldstr(sa+(i>>1)*512+(i&1)*32+64), hi=ldstr(sa+(i>>1)*512+(i&1)*32); fa[i>>1][i&1]=u4{lo[0],lo[1],hi[0],hi[1]}; }
  SCHED();
  y[0][0]=mfma16h(a2,b2,y[0][0]); y[0][1]=mfma16h(a2,b3,y[0][1]);
  y[0][0]=mfma16h(a0,b0,y[0][0]); y[1][0]=mfma16h(a1,b0,y[1][0]); y[0][1]=mfma16h(a0,b1,y[0][1]); y[1][1]=mfma16h(a1,b1,y[1][1]);
  #pragma unroll
  for(int ks=0;ks<4;ks++)
    #pragma unroll
    for(int m=0;m<2;m++){ y[m][1]=mfma16h(fa[ks][m],bz[ks],y[m][1]); if(ks<2) y[m][0]=mfma16h(fa[ks][m],bz[ks+2],y[m][0]); }
  #pragma unroll
  for(int i=0;i<4;i++) *(LAS f4*)(cb+S7O+i*1024+lane*16)=y[i&1][i>>1];
}
#define RDA(S,i,m) asm volatile("ds_read2_b64 %0, %1 offset0:%2 offset1:%3" : "=v"(AF[S][i][m]) : "v"(ka), "n"((1-(i))*128+(1-(m))*4), "n"((1-(i))*128+(1-(m))*4+1))
#define RDB(S,i,ad,o) asm volatile("ds_read_b128 %0, %1 offset:%2" : "=v"(BF[S][i]) : "v"(ad), "n"(o))
#define TIEA(S,i) "+v"(AF[S][i][0]),"+v"(AF[S][i][1])
#define CV_M(S,i,n) do{ y[0][n]=mfma16h(AF[S][i][0],BF[S][(n)-(i)],y[0][n]); y[1][n]=mfma16h(AF[S][i][1],BF[S][(n)-(i)],y[1][n]); SCHED(); }while(0)
#define CV_U0() u32 u0=u1-256; u0=((int)u0<(int)uz)?uz:u0
DI void conv_wave(const Params& p,rsrc_t rs,int d,u32 cb,u32 flag,u32 turn,u32 need,int lane,float ksc,float dsk){
  const int r=lane&15,q=lane>>4; u32 ka=cb+(r&3)*KP+16*q-8*(r>>2)+1088; const u32 uz=cb+UOF+q*1040; u32 u1=uz+(r+17)*16; u4 AF[2][2][2]; u4 BF[2][4]; f4 y[2][4]={};
  #define CV_WT(S) asm volatile("s_waitcnt lgkmcnt(0)" : TIEA(S,0),TIEA(S,1),"+v"(BF[S][0]),"+v"(BF[S][1]),"+v"(BF[S][2]),"+v"(BF[S][3]))
  #define CV_STEP(C,N) do{ CV_U0(); CV_M(C,0,0); RDA(N,0,0); RDB(N,0,u0,0); RDA(N,0,1); SCHED(); CV_M(C,0,1); RDB(N,1,u1,0); RDA(N,1,0); RDB(N,2,u1,256); SCHED(); CV_M(C,0,2); RDA(N,1,1); RDB(N,3,u1,512); \
      ka-=64; u1-=16; SCHED(); CV_M(C,0,3); CV_M(C,1,1); CV_M(C,1,2); CV_WT(N); }while(0)
  { CV_U0(); RDA(0,0,0); RDA(0,0,1); RDA(0,1,0); RDA(0,1,1); RDB(0,0,u0,0); RDB(0,1,u1,0); RDB(0,2,u1,256); RDB(0,3,u1,512); ka-=64; u1-=16; CV_WT(0); }
  #pragma unroll 1
  for(int dl=0;dl<8;dl++){ const u32 tv=ldsv(turn); CV_STEP(0,1); if(uni(tv)>=need) __builtin_amdgcn_s_setprio(3); if(dl==7){ ka+=64; u1+=16; } CV_STEP(1,0); }
  signal(turn,lane);
  asm volatile("s_nop 15" : "+v"(y[0][0]),"+v"(y[0][1]),"+v"(y[0][2]),"+v"(y[0][3]),"+v"(y[1][0]),"+v"(y[1][1]),"+v"(y[1][2]),"+v"(y[1][3]));
  __builtin_amdgcn_s_setprio(3);
  u2 uv[2][4];
  #pragma unroll
  for(int n=0;n<4;n++)
    #pragma unroll
    for(int m=0;m<2;m++) uv[m][n]=ldsr64(cb+UOF+(2*m+(q>>1))*1040+(16*n+r+1)*16+8*(q&1));
  const u32 sc=cb+UOF+r*64+q*8;
  auto fin=[&](int n){
    #pragma unroll
    for(int m=0;m<2;m++){ u2 o;
      #pragma unroll
      for(int e=0;e<2;e++) o[e]=pkbf(fmaf1(hlo(uv[m][n][e]),dsk,mulf(y[m][n][2*e],ksc)),fmaf1(hhi(uv[m][n][e]),dsk,mulf(y[m][n][2*e+1],ksc)));
      *(LAS u2*)(sc+n*1024+m*32)=o; } };
  ldswait(flag,2u);
  { f4 hp[4];
    #pragma unroll
    for(int i=0;i<4;i++) hp[i]=*(LAS f4*)(cb+S7O+i*1024+lane*16);
    fin(0); fin(1);
    #pragma unroll
    for(int i=0;i<4;i++)
      #pragma unroll
      for(int e=0;e<4;e++) y[i&1][2+(i>>1)][e]=addf(y[i&1][2+(i>>1)][e],hp[i][e]);
    fin(2); fin(3); }
  u4 ov[4];
  #pragma unroll
  for(int n=0;n<4;n++) ov[n]=ldsr128(cb+UOF+n*1024+lane*16);
  #pragma unroll
  for(int n=0;n<4;n++) st16(rs,OFF_AT+d*4096+n*1024+lane*16,ov[n]);
  WAITV(0);
}

#define NB 3
constexpr int g2ops(int h){ return h<0? 7 : (h+NB<16?7:0)+((h>=3&&h<=10)?4:0)+(h==13-NB?1:0)+(h==16-NB?8:0); }
constexpr int g2cnt(int h){ int c=0; for(int i=h-NB+1;i<h;i++) c+=g2ops(i); return c; }
struct Frag { u4 x[4], w[4][2]; };
__global__ __launch_bounds__(512,2) void fused(Params p){
  extern __shared__ __attribute__((aligned(16))) char sm[];
  const u32 smb=(u32)(size_t)(LAS char*)sm;
  u64 pc0; asm volatile("s_getpc_b64 %0" : "=s"(pc0));
  const int b=blockIdx.x, xc=b&7, idx=b>>3;
  const int tid=threadIdx.x, w=uni(tid>>6); int lane=tid&63;
  const rsrc_t rs=__builtin_amdgcn_make_buffer_rsrc(p.ws,0,WS_END,0x00020000);
  const u4 srdS=mksrd(p.ws);
  const int rtB=4*xc+(idx&3), ct=idx>>2;
  const bool hask=idx<28 && idx+28*(w&3)<80, hasc=hask && w>=4;
  const int dch=80*xc+(hask? idx+28*(w&3) : 0);
  const u32 cb=smb+CONV+(w&3)*CVS;
  const int rt=4*(xc>>1)+(idx&3), j=7*(xc&1)+(idx>>2), mi=w&3, g1=w>>2;
  u32 tag; float ksc=0.f, dsk=0.f;
  STAMP(0,0);
  auto pre01=[&](){
    const u4 sa=mksrd((const char*)p.wsp+(size_t)rtB*64*4096+mi*1024-4096); const u32 v0=(lane>>3)*4096+(((lane&7)^(lane>>4))<<4);
    #pragma unroll 1
    for(int i=0;i<4;i++){ setm0<RB+8192>(smb+mi*RING+i*4096); const u32 g=(i&1)*4+2*mi, k=(i>>1)*128+4096;
      dma<0>(sa,v0,(g&7)*32768+k); dma<1024>(sa,v0^64u,(g&7)*32768+32768-1024+k); dma<2048>(sa,v0,((g+2)&7)*32768-2048+k); dma<3072>(sa,v0^64u,((g+2)&7)*32768+32768-3072+k); } };
  if(idx>=28){
    const int a=xc*4+idx-28, r=lane&15, q=lane>>4;
    { u4 sc=mksrd((const void*)(pc0&~127ull)); sc[2]=CODE_BYTES; setm0<JNK>(smb); touch(sc,lane*128,w*8192); }
    const u32 ep=ld4(rs,OFF_EP);
    u4 xr[10]; const char* xs=(const char*)p.x+(size_t)(64*a+8*w+(lane>>3))*1280+(lane&7)*16;
    #pragma unroll
    for(int i=0;i<10;i++) xr[i]=*(const u4*)(xs+i*128);
    if(tid<32) *(LAS u32*)(smb+PM+tid*4)=0u;
    tag=uni(ep)+TAGINC;
    float s0=0.f,s1=0.f,q0=0.f,q1=0.f;
    #pragma unroll
    for(int i=0;i<10;i++){ s0=dot2(xr[i][0],0x3f803f80u,s0); q0=dot2(xr[i][0],xr[i][0],q0); s1=dot2(xr[i][1],0x3f803f80u,s1); q1=dot2(xr[i][1],xr[i][1],q1);
      s0=dot2(xr[i][2],0x3f803f80u,s0); q0=dot2(xr[i][2],xr[i][2],q0); s1=dot2(xr[i][3],0x3f803f80u,s1); q1=dot2(xr[i][3],xr[i][3],q1); }
    s0+=s1; q0+=q1;
    #define DPPADD(v,c) v+=__int_as_float(__builtin_amdgcn_update_dpp(0,__float_as_int(v),c,0xf,0xf,false))
    DPPADD(s0,0xB1); DPPADD(q0,0xB1); DPPADD(s0,0x4E); DPPADD(q0,0x4E); DPPADD(s0,0x141); DPPADD(q0,0x141);
    float m1,ri; { float mean=s0*(1.f/640.f); ln_fin(mean,q0*(1.f/640.f)-mean*mean,m1,ri); }
    if((lane&7)==0) st16(rs,OFF_RS+(64*a+8*w+(lane>>3))*16,u4{__float_as_uint(m1),tag,__float_as_uint(ri),tag});
    STAMP(5,0);
    if(a<27){ const u4 srdO=mksrd((const char*)p.wout+a*(32*1280)+w*5120); const u32 mo=smb+w*5120; setm0<0>(mo);
      dma<0>(srdO,lane*16,0); dma<1024>(srdO,lane*16,0); dma<2048>(srdO,lane*16,0); dma<3072>(srdO,lane*16,0); setm0<4096>(mo); dma<0>(srdO,lane*16,4096); }
    #pragma unroll
    for(int i=0;i<10;i++){ u4 o;
      #pragma unroll
      for(int e=0;e<4;e++) o[e]=pkhf2(fma2(unpk(xr[i][e]),f2{ri,ri},f2{-m1*ri,-m1*ri}));
      ldsw128(smb+RB+(8*w+(lane>>3))*1296+(lane&7)*16+i*128,o); }
    WAITV(0); bar();
    #pragma unroll
    for(int i=0;i<10;i++){ const int c=w*10+i; const u32 ad=smb+RB+(32*((r>>1)&1)+8*q+(r>>2))*1296+c*16+(r&1)*8; u2 lo=ldstr(ad), hi=ldstr(ad+4*1296);
      const u32 sc=smb+40960+w*2048+(i&1)*1024; ldsw128(sc+(r&7)*128+((((r>>3)*4+q)^(r&7))<<4),u4{lo[0],lo[1],hi[0],hi[1]});
      st16(rs,OFF_XNT+a*81920+c*1024+lane*16,ldsr128(sc+(lane>>3)*128+(((lane&7)^(lane>>3))<<4))); }
    WAITV(0);
    if(arrive(smb+C_Z1,lane)==7u && lane==0) st4(rs,OFF_FX+a*128,tag);
    STAMP(6,7);
    if(a<27){
      #pragma unroll
      for(int i=0;i<5;i++){ u32 ad=smb+(8*q+(r>>2))*1280+(w*5+i)*32+(r&3)*8; u2 lo=ldstr(ad), hi=ldstr(ad+4*1280);
        st16(rs,OFF_WOF+(u32)((w*27+a)*5+i)*1024+lane*16,u4{lo[0],lo[1],hi[0],hi[1]}); } }
    WAITV(0); bar();
    if(tid==0) st4(rs,OFF_FW+a*128,tag);
    if(!g1){ pre01(); WAITV(0); }
    STAMP(31,0);
  } else {
    const int r=lane&31, h=lane>>5;
    const u4 srdX=mksrd((const char*)p.x+(size_t)rt*128*1280);
    const u4 srdW=mksrd((const char*)p.win+j*128-3072);
    const u32 mw=smb+w*4096;
    u32 vx0,vx1,vw1,vw2,epv;
    { u32 lr=lane>>3,c=lane&7; vx0=(16*w+lr)*1280+((c^(lr>>1))<<4); vx1=(vx0^64u)+9216;
      vw1=(8*w+lr)*3456+((c^((lr&2)<<1))<<4); vw2=vw1+1024+(j==13?0:14*128); }
    u32 kx[10]; { u32 k=KROT;
      #pragma unroll
      for(int i=0;i<10;i++){ kx[i]=k*128; k=k==9?0:k+1; } }
    auto issue1=[&](auto sc,int k){ constexpr int s=sc.value;
      if(k==0){ setm0<(s&3)*STG>(mw); dma<0>(srdX,vx0,kx[s]); } if(k==1) dma<1024>(srdX,vx1,kx[s]); if(k==2) dma<2048>(srdW,vw2,kx[s]*1728); if(k==3) dma<3072>(srdW,vw1,kx[s]*1728); };
    auto issue=[&](auto sc){ issue1(sc,0); issue1(sc,1); issue1(sc,2); issue1(sc,3); };
    issue(std::integral_constant<int,0>{});
    u32 pr[6];
    gload4<0>(epv,0u,p.ws+OFF_EP);
    gload4<0>(pr[0],lane*4,(const char*)(p.lar+dch*128)); gload4<0>(pr[1],lane*4,(const char*)(p.aim+dch*128)); gload4<0>(pr[2],lane*4,(const char*)(p.cre+dch*128)); gload4<0>(pr[3],lane*4,(const char*)(p.cim+dch*128));
    gload2(pr[4],0u,(const char*)(p.ldt+dch)); gload2(pr[5],0u,(const char*)(p.dsk+dch));
    asm volatile("s_barrier" ::: "memory");
    issue(std::integral_constant<int,1>{});
    if(tid<32) *(LAS u32*)(smb+PM+tid*4)=0u;
    u32 ax[2][4],aw[2][4][2];
    { u32 row=32*mi+r, a0=smb+(row>>4)*4096+(row&15)*128+((h^((r>>1)&7))<<4);
      u32 qq=(lane&15)>>2, w0=smb+2048+g1*1024+h*4096+qq*128+8*(lane&1)+(((2*((lane>>4)&1)+((lane>>1)&1))^((qq&2)<<1))<<4);
      #pragma unroll
      for(int i=0;i<2;i++)
        #pragma unroll
        for(int k=0;k<4;k++){ const u32 kk=(k+mi)&3, wo=i*2*STG; ax[i][k]=opq((a0^(kk*32u))+wo); aw[i][k][0]=opq(w0+kk*8192+wo); aw[i][k][1]=opq((w0^64u)+kk*8192+wo); } }
    u4 sel; { const u32 o=((lane&15)==((lane>>4)&1))?0x3f803f80u:0u; sel=u4{o,o,o,o}; asm volatile("" : "+v"(sel)); }
    u32 ga[11];
    #pragma unroll
    for(int i=0;i<11;i++) ga[i]=opq(cb+2u*(u32)rbf((float)(512-8*lane+3-i)));
    Frag F[2];
    asm volatile("s_waitcnt vmcnt(4)" : "+v"(epv),"+v"(pr[0]),"+v"(pr[1]),"+v"(pr[2]),"+v"(pr[3]),"+v"(pr[4]),"+v"(pr[5]) :: "memory");
    bar();
    tag=uni(epv)+TAGINC; dsk=bflo(pr[5]);
    #pragma unroll
    for(int k=0;k<4;k++){
      #pragma unroll
      for(int n=0;n<2;n++){ u2 lo=ldstr(aw[0][k][n]), hi=ldstr(aw[0][k][n]+512); F[0].w[k][n]=u4{lo[0],lo[1],hi[0],hi[1]}; }
      F[0].x[k]=ldsr128(ax[0][k]); }
    issue(std::integral_constant<int,2>{});
    STAMP(1,0);
    f16v acc[2]={}; f4 cacc[2]={}; u4 G;
    const u32 go=OFF_RS+rt*2048+mi*512, cp=smb+MISC+g1*1024;
    sfor<10>([&](auto ic){
      constexpr int s=ic.value; Frag& cur=F[s&1]; Frag& nxt=F[(s&1)^1];
      if constexpr(s<9){ WAITV(s==8? 3 : s==7? 7 : 4); asm volatile("s_barrier" ::: "memory"); }
      auto rw=[&](int k,int n){ if constexpr(s<9){ const int i=((s+1)>>1)&1; const u32 so=((s+1)&1)*STG; u2 lo=ldstr(aw[i][k][n]+so), hi=ldstr(aw[i][k][n]+so+512); nxt.w[k][n]=u4{lo[0],lo[1],hi[0],hi[1]}; } };
      auto rx=[&](int k){ if constexpr(s<9){ const int i=((s+1)>>1)&1; const u32 so=((s+1)&1)*STG; nxt.x[k]=ldsr128(ax[i][k]+so); } };
      #pragma unroll
      for(int k=0;k<4;k++){
        acc[0]=mfma32b(cur.w[k][0],cur.x[k],acc[0]); SCHED();
        if constexpr(s+3<=9) issue1(std::integral_constant<int,s+3>{},k);
        rw(k,0); rx(k);
        SCHED();
        acc[1]=mfma32b(cur.w[k][1],cur.x[k],acc[1]); SCHED();
        rw(k,1);
        if(k==0){ cacc[0]=mfma16(cur.w[0][0],sel,cacc[0]); cacc[1]=mfma16(cur.w[0][1],sel,cacc[1]); }
        if constexpr(s==6){ if(k==3){ gload<0>(G,r*16,p.ws+go); setm0<JNK>(smb);
            if(g1){ const u4 sp=mksrd((const char*)p.wsp+(size_t)rtB*64*4096); const u32 l0=(ct*4+mi)*74+lane, l1=(ct*4+mi)*74+64+(lane<10?lane:9);
              touch(sp,(l0<2048?l0:2047)*128,0u); touch(sp,(l1<2048?l1:2047)*128,0u); }
            else { touch(srdS,0u,go); touch(srdS,0u,go); } } }
        if constexpr(s==8){ if(k==3) signal(smb+C_DONE,lane); }
        if constexpr(s==9){ if(k==1){
          if((lane&14)==0){ *(LAS f4*)(cp+mi*256+(lane&1)*64+(lane>>4)*16)=cacc[0]; *(LAS f4*)(cp+mi*256+128+(lane&1)*64+(lane>>4)*16)=cacc[1]; }
          signal(smb+C_CS+g1*4,lane); } }
        SCHED();
      }
    });
    RST(0);
    if(!g1) __builtin_amdgcn_s_setprio(2);
    asm volatile("s_waitcnt vmcnt(2)" : "+v"(G) :: "memory");
    ldswait(smb+C_CS+g1*4,4u);
    const float cs0=*(LAS float*)(cp+h*512+r*4)+*(LAS float*)(cp+h*512+256+r*4), cs1=*(LAS float*)(cp+h*512+128+r*4)+*(LAS float*)(cp+h*512+384+r*4);
    RST(2);
    #pragma unroll 1
    for(int sp=0;sp<SPIN && !__all(G[1]==tag&&G[3]==tag);sp++)asm volatile("global_load_dwordx4 %0, %1, %2 sc1\n\ts_waitcnt vmcnt(0)" : "=&v"(G) : "v"(r*16), "s"(p.ws+go) : "memory");
    RST(3);
    const float nm=-__uint_as_float(G[0]), ri1=__uint_as_float(G[2]);
    acc[0]=__builtin_amdgcn_mfma_f32_32x32x2f32(cs0,nm,acc[0],0,0,0); acc[1]=__builtin_amdgcn_mfma_f32_32x32x2f32(cs1,nm,acc[1],0,0,0);
    u32 zp[2][8]; const u32 zqa=smb+RB+mi*RING+4096+lane*16;
    if(g1){
      ldswait(smb+C_DONE,8u);
      #pragma unroll
      for(int i=0;i<4;i++){ u4 o;
        #pragma unroll
        for(int e=0;e<4;e++) o[e]=pkbf2(f2{acc[i>>1][8*(i&1)+2*e],acc[i>>1][8*(i&1)+2*e+1]}*ri1);
        ldsw128(zqa+i*1024,o); }
      signal(smb+C_K2+mi*4,lane); RST(4);
    } else {

      #pragma unroll
      for(int e=0;e<16;e++) zp[e>>3][e&7]=pkbf2(gelu2(f2{acc[e>>3][2*(e&7)],acc[e>>3][2*(e&7)+1]}*ri1));
      float S=0.f,Q=0.f;
      if(j<13){
        #pragma unroll
        for(int e=0;e<8;e++){ S=dot2(zp[0][e],0x3f803f80u,S); Q=dot2(zp[0][e],zp[0][e],Q); } }
      #pragma unroll
      for(int e=0;e<8;e++){ S=dot2(zp[1][e],0x3f803f80u,S); Q=dot2(zp[1][e],zp[1][e],Q); }
      #define XSUM32(v) do{ auto t_=__builtin_amdgcn_permlane32_swap(__float_as_uint(v),__float_as_uint(v),false,false); v=__uint_as_float(t_[0])+__uint_as_float(t_[1]); }while(0)
      XSUM32(S); XSUM32(Q);
      const u32 sto=OFF_ST+rt*(14*2048)+(32*mi+r)*16;
      if(h==0) st16(rs,sto+j*2048,u4{__float_as_uint(S),tag,__float_as_uint(Q),tag});
      RST(6);
    }
    float kp[11]; u4 g[7]={};
    const u32 gof=OFF_ST+rt*(14*2048)+(32*mi+r)*16+h*(7*2048);
    auto samp=[&](){ asm volatile("" ::: "memory");
      #pragma unroll
      for(int i=0;i<7;i++) g[i]=ld16(rs,gof+i*2048);
      asm volatile("" ::: "memory"); };
    if(hasc) ksc=kgen_tab(cb,lane,pr,kp,[](){}); else if(!g1){
      pre01(); ldswait(smb+C_K2+mi*4,1u); u4 zq[4];
      #pragma unroll
      for(int i=0;i<4;i++){ const u4 v=ldsr128(zqa+i*1024);
        #pragma unroll
        for(int e=0;e<4;e++) zq[i][e]=pkbf2(gelu2(unpk(v[e]))); }
      SCHED(); samp();
      #pragma unroll
      for(int n=0;n<2;n++) if(2*j+n<27){ const u32 oo=OFF_Z1T+((rt*8+2*mi+(r>>4))*27+2*j+n)*1024+(h*16+(r&15))*16;
        st16(rs,oo,u4{zq[2*n][0],zq[2*n][1],zq[2*n+1][0],zq[2*n+1][1]}); st16(rs,oo+512,u4{zq[2*n][2],zq[2*n][3],zq[2*n+1][2],zq[2*n+1][3]}); }
    }
    if(g1){
      if(hasc){
        RST(6);
        kgen_exp(cb,lane,kp,ga);
        signal(smb+C_KP+mi*4,lane);
        RST(8);
        ldswait(smb+C_FULL,1u);
        RST(9);
      }
    } else {
      RST(1);
      float S,Q;
      #pragma unroll 1
      for(int sp=0;sp<SPIN;sp++){ bool ok=true;
        #pragma unroll
        for(int i=0;i<7;i++) ok=ok&&g[i][1]==tag&&g[i][3]==tag;
        if(__all(ok)) break;
        __builtin_amdgcn_s_sleep(4); samp(); }
      RST(7);
      S=0.f; Q=0.f;
      #pragma unroll
      for(int i=0;i<7;i++){ S+=__uint_as_float(g[i][0]); Q+=__uint_as_float(g[i][2]); }
      XSUM32(S); XSUM32(Q);
      float mean=S*(1.f/864.f), m2,r2; ln_fin(mean,Q*(1.f/864.f)-mean*mean,m2,r2); m2*=-r2;
      ldswait(smb+C_DONE,8u);
      const u32 tb=smb+RB+mi*RING, ad=tb+(8*(lane>>4)+((lane&15)>>2))*80+(lane&3)*8;
      const u32 zo=OFF_Z2F+((rt*4+mi)*54+(j==13?-2:2+4*j))*1024+lane*16;
      #pragma unroll
      for(int n=0;n<2;n++){
        #pragma unroll
        for(int g2=0;g2<4;g2++){ u2 o;
          #pragma unroll
          for(int e=0;e<2;e++) o[e]=pkbf2(fma2(unpk(zp[n][2*g2+e]),f2{r2,r2},f2{m2,m2}));
          *(LAS u2*)(tb+r*80+g2*16+h*8)=o; }
        u2 l0=ldstr(ad), h0=ldstr(ad+320), l1=ldstr(ad+32), h1=ldstr(ad+32+320);
        if(j<13||n){ st16(rs,zo+n*2048,u4{l0[0],l0[1],h0[0],h0[1]}); st16(rs,zo+n*2048+1024,u4{l1[0],l1[1],h1[0],h1[1]}); } }
      RST(8);
      WAITV(0);
      if(arrive(smb+C_Z2,lane)==3u && lane==0) st4(rs,OFF_F2+(rt*16+j)*128,tag);
      RST(9);
    }
  }
  const int ft0=ct<7?7*ct:47;
  bool adone=true; const u32 fao=OFF_FA+((lane<28?lane:0)*8+ct)*128;
  auto aload=[&](){ const u32 vo=(lane>>3)*4096+(lane&7)*16;
    #pragma unroll 1
    for(int i=0;i<10;i++){ setm0<ATL>(smb+i*1024); dma<0>(srdS,vo,OFF_AT+(u32)(80*ct+8*i)*4096+rtB*128); } };
  if(w>=4){
    if(hasc){
      conv_wave(p,rs,dch,cb,smb+C_KP+mi*4,smb+C_ZQ,STAG,lane,ksc,dsk);
      __builtin_amdgcn_s_setprio(0);
      if(arrive(smb+C_CV,lane)==(idx<24?2u:1u) && lane==0) st4(rs,OFF_FA+b*128,tag);
    }
    if(w==7){
      if(idx<28){
        __builtin_amdgcn_s_sleep(FXDLY);
        pollw<16>(rs,OFF_FX+(lane&31)*128,tag);
        u4 sw[3][4];
        #pragma unroll
        for(int c=0;c<3;c++)
          #pragma unroll
          for(int i=0;i<4;i++) sw[c][i]=ld16(rs,OFF_XNT+((lane>>3)+8*i)*81920+(80*xc+idx+((c<2||idx<24)?28*c:0))*128+(lane&7)*16);
        #pragma unroll
        for(int c=0;c<3;c++){
          #pragma unroll
          for(int i=0;i<4;i++){ const int x=lane+64*i; ldsw128(smb+c*CVS+UOF+(x&3)*1040+((x>>2)+1)*16,sw[c][i]); }
          if(lane<4) ldsw128(smb+c*CVS+UOF+lane*1040,u4{0,0,0,0}); }
        signal(smb+C_FULL,lane);
        RST(4);
        #pragma unroll 1
        for(int c=0;c<3;c++){
          #pragma unroll
          for(int i=0;i<4;i++){ sw[0][i]=sw[1][i]; sw[1][i]=sw[2][i]; }
          conv_win(smb+c*CVS,lane,sw[2]); }
        RST(5);
        __builtin_amdgcn_s_setprio(HPRIO);
        #pragma unroll 1
        for(int c=0;c<(idx<24?3:2);c++){ ldswait(smb+C_KP+c*4,1u); conv_help(smb+c*CVS,lane,sw[0]); signal(smb+C_KP+c*4,lane);
          #pragma unroll
          for(int i=0;i<4;i++){ sw[0][i]=sw[1][i]; sw[1][i]=sw[2][i]; } } }
      __builtin_amdgcn_s_setprio(0);
      RST(6);
    }
    RST(10);
    ldswait(smb+C_Z1R,4u);
    u4 wq[24];
    #pragma unroll
    for(int i=0;i<24;i++) wq[i]=ld16(rs,OFF_WOF+ct*(135*1024)+40960+mi*RING+((i==23&&mi==3)?22:i)*1024+lane*16);
    if(w<6){
      #pragma unroll 1
      for(u32 i=0;i<20480;i+=4096){ setm0<0>(cb+i); const u32 o=OFF_WOF+ct*(135*1024)+mi*20480+i; dma<0>(srdS,lane*16,o); dma<1024>(srdS,lane*16,o); dma<2048>(srdS,lane*16,o); dma<3072>(srdS,lane*16,o); } }
    if(w==6){ adone=__all(ld4(rs,fao)==tag); if(adone) aload(); }
    ldswait(smb+C_FREE,4u);
    #pragma unroll
    for(int i=0;i<24;i++) ldsw128(smb+RB+mi*RING+i*1024+lane*16,wq[i]);
    if(w<6) WAITV(0);
    signal(smb+C_WR,lane);
    if(!adone){ adone=__all(ld4(rs,fao)==tag); if(adone) aload(); }
    if(w==7 && b==255){
      #pragma unroll 1
      for(int sp=0;sp<SPIN;sp++){ const u32 o=OFF_FA+lane*128; u32 v0=ld4(rs,o),v1=ld4(rs,o+8192),v2=ld4(rs,o+16384),v3=ld4(rs,lane<32? o+24576 : OFF_FW+(lane-32)*128); asm volatile("" ::: "memory");
        if(__all(v0==tag&&v1==tag&&v2==tag&&v3==tag)) break; __builtin_amdgcn_s_sleep(4); }
      if(lane==0) st4(rs,OFF_EP,tag); }
  }
  int w2=w; asm volatile("" : "+s"(w2));
  if(w2<4){
    __builtin_amdgcn_s_setprio(G2PRIO);
    int r=lane&15, q=lane>>4;
    const u32 rb=smb+RB+w*RING;
    u4 srdA=mksrd((const char*)p.wsp+(size_t)rtB*64*4096+w*1024-4096+256);
    const char* zb=p.ws+OFF_Z2F+(u32)(16*w*54+ft0+3)*1024;
    u32 voA[2]; { u32 lr=lane>>3,c=lane&7; voA[0]=lr*4096+((c^(lr>>1))<<4); voA[1]=voA[0]^64u; }
    u32 sofa[8];
    #pragma unroll
    for(int g=0;g<8;g++) sofa[g]=((g+2*w)&7)*32768+4096-(g&3)*1024;
    const u32 voB=lane*16;
    u32 la[2]; la[0]=rb+r*128+((q^(r>>1))<<4); la[1]=la[0]^64u;
    f4 acc[4][7]={}; u4 B[NB][7]; u4 A[2][4]; u2 z1[7];
    auto issueA=[&](auto sc,auto gc){ constexpr int s=sc.value,g=gc.value; if constexpr((g&3)==0) setm0<((s+1)%3)*8192+g*1024>(rb); dma<(g&3)*1024>(srdA,voA[g&1],sofa[g]);
      if constexpr(g==7){ u64 a=((u64)srdA[1]<<32|srdA[0])+128; srdA[0]=(u32)a; srdA[1]=(u32)(a>>32); } };

    u32 bsr,fv;
    auto fof=[&](){ const u32 l=voB>>4; return l<27? OFF_FW+l*128 : OFF_F2+((rtB>>1)*16+(((ft0>>1)+(l&3))>>1))*128; };
    #pragma unroll
    for(int m=0;m<4;m++){ A[0][m]=ldsr128(la[0]+m*2048+8192); A[1][m]=ldsr128(la[1]+m*2048+8192); }
    sfor<8>([&](auto g){ issueA(std::integral_constant<int,2>{},g); });
    waitA(A[0]); waitA(A[1]);
    sfor<8>([&](auto g){ issueA(std::integral_constant<int,3>{},g); });
    #pragma unroll
    for(int m=0;m<4;m++)
      #pragma unroll
      for(int n=0;n<7;n++) asm volatile("" : "+v"(acc[m][n]));
    RST(2);
    { const int hf=(ft0>>1)+q; pollw(rs,OFF_F2+((4*w+(lane&3))*16+(hf==0?13:(hf-1)>>1))*128,tag); }
    RST(10);
    sfor<NB>([&](auto hh){ const char* s=zb+(u32)hh.value*(54*1024); sfor<7>([&](auto n){ gload<(n.value-3)*1024>(B[hh][n],voB,s); }); });
    auto redw=[&](auto nc){ constexpr int n=nc.value; *(LAS f4*)(rb+n*1024+voB)=acc[1][n]; *(LAS f4*)(rb+(7+n)*1024+voB)=acc[2][n]; *(LAS f4*)(rb+(14+n)*1024+voB)=acc[3][n]; };
    const char* z1b=p.ws+OFF_Z1T+(u32)((rtB*4+w)*27+(ft0>>1))*1024;
    sfor<16>([&](auto hc){
      constexpr int h=hc.value, S=h%NB, P=h&1, HZ=16-NB, cnt=g2cnt(h);
      waitB<cnt>(B[S]); waitA(A[P]);
      if constexpr(h==HZ){ tie1(fv); if(!__all(fv==tag)) pollw(rs,fof(),tag); }
      if constexpr(h+1<16 && h>0){
        #pragma unroll
        for(int m=0;m<4;m++) A[P^1][m]=ldsr128(la[(h+1)&1]+m*2048+((((h+1)>>1)+1)%3)*8192);
      }
      if constexpr(h==HZ-3) gload4<0>(fv,fof(),p.ws);
      const char* s=zb+(u32)(h+NB)*(54*1024);
      sfor<7>([&](auto n){
        mfma4(acc[0][n],acc[1][n],acc[2][n],acc[3][n],B[S][n],A[P]);
        if constexpr(h>=3 && h<=10 && (n.value&1)==0) issueA(std::integral_constant<int,(h-1)/2+3>{},std::integral_constant<int,((h-1)&1)*4+n.value/2>{});
        if constexpr(h+NB<16) gload_t<(n.value-3)*1024>(B[S][n],voB,s);
        if constexpr(h==HZ){ gload8<(n.value>>1)*1024>(z1[n],voB,z1b+((n.value&1)? ((ft0&1)?1024:8) : (ft0&1)*8));
          if constexpr(n.value==6) { gload2(bsr,(voB>>3)&30u,(const char*)(p.bsp+(rtB*4+w)*16)); signal(smb+C_Z1R,lane); } }
        if constexpr(h==15){ SCHED(); if constexpr(n.value>=2) redw(std::integral_constant<int,n.value-2>{}); SCHED(); }
      });
    });
    asm volatile("s_nop 15\n\ts_nop 3" ::: "memory");
    SCHED();
    STAMP(10,0);
    lane=__builtin_amdgcn_mbcnt_hi(~0u,__builtin_amdgcn_mbcnt_lo(~0u,0u)); asm volatile("" : "+v"(lane)); r=lane&15; q=lane>>4;
    redw(std::integral_constant<int,5>{}); redw(std::integral_constant<int,6>{});
    signal(smb+C_RED,lane);
    ldswait(smb+C_RED,4u);
    #pragma unroll
    for(int m=1;m<4;m++){ u32 src=smb+RB+((w-m)&3)*RING+(m-1)*7168+lane*16;
      #pragma unroll
      for(int n=0;n<7;n++) acc[0][n]+=*(LAS f4*)(src+n*1024); }
    asm volatile("s_waitcnt vmcnt(0) lgkmcnt(0)" : "+v"(z1[0]),"+v"(z1[1]),"+v"(z1[2]),"+v"(z1[3]),"+v"(z1[4]),"+v"(z1[5]),"+v"(z1[6]),"+v"(bsr) :: "memory");
    const float bs=bflo(bsr);
    signal(smb+C_FREE,lane);
    u2 o[7];
    #pragma unroll
    for(int n=0;n<7;n++)
      #pragma unroll
      for(int e=0;e<2;e++)o[n][e]=pkbf2(unpk(z1[n][e])*(f2{acc[0][n][2*e],acc[0][n][2*e+1]}+bs));
    const u32 ho=OFF_HF+(u32)((rtB*4+w)*54+ft0)*512+(q>>1)*256+r*16;
    #pragma unroll
    for(int n=0;n<6;n+=2){ auto sx=__builtin_amdgcn_permlane16_swap(o[n][0],o[n+1][0],false,false); auto sy=__builtin_amdgcn_permlane16_swap(o[n][1],o[n+1][1],false,false);
      if(ct<7||n>=2) st16(rs,ho+(n+(q&1))*512,u4{sx[0],sy[0],sx[1],sy[1]}); }
    st8(rs,ho+6*512+(q&1)*8,o[6]);
    STAMP(11,0);
    WAITV(0);
    if(arrive(smb+C_H,lane)==3u && lane==0) st4(rs,OFF_FH+(rtB*8+ct)*128,tag);
    STAMP(12,0);
    __builtin_amdgcn_s_setprio(0);
  }
  { const int r=lane&15, q=lane>>4, m=w&3, kh=w>>2;
    u2 xv[5]={};
    if(kh){
      #pragma unroll
      for(int n=0;n<5;n++) xv[n]=*(const u2*)(p.x+(rtB*64+m*16+r)*640+80*ct+16*n+4*q);
    }
    f4 acc[5]={}; u4 W[3][5];
    const u32 wl2=smb+lane*16+RB-40960+kh*14*5120, wl0=kh? wl2 : smb+lane*16, wl1=kh? wl2 : smb+lane*16+CVS-20480;
    #define WAD(k,n) (((k)<4? wl0 : (k)<8? wl1 : wl2)+((k)*5+(n))*1024)
    f4 ax[5]={};
    auto addax=[&](){
      #pragma unroll
      for(int n=0;n<5;n++){ u2 av=ldstr(smb+ATL+(16*n+4*q+(r>>2))*128+(16*m+4*(r&3))*2); ax[n]=f4{bflo(av[0]),bfhi(av[0]),bflo(av[1]),bfhi(av[1])}+f4{bflo(xv[n][0]),bfhi(xv[n][0]),bflo(xv[n][1]),bfhi(xv[n][1])}; } };
    if(w==6 && adone){ WAITV(0); signal(smb+C_AT,lane); }
    u32 oo[3],to[3]; const u32 tb=smb+CVS+m*3072;
    #pragma unroll
    for(int i=0;i<3;i++){ const int it=lane+64*i, rr=(it*205)>>11, cc=it-rr*10; oo[i]=opq((rtB*64+m*16+rr)*1280+ct*160+cc*16); to[i]=opq(tb+rr*176+cc*16); }
    if(m==0){ ldswait(smb+C_H,4u); pollw(rs,OFF_FH+(rtB*8+kh*4+(lane&3))*128,tag); signal(smb+C_TAG+kh*4,lane); } else ldswait(smb+C_TAG+kh*4,1u);
    STAMP(16,0);
    u4 hf[14];
    const u32 ho=OFF_HF+(u32)((rtB*4+m)*27+kh*14)*1024+lane*16;
    #pragma unroll
    for(int k=0;k<HPF;k++) hf[k]=ld16(rs,ho+k*1024);
    const bool early=kh && uni(ldsv(smb+C_AT))!=0u;
    if(early) addax();
    ldswait(smb+C_WR,4u);
    STAMP(17,0);
    const u32 wl3=kh? wl2-5120 : wl2;
    #pragma unroll
    for(int n=0;n<10;n++) W[n/5][n%5]=ldsr128(WAD(n/5,n%5));
    #pragma unroll
    for(int k=0;k<13;k++){
      if(k+HPF<14) hf[k+HPF]=ld16(rs,ho+((k+HPF==13&&kh)?12:k+HPF)*1024);
      SCHED();
      #pragma unroll
      for(int n=0;n<5;n++){ acc[n]=mfma16(W[k%3][n],hf[k],acc[n]); SCHED();
        if(k<11) W[(k+2)%3][n]=ldsr128(WAD(k+2,n)); else if(k==11) W[1][n]=ldsr128(wl3+(65+n)*1024);
        SCHED(); }
      if(k==5 && !kh) signal(smb+C_G1,lane);
    }
    const u32 pb=smb+m*5120+lane*16;
    if(!kh){
      #pragma unroll
      for(int n=0;n<5;n++) acc[n]=mfma16(W[1][n],hf[13],acc[n]);
      STAMP(18,0);
      ldswait(smb+C_G1,4u);
      #pragma unroll
      for(int n=0;n<5;n++) *(LAS f4*)(pb+n*1024)=acc[n];
      signal(smb+C_P+m*4,lane);
    } else {
      if(w==6 && !adone){ pollw(rs,fao,tag); aload(); WAITV(0); signal(smb+C_AT,lane); }
      if(!early){ ldswait(smb+C_AT,1u); addax(); }
      RST(7);
      ldswait(smb+C_P+m*4,1u);
      #pragma unroll
      for(int n=0;n<5;n++){ const f4 o=(acc[n]+ax[n])+*(LAS f4*)(pb+n*1024); *(LAS u2*)(tb+r*176+n*32+q*8)=u2{pkbf(o[0],o[1]),pkbf(o[2],o[3])}; }
      #pragma unroll
      for(int i=0;i<3;i++) if(i<2||lane<32) __builtin_amdgcn_raw_buffer_store_b128(ldsr128(to[i]),__builtin_amdgcn_make_buffer_rsrc(p.out,0,2048*1280,0x00020000),oo[i],0,16);
      STAMP(19,4);
    }
  }
}
extern "C" void kernel_launch(void* const* d_in,const int* in_sizes,int n_in,void* d_out,int out_size,void* d_ws,size_t ws_size,hipStream_t stream){
  Params p;
  p.x=(const u16*)d_in[0]; p.lar=(const u16*)d_in[1]; p.aim=(const u16*)d_in[2]; p.cre=(const u16*)d_in[3]; p.cim=(const u16*)d_in[4];
  p.ldt=(const u16*)d_in[5]; p.dsk=(const u16*)d_in[6]; p.win=(const u16*)d_in[8]; p.wsp=(const u16*)d_in[9]; p.bsp=(const u16*)d_in[10]; p.wout=(const u16*)d_in[11];
  p.out=(u16*)d_out; p.ws=(char*)d_ws;
  (void)hipFuncSetAttribute((const void*)fused,hipFuncAttributeMaxDynamicSharedMemorySize,LDS_TOTAL);
  fused<<<256,512,LDS_TOTAL,stream>>>(p);
}
```

```cpp
#include <hip/hip_runtime.h>
#include <utility>
#pragma clang diagnostic ignored "-Wint-to-pointer-cast"
typedef unsigned short u16;
typedef unsigned int u32;
typedef unsigned long long u64;
typedef __attribute__((ext_vector_type(8))) __bf16 bf8;
typedef __attribute__((ext_vector_type(8))) _Float16 hf8;
typedef __attribute__((ext_vector_type(2))) __bf16 bf2;
typedef __attribute__((ext_vector_type(2))) _Float16 hf2;
typedef __attribute__((ext_vector_type(2))) float f2;
typedef __attribute__((ext_vector_type(4))) float f4;
typedef __attribute__((ext_vector_type(16))) float f16v;
typedef __attribute__((ext_vector_type(4))) u32 u4;
typedef __attribute__((ext_vector_type(2))) u32 u2;
typedef __attribute__((ext_vector_type(4))) short s4;
#define LAS __attribute__((address_space(3)))
#define GAS __attribute__((address_space(1)))
#define DI __device__ __forceinline__

constexpr u32 OFF_Z2F = 0;
constexpr u32 OFF_Z1T = OFF_Z2F + 64*54*1024;
constexpr u32 OFF_XNT = OFF_Z1T + 128*27*1024;
constexpr u32 OFF_WOF = OFF_XNT + 640*4096;
constexpr u32 OFF_HF  = OFF_WOF + 8*27*5*1024;
constexpr u32 OFF_AT  = OFF_HF + 128*27*1024;
constexpr u32 OFF_ST  = OFF_AT + 640*4096;
constexpr u32 OFF_RS  = OFF_ST + 16*14*2048+8192;
constexpr u32 OFF_EP  = OFF_RS + 2048*16;
constexpr u32 OFF_F2  = OFF_EP + 256;
constexpr u32 OFF_F1  = OFF_F2 + 256*128;
constexpr u32 OFF_FX  = OFF_F1 + 256*128;
constexpr u32 OFF_FW  = OFF_FX + 256*128;
constexpr u32 OFF_FH  = OFF_FW + 32*128;
constexpr u32 OFF_FA  = OFF_FH + 256*128;
constexpr u32 WS_END  = OFF_FA + 256*128;

struct Params {
  const u16 *x, *lar, *aim, *cre, *cim, *ldt, *dsk, *win, *wsp, *bsp, *wout;
  u16* out; char* ws;
};
#define STAMP(id,wv) do{}while(0)
#define RST(i) do{}while(0)
template<class F,int... I> DI void sfor_impl(F&& f, std::integer_sequence<int,I...>){ (f(std::integral_constant<int,I>{}),...); }
template<int N,class F> DI void sfor(F&& f){ sfor_impl(f, std::make_integer_sequence<int,N>{}); }

DI float bflo(u32 v){ return __uint_as_float(v<<16); }
DI float bfhi(u32 v){ return __uint_as_float(v&0xffff0000u); }
DI float bf1(u16 v){ return __uint_as_float(((u32)v)<<16); }
DI u32 pkbf(float a,float b){ f2 v={a,b}; return __builtin_bit_cast(u32,__builtin_convertvector(v,bf2)); }
DI u32 pkhf(float a,float b){ f2 v={a,b}; return __builtin_bit_cast(u32,__builtin_convertvector(v,hf2)); }
DI float rbf(float a){ return bflo(pkbf(a,a)); }
DI float hlo(u32 v){ return (float)__builtin_bit_cast(hf2,v)[0]; }
DI float hhi(u32 v){ return (float)__builtin_bit_cast(hf2,v)[1]; }
DI float dot2(u32 a,u32 b,float c){ return __builtin_amdgcn_fdot2_f32_bf16(__builtin_bit_cast(bf2,a),__builtin_bit_cast(bf2,b),c,false); }
DI f4 mfma16(u4 a,u4 b,f4 c){ return __builtin_amdgcn_mfma_f32_16x16x32_bf16(__builtin_bit_cast(bf8,a),__builtin_bit_cast(bf8,b),c,0,0,0); }
DI f16v mfma32h(u4 a,u4 b,f16v c){ return __builtin_amdgcn_mfma_f32_32x32x16_f16(__builtin_bit_cast(hf8,a),__builtin_bit_cast(hf8,b),c,0,0,0); }
DI u4 ldsr128(u32 a){ return *(LAS u4*)a; }
DI u2 ldsr64(u32 a){ return *(LAS u2*)a; }
DI void ldsw128(u32 a,u4 v){ *(LAS u4*)a=v; }
DI u2 ldstr(u32 a){ return __builtin_bit_cast(u2,__builtin_amdgcn_ds_read_tr16_b64_v4i16((LAS s4*)a)); }
DI u32 uni(u32 v){ return __builtin_amdgcn_readfirstlane(v); }

template<int OFF> DI void gload(u4& dst,u32 voff,const char* base){ asm volatile("global_load_dwordx4 %0, %1, %2 offset:%3 sc1" : "=v"(dst) : "v"(voff), "s"(base), "n"(OFF)); }
template<int OFF> DI void gload_t(u4& dst,u32 voff,const char* base){ asm volatile("global_load_dwordx4 %0, %1, %2 offset:%3 sc1" : "+v"(dst) : "v"(voff), "s"(base), "n"(OFF)); }
DI void mfma4(f4& c0,f4& c1,f4& c2,f4& c3,const u4& a,const u4 (&b)[4]){
  asm volatile("v_mfma_f32_16x16x32_bf16 %0, %4, %5, %0\n\tv_mfma_f32_16x16x32_bf16 %1, %4, %6, %1\n\tv_mfma_f32_16x16x32_bf16 %2, %4, %7, %2\n\tv_mfma_f32_16x16x32_bf16 %3, %4, %8, %3"
    : "+v"(c0),"+v"(c1),"+v"(c2),"+v"(c3) : "v"(a),"v"(b[0]),"v"(b[1]),"v"(b[2]),"v"(b[3])); }
template<int CNT> DI void waitB(u4 (&B)[7]){ asm volatile("s_waitcnt vmcnt(%7)" : "+v"(B[0]),"+v"(B[1]),"+v"(B[2]),"+v"(B[3]),"+v"(B[4]),"+v"(B[5]),"+v"(B[6]) : "n"(CNT) : "memory"); }
DI void waitA(u4 (&A)[4]){ asm volatile("s_waitcnt lgkmcnt(0)" : "+v"(A[0]),"+v"(A[1]),"+v"(A[2]),"+v"(A[3]) :: "memory"); }
#define WAITV(n) asm volatile("s_waitcnt vmcnt(%0)" :: "n"(n) : "memory")
#define WAITL0() asm volatile("s_waitcnt lgkmcnt(0)" ::: "memory")
#define SCHED() __builtin_amdgcn_sched_barrier(0)
DI void bar(){ asm volatile("s_waitcnt lgkmcnt(0)\n\ts_barrier" ::: "memory"); }
DI f2 unpk(u32 v){ return f2{bflo(v),bfhi(v)}; }
DI u32 pkbf2(f2 v){ return __builtin_bit_cast(u32,__builtin_convertvector(v,bf2)); }
DI u32 pkhf2(f2 v){ return __builtin_bit_cast(u32,__builtin_convertvector(v,hf2)); }
DI f2 fma2(f2 a,f2 b,f2 c){ return __builtin_elementwise_fma(a,b,c); }
DI f2 gelu2(f2 x){
  f2 u=x*fma2(x*x,f2{-0.10294324f,-0.10294324f},f2{-2.30220820f,-2.30220820f});
  f2 d=f2{__builtin_amdgcn_exp2f(u[0]),__builtin_amdgcn_exp2f(u[1])}+f2{1.f,1.f};
  return x*f2{__builtin_amdgcn_rcpf(d[0]),__builtin_amdgcn_rcpf(d[1])};
}
DI void ln_fin(float mean,float var,float& m,float& rinv){
  m=rbf(mean); float v=rbf(__builtin_fmaxf(var,0.f));
  float s=rbf(__builtin_amdgcn_sqrtf(rbf(v+1e-5f))); float r=__builtin_amdgcn_rcpf(s); rinv=__builtin_fmaf(__builtin_fmaf(-s,r,1.f),r,r);
}
typedef __amdgpu_buffer_rsrc_t rsrc_t;
DI void st16(rsrc_t rs,u32 off,u4 v){ __builtin_amdgcn_raw_buffer_store_b128(v,rs,off,0,16); }
DI void st8(rsrc_t rs,u32 off,u2 v){ __builtin_amdgcn_raw_buffer_store_b64(v,rs,off,0,16); }
DI void st4(rsrc_t rs,u32 off,u32 v){ __builtin_amdgcn_raw_buffer_store_b32(v,rs,off,0,16); }
DI u4 ld16(rsrc_t rs,u32 off){ return __builtin_amdgcn_raw_buffer_load_b128(rs,off,0,16); }
DI u2 ld8(rsrc_t rs,u32 off){ return __builtin_amdgcn_raw_buffer_load_b64(rs,off,0,16); }
DI u32 ld4(rsrc_t rs,u32 off){ return __builtin_amdgcn_raw_buffer_load_b32(rs,off,0,16); }
constexpr int SPIN=1<<17;
constexpr u32 TAGINC=0x9E3779B9u;
template<int SL=2> DI void pollw(rsrc_t rs,u32 off,u32 tag){
  #pragma unroll 1
  for(int s=0;s<SPIN;s++){ u32 v=ld4(rs,off); asm volatile("" ::: "memory"); if(__all(v==tag)) break; __builtin_amdgcn_s_sleep(SL); }
}

DI f16v mfma32b(u4 a,u4 b,f16v c){ return __builtin_amdgcn_mfma_f32_32x32x16_bf16(__builtin_bit_cast(bf8,a),__builtin_bit_cast(bf8,b),c,0,0,0); }
DI u4 mksrd(const void* b){ u64 a=(u64)b; return u4{(u32)a,(u32)(a>>32)&0xffffu,0xffffffffu,0x00020000u}; }
template<int IMM> DI void setm0(u32 base){ asm volatile("s_add_u32 m0, %0, %1\n\ts_nop 0" :: "s"(base), "n"(IMM) : "scc","memory"); }
template<int OFF> DI void dma(u4 srd,u32 voff,u32 soff){ asm volatile("buffer_load_dwordx4 %0, %1, %2 offen offset:%3 sc1 lds" :: "v"(voff), "s"(srd), "s"(soff), "n"(OFF) : "memory"); }
DI void touch(u4 srd,u32 voff,u32 soff){ asm volatile("buffer_load_dword %0, %1, %2 offen sc1 lds" :: "v"(voff), "s"(srd), "s"(soff) : "memory"); }
template<int OFF> DI void gload8(u2& dst,u32 voff,const char* base){ asm volatile("global_load_dwordx2 %0, %1, %2 offset:%3 sc1" : "=v"(dst) : "v"(voff), "s"(base), "n"(OFF)); }
DI void gload2(u32& dst,u32 voff,const char* base){ asm volatile("global_load_ushort %0, %1, %2" : "=v"(dst) : "v"(voff), "s"(base)); }
DI u32 arrive(u32 a,int){ u32 o; asm volatile("s_mov_b64 exec, 1\n\tds_add_rtn_u32 %0, %1, %2\n\ts_mov_b64 exec, -1\n\ts_waitcnt lgkmcnt(0)" : "=&v"(o) : "v"(a), "v"(1u) : "memory"); return uni(o); }
DI void ldswait(u32 a,u32 n){ while(__hip_atomic_load((LAS u32*)a,__ATOMIC_RELAXED,__HIP_MEMORY_SCOPE_WORKGROUP)<n) __builtin_amdgcn_s_sleep(1); asm volatile("" ::: "memory"); }
DI float addf(float a,float b){ float o; asm("v_add_f32 %0, %1, %2" : "=v"(o) : "v"(a), "v"(b)); return o; }
DI void tie1(u32& g){ asm volatile("" : "+v"(g) :: "memory"); }
DI u32 opq(u32 v){ asm volatile("" : "+v"(v)); return v; }

constexpr u32 STG=32768;
constexpr u32 RING=24576, RB=62848, CONV=0, KP=2208, UOF=16768, CVS=UOF+4160, S7O=4*KP, S9O=S7O+2112, KZO=S9O+2112, MISC=RB+4*RING, JNK=MISC+2048, PM=163840-128, LDS_TOTAL=163840;
constexpr u32 ATL=2*CVS;
constexpr u32 C_FULL=PM, C_G1=PM+4, C_Z2=PM+8, C_Z1=PM+12, C_RED=PM+16, C_H=PM+20, C_FREE=PM+24, C_CV=PM+28, C_Z1R=PM+32, C_TAG=PM+40, C_KP=PM+48, C_DONE=PM+64, C_ZQ=PM+68, C_CS=PM+72, C_K2=PM+80, C_P=PM+96, C_AT=PM+112, C_WR=PM+116;
constexpr u32 CODE_BYTES=39936;
#define LA 3
#define TC_S 7
#define G2PRIO 2
#define XMAX 9
#define GS_K 35
#define WNT ""
#define TS1 6
#define HPRIO 3
#define CPRIO 3
#define POLLM 1
#define HPF 5
#define CVSKIP 0
#define BROT 0
#define STAG 0u
#define FXDLY 24
#define HSH 0
#define PDLY 8
#define RS_S 7
#define KROT ((((j+rt)&3)*5)>>1)
DI void signal(u32 a,int){ asm volatile("s_mov_b64 exec, 1\n\tds_add_u32 %0, %1\n\ts_mov_b64 exec, -1" :: "v"(a), "v"(1u) : "memory"); }
template<int OFF> DI void gload4(u32& dst,u32 voff,const char* base){ asm volatile("global_load_dword %0, %1, %2 offset:%3 sc1" : "=v"(dst) : "v"(voff), "s"(base), "n"(OFF)); }
template<int CNT> DI void waitG(u4& g){ asm volatile("s_waitcnt vmcnt(%1)" : "+v"(g) : "n"(CNT) : "memory"); }

DI f2 cmulp(f2 a,f2 b,f2 bs){ return fma2(f2{a[1],a[1]},bs,f2{a[0],a[0]}*b); }
DI float mulf(float a,float b){ float o; asm("v_mul_f32 %0, %1, %2" : "=v"(o) : "v"(a),"v"(b)); return o; }
DI float fmaf1(float a,float b,float c){ float o; asm("v_fma_f32 %0, %1, %2, %3" : "=v"(o) : "v"(a),"v"(b),"v"(c)); return o; }
DI u32 ldsv(u32 a){ return __hip_atomic_load((LAS u32*)a,__ATOMIC_RELAXED,__HIP_MEMORY_SCOPE_WORKGROUP); }
DI void ldswait4(u32 a,int lane,u32 n){ while(__any(ldsv(a+(lane&3)*4)<n)) __builtin_amdgcn_s_sleep(1); asm volatile("" ::: "memory"); }
DI u32 gettag(u32 a){ ldswait(a+4,1u); return uni(ldsv(a)); }
DI f2 cpow_dir(float lr,float rv,float pw){ float e=__builtin_amdgcn_exp2f(lr*pw); float f=__builtin_amdgcn_fractf(rv*pw);
  return f2{e*__builtin_amdgcn_cosf(f),e*__builtin_amdgcn_sinf(f)}; }
DI u32 dup(u32 a){ return __builtin_amdgcn_perm(a,a,0x01000100u); }
DI u32 cat(u32 lo,u32 hi){ return __builtin_amdgcn_perm(hi,lo,0x05040100u); }
DI u32 lds16(u32 a){ return *(LAS u16*)a; }

template<class CB> DI float kgen_tab(u32 cb,int lane,const u32 (&pr)[6],float (&kp)[11],CB&& mid){
  const float dt=rbf(__expf(bflo(pr[4])));
  const float cr[2]={bflo(pr[2]),bfhi(pr[2])}, cm[2]={bflo(pr[3]),bfhi(pr[3])};
  float mx=fmaxf(fmaxf(fabsf(cr[0]),fabsf(cm[0])),fmaxf(fabsf(cr[1]),fabsf(cm[1])));
  u32 eb=__float_as_uint(mx)&0x7f800000u;
  #define DPPMAX(c) eb=max(eb,(u32)__builtin_amdgcn_update_dpp(0,(int)eb,c,0xf,0xf,false))
  DPPMAX(0x128); DPPMAX(0x124); DPPMAX(0x122); DPPMAX(0x121);
  eb=max(max((u32)__builtin_amdgcn_readlane(eb,0),(u32)__builtin_amdgcn_readlane(eb,16)),max((u32)__builtin_amdgcn_readlane(eb,32),(u32)__builtin_amdgcn_readlane(eb,48))); eb=eb<0x01000000u?0x3f800000u:(eb>0x7e000000u?0x7e000000u:eb);
  const float ksc=2.f*dt*__uint_as_float(eb), isc=__uint_as_float(0x7f000000u-eb);
  const int r=lane&31,h=lane>>5;
  const u32 TP=272, TA=cb, TB=cb+22*TP;
  f16v acc={};
  f2 an[2][4];
  #pragma unroll
  for(int c=0;c<2;c++){ const float l_a=c?bfhi(pr[0]):bflo(pr[0]), i_m=c?bfhi(pr[1]):bflo(pr[1]);
    const float lr=(-rbf(__expf(l_a))*dt)*1.44269504f, rv=(i_m*dt)*0.15915494f;
    an[c][0]=cpow_dir(lr,rv,1.f); an[c][1]=cpow_dir(lr,-rv,8.f); an[c][2]=cpow_dir(lr,-rv,16.f); an[c][3]=cpow_dir(lr,rv,256.f); }
  #pragma unroll
  for(int c=0;c<2;c++){
    const float c_r=cr[c], c_i=cm[c];
    const f2 z1=an[c][0], z8=an[c][1], zs=an[c][2], zk=an[c][3];
    const f2 z1s={-z1[1],z1[0]}, zss={-zs[1],zs[0]}, zks={-zk[1],zk[0]};
    const u32 wa=TA+lane*4, wb=TB+lane*4;
    #define PUT(base,row,v) do{ *(LAS u32*)((base)+(row)*TP)=pkhf2(v); }while(0)
    f2 e={1.f,0.f}, o=z8;
    #pragma unroll
    for(int k=0;k<16;k++){ PUT(wb,2*k,e); PUT(wb,2*k+1,o); if(k<15){ e=k?cmulp(e,zs,zss):zs; o=cmulp(o,zs,zss);} }
    f2 b0={c_r*isc,c_i*isc}; PUT(wa,0,b0);
    f2 t=cmulp(b0,zk,zks); PUT(wa,8,t);
    f2 b1=cmulp(b0,z1,z1s); PUT(wa,1,b1); t=cmulp(t,zk,zks); PUT(wa,10,t);
    f2 b2=cmulp(b1,z1,z1s); PUT(wa,2,b2); t=cmulp(t,zk,zks); PUT(wa,11,t);
    f2 b3=cmulp(b2,z1,z1s); PUT(wa,3,b3); t=cmulp(t,zk,zks); PUT(wa,16,t);
    f2 b4=cmulp(b3,z1,z1s); PUT(wa,4,b4); t=cmulp(t,zk,zks); PUT(wa,17,t);
    f2 b5=cmulp(b4,z1,z1s); PUT(wa,5,b5); t=cmulp(t,zk,zks); PUT(wa,20,t);
    f2 b6=cmulp(b5,z1,z1s); PUT(wa,6,b6); t=cmulp(t,zk,zks); PUT(wa,21,t);
    f2 b7=cmulp(b6,z1,z1s); PUT(wa,7,b7); t=cmulp(t,zk,zks); PUT(wa,18,t);
    f2 s=cmulp(b4,zk,zks); PUT(wa,12,s); t=cmulp(b2,zk,zks); PUT(wa,9,t);
    s=cmulp(s,zk,zks); PUT(wa,14,s); t=cmulp(b6,zk,zks); PUT(wa,13,t);
    s=cmulp(s,zk,zks); PUT(wa,15,s);
    #undef PUT
    if(c==0) mid();
    const int ra=(r>21||r==19)?0:r;
    #pragma unroll
    for(int ks=0;ks<8;ks++){ u4 fa=ldsr128(TA+ra*TP+ks*32+h*16), fb=ldsr128(TB+r*TP+ks*32+h*16); acc=mfma32h(fa,fb,acc); }
  }
  #pragma unroll
  for(int i=0;i<11;i++) kp[i]=acc[i];
  return ksc;
}
DI void kgen_exp(u32 cb,int lane,const float (&acc)[11],const u32 (&ga)[11]){
  const int r=lane&31,h=lane>>5;
  { const u32 a4=cb+r*16+h*8, a5=cb+r*16+h*1024, az=cb+KZO+r*2+h*128;
    #define PK(addr,base,reg) do{ *(LAS _Float16*)((addr)+2*(base))=(_Float16)acc[reg]; }while(0)
    PK(a4,0,0);PK(a4,1,1);PK(a4,2,2);PK(a4,3,3); PK(a4,256,4);PK(a4,258,5);PK(a4,512,6);PK(a4,768,7);
    PK(a5,1024,8); PK(az,255,10); PK(az,127,8); PK(az,159,9);
    #undef PK
    if(lane<16) ldsw128(cb+KZO+lane*16,u4{0,0,0,0});
  }
  { const u32 g0=cb+2*(1024-8*lane);
    u32 f2_=lds16(g0+(lane?8:0)), a2=lds16(g0), m2=lds16(g0-8), b2=lds16(g0-16);
    u32 e[11];
    #pragma unroll
    for(int j=0;j<11;j++) e[j]=lds16(ga[j]);
    u32 k01=*(LAS u32*)cb, k23=*(LAS u32*)(cb+4), kl=lds16(cb+2048);
    WAITL0();
    const u32 wo=cb+96+lane*16;
    { u32 aa=dup(a2),mm=dup(m2),bb=dup(b2),am=cat(a2,m2),fa=cat(f2_,a2),mb=cat(m2,b2);
      ldsw128(wo,u4{aa,am,mm,bb}); ldsw128(wo+KP,u4{aa,aa,mm,mb}); ldsw128(wo+2*KP,u4{aa,aa,am,mm}); ldsw128(wo+3*KP,u4{fa,aa,aa,mm}); }
    { u32 o0=cat(e[0],e[1]),o1=cat(e[2],e[3]),o2=cat(e[4],e[5]),o3=cat(e[6],e[7]),o4=cat(e[8],e[9]);
      u32 p1=cat(e[1],e[2]),d0=cat(e[3],e[4]),d1=cat(e[5],e[6]),d2=cat(e[7],e[8]),d3=cat(e[9],e[10]);
      ldsw128(wo+1024,u4{d0,d1,d2,d3}); ldsw128(wo+1024+KP,u4{o1,o2,o3,o4}); ldsw128(wo+1024+2*KP,u4{p1,d0,d1,d2}); ldsw128(wo+1024+3*KP,u4{o0,o1,o2,o3}); }
    if(lane<10){ if(lane){k01=0;k23=0;} if(lane!=9) kl=0;
      const u32 d0=k01&0xffffu, o1=__builtin_amdgcn_alignbit(k01,k01,16), o0=__builtin_amdgcn_alignbit(k23,k23,16), p1=(k23&0xffffu)|(k01&0xffff0000u), kk=dup(kl), zk=kl<<16;
      const u32 w2=lane<4? wo+2048 : wo-160;
      ldsw128(w2,u4{d0,0,kk,kk}); ldsw128(w2+KP,u4{o1,0,zk,kk}); ldsw128(w2+2*KP,u4{p1,d0,0,kk}); ldsw128(w2+3*KP,u4{o0,o1,0,zk}); }
  }
}
DI f4 mfma16h(u4 a,u4 b,f4 c){ return __builtin_amdgcn_mfma_f32_16x16x32_f16(__builtin_bit_cast(hf8,a),__builtin_bit_cast(hf8,b),c,0,0,0); }
DI u4 lds2x64(u32 a){ const u2 lo=ldsr64(a), hi=ldsr64(a+8); return u4{lo[0],lo[1],hi[0],hi[1]}; }
DI void conv_win(u32 cb,int lane,u4 (&sw)[4]){
  float P[25]; P[0]=0.f;
  #pragma unroll
  for(int c=0;c<4;c++){ const int X=2*lane-2+c; const u4 v=ldsr128(cb+UOF+(X&3)*1040+((X>>2)+1)*16);
    #pragma unroll
    for(int e=0;e<8;e++){ const int i=8*c+e-4; if(i>=0&&i<24) P[i+1]=P[i]+((e&1)?hhi(v[e>>1]):hlo(v[e>>1])); } }
  #pragma unroll
  for(int k=0;k<8;k++){ sw[k>>2][k&3]=pkhf(P[2*k+8]-P[2*k+1],P[2*k+9]-P[2*k+2]); sw[2+(k>>2)][k&3]=pkhf(P[2*k+9]-P[2*k],P[2*k+10]-P[2*k+1]); }
}
DI void conv_help(u32 cb,int lane,const u4 (&sw)[4]){
  const int r=lane&15,q=lane>>4; f4 y[2][2]={};
  ldsw128(cb+S7O+48+lane*32,sw[0]); ldsw128(cb+S7O+64+lane*32,sw[1]); ldsw128(cb+S9O+48+lane*32,sw[2]); ldsw128(cb+S9O+64+lane*32,sw[3]);
  if(lane<3){ ldsw128(cb+S7O+lane*16,u4{0,0,0,0}); ldsw128(cb+S9O+lane*16,u4{0,0,0,0}); }
  const u32 ka=cb+(r&3)*KP+16*q-8*(r>>2)+64, ub=cb+UOF+q*1040+(r+1)*16;
  u4 a[2][4][2],bb[2][4]; const u32 kg=ka+64, ug=ub+272;
  auto rdn=[&](int g){
    #pragma unroll
    for(int i=0;i<4;i++){ a[g&1][i][0]=lds2x64(kg+(3-g)*256+i*64+32); a[g&1][i][1]=lds2x64(kg+(3-g)*256+i*64); bb[g&1][i]=ldsr128(ug+(3-g)*64+i*16); } };
  auto mmn=[&](int g){
    #pragma unroll
    for(int i=0;i<4;i++){ y[0][1]=mfma16h(a[g&1][i][0],bb[g&1][i],y[0][1]); y[1][1]=mfma16h(a[g&1][i][1],bb[g&1][i],y[1][1]); } };
  if(HSH==4){ rdn(0); rdn(1); SCHED(); mmn(0); SCHED(); rdn(2); SCHED(); mmn(1); SCHED(); rdn(3); SCHED(); mmn(2); SCHED(); }
  if(HSH==2){ rdn(2); rdn(3); SCHED(); mmn(2); SCHED(); }
  const u4 a0=lds2x64(ka+32), a1=lds2x64(ka), b0=ldsr128(ub), b1=ldsr128(ub+256), a2=lds2x64(ka-32), b2=ldsr128(ub-16), b3=ldsr128(ub+240);
  const u32 sa=cb+(((r>>2)&1)?S9O:S7O)+2*(64*q+24-8*(r>>2)+4*(r&3)), kz=cb+KZO+8*(r-2*q+23); u4 bz[4],fa[4][2];
  #pragma unroll
  for(int ks=0;ks<4;ks++) bz[ks]=lds2x64(kz+(3-ks)*64);
  SCHED(); if(HSH) mmn(3); SCHED();
  #pragma unroll
  for(int i=0;i<8;i++){ const u2 lo=ldstr(sa+(i>>1)*512+(i&1)*32+64), hi=ldstr(sa+(i>>1)*512+(i&1)*32); fa[i>>1][i&1]=u4{lo[0],lo[1],hi[0],hi[1]}; }
  SCHED();
  y[0][0]=mfma16h(a2,b2,y[0][0]); y[0][1]=mfma16h(a2,b3,y[0][1]);
  y[0][0]=mfma16h(a0,b0,y[0][0]); y[1][0]=mfma16h(a1,b0,y[1][0]); y[0][1]=mfma16h(a0,b1,y[0][1]); y[1][1]=mfma16h(a1,b1,y[1][1]);
  #pragma unroll
  for(int ks=0;ks<4;ks++)
    #pragma unroll
    for(int m=0;m<2;m++){ y[m][1]=mfma16h(fa[ks][m],bz[ks],y[m][1]); if(ks<2) y[m][0]=mfma16h(fa[ks][m],bz[ks+2],y[m][0]); }
  #pragma unroll
  for(int i=0;i<4;i++) *(LAS f4*)(cb+S7O+i*1024+lane*16)=y[i&1][i>>1];
}
#define RDA(S,i,m) asm volatile("ds_read2_b64 %0, %1 offset0:%2 offset1:%3" : "=v"(AF[S][i][m]) : "v"(ka), "n"((1-(i))*128+(1-(m))*4), "n"((1-(i))*128+(1-(m))*4+1))
#define RDB(S,i,ad,o) asm volatile("ds_read_b128 %0, %1 offset:%2" : "=v"(BF[S][i]) : "v"(ad), "n"(o))
#define TIEA(S,i) "+v"(AF[S][i][0]),"+v"(AF[S][i][1])
#define CV_M(S,i,n) do{ y[0][n]=mfma16h(AF[S][i][0],BF[S][(n)-(i)],y[0][n]); y[1][n]=mfma16h(AF[S][i][1],BF[S][(n)-(i)],y[1][n]); SCHED(); }while(0)
#define CV_U0() u32 u0=u1-256; u0=((int)u0<(int)uz)?uz:u0
DI void conv_wave(const Params& p,rsrc_t rs,int d,u32 cb,u32 flag,u32 turn,u32 need,int lane,float ksc,float dsk){
  const int r=lane&15,q=lane>>4; u32 ka=cb+(r&3)*KP+16*q-8*(r>>2)+1088; const u32 uz=cb+UOF+q*1040; u32 u1=uz+(r+17)*16; u4 AF[2][2][2]; u4 BF[2][4]; f4 y[2][4]={};
  #define CV_WT(S) asm volatile("s_waitcnt lgkmcnt(0)" : TIEA(S,0),TIEA(S,1),"+v"(BF[S][0]),"+v"(BF[S][1]),"+v"(BF[S][2]),"+v"(BF[S][3]))
  #define CV_STEP(C,N) do{ CV_U0(); CV_M(C,0,0); RDA(N,0,0); RDB(N,0,u0,0); RDA(N,0,1); SCHED(); CV_M(C,0,1); RDB(N,1,u1,0); RDA(N,1,0); RDB(N,2,u1,256); SCHED(); CV_M(C,0,2); RDA(N,1,1); RDB(N,3,u1,512); \
      ka-=64; u1-=16; SCHED(); CV_M(C,0,3); CV_M(C,1,1); CV_M(C,1,2); if(dl<8-2*HSH) CV_M(C,1,3); CV_WT(N); }while(0)
  { CV_U0(); RDA(0,0,0); RDA(0,0,1); RDA(0,1,0); RDA(0,1,1); RDB(0,0,u0,0); RDB(0,1,u1,0); RDB(0,2,u1,256); RDB(0,3,u1,512); ka-=64; u1-=16; CV_WT(0); }
  #pragma unroll 1
  for(int dl=0;dl<8;dl++){ const u32 tv=ldsv(turn); CV_STEP(0,1); if(uni(tv)>=need) __builtin_amdgcn_s_setprio(3); if(dl==7){ ka+=64; u1+=16; } CV_STEP(1,0); }
  signal(turn,lane);
  asm volatile("s_nop 15" : "+v"(y[0][0]),"+v"(y[0][1]),"+v"(y[0][2]),"+v"(y[0][3]),"+v"(y[1][0]),"+v"(y[1][1]),"+v"(y[1][2]),"+v"(y[1][3]));
  __builtin_amdgcn_s_setprio(3);
  u2 uv[2][4];
  #pragma unroll
  for(int n=0;n<4;n++)
    #pragma unroll
    for(int m=0;m<2;m++) uv[m][n]=ldsr64(cb+UOF+(2*m+(q>>1))*1040+(16*n+r+1)*16+8*(q&1));
  const u32 sc=cb+UOF+r*64+q*8;
  auto fin=[&](int n){
    #pragma unroll
    for(int m=0;m<2;m++){ u2 o;
      #pragma unroll
      for(int e=0;e<2;e++) o[e]=pkbf(fmaf1(hlo(uv[m][n][e]),dsk,mulf(y[m][n][2*e],ksc)),fmaf1(hhi(uv[m][n][e]),dsk,mulf(y[m][n][2*e+1],ksc)));
      *(LAS u2*)(sc+n*1024+m*32)=o; } };
  ldswait(flag,2u);
  { f4 hp[4];
    #pragma unroll
    for(int i=0;i<4;i++) hp[i]=*(LAS f4*)(cb+S7O+i*1024+lane*16);
    fin(0); fin(1);
    #pragma unroll
    for(int i=0;i<4;i++)
      #pragma unroll
      for(int e=0;e<4;e++) y[i&1][2+(i>>1)][e]=addf(y[i&1][2+(i>>1)][e],hp[i][e]);
    fin(2); fin(3); }
  u4 ov[4];
  #pragma unroll
  for(int n=0;n<4;n++) ov[n]=ldsr128(cb+UOF+n*1024+lane*16);
  #pragma unroll
  for(int n=0;n<4;n++) st16(rs,OFF_AT+d*4096+n*1024+lane*16,ov[n]);
  WAITV(0);
}

#define NB 3
constexpr int g2ops(int h){ return h<0? 7 : (h+NB<16?7:0)+((h>=3&&h<=10)?4:0)+(h==13-NB?1:0)+(h==16-NB?8:0); }
constexpr int g2cnt(int h){ int c=0; for(int i=h-NB+1;i<h;i++) c+=g2ops(i); return c; }
struct Frag { u4 x[4], w[4][2]; };
__global__ __launch_bounds__(512,2) void fused(Params p){
  extern __shared__ __attribute__((aligned(16))) char sm[];
  const u32 smb=(u32)(size_t)(LAS char*)sm;
  u64 pc0; asm volatile("s_getpc_b64 %0" : "=s"(pc0));
  const int b=blockIdx.x, xc=b&7, idx=b>>3;
  const int tid=threadIdx.x, w=uni(tid>>6); int lane=tid&63;
  auto cpf=[&](u32 o){ u4 sc=mksrd((const void*)(pc0&~127ull)); sc[2]=CODE_BYTES; setm0<JNK>(smb); touch(sc,lane*128,o); };
  if(idx>=28) cpf(w*8192);
  SCHED();
  const int rt=4*(xc>>1)+(idx&3), j=7*(xc&1)+(idx>>2), mi=w&3, g1=w>>2;
  u32 vx0,vx1,vw1,vw2,k0=KROT*128,k0w=KROT*(64*3456); const u32 mw=smb+w*4096;
  { const u32 lr=lane>>3,c=lane&7; vx0=(rt*128+16*w+lr)*1280+((c^(lr>>1))<<4); vx1=(vx0^64u)+9216;
    vw1=(8*w+lr)*3456+((c^((lr&2)<<1))<<4)+j*128; vw2=vw1+1024+(j==13?0:14*128); }
  asm volatile("" : "+v"(vx0),"+v"(vx1),"+v"(vw1),"+v"(vw2),"+s"(k0),"+s"(k0w));
  setm0<0>(mw);
  SCHED();
  const u4 srdX=mksrd(p.x), srdW=mksrd((const char*)p.win-3072);
  if(idx<28){ dma<0>(srdX,vx0,k0); dma<1024>(srdX,vx1,k0); dma<2048>(srdW,vw2,k0w); dma<3072>(srdW,vw1,k0w); }
  SCHED();
  const rsrc_t rs=__builtin_amdgcn_make_buffer_rsrc(p.ws,0,WS_END,0x00020000);
  const u4 srdS=mksrd(p.ws);
  const int rtB=4*xc+(idx&3), ct=idx>>2;
  const bool hask=idx<28 && idx+28*(w&3)<80, hasc=hask && w>=4;
  const int dch=80*xc+(hask? idx+28*(w&3) : 0);
  const u32 cb=smb+CONV+(w&3)*CVS;
  u32 tag; float ksc=0.f, dsk=0.f;
  STAMP(0,0);
  auto pre01=[&](){
    const u4 sa=mksrd((const char*)p.wsp+(size_t)rtB*64*4096+mi*1024-4096); const u32 v0=(lane>>3)*4096+(((lane&7)^(lane>>4))<<4);
    #pragma unroll 1
    for(int i=0;i<4;i++){ setm0<RB+8192>(smb+mi*RING+i*4096); const u32 g=(i&1)*4+2*mi, k=(i>>1)*128+4096;
      dma<0>(sa,v0,(g&7)*32768+k); dma<1024>(sa,v0^64u,(g&7)*32768+32768-1024+k); dma<2048>(sa,v0,((g+2)&7)*32768-2048+k); dma<3072>(sa,v0^64u,((g+2)&7)*32768+32768-3072+k); } };
  if(idx>=28){
    const int a=xc*4+idx-28, r=lane&15, q=lane>>4;
    const u32 ep=ld4(rs,OFF_EP);
    u4 xr[10]; const char* xs=(const char*)p.x+(size_t)(64*a+8*w+(lane>>3))*1280+(lane&7)*16;
    #pragma unroll
    for(int i=0;i<10;i++) xr[i]=*(const u4*)(xs+i*128);
    if(tid<32) *(LAS u32*)(smb+PM+tid*4)=0u;
    tag=uni(ep)+TAGINC;
    float s0=0.f,s1=0.f,q0=0.f,q1=0.f;
    #pragma unroll
    for(int i=0;i<10;i++){ s0=dot2(xr[i][0],0x3f803f80u,s0); q0=dot2(xr[i][0],xr[i][0],q0); s1=dot2(xr[i][1],0x3f803f80u,s1); q1=dot2(xr[i][1],xr[i][1],q1);
      s0=dot2(xr[i][2],0x3f803f80u,s0); q0=dot2(xr[i][2],xr[i][2],q0); s1=dot2(xr[i][3],0x3f803f80u,s1); q1=dot2(xr[i][3],xr[i][3],q1); }
    s0+=s1; q0+=q1;
    #define DPPADD(v,c) v+=__int_as_float(__builtin_amdgcn_update_dpp(0,__float_as_int(v),c,0xf,0xf,false))
    DPPADD(s0,0xB1); DPPADD(q0,0xB1); DPPADD(s0,0x4E); DPPADD(q0,0x4E); DPPADD(s0,0x141); DPPADD(q0,0x141);
    float m1,ri; { float mean=s0*(1.f/640.f); ln_fin(mean,q0*(1.f/640.f)-mean*mean,m1,ri); }
    if((lane&7)==0) st16(rs,OFF_RS+(64*a+8*w+(lane>>3))*16,u4{__float_as_uint(m1),tag,__float_as_uint(ri),tag});
    STAMP(5,0);
    if(a<27){ const u4 srdO=mksrd((const char*)p.wout+a*(32*1280)+w*5120); const u32 mo=smb+w*5120; setm0<0>(mo);
      dma<0>(srdO,lane*16,0); dma<1024>(srdO,lane*16,0); dma<2048>(srdO,lane*16,0); dma<3072>(srdO,lane*16,0); setm0<4096>(mo); dma<0>(srdO,lane*16,4096); }
    #pragma unroll
    for(int i=0;i<10;i++){ u4 o;
      #pragma unroll
      for(int e=0;e<4;e++) o[e]=pkhf2(fma2(unpk(xr[i][e]),f2{ri,ri},f2{-m1*ri,-m1*ri}));
      ldsw128(smb+RB+(8*w+(lane>>3))*1296+(lane&7)*16+i*128,o); }
    WAITV(0); bar();
    #pragma unroll
    for(int i=0;i<10;i++){ const int c=w*10+i; const u32 ad=smb+RB+(32*((r>>1)&1)+8*q+(r>>2))*1296+c*16+(r&1)*8; u2 lo=ldstr(ad), hi=ldstr(ad+4*1296);
      const u32 sc=smb+40960+w*2048+(i&1)*1024; ldsw128(sc+(r&7)*128+((((r>>3)*4+q)^(r&7))<<4),u4{lo[0],lo[1],hi[0],hi[1]});
      st16(rs,OFF_XNT+a*81920+c*1024+lane*16,ldsr128(sc+(lane>>3)*128+(((lane&7)^(lane>>3))<<4))); }
    WAITV(0);
    if(arrive(smb+C_Z1,lane)==7u && lane==0) st4(rs,OFF_FX+a*128,tag);
    STAMP(6,7);
    if(a<27){
      #pragma unroll
      for(int i=0;i<5;i++){ u32 ad=smb+(8*q+(r>>2))*1280+(w*5+i)*32+(r&3)*8; u2 lo=ldstr(ad), hi=ldstr(ad+4*1280);
        st16(rs,OFF_WOF+(u32)((w*27+a)*5+i)*1024+lane*16,u4{lo[0],lo[1],hi[0],hi[1]}); } }
    WAITV(0); bar();
    if(tid==0) st4(rs,OFF_FW+a*128,tag);
    if(!g1){ pre01(); WAITV(0); }
    STAMP(31,0);
  } else {
    const int r=lane&31, h=lane>>5;
    u32 epv;
    u32 kx[10]; { u32 k=KROT;
      #pragma unroll
      for(int i=0;i<10;i++){ kx[i]=k*128; k=k==9?0:k+1; } }
    auto issue1=[&](auto sc,int k){ constexpr int s=sc.value;
      if(k==0){ setm0<(s&3)*STG>(mw); dma<0>(srdX,vx0,kx[s]); } if(k==1) dma<1024>(srdX,vx1,kx[s]); if(k==2) dma<2048>(srdW,vw2,kx[s]*1728); if(k==3) dma<3072>(srdW,vw1,kx[s]*1728); };
    auto issue=[&](auto sc){ issue1(sc,0); issue1(sc,1); issue1(sc,2); issue1(sc,3); };
    u32 pr[6];
    gload4<0>(epv,0u,p.ws+OFF_EP);
    gload4<0>(pr[0],lane*4,(const char*)(p.lar+dch*128)); gload4<0>(pr[1],lane*4,(const char*)(p.aim+dch*128)); gload4<0>(pr[2],lane*4,(const char*)(p.cre+dch*128)); gload4<0>(pr[3],lane*4,(const char*)(p.cim+dch*128));
    gload2(pr[4],0u,(const char*)(p.ldt+dch)); gload2(pr[5],0u,(const char*)(p.dsk+dch));
    asm volatile("s_barrier" ::: "memory");
    issue(std::integral_constant<int,1>{});
    if(tid<32) *(LAS u32*)(smb+PM+tid*4)=0u;
    u32 ax[2][4],aw[2][4][2];
    { u32 row=32*mi+r, a0=smb+(row>>4)*4096+(row&15)*128+((h^((r>>1)&7))<<4);
      u32 qq=(lane&15)>>2, w0=smb+2048+g1*1024+h*4096+qq*128+8*(lane&1)+(((2*((lane>>4)&1)+((lane>>1)&1))^((qq&2)<<1))<<4);
      #pragma unroll
      for(int i=0;i<2;i++)
        #pragma unroll
        for(int k=0;k<4;k++){ const u32 kk=(k+mi)&3, wo=i*2*STG; ax[i][k]=opq((a0^(kk*32u))+wo); aw[i][k][0]=opq(w0+kk*8192+wo); aw[i][k][1]=opq((w0^64u)+kk*8192+wo); } }
    u4 sel; { const u32 o=((lane&15)==((lane>>4)&1))?0x3f803f80u:0u; sel=u4{o,o,o,o}; asm volatile("" : "+v"(sel)); }
    u32 ga[11];
    #pragma unroll
    for(int i=0;i<11;i++) ga[i]=opq(cb+2u*(u32)rbf((float)(512-8*lane+3-i)));
    Frag F[2];
    asm volatile("s_waitcnt vmcnt(4)" : "+v"(epv),"+v"(pr[0]),"+v"(pr[1]),"+v"(pr[2]),"+v"(pr[3]),"+v"(pr[4]),"+v"(pr[5]) :: "memory");
    bar();
    tag=uni(epv)+TAGINC; dsk=bflo(pr[5]);
    #pragma unroll
    for(int k=0;k<4;k++){
      #pragma unroll
      for(int n=0;n<2;n++){ u2 lo=ldstr(aw[0][k][n]), hi=ldstr(aw[0][k][n]+512); F[0].w[k][n]=u4{lo[0],lo[1],hi[0],hi[1]}; }
      F[0].x[k]=ldsr128(ax[0][k]); }
    issue(std::integral_constant<int,2>{});
    STAMP(1,0);
    f16v acc[2]={}; f4 cacc[2]={}; u4 G;
    const u32 go=OFF_RS+rt*2048+mi*512, cp=smb+MISC+g1*1024;
    sfor<10>([&](auto ic){
      constexpr int s=ic.value; Frag& cur=F[s&1]; Frag& nxt=F[(s&1)^1];
      if constexpr(s<9){ WAITV(s==8? 3 : s==7? 7 : 4); asm volatile("s_barrier" ::: "memory"); }
      auto rw=[&](int k,int n){ if constexpr(s<9){ const int i=((s+1)>>1)&1; const u32 so=((s+1)&1)*STG; u2 lo=ldstr(aw[i][k][n]+so), hi=ldstr(aw[i][k][n]+so+512); nxt.w[k][n]=u4{lo[0],lo[1],hi[0],hi[1]}; } };
      auto rx=[&](int k){ if constexpr(s<9){ const int i=((s+1)>>1)&1; const u32 so=((s+1)&1)*STG; nxt.x[k]=ldsr128(ax[i][k]+so); } };
      #pragma unroll
      for(int k=0;k<4;k++){
        acc[0]=mfma32b(cur.w[k][0],cur.x[k],acc[0]); SCHED();
        if constexpr(s+3<=9) issue1(std::integral_constant<int,s+3>{},k);
        rw(k,0); rx(k);
        SCHED();
        acc[1]=mfma32b(cur.w[k][1],cur.x[k],acc[1]); SCHED();
        rw(k,1);
        if(k==0){ cacc[0]=mfma16(cur.w[0][0],sel,cacc[0]); cacc[1]=mfma16(cur.w[0][1],sel,cacc[1]); }
        if constexpr(s==6){ if(k==3){ gload<0>(G,r*16,p.ws+go); setm0<JNK>(smb);
            if(g1){ const u4 sp=mksrd((const char*)p.wsp+(size_t)rtB*64*4096); const u32 l0=(ct*4+mi)*74+lane, l1=(ct*4+mi)*74+64+(lane<10?lane:9);
              touch(sp,(l0<2048?l0:2047)*128,0u); touch(sp,(l1<2048?l1:2047)*128,0u); }
            else { touch(srdS,0u,go); touch(srdS,0u,go); } } }
        if constexpr(s==8){ if(k==3) signal(smb+C_DONE,lane); }
        if constexpr(s==9){ if(k==1){
          if((lane&14)==0){ *(LAS f4*)(cp+mi*256+(lane&1)*64+(lane>>4)*16)=cacc[0]; *(LAS f4*)(cp+mi*256+128+(lane&1)*64+(lane>>4)*16)=cacc[1]; }
          signal(smb+C_CS+g1*4,lane); } }
        SCHED();
      }
    });
    RST(0);
    if(!g1) __builtin_amdgcn_s_setprio(2);
    asm volatile("s_waitcnt vmcnt(2)" : "+v"(G) :: "memory");
    ldswait(smb+C_CS+g1*4,4u);
    const float cs0=*(LAS float*)(cp+h*512+r*4)+*(LAS float*)(cp+h*512+256+r*4), cs1=*(LAS float*)(cp+h*512+128+r*4)+*(LAS float*)(cp+h*512+384+r*4);
    RST(2);
    #pragma unroll 1
    for(int sp=0;sp<SPIN && !__all(G[1]==tag&&G[3]==tag);sp++)asm volatile("global_load_dwordx4 %0, %1, %2 sc1\n\ts_waitcnt vmcnt(0)" : "=&v"(G) : "v"(r*16), "s"(p.ws+go) : "memory");
    RST(3);
    const float nm=-__uint_as_float(G[0]), ri1=__uint_as_float(G[2]);
    acc[0]=__builtin_amdgcn_mfma_f32_32x32x2f32(cs0,nm,acc[0],0,0,0); acc[1]=__builtin_amdgcn_mfma_f32_32x32x2f32(cs1,nm,acc[1],0,0,0);
    u32 zp[2][8]; const u32 zqa=smb+RB+mi*RING+4096+lane*16;
    if(g1){
      ldswait(smb+C_DONE,8u);
      #pragma unroll
      for(int i=0;i<4;i++){ u4 o;
        #pragma unroll
        for(int e=0;e<4;e++) o[e]=pkbf2(f2{acc[i>>1][8*(i&1)+2*e],acc[i>>1][8*(i&1)+2*e+1]}*ri1);
        ldsw128(zqa+i*1024,o); }
      signal(smb+C_K2+mi*4,lane); RST(4);
    } else {

      #pragma unroll
      for(int e=0;e<16;e++) zp[e>>3][e&7]=pkbf2(gelu2(f2{acc[e>>3][2*(e&7)],acc[e>>3][2*(e&7)+1]}*ri1));
      float S=0.f,Q=0.f;
      if(j<13){
        #pragma unroll
        for(int e=0;e<8;e++){ S=dot2(zp[0][e],0x3f803f80u,S); Q=dot2(zp[0][e],zp[0][e],Q); } }
      #pragma unroll
      for(int e=0;e<8;e++){ S=dot2(zp[1][e],0x3f803f80u,S); Q=dot2(zp[1][e],zp[1][e],Q); }
      #define XSUM32(v) do{ auto t_=__builtin_amdgcn_permlane32_swap(__float_as_uint(v),__float_as_uint(v),false,false); v=__uint_as_float(t_[0])+__uint_as_float(t_[1]); }while(0)
      XSUM32(S); XSUM32(Q);
      const u32 sto=OFF_ST+rt*(14*2048)+(32*mi+r)*16;
      if(h==0) st16(rs,sto+j*2048,u4{__float_as_uint(S),tag,__float_as_uint(Q),tag});
      RST(6);
    }
    float kp[11]; u4 g[7]={};
    const u32 gof=OFF_ST+rt*(14*2048)+(32*mi+r)*16+h*(7*2048);
    auto samp=[&](){ asm volatile("" ::: "memory");
      #pragma unroll
      for(int i=0;i<7;i++) g[i]=ld16(rs,gof+i*2048);
      asm volatile("" ::: "memory"); };
    if(hasc) ksc=kgen_tab(cb,lane,pr,kp,[](){}); else if(!g1){
      pre01(); ldswait(smb+C_K2+mi*4,1u); u4 zq[4];
      #pragma unroll
      for(int i=0;i<4;i++){ const u4 v=ldsr128(zqa+i*1024);
        #pragma unroll
        for(int e=0;e<4;e++) zq[i][e]=pkbf2(gelu2(unpk(v[e]))); }
      SCHED(); samp();
      #pragma unroll
      for(int n=0;n<2;n++) if(2*j+n<27){ const u32 oo=OFF_Z1T+((rt*8+2*mi+(r>>4))*27+2*j+n)*1024+(h*16+(r&15))*16;
        st16(rs,oo,u4{zq[2*n][0],zq[2*n][1],zq[2*n+1][0],zq[2*n+1][1]}); st16(rs,oo+512,u4{zq[2*n][2],zq[2*n][3],zq[2*n+1][2],zq[2*n+1][3]}); }
    }
    if(g1){
      if(hasc){
        RST(6);
        kgen_exp(cb,lane,kp,ga);
        signal(smb+C_KP+mi*4,lane);
        RST(8);
        ldswait(smb+C_FULL,1u);
        RST(9);
      }
    } else {
      RST(1);
      float S,Q;
      #pragma unroll 1
      for(int sp=0;sp<SPIN;sp++){ bool ok=true;
        #pragma unroll
        for(int i=0;i<7;i++) ok=ok&&g[i][1]==tag&&g[i][3]==tag;
        if(__all(ok)) break;
        __builtin_amdgcn_s_sleep(4); samp(); }
      RST(7);
      S=0.f; Q=0.f;
      #pragma unroll
      for(int i=0;i<7;i++){ S+=__uint_as_float(g[i][0]); Q+=__uint_as_float(g[i][2]); }
      XSUM32(S); XSUM32(Q);
      float mean=S*(1.f/864.f), m2,r2; ln_fin(mean,Q*(1.f/864.f)-mean*mean,m2,r2); m2*=-r2;
      ldswait(smb+C_DONE,8u);
      const u32 tb=smb+RB+mi*RING, ad=tb+(8*(lane>>4)+((lane&15)>>2))*80+(lane&3)*8;
      const u32 zo=OFF_Z2F+((rt*4+mi)*54+(j==13?-2:2+4*j))*1024+lane*16;
      #pragma unroll
      for(int n=0;n<2;n++){
        #pragma unroll
        for(int g2=0;g2<4;g2++){ u2 o;
          #pragma unroll
          for(int e=0;e<2;e++) o[e]=pkbf2(fma2(unpk(zp[n][2*g2+e]),f2{r2,r2},f2{m2,m2}));
          *(LAS u2*)(tb+r*80+g2*16+h*8)=o; }
        u2 l0=ldstr(ad), h0=ldstr(ad+320), l1=ldstr(ad+32), h1=ldstr(ad+32+320);
        if(j<13||n){ st16(rs,zo+n*2048,u4{l0[0],l0[1],h0[0],h0[1]}); st16(rs,zo+n*2048+1024,u4{l1[0],l1[1],h1[0],h1[1]}); } }
      RST(8);
      WAITV(0);
      if(arrive(smb+C_Z2,lane)==3u && lane==0) st4(rs,OFF_F2+(rt*16+j)*128,tag);
      RST(9);
    }
  }
  const int ft0=ct<7?7*ct:47;
  bool adone=true; const u32 fao=OFF_FA+((lane<28?lane:0)*8+ct)*128;
  auto aload=[&](){ const u32 vo=(lane>>3)*4096+(lane&7)*16;
    #pragma unroll 1
    for(int i=0;i<10;i++){ setm0<ATL>(smb+i*1024); dma<0>(srdS,vo,OFF_AT+(u32)(80*ct+8*i)*4096+rtB*128); } };
  if(w>=4){
    if(hasc){
      conv_wave(p,rs,dch,cb,smb+C_KP+mi*4,smb+C_ZQ,STAG,lane,ksc,dsk);
      __builtin_amdgcn_s_setprio(0);
      if(arrive(smb+C_CV,lane)==(idx<24?2u:1u) && lane==0) st4(rs,OFF_FA+b*128,tag);
    }
    if(w==7){
      if(idx<28){
        __builtin_amdgcn_s_sleep(FXDLY);
        pollw<16>(rs,OFF_FX+(lane&31)*128,tag);
        u4 sw[3][4];
        #pragma unroll
        for(int c=0;c<3;c++)
          #pragma unroll
          for(int i=0;i<4;i++) sw[c][i]=ld16(rs,OFF_XNT+((lane>>3)+8*i)*81920+(80*xc+idx+((c<2||idx<24)?28*c:0))*128+(lane&7)*16);
        #pragma unroll
        for(int c=0;c<3;c++){
          #pragma unroll
          for(int i=0;i<4;i++){ const int x=lane+64*i; ldsw128(smb+c*CVS+UOF+(x&3)*1040+((x>>2)+1)*16,sw[c][i]); }
          if(lane<4) ldsw128(smb+c*CVS+UOF+lane*1040,u4{0,0,0,0}); }
        signal(smb+C_FULL,lane);
        RST(4);
        #pragma unroll 1
        for(int c=0;c<3;c++){
          #pragma unroll
          for(int i=0;i<4;i++){ sw[0][i]=sw[1][i]; sw[1][i]=sw[2][i]; }
          conv_win(smb+c*CVS,lane,sw[2]); }
        RST(5);
        __builtin_amdgcn_s_setprio(HPRIO);
        #pragma unroll 1
        for(int c=0;c<(idx<24?3:2);c++){ ldswait(smb+C_KP+c*4,1u); conv_help(smb+c*CVS,lane,sw[0]); signal(smb+C_KP+c*4,lane);
          #pragma unroll
          for(int i=0;i<4;i++){ sw[0][i]=sw[1][i]; sw[1][i]=sw[2][i]; } } }
      __builtin_amdgcn_s_setprio(0);
      RST(6);
    }
    RST(10);
    ldswait(smb+C_Z1R,4u);
    u4 wq[24];
    #pragma unroll
    for(int i=0;i<24;i++) wq[i]=ld16(rs,OFF_WOF+ct*(135*1024)+40960+mi*RING+((i==23&&mi==3)?22:i)*1024+lane*16);
    if(w<6){
      #pragma unroll 1
      for(u32 i=0;i<20480;i+=4096){ setm0<0>(cb+i); const u32 o=OFF_WOF+ct*(135*1024)+mi*20480+i; dma<0>(srdS,lane*16,o); dma<1024>(srdS,lane*16,o); dma<2048>(srdS,lane*16,o); dma<3072>(srdS,lane*16,o); } }
    if(w==6){ adone=__all(ld4(rs,fao)==tag); if(adone) aload(); }
    ldswait(smb+C_FREE,4u);
    #pragma unroll
    for(int i=0;i<24;i++) ldsw128(smb+RB+mi*RING+i*1024+lane*16,wq[i]);
    if(w<6) WAITV(0);
    signal(smb+C_WR,lane);
    if(!adone){ adone=__all(ld4(rs,fao)==tag); if(adone) aload(); }
    if(w==7 && b==255){
      #pragma unroll 1
      for(int sp=0;sp<SPIN;sp++){ const u32 o=OFF_FA+lane*128; u32 v0=ld4(rs,o),v1=ld4(rs,o+8192),v2=ld4(rs,o+16384),v3=ld4(rs,lane<32? o+24576 : OFF_FW+(lane-32)*128); asm volatile("" ::: "memory");
        if(__all(v0==tag&&v1==tag&&v2==tag&&v3==tag)) break; __builtin_amdgcn_s_sleep(4); }
      if(lane==0) st4(rs,OFF_EP,tag); }
  }
  int w2=w; asm volatile("" : "+s"(w2));
  if(w2<4){
    __builtin_amdgcn_s_setprio(G2PRIO);
    int r=lane&15, q=lane>>4;
    const u32 rb=smb+RB+w*RING;
    u4 srdA=mksrd((const char*)p.wsp+(size_t)rtB*64*4096+w*1024-4096+256);
    const char* zb=p.ws+OFF_Z2F+(u32)(16*w*54+ft0+3)*1024;
    u32 voA[2]; { u32 lr=lane>>3,c=lane&7; voA[0]=lr*4096+((c^(lr>>1))<<4); voA[1]=voA[0]^64u; }
    u32 sofa[8];
    #pragma unroll
    for(int g=0;g<8;g++) sofa[g]=((g+2*w)&7)*32768+4096-(g&3)*1024;
    const u32 voB=lane*16;
    u32 la[2]; la[0]=rb+r*128+((q^(r>>1))<<4); la[1]=la[0]^64u;
    f4 acc[4][7]={}; u4 B[NB][7]; u4 A[2][4]; u2 z1[7];
    auto issueA=[&](auto sc,auto gc){ constexpr int s=sc.value,g=gc.value; if constexpr((g&3)==0) setm0<((s+1)%3)*8192+g*1024>(rb); dma<(g&3)*1024>(srdA,voA[g&1],sofa[g]);
      if constexpr(g==7){ u64 a=((u64)srdA[1]<<32|srdA[0])+128; srdA[0]=(u32)a; srdA[1]=(u32)(a>>32); } };

    u32 bsr,fv;
    auto fof=[&](){ const u32 l=voB>>4; return l<27? OFF_FW+l*128 : OFF_F2+((rtB>>1)*16+(((ft0>>1)+(l&3))>>1))*128; };
    #pragma unroll
    for(int m=0;m<4;m++){ A[0][m]=ldsr128(la[0]+m*2048+8192); A[1][m]=ldsr128(la[1]+m*2048+8192); }
    sfor<8>([&](auto g){ issueA(std::integral_constant<int,2>{},g); });
    waitA(A[0]); waitA(A[1]);
    sfor<8>([&](auto g){ issueA(std::integral_constant<int,3>{},g); });
    #pragma unroll
    for(int m=0;m<4;m++)
      #pragma unroll
      for(int n=0;n<7;n++) asm volatile("" : "+v"(acc[m][n]));
    RST(2);
    { const int hf=(ft0>>1)+q; pollw(rs,OFF_F2+((4*w+(lane&3))*16+(hf==0?13:(hf-1)>>1))*128,tag); }
    RST(10);
    sfor<NB>([&](auto hh){ const char* s=zb+(u32)hh.value*(54*1024); sfor<7>([&](auto n){ gload<(n.value-3)*1024>(B[hh][n],voB,s); }); });
    auto redw=[&](auto nc){ constexpr int n=nc.value; *(LAS f4*)(rb+n*1024+voB)=acc[1][n]; *(LAS f4*)(rb+(7+n)*1024+voB)=acc[2][n]; *(LAS f4*)(rb+(14+n)*1024+voB)=acc[3][n]; };
    const char* z1b=p.ws+OFF_Z1T+(u32)((rtB*4+w)*27+(ft0>>1))*1024;
    sfor<16>([&](auto hc){
      constexpr int h=hc.value, S=h%NB, P=h&1, HZ=16-NB, cnt=g2cnt(h);
      waitB<cnt>(B[S]); waitA(A[P]);
      if constexpr(h==HZ){ tie1(fv); if(!__all(fv==tag)) pollw(rs,fof(),tag); }
      if constexpr(h+1<16 && h>0){
        #pragma unroll
        for(int m=0;m<4;m++) A[P^1][m]=ldsr128(la[(h+1)&1]+m*2048+((((h+1)>>1)+1)%3)*8192);
      }
      if constexpr(h==HZ-3) gload4<0>(fv,fof(),p.ws);
      const char* s=zb+(u32)(h+NB)*(54*1024);
      sfor<7>([&](auto n){
        mfma4(acc[0][n],acc[1][n],acc[2][n],acc[3][n],B[S][n],A[P]);
        if constexpr(h>=3 && h<=10 && (n.value&1)==0) issueA(std::integral_constant<int,(h-1)/2+3>{},std::integral_constant<int,((h-1)&1)*4+n.value/2>{});
        if constexpr(h+NB<16) gload_t<(n.value-3)*1024>(B[S][n],voB,s);
        if constexpr(h==HZ){ gload8<(n.value>>1)*1024>(z1[n],voB,z1b+((n.value&1)? ((ft0&1)?1024:8) : (ft0&1)*8));
          if constexpr(n.value==6) { gload2(bsr,(voB>>3)&30u,(const char*)(p.bsp+(rtB*4+w)*16)); signal(smb+C_Z1R,lane); } }
        if constexpr(h==15){ SCHED(); if constexpr(n.value>=2) redw(std::integral_constant<int,n.value-2>{}); SCHED(); }
      });
    });
    asm volatile("s_nop 15\n\ts_nop 3" ::: "memory");
    SCHED();
    STAMP(10,0);
    lane=__builtin_amdgcn_mbcnt_hi(~0u,__builtin_amdgcn_mbcnt_lo(~0u,0u)); asm volatile("" : "+v"(lane)); r=lane&15; q=lane>>4;
    redw(std::integral_constant<int,5>{}); redw(std::integral_constant<int,6>{});
    signal(smb+C_RED,lane);
    ldswait(smb+C_RED,4u);
    #pragma unroll
    for(int m=1;m<4;m++){ u32 src=smb+RB+((w-m)&3)*RING+(m-1)*7168+lane*16;
      #pragma unroll
      for(int n=0;n<7;n++) acc[0][n]+=*(LAS f4*)(src+n*1024); }
    asm volatile("s_waitcnt vmcnt(0) lgkmcnt(0)" : "+v"(z1[0]),"+v"(z1[1]),"+v"(z1[2]),"+v"(z1[3]),"+v"(z1[4]),"+v"(z1[5]),"+v"(z1[6]),"+v"(bsr) :: "memory");
    const float bs=bflo(bsr);
    signal(smb+C_FREE,lane);
    u2 o[7];
    #pragma unroll
    for(int n=0;n<7;n++)
      #pragma unroll
      for(int e=0;e<2;e++)o[n][e]=pkbf2(unpk(z1[n][e])*(f2{acc[0][n][2*e],acc[0][n][2*e+1]}+bs));
    const u32 ho=OFF_HF+(u32)((rtB*4+w)*54+ft0)*512+(q>>1)*256+r*16;
    #pragma unroll
    for(int n=0;n<6;n+=2){ auto sx=__builtin_amdgcn_permlane16_swap(o[n][0],o[n+1][0],false,false); auto sy=__builtin_amdgcn_permlane16_swap(o[n][1],o[n+1][1],false,false);
      if(ct<7||n>=2) st16(rs,ho+(n+(q&1))*512,u4{sx[0],sy[0],sx[1],sy[1]}); }
    st8(rs,ho+6*512+(q&1)*8,o[6]);
    STAMP(11,0);
    WAITV(0);
    if(arrive(smb+C_H,lane)==3u && lane==0) st4(rs,OFF_FH+(rtB*8+ct)*128,tag);
    STAMP(12,0);
    __builtin_amdgcn_s_setprio(0);
  }
  { const int r=lane&15, q=lane>>4, m=w&3, kh=w>>2;
    u2 xv[5]={};
    if(kh){
      #pragma unroll
      for(int n=0;n<5;n++) xv[n]=*(const u2*)(p.x+(rtB*64+m*16+r)*640+80*ct+16*n+4*q);
    }
    f4 acc[5]={}; u4 W[3][5];
    const u32 wl2=smb+lane*16+RB-40960+kh*14*5120, wl0=kh? wl2 : smb+lane*16, wl1=kh? wl2 : smb+lane*16+CVS-20480;
    #define WAD(k,n) (((k)<4? wl0 : (k)<8? wl1 : wl2)+((k)*5+(n))*1024)
    f4 ax[5]={};
    auto addax=[&](){
      #pragma unroll
      for(int n=0;n<5;n++){ u2 av=ldstr(smb+ATL+(16*n+4*q+(r>>2))*128+(16*m+4*(r&3))*2); ax[n]=f4{bflo(av[0]),bfhi(av[0]),bflo(av[1]),bfhi(av[1])}+f4{bflo(xv[n][0]),bfhi(xv[n][0]),bflo(xv[n][1]),bfhi(xv[n][1])}; } };
    if(w==6 && adone){ WAITV(0); signal(smb+C_AT,lane); }
    u32 oo[3],to[3]; const u32 tb=smb+CVS+m*3072;
    #pragma unroll
    for(int i=0;i<3;i++){ const int it=lane+64*i, rr=(it*205)>>11, cc=it-rr*10; oo[i]=opq((rtB*64+m*16+rr)*1280+ct*160+cc*16); to[i]=opq(tb+rr*176+cc*16); }
    if(m==0){ ldswait(smb+C_H,4u); pollw(rs,OFF_FH+(rtB*8+kh*4+(lane&3))*128,tag); signal(smb+C_TAG+kh*4,lane); } else ldswait(smb+C_TAG+kh*4,1u);
    STAMP(16,0);
    u4 hf[14];
    const u32 ho=OFF_HF+(u32)((rtB*4+m)*27+kh*14)*1024+lane*16;
    #pragma unroll
    for(int k=0;k<HPF;k++) hf[k]=ld16(rs,ho+k*1024);
    const bool early=kh && uni(ldsv(smb+C_AT))!=0u;
    if(early) addax();
    ldswait(smb+C_WR,4u);
    STAMP(17,0);
    const u32 wl3=kh? wl2-5120 : wl2;
    #pragma unroll
    for(int n=0;n<10;n++) W[n/5][n%5]=ldsr128(WAD(n/5,n%5));
    #pragma unroll
    for(int k=0;k<13;k++){
      if(k+HPF<14) hf[k+HPF]=ld16(rs,ho+((k+HPF==13&&kh)?12:k+HPF)*1024);
      SCHED();
      #pragma unroll
      for(int n=0;n<5;n++){ acc[n]=mfma16(W[k%3][n],hf[k],acc[n]); SCHED();
        if(k<11) W[(k+2)%3][n]=ldsr128(WAD(k+2,n)); else if(k==11) W[1][n]=ldsr128(wl3+(65+n)*1024);
        SCHED(); }
      if(k==5 && !kh) signal(smb+C_G1,lane);
    }
    const u32 pb=smb+m*5120+lane*16;
    if(!kh){
      #pragma unroll
      for(int n=0;n<5;n++) acc[n]=mfma16(W[1][n],hf[13],acc[n]);
      STAMP(18,0);
      ldswait(smb+C_G1,4u);
      #pragma unroll
      for(int n=0;n<5;n++) *(LAS f4*)(pb+n*1024)=acc[n];
      signal(smb+C_P+m*4,lane);
    } else {
      if(w==6 && !adone){ pollw(rs,fao,tag); aload(); WAITV(0); signal(smb+C_AT,lane); }
      if(!early){ ldswait(smb+C_AT,1u); addax(); }
      RST(7);
      ldswait(smb+C_P+m*4,1u);
      #pragma unroll
      for(int n=0;n<5;n++){ const f4 o=(acc[n]+ax[n])+*(LAS f4*)(pb+n*1024); *(LAS u2*)(tb+r*176+n*32+q*8)=u2{pkbf(o[0],o[1]),pkbf(o[2],o[3])}; }
      #pragma unroll
      for(int i=0;i<3;i++) if(i<2||lane<32) __builtin_amdgcn_raw_buffer_store_b128(ldsr128(to[i]),__builtin_amdgcn_make_buffer_rsrc(p.out,0,2048*1280,0x00020000),oo[i],0,16);
      STAMP(19,4);
    }
  }
}
extern "C" void kernel_launch(void* const* d_in,const int* in_sizes,int n_in,void* d_out,int out_size,void* d_ws,size_t ws_size,hipStream_t stream){
  Params p;
  p.x=(const u16*)d_in[0]; p.lar=(const u16*)d_in[1]; p.aim=(const u16*)d_in[2]; p.cre=(const u16*)d_in[3]; p.cim=(const u16*)d_in[4];
  p.ldt=(const u16*)d_in[5]; p.dsk=(const u16*)d_in[6]; p.win=(const u16*)d_in[8]; p.wsp=(const u16*)d_in[9]; p.bsp=(const u16*)d_in[10]; p.wout=(const u16*)d_in[11];
  p.out=(u16*)d_out; p.ws=(char*)d_ws;
  (void)hipFuncSetAttribute((const void*)fused,hipFuncAttributeMaxDynamicSharedMemorySize,LDS_TOTAL);
  fused<<<256,512,LDS_TOTAL,stream>>>(p);
}
```

```cpp
#include <hip/hip_runtime.h>
#include <utility>
#pragma clang diagnostic ignored "-Wint-to-pointer-cast"
typedef unsigned short u16;
typedef unsigned int u32;
typedef unsigned long long u64;
typedef __attribute__((ext_vector_type(8))) __bf16 bf8;
typedef __attribute__((ext_vector_type(8))) _Float16 hf8;
typedef __attribute__((ext_vector_type(2))) __bf16 bf2;
typedef __attribute__((ext_vector_type(2))) _Float16 hf2;
typedef __attribute__((ext_vector_type(2))) float f2;
typedef __attribute__((ext_vector_type(4))) float f4;
typedef __attribute__((ext_vector_type(16))) float f16v;
typedef __attribute__((ext_vector_type(4))) u32 u4;
typedef __attribute__((ext_vector_type(2))) u32 u2;
typedef __attribute__((ext_vector_type(4))) short s4;
#define LAS __attribute__((address_space(3)))
#define DI __device__ __forceinline__

constexpr u32 OFF_Z2F = 0;
constexpr u32 OFF_Z1T = OFF_Z2F + 64*54*1024;
constexpr u32 OFF_XNT = OFF_Z1T + 128*27*1024;
constexpr u32 OFF_WOF = OFF_XNT + 640*4096;
constexpr u32 OFF_HF  = OFF_WOF + 8*27*5*1024;
constexpr u32 OFF_AT  = OFF_HF + 128*27*1024;
constexpr u32 OFF_ST  = OFF_AT + 640*4096;
constexpr u32 OFF_RS  = OFF_ST + 16*14*2048+8192;
constexpr u32 OFF_EP  = OFF_RS + 2048*16;
constexpr u32 OFF_F2  = OFF_EP + 256;
constexpr u32 OFF_FX  = OFF_F2 + 256*128;
constexpr u32 OFF_FW  = OFF_FX + 32*128;
constexpr u32 OFF_FH  = OFF_FW + 32*128;
constexpr u32 OFF_FA  = OFF_FH + 256*128;
constexpr u32 WS_END  = OFF_FA + 256*128;

struct Params {
  const u16 *x, *lar, *aim, *cre, *cim, *ldt, *dsk, *win, *wsp, *bsp, *wout;
  u16* out; char* ws;
};
template<class F,int... I> DI void sfor_impl(F&& f, std::integer_sequence<int,I...>){ (f(std::integral_constant<int,I>{}),...); }
template<int N,class F> DI void sfor(F&& f){ sfor_impl(f, std::make_integer_sequence<int,N>{}); }

DI float bflo(u32 v){ return __uint_as_float(v<<16); }
DI float bfhi(u32 v){ return __uint_as_float(v&0xffff0000u); }
DI u32 pkbf(float a,float b){ f2 v={a,b}; return __builtin_bit_cast(u32,__builtin_convertvector(v,bf2)); }
DI u32 pkhf(float a,float b){ f2 v={a,b}; return __builtin_bit_cast(u32,__builtin_convertvector(v,hf2)); }
DI float rbf(float a){ return bflo(pkbf(a,a)); }
DI float hlo(u32 v){ return (float)__builtin_bit_cast(hf2,v)[0]; }
DI float hhi(u32 v){ return (float)__builtin_bit_cast(hf2,v)[1]; }
DI float dot2(u32 a,u32 b,float c){ return __builtin_amdgcn_fdot2_f32_bf16(__builtin_bit_cast(bf2,a),__builtin_bit_cast(bf2,b),c,false); }
DI f4 mfma16(u4 a,u4 b,f4 c){ return __builtin_amdgcn_mfma_f32_16x16x32_bf16(__builtin_bit_cast(bf8,a),__builtin_bit_cast(bf8,b),c,0,0,0); }
DI f16v mfma32h(u4 a,u4 b,f16v c){ return __builtin_amdgcn_mfma_f32_32x32x16_f16(__builtin_bit_cast(hf8,a),__builtin_bit_cast(hf8,b),c,0,0,0); }
DI u4 ldsr128(u32 a){ return *(LAS u4*)a; }
DI u2 ldsr64(u32 a){ return *(LAS u2*)a; }
DI void ldsw128(u32 a,u4 v){ *(LAS u4*)a=v; }
DI u2 ldstr(u32 a){ return __builtin_bit_cast(u2,__builtin_amdgcn_ds_read_tr16_b64_v4i16((LAS s4*)a)); }
DI u32 uni(u32 v){ return __builtin_amdgcn_readfirstlane(v); }

template<int OFF> DI void gload(u4& dst,u32 voff,const char* base){ asm volatile("global_load_dwordx4 %0, %1, %2 offset:%3 sc1" : "=v"(dst) : "v"(voff), "s"(base), "n"(OFF)); }
template<int OFF> DI void gload_t(u4& dst,u32 voff,const char* base){ asm volatile("global_load_dwordx4 %0, %1, %2 offset:%3 sc1" : "+v"(dst) : "v"(voff), "s"(base), "n"(OFF)); }
DI void mfma4(f4& c0,f4& c1,f4& c2,f4& c3,const u4& a,const u4 (&b)[4]){
  asm volatile("v_mfma_f32_16x16x32_bf16 %0, %4, %5, %0\n\tv_mfma_f32_16x16x32_bf16 %1, %4, %6, %1\n\tv_mfma_f32_16x16x32_bf16 %2, %4, %7, %2\n\tv_mfma_f32_16x16x32_bf16 %3, %4, %8, %3"
    : "+v"(c0),"+v"(c1),"+v"(c2),"+v"(c3) : "v"(a),"v"(b[0]),"v"(b[1]),"v"(b[2]),"v"(b[3])); }
template<int CNT> DI void waitB(u4 (&B)[7]){ asm volatile("s_waitcnt vmcnt(%7)" : "+v"(B[0]),"+v"(B[1]),"+v"(B[2]),"+v"(B[3]),"+v"(B[4]),"+v"(B[5]),"+v"(B[6]) : "n"(CNT) : "memory"); }
DI void waitA(u4 (&A)[4]){ asm volatile("s_waitcnt lgkmcnt(0)" : "+v"(A[0]),"+v"(A[1]),"+v"(A[2]),"+v"(A[3]) :: "memory"); }
#define WAITV(n) asm volatile("s_waitcnt vmcnt(%0)" :: "n"(n) : "memory")
#define WAITL0() asm volatile("s_waitcnt lgkmcnt(0)" ::: "memory")
#define CFENCE() asm volatile("" ::: "memory")
#define SCHED() __builtin_amdgcn_sched_barrier(0)
DI void bar(){ asm volatile("s_waitcnt lgkmcnt(0)\n\ts_barrier" ::: "memory"); }
DI f2 unpk(u32 v){ return f2{bflo(v),bfhi(v)}; }
DI u32 pkbf2(f2 v){ return __builtin_bit_cast(u32,__builtin_convertvector(v,bf2)); }
DI u32 pkhf2(f2 v){ return __builtin_bit_cast(u32,__builtin_convertvector(v,hf2)); }
DI f2 fma2(f2 a,f2 b,f2 c){ return __builtin_elementwise_fma(a,b,c); }
DI f2 gelu2(f2 x){
  f2 u=x*fma2(x*x,f2{-0.10294324f,-0.10294324f},f2{-2.30220820f,-2.30220820f});
  f2 d=f2{__builtin_amdgcn_exp2f(u[0]),__builtin_amdgcn_exp2f(u[1])}+f2{1.f,1.f};
  return x*f2{__builtin_amdgcn_rcpf(d[0]),__builtin_amdgcn_rcpf(d[1])};
}
DI void ln_fin(float mean,float var,float& m,float& rinv){
  m=rbf(mean); float v=rbf(__builtin_fmaxf(var,0.f));
  float s=rbf(__builtin_amdgcn_sqrtf(rbf(v+1e-5f))); float r=__builtin_amdgcn_rcpf(s); rinv=__builtin_fmaf(__builtin_fmaf(-s,r,1.f),r,r);
}
typedef __amdgpu_buffer_rsrc_t rsrc_t;
DI void st16(rsrc_t rs,u32 off,u4 v){ __builtin_amdgcn_raw_buffer_store_b128(v,rs,off,0,16); }
DI void st8(rsrc_t rs,u32 off,u2 v){ __builtin_amdgcn_raw_buffer_store_b64(v,rs,off,0,16); }
DI void st4(rsrc_t rs,u32 off,u32 v){ __builtin_amdgcn_raw_buffer_store_b32(v,rs,off,0,16); }
DI u4 ld16(rsrc_t rs,u32 off){ return __builtin_amdgcn_raw_buffer_load_b128(rs,off,0,16); }
DI u32 ld4(rsrc_t rs,u32 off){ return __builtin_amdgcn_raw_buffer_load_b32(rs,off,0,16); }
constexpr int SPIN=1<<17;
constexpr u32 TAGINC=0x9E3779B9u;
template<int SL=2> DI void pollw(rsrc_t rs,u32 off,u32 tag){
  #pragma unroll 1
  for(int s=0;s<SPIN;s++){ u32 v=ld4(rs,off); asm volatile("" ::: "memory"); if(__all(v==tag)) break; __builtin_amdgcn_s_sleep(SL); }
}

DI f16v mfma32b(u4 a,u4 b,f16v c){ return __builtin_amdgcn_mfma_f32_32x32x16_bf16(__builtin_bit_cast(bf8,a),__builtin_bit_cast(bf8,b),c,0,0,0); }
DI u4 mksrd(const void* b){ u64 a=(u64)b; return u4{(u32)a,(u32)(a>>32)&0xffffu,0xffffffffu,0x00020000u}; }
template<int IMM> DI void setm0(u32 base){ asm volatile("s_add_u32 m0, %0, %1\n\ts_nop 0" :: "s"(base), "n"(IMM) : "scc","memory"); }
template<int OFF> DI void dma(u4 srd,u32 voff,u32 soff){ asm volatile("buffer_load_dwordx4 %0, %1, %2 offen offset:%3 sc1 lds" :: "v"(voff), "s"(srd), "s"(soff), "n"(OFF) : "memory"); }
DI void touch(u4 srd,u32 voff,u32 soff){ asm volatile("buffer_load_dword %0, %1, %2 offen sc1 lds" :: "v"(voff), "s"(srd), "s"(soff) : "memory"); }
template<int OFF> DI void gload8(u2& dst,u32 voff,const char* base){ asm volatile("global_load_dwordx2 %0, %1, %2 offset:%3 sc1" : "=v"(dst) : "v"(voff), "s"(base), "n"(OFF)); }
DI void gload2(u32& dst,u32 voff,const char* base){ asm volatile("global_load_ushort %0, %1, %2" : "=v"(dst) : "v"(voff), "s"(base)); }
DI u32 arrive(u32 a,int){ u32 o; asm volatile("s_mov_b64 exec, 1\n\tds_add_rtn_u32 %0, %1, %2\n\ts_mov_b64 exec, -1\n\ts_waitcnt lgkmcnt(0)" : "=&v"(o) : "v"(a), "v"(1u) : "memory"); return uni(o); }
template<int SL=1> DI void ldswait(u32 a,u32 n){ while(__hip_atomic_load((LAS u32*)a,__ATOMIC_RELAXED,__HIP_MEMORY_SCOPE_WORKGROUP)<n) __builtin_amdgcn_s_sleep(SL); asm volatile("" ::: "memory"); }
DI float addf(float a,float b){ float o; asm("v_add_f32 %0, %1, %2" : "=v"(o) : "v"(a), "v"(b)); return o; }
DI void tie1(u32& g){ asm volatile("" : "+v"(g) :: "memory"); }
DI u32 opq(u32 v){ asm volatile("" : "+v"(v)); return v; }

constexpr u32 STG=32768;
constexpr u32 RING=24576, RB=62848, CONV=0, KP=2208, UOF=16768, CVS=UOF+4160, S7O=4*KP, S9O=S7O+2112, KZO=S9O+2112, MISC=RB+4*RING, JNK=MISC+2048, PM=163840-128, LDS_TOTAL=163840;
constexpr u32 ATL=2*CVS;
constexpr u32 C_FULL=PM, C_G1=PM+4, C_Z2=PM+8, C_Z1=PM+12, C_RED=PM+16, C_H=PM+20, C_FREE=PM+24, C_CV=PM+28, C_Z1R=PM+32, C_TAG=PM+40, C_KP=PM+48, C_DONE=PM+64, C_CS=PM+72, C_K2=PM+80, C_P=PM+96, C_AT=PM+112, C_WR=PM+116;
constexpr u32 CODE_BYTES=39424;
constexpr int HPF=5;
DI void signal(u32 a,int){ asm volatile("s_mov_b64 exec, 1\n\tds_add_u32 %0, %1\n\ts_mov_b64 exec, -1" :: "v"(a), "v"(1u) : "memory"); }
template<int OFF> DI void gload4(u32& dst,u32 voff,const char* base){ asm volatile("global_load_dword %0, %1, %2 offset:%3 sc1" : "=v"(dst) : "v"(voff), "s"(base), "n"(OFF)); }

DI f2 cmulp(f2 a,f2 b,f2 bs){ return fma2(f2{a[1],a[1]},bs,f2{a[0],a[0]}*b); }
DI float mulf(float a,float b){ float o; asm("v_mul_f32 %0, %1, %2" : "=v"(o) : "v"(a),"v"(b)); return o; }
DI float fmaf1(float a,float b,float c){ float o; asm("v_fma_f32 %0, %1, %2, %3" : "=v"(o) : "v"(a),"v"(b),"v"(c)); return o; }
DI u32 ldsv(u32 a){ return __hip_atomic_load((LAS u32*)a,__ATOMIC_RELAXED,__HIP_MEMORY_SCOPE_WORKGROUP); }
DI f2 cpow_dir(float lr,float rv,float pw){ float e=__builtin_amdgcn_exp2f(lr*pw); float f=__builtin_amdgcn_fractf(rv*pw);
  return f2{e*__builtin_amdgcn_cosf(f),e*__builtin_amdgcn_sinf(f)}; }
DI u32 dup(u32 a){ return __builtin_amdgcn_perm(a,a,0x01000100u); }
DI u32 cat(u32 lo,u32 hi){ return __builtin_amdgcn_perm(hi,lo,0x05040100u); }
DI u32 lds16(u32 a){ return *(LAS u16*)a; }

DI float kgen_tab(u32 cb,int lane,const u32 (&pr)[6],float (&kp)[11]){
  const float dt=rbf(__expf(bflo(pr[4])));
  const float cr[2]={bflo(pr[2]),bfhi(pr[2])}, cm[2]={bflo(pr[3]),bfhi(pr[3])};
  float mx=fmaxf(fmaxf(fabsf(cr[0]),fabsf(cm[0])),fmaxf(fabsf(cr[1]),fabsf(cm[1])));
  u32 eb=__float_as_uint(mx)&0x7f800000u;
  #define DPPMAX(c) eb=max(eb,(u32)__builtin_amdgcn_update_dpp(0,(int)eb,c,0xf,0xf,false))
  DPPMAX(0x128); DPPMAX(0x124); DPPMAX(0x122); DPPMAX(0x121);
  eb=max(max((u32)__builtin_amdgcn_readlane(eb,0),(u32)__builtin_amdgcn_readlane(eb,16)),max((u32)__builtin_amdgcn_readlane(eb,32),(u32)__builtin_amdgcn_readlane(eb,48))); eb=eb<0x01000000u?0x3f800000u:(eb>0x7e000000u?0x7e000000u:eb);
  const float ksc=2.f*dt*__uint_as_float(eb), isc=__uint_as_float(0x7f000000u-eb);
  const int r=lane&31,h=lane>>5;
  const u32 TP=272, TA=cb, TB=cb+22*TP;
  f16v acc={};
  f2 an[2][4];
  #pragma unroll
  for(int c=0;c<2;c++){ const float l_a=c?bfhi(pr[0]):bflo(pr[0]), i_m=c?bfhi(pr[1]):bflo(pr[1]);
    const float lr=(-rbf(__expf(l_a))*dt)*1.44269504f, rv=(i_m*dt)*0.15915494f;
    an[c][0]=cpow_dir(lr,rv,1.f); an[c][1]=cpow_dir(lr,-rv,8.f); an[c][2]=cpow_dir(lr,-rv,16.f); an[c][3]=cpow_dir(lr,rv,256.f); }
  u32 hold[54]; const u32 wt=TA+lane*4;
  struct P2 { f2 v[2]; };
  auto mul=[&](const P2& x,int k){ P2 o;
    #pragma unroll
    for(int c=0;c<2;c++) o.v[c]=cmulp(x.v[c],an[c][k],f2{-an[c][k][1],an[c][k][0]});
    return o; };
  auto put=[&](int row,const P2& x){ *(LAS u32*)(wt+row*TP)=pkhf2(x.v[0]); hold[row]=pkhf2(x.v[1]); };
  P2 e={{f2{1.f,0.f},f2{1.f,0.f}}}, o={{an[0][1],an[1][1]}}, bk[8], t;
  bk[0].v[0]=f2{cr[0]*isc,cm[0]*isc}; bk[0].v[1]=f2{cr[1]*isc,cm[1]*isc}; put(0,bk[0]); t=bk[0];
  constexpr int tr[8]={8,10,11,16,17,20,21,18};
  #pragma unroll
  for(int k=0;k<16;k++){ put(22+2*k,e); put(23+2*k,o);
    if(k<15){ if(k) e=mul(e,2); else { e.v[0]=an[0][2]; e.v[1]=an[1][2]; } o=mul(o,2); }
    if(k<8){ t=mul(t,3); put(tr[k],t); }
    if(k<7){ bk[k+1]=mul(bk[k],0); put(k+1,bk[k+1]); }
    if(k==8){ put(9,mul(bk[2],3)); put(13,mul(bk[6],3)); t=mul(bk[4],3); put(12,t); }
    if(k==9||k==10){ t=mul(t,3); put(5+k,t); }
    SCHED(); }
  const int ra=(r>21||r==19)?0:r;
  #pragma unroll
  for(int c=0;c<2;c++){
    asm volatile("" ::: "memory");
    u4 fa[8],fb[8];
    #pragma unroll
    for(int ks=0;ks<8;ks++){ fa[ks]=ldsr128(TA+ra*TP+ks*32+h*16); fb[ks]=ldsr128(TB+r*TP+ks*32+h*16); }
    asm volatile("" ::: "memory");
    if(c==0){
      #pragma unroll
      for(int i=0;i<54;i++) if(i!=19) *(LAS u32*)(wt+i*TP)=hold[i]; }
    #pragma unroll
    for(int ks=0;ks<8;ks++) acc=mfma32h(fa[ks],fb[ks],acc);
  }
  asm volatile("" ::: "memory");
  #pragma unroll
  for(int i=0;i<11;i++) kp[i]=acc[i];
  return ksc;
}
DI void kgen_exp(u32 cb,int lane,const float (&acc)[11],const u32 (&ga)[11]){
  const int r=lane&31,h=lane>>5;
  { const u32 a4=cb+r*16+h*8, a5=cb+r*16+h*1024, az=cb+KZO+r*2+h*128;
    #define PK(addr,base,reg) do{ *(LAS _Float16*)((addr)+2*(base))=(_Float16)acc[reg]; }while(0)
    PK(a4,0,0);PK(a4,1,1);PK(a4,2,2);PK(a4,3,3); PK(a4,256,4);PK(a4,258,5);PK(a4,512,6);PK(a4,768,7);
    PK(a5,1024,8); PK(az,255,10); PK(az,127,8); PK(az,159,9);
    #undef PK
    CFENCE();
    if(lane<16) ldsw128(cb+KZO+lane*16,u4{0,0,0,0});
    CFENCE();
  }
  { const u32 g0=cb+2*(1024-8*lane);
    u32 f2_=lds16(g0+(lane?8:0)), a2=lds16(g0), m2=lds16(g0-8), b2=lds16(g0-16);
    u32 e[11];
    #pragma unroll
    for(int j=0;j<11;j++) e[j]=lds16(ga[j]);
    u32 k01=*(LAS u32*)cb, k23=*(LAS u32*)(cb+4), kl=lds16(cb+2048);
    WAITL0();
    const u32 wo=cb+96+lane*16;
    { u32 aa=dup(a2),mm=dup(m2),bb=dup(b2),am=cat(a2,m2),fa=cat(f2_,a2),mb=cat(m2,b2);
      ldsw128(wo,u4{aa,am,mm,bb}); ldsw128(wo+KP,u4{aa,aa,mm,mb}); ldsw128(wo+2*KP,u4{aa,aa,am,mm}); ldsw128(wo+3*KP,u4{fa,aa,aa,mm}); }
    { u32 o0=cat(e[0],e[1]),o1=cat(e[2],e[3]),o2=cat(e[4],e[5]),o3=cat(e[6],e[7]),o4=cat(e[8],e[9]);
      u32 p1=cat(e[1],e[2]),d0=cat(e[3],e[4]),d1=cat(e[5],e[6]),d2=cat(e[7],e[8]),d3=cat(e[9],e[10]);
      ldsw128(wo+1024,u4{d0,d1,d2,d3}); ldsw128(wo+1024+KP,u4{o1,o2,o3,o4}); ldsw128(wo+1024+2*KP,u4{p1,d0,d1,d2}); ldsw128(wo+1024+3*KP,u4{o0,o1,o2,o3}); }
    if(lane<10){ if(lane){k01=0;k23=0;} if(lane!=9) kl=0;
      const u32 d0=k01&0xffffu, o1=__builtin_amdgcn_alignbit(k01,k01,16), o0=__builtin_amdgcn_alignbit(k23,k23,16), p1=(k23&0xffffu)|(k01&0xffff0000u), kk=dup(kl), zk=kl<<16;
      const u32 w2=lane<4? wo+2048 : wo-160;
      ldsw128(w2,u4{d0,0,kk,kk}); ldsw128(w2+KP,u4{o1,0,zk,kk}); ldsw128(w2+2*KP,u4{p1,d0,0,kk}); ldsw128(w2+3*KP,u4{o0,o1,0,zk}); }
  }
}
DI f4 mfma16h(u4 a,u4 b,f4 c){ return __builtin_amdgcn_mfma_f32_16x16x32_f16(__builtin_bit_cast(hf8,a),__builtin_bit_cast(hf8,b),c,0,0,0); }
DI u4 lds2x64(u32 a){ const u2 lo=ldsr64(a), hi=ldsr64(a+8); return u4{lo[0],lo[1],hi[0],hi[1]}; }
DI void conv_win(u32 cb,int lane,u4 (&sw)[4]){
  float P[25]; P[0]=0.f;
  #pragma unroll
  for(int c=0;c<4;c++){ const int X=2*lane-2+c; const u4 v=ldsr128(cb+UOF+(X&3)*1040+((X>>2)+1)*16);
    #pragma unroll
    for(int e=0;e<8;e++){ const int i=8*c+e-4; if(i>=0&&i<24) P[i+1]=P[i]+((e&1)?hhi(v[e>>1]):hlo(v[e>>1])); } }
  #pragma unroll
  for(int k=0;k<8;k++){ sw[k>>2][k&3]=pkhf(P[2*k+8]-P[2*k+1],P[2*k+9]-P[2*k+2]); sw[2+(k>>2)][k&3]=pkhf(P[2*k+9]-P[2*k],P[2*k+10]-P[2*k+1]); }
}
DI void conv_help(u32 cb,int lane,const u4 (&sw)[4]){
  const int r=lane&15,q=lane>>4; f4 y[2][2]={};
  ldsw128(cb+S7O+48+lane*32,sw[0]); ldsw128(cb+S7O+64+lane*32,sw[1]); ldsw128(cb+S9O+48+lane*32,sw[2]); ldsw128(cb+S9O+64+lane*32,sw[3]);
  if(lane<3){ ldsw128(cb+S7O+lane*16,u4{0,0,0,0}); ldsw128(cb+S9O+lane*16,u4{0,0,0,0}); }
  CFENCE();
  const u32 ka=cb+(r&3)*KP+16*q-8*(r>>2)+64, ub=cb+UOF+q*1040+(r+1)*16;
  const u4 a0=lds2x64(ka+32), a1=lds2x64(ka), b0=ldsr128(ub), b1=ldsr128(ub+256), a2=lds2x64(ka-32), b2=ldsr128(ub-16), b3=ldsr128(ub+240);
  const u32 sa=cb+(((r>>2)&1)?S9O:S7O)+2*(64*q+24-8*(r>>2)+4*(r&3)), kz=cb+KZO+8*(r-2*q+23); u4 bz[4],fa[4][2];
  #pragma unroll
  for(int ks=0;ks<4;ks++) bz[ks]=lds2x64(kz+(3-ks)*64);
  #pragma unroll
  for(int i=0;i<8;i++){ const u2 lo=ldstr(sa+(i>>1)*512+(i&1)*32+64), hi=ldstr(sa+(i>>1)*512+(i&1)*32); fa[i>>1][i&1]=u4{lo[0],lo[1],hi[0],hi[1]}; }
  SCHED();
  y[0][0]=mfma16h(a2,b2,y[0][0]); y[0][1]=mfma16h(a2,b3,y[0][1]);
  y[0][0]=mfma16h(a0,b0,y[0][0]); y[1][0]=mfma16h(a1,b0,y[1][0]); y[0][1]=mfma16h(a0,b1,y[0][1]); y[1][1]=mfma16h(a1,b1,y[1][1]);
  #pragma unroll
  for(int ks=0;ks<4;ks++)
    #pragma unroll
    for(int m=0;m<2;m++){ y[m][1]=mfma16h(fa[ks][m],bz[ks],y[m][1]); if(ks<2) y[m][0]=mfma16h(fa[ks][m],bz[ks+2],y[m][0]); }
  CFENCE();
  #pragma unroll
  for(int i=0;i<4;i++) *(LAS f4*)(cb+S7O+i*1024+lane*16)=y[i&1][i>>1];
}
#define RDA(S,i,m) asm volatile("ds_read2_b64 %0, %1 offset0:%2 offset1:%3" : "=v"(AF[S][i][m]) : "v"(ka), "n"((1-(i))*128+(1-(m))*4), "n"((1-(i))*128+(1-(m))*4+1))
#define RDB(S,i,ad,o) asm volatile("ds_read_b128 %0, %1 offset:%2" : "=v"(BF[S][i]) : "v"(ad), "n"(o))
#define TIEA(S,i) "+v"(AF[S][i][0]),"+v"(AF[S][i][1])
#define CV_M(S,i,n) do{ y[0][n]=mfma16h(AF[S][i][0],BF[S][(n)-(i)],y[0][n]); y[1][n]=mfma16h(AF[S][i][1],BF[S][(n)-(i)],y[1][n]); SCHED(); }while(0)
#define CV_U0() u32 u0=u1-256; u0=((int)u0<(int)uz)?uz:u0
DI void conv_wave(rsrc_t rs,int d,u32 cb,u32 flag,int lane,float ksc,float dsk){
  const int r=lane&15,q=lane>>4; u32 ka=cb+(r&3)*KP+16*q-8*(r>>2)+1088; const u32 uz=cb+UOF+q*1040; u32 u1=uz+(r+17)*16; u4 AF[2][2][2]; u4 BF[2][4]; f4 y[2][4]={};
  #define CV_WT(S) asm volatile("s_waitcnt lgkmcnt(0)" : TIEA(S,0),TIEA(S,1),"+v"(BF[S][0]),"+v"(BF[S][1]),"+v"(BF[S][2]),"+v"(BF[S][3]))
  #define CV_STEP(C,N) do{ CV_U0(); CV_M(C,0,0); RDA(N,0,0); RDB(N,0,u0,0); RDA(N,0,1); SCHED(); CV_M(C,0,1); RDB(N,1,u1,0); RDA(N,1,0); RDB(N,2,u1,256); SCHED(); CV_M(C,0,2); RDA(N,1,1); RDB(N,3,u1,512); \
      ka-=64; u1-=16; SCHED(); CV_M(C,0,3); CV_M(C,1,1); CV_M(C,1,2); CV_M(C,1,3); CV_WT(N); }while(0)
  { CV_U0(); RDA(0,0,0); RDA(0,0,1); RDA(0,1,0); RDA(0,1,1); RDB(0,0,u0,0); RDB(0,1,u1,0); RDB(0,2,u1,256); RDB(0,3,u1,512); ka-=64; u1-=16; CV_WT(0); }
  #pragma unroll 1
  for(int dl=0;dl<8;dl++){ CV_STEP(0,1); if(dl==7){ ka+=64; u1+=16; } CV_STEP(1,0); }
  asm volatile("s_nop 15" : "+v"(y[0][0]),"+v"(y[0][1]),"+v"(y[0][2]),"+v"(y[0][3]),"+v"(y[1][0]),"+v"(y[1][1]),"+v"(y[1][2]),"+v"(y[1][3]));
  u2 uv[2][4];
  #pragma unroll
  for(int n=0;n<4;n++)
    #pragma unroll
    for(int m=0;m<2;m++) uv[m][n]=ldsr64(cb+UOF+(2*m+(q>>1))*1040+(16*n+r+1)*16+8*(q&1));
  const u32 sc=cb+UOF+r*64+q*8;
  auto fin=[&](int n){
    #pragma unroll
    for(int m=0;m<2;m++){ u2 o;
      #pragma unroll
      for(int e=0;e<2;e++) o[e]=pkbf(fmaf1(hlo(uv[m][n][e]),dsk,mulf(y[m][n][2*e],ksc)),fmaf1(hhi(uv[m][n][e]),dsk,mulf(y[m][n][2*e+1],ksc)));
      *(LAS u2*)(sc+n*1024+m*32)=o; } };
  ldswait(flag,2u);
  { f4 hp[4];
    #pragma unroll
    for(int i=0;i<4;i++) hp[i]=*(LAS f4*)(cb+S7O+i*1024+lane*16);
    fin(0); fin(1);
    #pragma unroll
    for(int i=0;i<4;i++)
      #pragma unroll
      for(int e=0;e<4;e++) y[i&1][2+(i>>1)][e]=addf(y[i&1][2+(i>>1)][e],hp[i][e]);
    fin(2); fin(3); }
  CFENCE();
  u4 ov[4];
  #pragma unroll
  for(int n=0;n<4;n++) ov[n]=ldsr128(cb+UOF+n*1024+lane*16);
  #pragma unroll
  for(int n=0;n<4;n++) st16(rs,OFF_AT+d*4096+n*1024+lane*16,ov[n]);
  WAITV(0);
}

constexpr int NB=3;
constexpr int g2ops(int h){ return h<0? 7 : (h+NB<16?7:0)+((h>=3&&h<=10)?4:0)+(h==13-NB?1:0)+(h==16-NB?8:0); }
constexpr int g2cnt(int h){ int c=0; for(int i=h-NB+1;i<h;i++) c+=g2ops(i); return c; }
struct Frag { u4 x[4], w[4][2]; };
__global__ __launch_bounds__(512,2) void fused(Params p){
  extern __shared__ __attribute__((aligned(16))) char sm[];
  const u32 smb=(u32)(size_t)(LAS char*)sm;
  u64 pc0; asm volatile("s_getpc_b64 %0" : "=s"(pc0));
  const int b=blockIdx.x, xc=b&7, idx=b>>3;
  const int tid=threadIdx.x, w=uni(tid>>6); int lane=tid&63;
  auto cpf=[&](u32 o){ u4 sc=mksrd((const void*)(pc0&~127ull)); sc[2]=CODE_BYTES; setm0<JNK>(smb); touch(sc,lane*128,o); };
  if(idx>=28) cpf(w*8192);
  SCHED();
  const int rt=4*(xc>>1)+(idx&3), j=7*(xc&1)+(idx>>2), mi=w&3, g1=w>>2;
  const u32 kr0=(((j+rt)&3)*5)>>1;
  u32 vx0,vx1,vw1,vw2,k0=kr0*128,k0w=kr0*(64*3456); const u32 mw=smb+w*4096;
  { const u32 lr=lane>>3,c=lane&7; vx0=(rt*128+16*w+lr)*1280+((c^(lr>>1))<<4); vx1=(vx0^64u)+9216;
    vw1=(8*w+lr)*3456+((c^((lr&2)<<1))<<4)+j*128; vw2=vw1+1024+(j==13?0:14*128); }
  asm volatile("" : "+v"(vx0),"+v"(vx1),"+v"(vw1),"+v"(vw2),"+s"(k0),"+s"(k0w));
  setm0<0>(mw);
  SCHED();
  const u4 srdX=mksrd(p.x), srdW=mksrd((const char*)p.win-3072);
  if(idx<28){ dma<0>(srdX,vx0,k0); dma<1024>(srdX,vx1,k0); dma<2048>(srdW,vw2,k0w); dma<3072>(srdW,vw1,k0w); }
  SCHED();
  const rsrc_t rs=__builtin_amdgcn_make_buffer_rsrc(p.ws,0,WS_END,0x00020000);
  const u4 srdS=mksrd(p.ws);
  const int rtB=8*(xc>>1)+(idx&7), ct=4*(xc&1)+(idx>>3);
  const bool hask=idx<28 && idx+28*(w&3)<80, hasc=hask && w>=4;
  const int dch=80*xc+(hask? idx+28*(w&3) : 0);
  const u32 cb=smb+CONV+(w&3)*CVS;
  u32 tag; float ksc=0.f, dsk=0.f;
  auto pre01=[&](){
    const u4 sa=mksrd((const char*)p.wsp+(size_t)rtB*64*4096+mi*1024-4096); const u32 v0=(lane>>3)*4096+(((lane&7)^(lane>>4))<<4);
    #pragma unroll 1
    for(int i=0;i<4;i++){ setm0<RB+8192>(smb+mi*RING+i*4096); const u32 g=(i&1)*4+2*mi, k=(i>>1)*128+4096;
      dma<0>(sa,v0,(g&7)*32768+k); dma<1024>(sa,v0^64u,(g&7)*32768+32768-1024+k); dma<2048>(sa,v0,((g+2)&7)*32768-2048+k); dma<3072>(sa,v0^64u,((g+2)&7)*32768+32768-3072+k); } };
  if(idx>=28){
    const int a=xc*4+idx-28, r=lane&15, q=lane>>4;
    const u32 ep=ld4(rs,OFF_EP);
    u4 xr[10]; const char* xs=(const char*)p.x+(size_t)(64*a+8*w+(lane>>3))*1280+(lane&7)*16;
    #pragma unroll
    for(int i=0;i<10;i++) xr[i]=*(const u4*)(xs+i*128);
    if(tid<32) *(LAS u32*)(smb+PM+tid*4)=0u;
    tag=uni(ep)+TAGINC;
    float s0=0.f,s1=0.f,q0=0.f,q1=0.f;
    #pragma unroll
    for(int i=0;i<10;i++){ s0=dot2(xr[i][0],0x3f803f80u,s0); q0=dot2(xr[i][0],xr[i][0],q0); s1=dot2(xr[i][1],0x3f803f80u,s1); q1=dot2(xr[i][1],xr[i][1],q1);
      s0=dot2(xr[i][2],0x3f803f80u,s0); q0=dot2(xr[i][2],xr[i][2],q0); s1=dot2(xr[i][3],0x3f803f80u,s1); q1=dot2(xr[i][3],xr[i][3],q1); }
    s0+=s1; q0+=q1;
    #define DPPADD(v,c) v+=__int_as_float(__builtin_amdgcn_update_dpp(0,__float_as_int(v),c,0xf,0xf,false))
    DPPADD(s0,0xB1); DPPADD(q0,0xB1); DPPADD(s0,0x4E); DPPADD(q0,0x4E); DPPADD(s0,0x141); DPPADD(q0,0x141);
    float m1,ri; { float mean=s0*(1.f/640.f); ln_fin(mean,q0*(1.f/640.f)-mean*mean,m1,ri); }
    if((lane&7)==0) st16(rs,OFF_RS+(64*a+8*w+(lane>>3))*16,u4{__float_as_uint(m1),tag,__float_as_uint(ri),tag});
    if(a<27){ const u4 srdO=mksrd((const char*)p.wout+a*(32*1280)+w*5120); const u32 mo=smb+w*5120; setm0<0>(mo);
      dma<0>(srdO,lane*16,0); dma<1024>(srdO,lane*16,0); dma<2048>(srdO,lane*16,0); dma<3072>(srdO,lane*16,0); setm0<4096>(mo); dma<0>(srdO,lane*16,4096); }
    #pragma unroll
    for(int i=0;i<10;i++){ u4 o;
      #pragma unroll
      for(int e=0;e<4;e++) o[e]=pkhf2(fma2(unpk(xr[i][e]),f2{ri,ri},f2{-m1*ri,-m1*ri}));
      ldsw128(smb+RB+(8*w+(lane>>3))*1296+(lane&7)*16+i*128,o); }
    WAITV(0); bar();
    #pragma unroll
    for(int i=0;i<10;i++){ const int c=w*10+i; const u32 ad=smb+RB+(32*((r>>1)&1)+8*q+(r>>2))*1296+c*16+(r&1)*8; u2 lo=ldstr(ad), hi=ldstr(ad+4*1296);
      const u32 sc=smb+40960+w*2048+(i&1)*1024; ldsw128(sc+(r&7)*128+((((r>>3)*4+q)^(r&7))<<4),u4{lo[0],lo[1],hi[0],hi[1]});
      CFENCE(); const u4 o=ldsr128(sc+(lane>>3)*128+(((lane&7)^(lane>>3))<<4)); CFENCE();
      st16(rs,OFF_XNT+a*81920+c*1024+lane*16,o); }
    WAITV(0);
    if(arrive(smb+C_Z1,lane)==7u && lane==0) st4(rs,OFF_FX+a*128,tag);
    if(a<27){
      #pragma unroll
      for(int i=0;i<5;i++){ u32 ad=smb+(8*q+(r>>2))*1280+(w*5+i)*32+(r&3)*8; u2 lo=ldstr(ad), hi=ldstr(ad+4*1280);
        st16(rs,OFF_WOF+(u32)((w*27+a)*5+i)*1024+lane*16,u4{lo[0],lo[1],hi[0],hi[1]}); } }
    WAITV(0); bar();
    if(tid==0) st4(rs,OFF_FW+a*128,tag);
    if(!g1){ pre01(); WAITV(0); }
  } else {
    const int r=lane&31, h=lane>>5;
    u32 epv;
    u32 kx[10]; { u32 k=kr0;
      #pragma unroll
      for(int i=0;i<10;i++){ kx[i]=k*128; k=k==9?0:k+1; } }
    auto issue1=[&](auto sc,int k){ constexpr int s=sc.value;
      if(k==0){ setm0<(s&3)*STG>(mw); dma<0>(srdX,vx0,kx[s]); } if(k==1) dma<1024>(srdX,vx1,kx[s]); if(k==2) dma<2048>(srdW,vw2,kx[s]*1728); if(k==3) dma<3072>(srdW,vw1,kx[s]*1728); };
    auto issue=[&](auto sc){ issue1(sc,0); issue1(sc,1); issue1(sc,2); issue1(sc,3); };
    u32 pr[6];
    gload4<0>(epv,0u,p.ws+OFF_EP);
    gload4<0>(pr[0],lane*4,(const char*)(p.lar+dch*128)); gload4<0>(pr[1],lane*4,(const char*)(p.aim+dch*128)); gload4<0>(pr[2],lane*4,(const char*)(p.cre+dch*128)); gload4<0>(pr[3],lane*4,(const char*)(p.cim+dch*128));
    gload2(pr[4],0u,(const char*)(p.ldt+dch)); gload2(pr[5],0u,(const char*)(p.dsk+dch));
    asm volatile("s_barrier" ::: "memory");
    issue(std::integral_constant<int,1>{});
    if(tid<32) *(LAS u32*)(smb+PM+tid*4)=0u;
    u32 ax[2][4],aw[2][4][2];
    { u32 row=32*mi+r, a0=smb+(row>>4)*4096+(row&15)*128+((h^((r>>1)&7))<<4);
      u32 qq=(lane&15)>>2, w0=smb+2048+g1*1024+h*4096+qq*128+8*(lane&1)+(((2*((lane>>4)&1)+((lane>>1)&1))^((qq&2)<<1))<<4);
      #pragma unroll
      for(int i=0;i<2;i++)
        #pragma unroll
        for(int k=0;k<4;k++){ const u32 kk=(k+mi)&3, wo=i*2*STG; ax[i][k]=opq((a0^(kk*32u))+wo); aw[i][k][0]=opq(w0+kk*8192+wo); aw[i][k][1]=opq((w0^64u)+kk*8192+wo); } }
    u4 sel; { const u32 o=((lane&15)==((lane>>4)&1))?0x3f803f80u:0u; sel=u4{o,o,o,o}; asm volatile("" : "+v"(sel)); }
    u32 ga[11];
    #pragma unroll
    for(int i=0;i<11;i++) ga[i]=opq(cb+2u*(u32)rbf((float)(512-8*lane+3-i)));
    Frag F[2];
    asm volatile("s_waitcnt vmcnt(4)" : "+v"(epv),"+v"(pr[0]),"+v"(pr[1]),"+v"(pr[2]),"+v"(pr[3]),"+v"(pr[4]),"+v"(pr[5]) :: "memory");
    bar();
    tag=uni(epv)+TAGINC; dsk=bflo(pr[5]);
    #pragma unroll
    for(int k=0;k<4;k++){
      #pragma unroll
      for(int n=0;n<2;n++){ u2 lo=ldstr(aw[0][k][n]), hi=ldstr(aw[0][k][n]+512); F[0].w[k][n]=u4{lo[0],lo[1],hi[0],hi[1]}; }
      F[0].x[k]=ldsr128(ax[0][k]); }
    issue(std::integral_constant<int,2>{});
    f16v acc[2]={}; f4 cacc[2]={}; u4 G;
    const u32 go=OFF_RS+rt*2048+mi*512, cp=smb+MISC+g1*1024;
    sfor<10>([&](auto ic){
      constexpr int s=ic.value; Frag& cur=F[s&1]; Frag& nxt=F[(s&1)^1];
      if constexpr(s<9){ WAITV(s==8? 3 : s==7? 7 : 4); asm volatile("s_barrier" ::: "memory"); }
      auto rw=[&](int k,int n){ if constexpr(s<9){ const int i=((s+1)>>1)&1; const u32 so=((s+1)&1)*STG; u2 lo=ldstr(aw[i][k][n]+so), hi=ldstr(aw[i][k][n]+so+512); nxt.w[k][n]=u4{lo[0],lo[1],hi[0],hi[1]}; } };
      auto rx=[&](int k){ if constexpr(s<9){ const int i=((s+1)>>1)&1; const u32 so=((s+1)&1)*STG; nxt.x[k]=ldsr128(ax[i][k]+so); } };
      #pragma unroll
      for(int k=0;k<4;k++){
        acc[0]=mfma32b(cur.w[k][0],cur.x[k],acc[0]); SCHED();
        if constexpr(s+3<=9) issue1(std::integral_constant<int,s+3>{},k);
        rw(k,0); rx(k);
        SCHED();
        acc[1]=mfma32b(cur.w[k][1],cur.x[k],acc[1]); SCHED();
        rw(k,1);
        if(k==0){ cacc[0]=mfma16(cur.w[0][0],sel,cacc[0]); cacc[1]=mfma16(cur.w[0][1],sel,cacc[1]); }
        if constexpr(s==6){ if(k==3){ gload<0>(G,r*16,p.ws+go); setm0<JNK>(smb);
            if(g1){ const u4 sp=mksrd((const char*)p.wsp+(size_t)(xc>>1)*512*4096); const u32 lb=(((xc&1)*28+idx)*4+mi)*74, l0=lb+lane, l1=lb+64+(lane<10?lane:9);
              touch(sp,(l0<16384?l0:16383)*128,0u); touch(sp,(l1<16384?l1:16383)*128,0u); }
            else { touch(srdS,0u,go); touch(srdS,0u,go); } } }
        if constexpr(s==8){ if(k==3) signal(smb+C_DONE,lane); }
        if constexpr(s==9){ if(k==1){
          if((lane&14)==0){ *(LAS f4*)(cp+mi*256+(lane&1)*64+(lane>>4)*16)=cacc[0]; *(LAS f4*)(cp+mi*256+128+(lane&1)*64+(lane>>4)*16)=cacc[1]; }
          signal(smb+C_CS+g1*4,lane); } }
        SCHED();
      }
    });
    if(!g1) __builtin_amdgcn_s_setprio(2);
    asm volatile("s_waitcnt vmcnt(2)" : "+v"(G) :: "memory");
    ldswait(smb+C_CS+g1*4,4u);
    const float cs0=*(LAS float*)(cp+h*512+r*4)+*(LAS float*)(cp+h*512+256+r*4), cs1=*(LAS float*)(cp+h*512+128+r*4)+*(LAS float*)(cp+h*512+384+r*4);
    #pragma unroll 1
    for(int sp=0;sp<SPIN && !__all(G[1]==tag&&G[3]==tag);sp++)asm volatile("global_load_dwordx4 %0, %1, %2 sc1\n\ts_waitcnt vmcnt(0)" : "=&v"(G) : "v"(r*16), "s"(p.ws+go) : "memory");
    const float nm=-__uint_as_float(G[0]), ri1=__uint_as_float(G[2]);
    acc[0]=__builtin_amdgcn_mfma_f32_32x32x2f32(cs0,nm,acc[0],0,0,0); acc[1]=__builtin_amdgcn_mfma_f32_32x32x2f32(cs1,nm,acc[1],0,0,0);
    u32 zp[2][8]; const u32 zqa=smb+RB+mi*RING+4096+lane*16;
    if(g1){
      ldswait(smb+C_DONE,8u);
      #pragma unroll
      for(int i=0;i<4;i++){ u4 o;
        #pragma unroll
        for(int e=0;e<4;e++) o[e]=pkbf2(f2{acc[i>>1][8*(i&1)+2*e],acc[i>>1][8*(i&1)+2*e+1]}*ri1);
        ldsw128(zqa+i*1024,o); }
      signal(smb+C_K2+mi*4,lane);
    } else {

      #pragma unroll
      for(int e=0;e<16;e++) zp[e>>3][e&7]=pkbf2(gelu2(f2{acc[e>>3][2*(e&7)],acc[e>>3][2*(e&7)+1]}*ri1));
      float S=0.f,Q=0.f;
      if(j<13){
        #pragma unroll
        for(int e=0;e<8;e++){ S=dot2(zp[0][e],0x3f803f80u,S); Q=dot2(zp[0][e],zp[0][e],Q); } }
      #pragma unroll
      for(int e=0;e<8;e++){ S=dot2(zp[1][e],0x3f803f80u,S); Q=dot2(zp[1][e],zp[1][e],Q); }
      #define XSUM32(v) do{ auto t_=__builtin_amdgcn_permlane32_swap(__float_as_uint(v),__float_as_uint(v),false,false); v=__uint_as_float(t_[0])+__uint_as_float(t_[1]); }while(0)
      XSUM32(S); XSUM32(Q);
      const u32 sto=OFF_ST+rt*(14*2048)+(32*mi+r)*16;
      if(h==0) st16(rs,sto+j*2048,u4{__float_as_uint(S),tag,__float_as_uint(Q),tag});
    }
    float kp[11]; u4 g[7]={};
    const u32 gof=OFF_ST+rt*(14*2048)+(32*mi+r)*16+h*(7*2048);
    auto samp=[&](){ asm volatile("" ::: "memory");
      #pragma unroll
      for(int i=0;i<7;i++) g[i]=ld16(rs,gof+i*2048);
      asm volatile("" ::: "memory"); };
    if(hasc) ksc=kgen_tab(cb,lane,pr,kp); else if(!g1){
      pre01(); ldswait(smb+C_K2+mi*4,1u); u4 zq[4];
      #pragma unroll
      for(int i=0;i<4;i++){ const u4 v=ldsr128(zqa+i*1024);
        #pragma unroll
        for(int e=0;e<4;e++) zq[i][e]=pkbf2(gelu2(unpk(v[e]))); }
      SCHED(); samp();
      #pragma unroll
      for(int n=0;n<2;n++) if(2*j+n<27){ const u32 oo=OFF_Z1T+((rt*8+2*mi+(r>>4))*27+2*j+n)*1024+(h*16+(r&15))*16;
        st16(rs,oo,u4{zq[2*n][0],zq[2*n][1],zq[2*n+1][0],zq[2*n+1][1]}); st16(rs,oo+512,u4{zq[2*n][2],zq[2*n][3],zq[2*n+1][2],zq[2*n+1][3]}); }
    }
    if(g1){
      if(hasc){
        kgen_exp(cb,lane,kp,ga);
        signal(smb+C_KP+mi*4,lane);
        ldswait(smb+C_FULL,1u);
      }
    } else {
      float S,Q;
      #pragma unroll 1
      for(int sp=0;sp<SPIN;sp++){ bool ok=true;
        #pragma unroll
        for(int i=0;i<7;i++) ok=ok&&g[i][1]==tag&&g[i][3]==tag;
        if(__all(ok)) break;
        __builtin_amdgcn_s_sleep(4); samp(); }
      S=0.f; Q=0.f;
      #pragma unroll
      for(int i=0;i<7;i++){ S+=__uint_as_float(g[i][0]); Q+=__uint_as_float(g[i][2]); }
      XSUM32(S); XSUM32(Q);
      float mean=S*(1.f/864.f), m2,r2; ln_fin(mean,Q*(1.f/864.f)-mean*mean,m2,r2); m2*=-r2;
      ldswait(smb+C_DONE,8u);
      const u32 tb=smb+RB+mi*RING, ad=tb+(8*(lane>>4)+((lane&15)>>2))*80+(lane&3)*8;
      const u32 zo=OFF_Z2F+((rt*4+mi)*54+(j==13?-2:2+4*j))*1024+lane*16;
      #pragma unroll
      for(int n=0;n<2;n++){
        #pragma unroll
        for(int g2=0;g2<4;g2++){ u2 o;
          #pragma unroll
          for(int e=0;e<2;e++) o[e]=pkbf2(fma2(unpk(zp[n][2*g2+e]),f2{r2,r2},f2{m2,m2}));
          *(LAS u2*)(tb+r*80+g2*16+h*8)=o; }
        CFENCE();
        u2 l0=ldstr(ad), h0=ldstr(ad+320), l1=ldstr(ad+32), h1=ldstr(ad+32+320);
        CFENCE();
        if(j<13||n){ st16(rs,zo+n*2048,u4{l0[0],l0[1],h0[0],h0[1]}); st16(rs,zo+n*2048+1024,u4{l1[0],l1[1],h1[0],h1[1]}); } }
      WAITV(0);
      if(arrive(smb+C_Z2,lane)==3u && lane==0) st4(rs,OFF_F2+(rt*16+j)*128,tag);
    }
  }
  const int ft0=ct<7?7*ct:47;
  bool adone=true; const u32 fao=OFF_FA+((lane<28?lane:0)*8+ct)*128;
  auto aload=[&](){ const u32 vo=(lane>>3)*4096+(lane&7)*16;
    #pragma unroll 1
    for(int i=0;i<10;i++){ setm0<ATL>(smb+i*1024); dma<0>(srdS,vo,OFF_AT+(u32)(80*ct+8*i)*4096+rtB*128); } };
  if(w>=4){
    if(hasc){
      __builtin_amdgcn_s_setprio(3);
      conv_wave(rs,dch,cb,smb+C_KP+mi*4,lane,ksc,dsk);
      __builtin_amdgcn_s_setprio(0);
      if(arrive(smb+C_CV,lane)==(idx<24?2u:1u) && lane==0) st4(rs,OFF_FA+b*128,tag);
    }
    if(w==7){
      if(idx<28){
        __builtin_amdgcn_s_sleep(24);
        pollw<16>(rs,OFF_FX+(lane&31)*128,tag);
        u4 sw[3][4];
        #pragma unroll
        for(int c=0;c<3;c++)
          #pragma unroll
          for(int i=0;i<4;i++) sw[c][i]=ld16(rs,OFF_XNT+((lane>>3)+8*i)*81920+(80*xc+idx+((c<2||idx<24)?28*c:0))*128+(lane&7)*16);
        #pragma unroll
        for(int c=0;c<3;c++){
          #pragma unroll
          for(int i=0;i<4;i++){ const int x=lane+64*i; ldsw128(smb+c*CVS+UOF+(x&3)*1040+((x>>2)+1)*16,sw[c][i]); }
          if(lane<4) ldsw128(smb+c*CVS+UOF+lane*1040,u4{0,0,0,0}); }
        signal(smb+C_FULL,lane);
        #pragma unroll 1
        for(int c=0;c<3;c++){
          #pragma unroll
          for(int i=0;i<4;i++){ sw[0][i]=sw[1][i]; sw[1][i]=sw[2][i]; }
          conv_win(smb+c*CVS,lane,sw[2]); }
        __builtin_amdgcn_s_setprio(3);
        #pragma unroll 1
        for(int c=0;c<(idx<24?3:2);c++){ ldswait<4>(smb+C_KP+c*4,1u); conv_help(smb+c*CVS,lane,sw[0]); signal(smb+C_KP+c*4,lane);
          #pragma unroll
          for(int i=0;i<4;i++){ sw[0][i]=sw[1][i]; sw[1][i]=sw[2][i]; } } }
      __builtin_amdgcn_s_setprio(0);
    }
    ldswait<6>(smb+C_Z1R,4u);
    u4 wq[24];
    #pragma unroll
    for(int i=0;i<24;i++) wq[i]=ld16(rs,OFF_WOF+ct*(135*1024)+40960+mi*RING+((i==23&&mi==3)?22:i)*1024+lane*16);
    if(w<6){
      #pragma unroll 1
      for(u32 i=0;i<20480;i+=4096){ setm0<0>(cb+i); const u32 o=OFF_WOF+ct*(135*1024)+mi*20480+i; dma<0>(srdS,lane*16,o); dma<1024>(srdS,lane*16,o); dma<2048>(srdS,lane*16,o); dma<3072>(srdS,lane*16,o); } }
    if(w==6){ adone=__all(ld4(rs,fao)==tag); if(adone) aload(); }
    ldswait(smb+C_FREE,4u);
    #pragma unroll
    for(int i=0;i<24;i++) ldsw128(smb+RB+mi*RING+i*1024+lane*16,wq[i]);
    if(w<6) WAITV(0);
    signal(smb+C_WR,lane);
    if(!adone){ adone=__all(ld4(rs,fao)==tag); if(adone) aload(); }
    if(w==7 && b==255){
      #pragma unroll 1
      for(int sp=0;sp<SPIN;sp++){ const u32 o=OFF_FA+lane*128; u32 v0=ld4(rs,o),v1=ld4(rs,o+8192),v2=ld4(rs,o+16384),v3=ld4(rs,lane<32? o+24576 : OFF_FW+(lane-32)*128); asm volatile("" ::: "memory");
        if(__all(v0==tag&&v1==tag&&v2==tag&&v3==tag)) break; __builtin_amdgcn_s_sleep(4); }
      if(lane==0) st4(rs,OFF_EP,tag); }
  }
  int w2=w; asm volatile("" : "+s"(w2));
  if(w2<4){
    __builtin_amdgcn_s_setprio(2);
    int r=lane&15, q=lane>>4;
    const u32 rb=smb+RB+w*RING;
    u4 srdA=mksrd((const char*)p.wsp+(size_t)rtB*64*4096+w*1024-4096+256);
    const char* zb=p.ws+OFF_Z2F+(u32)(16*w*54+ft0+3)*1024;
    u32 voA[2]; { u32 lr=lane>>3,c=lane&7; voA[0]=lr*4096+((c^(lr>>1))<<4); voA[1]=voA[0]^64u; }
    u32 sofa[8];
    #pragma unroll
    for(int g=0;g<8;g++) sofa[g]=((g+2*w)&7)*32768+4096-(g&3)*1024;
    const u32 voB=lane*16;
    u32 la[2]; la[0]=rb+r*128+((q^(r>>1))<<4); la[1]=la[0]^64u;
    f4 acc[4][7]={}; u4 B[NB][7]; u4 A[2][4]; u2 z1[7];
    auto issueA=[&](auto sc,auto gc){ constexpr int s=sc.value,g=gc.value; if constexpr((g&3)==0) setm0<((s+1)%3)*8192+g*1024>(rb); dma<(g&3)*1024>(srdA,voA[g&1],sofa[g]);
      if constexpr(g==7){ u64 a=((u64)srdA[1]<<32|srdA[0])+128; srdA[0]=(u32)a; srdA[1]=(u32)(a>>32); } };

    u32 bsr,fv;
    auto fof=[&](){ const u32 l=voB>>4; return l<27? OFF_FW+l*128 : OFF_F2+((rtB>>1)*16+(((ft0>>1)+(l&3))>>1))*128; };
    #pragma unroll
    for(int m=0;m<4;m++){ A[0][m]=ldsr128(la[0]+m*2048+8192); A[1][m]=ldsr128(la[1]+m*2048+8192); }
    sfor<8>([&](auto g){ issueA(std::integral_constant<int,2>{},g); });
    waitA(A[0]); waitA(A[1]);
    sfor<8>([&](auto g){ issueA(std::integral_constant<int,3>{},g); });
    #pragma unroll
    for(int m=0;m<4;m++)
      #pragma unroll
      for(int n=0;n<7;n++) asm volatile("" : "+v"(acc[m][n]));
    { const int hf=(ft0>>1)+q; pollw(rs,OFF_F2+((4*w+(lane&3))*16+(hf==0?13:(hf-1)>>1))*128,tag); }
    sfor<NB>([&](auto hh){ const char* s=zb+(u32)hh.value*(54*1024); sfor<7>([&](auto n){ gload<(n.value-3)*1024>(B[hh][n],voB,s); }); });
    auto redw=[&](auto nc){ constexpr int n=nc.value; *(LAS f4*)(rb+n*1024+voB)=acc[1][n]; *(LAS f4*)(rb+(7+n)*1024+voB)=acc[2][n]; *(LAS f4*)(rb+(14+n)*1024+voB)=acc[3][n]; };
    const char* z1b=p.ws+OFF_Z1T+(u32)((rtB*4+w)*27+(ft0>>1))*1024;
    sfor<16>([&](auto hc){
      constexpr int h=hc.value, S=h%NB, P=h&1, HZ=16-NB, cnt=g2cnt(h);
      waitB<cnt>(B[S]); waitA(A[P]);
      if constexpr(h==HZ){ tie1(fv); if(!__all(fv==tag)) pollw(rs,fof(),tag); }
      if constexpr(h+1<16 && h>0){
        #pragma unroll
        for(int m=0;m<4;m++) A[P^1][m]=ldsr128(la[(h+1)&1]+m*2048+((((h+1)>>1)+1)%3)*8192);
      }
      if constexpr(h==HZ-3) gload4<0>(fv,fof(),p.ws);
      const char* s=zb+(u32)(h+NB)*(54*1024);
      sfor<7>([&](auto n){
        mfma4(acc[0][n],acc[1][n],acc[2][n],acc[3][n],B[S][n],A[P]);
        if constexpr(h>=3 && h<=10 && (n.value&1)==0) issueA(std::integral_constant<int,(h-1)/2+3>{},std::integral_constant<int,((h-1)&1)*4+n.value/2>{});
        if constexpr(h+NB<16) gload_t<(n.value-3)*1024>(B[S][n],voB,s);
        if constexpr(h==HZ){ gload8<(n.value>>1)*1024>(z1[n],voB,z1b+((n.value&1)? ((ft0&1)?1024:8) : (ft0&1)*8));
          if constexpr(n.value==6) { gload2(bsr,(voB>>3)&30u,(const char*)(p.bsp+(rtB*4+w)*16)); signal(smb+C_Z1R,lane); } }
        if constexpr(h==15){ SCHED(); if constexpr(n.value>=2) redw(std::integral_constant<int,n.value-2>{}); SCHED(); }
      });
    });
    asm volatile("s_nop 15\n\ts_nop 3" ::: "memory");
    SCHED();
    lane=__builtin_amdgcn_mbcnt_hi(~0u,__builtin_amdgcn_mbcnt_lo(~0u,0u)); asm volatile("" : "+v"(lane)); r=lane&15; q=lane>>4;
    redw(std::integral_constant<int,5>{}); redw(std::integral_constant<int,6>{});
    signal(smb+C_RED,lane);
    ldswait(smb+C_RED,4u);
    #pragma unroll
    for(int m=1;m<4;m++){ u32 src=smb+RB+((w-m)&3)*RING+(m-1)*7168+lane*16;
      #pragma unroll
      for(int n=0;n<7;n++) acc[0][n]+=*(LAS f4*)(src+n*1024); }
    asm volatile("s_waitcnt vmcnt(0) lgkmcnt(0)" : "+v"(z1[0]),"+v"(z1[1]),"+v"(z1[2]),"+v"(z1[3]),"+v"(z1[4]),"+v"(z1[5]),"+v"(z1[6]),"+v"(bsr) :: "memory");
    const float bs=bflo(bsr);
    signal(smb+C_FREE,lane);
    u2 o[7];
    #pragma unroll
    for(int n=0;n<7;n++)
      #pragma unroll
      for(int e=0;e<2;e++)o[n][e]=pkbf2(unpk(z1[n][e])*(f2{acc[0][n][2*e],acc[0][n][2*e+1]}+bs));
    const u32 ho=OFF_HF+(u32)((rtB*4+w)*54+ft0)*512+(q>>1)*256+r*16;
    #pragma unroll
    for(int n=0;n<6;n+=2){ auto sx=__builtin_amdgcn_permlane16_swap(o[n][0],o[n+1][0],false,false); auto sy=__builtin_amdgcn_permlane16_swap(o[n][1],o[n+1][1],false,false);
      if(ct<7||n>=2) st16(rs,ho+(n+(q&1))*512,u4{sx[0],sy[0],sx[1],sy[1]}); }
    st8(rs,ho+6*512+(q&1)*8,o[6]);
    WAITV(0);
    if(arrive(smb+C_H,lane)==3u && lane==0) st4(rs,OFF_FH+(rtB*8+ct)*128,tag);
    __builtin_amdgcn_s_setprio(0);
  }
  { const int r=lane&15, q=lane>>4, m=w&3, kh=w>>2;
    u2 xv[5]={};
    if(kh){
      #pragma unroll
      for(int n=0;n<5;n++) xv[n]=*(const u2*)(p.x+(rtB*64+m*16+r)*640+80*ct+16*n+4*q);
    }
    f4 acc[5]={}; u4 W[3][5];
    const u32 wl2=smb+lane*16+RB-40960+kh*14*5120, wl0=kh? wl2 : smb+lane*16, wl1=kh? wl2 : smb+lane*16+CVS-20480;
    #define WAD(k,n) (((k)<4? wl0 : (k)<8? wl1 : wl2)+((k)*5+(n))*1024)
    f4 ax[5]={};
    auto addax=[&](){
      #pragma unroll
      for(int n=0;n<5;n++){ u2 av=ldstr(smb+ATL+(16*n+4*q+(r>>2))*128+(16*m+4*(r&3))*2); ax[n]=f4{bflo(av[0]),bfhi(av[0]),bflo(av[1]),bfhi(av[1])}+f4{bflo(xv[n][0]),bfhi(xv[n][0]),bflo(xv[n][1]),bfhi(xv[n][1])}; } };
    if(w==6 && adone){ WAITV(0); signal(smb+C_AT,lane); }
    u32 oo[3],to[3]; const u32 tb=smb+CVS+m*3072;
    #pragma unroll
    for(int i=0;i<3;i++){ const int it=lane+64*i, rr=(it*205)>>11, cc=it-rr*10; oo[i]=opq((rtB*64+m*16+rr)*1280+ct*160+cc*16); to[i]=opq(tb+rr*176+cc*16); }
    if(m==0){ ldswait(smb+C_H,4u); pollw(rs,OFF_FH+(rtB*8+kh*4+(lane&3))*128,tag); signal(smb+C_TAG+kh*4,lane); } else ldswait(smb+C_TAG+kh*4,1u);
    u4 hf[14];
    const u32 ho=OFF_HF+(u32)((rtB*4+m)*27+kh*14)*1024+lane*16;
    #pragma unroll
    for(int k=0;k<HPF;k++) hf[k]=ld16(rs,ho+k*1024);
    const bool early=kh && uni(ldsv(smb+C_AT))!=0u;
    if(early) addax();
    ldswait(smb+C_WR,4u);
    const u32 wl3=kh? wl2-5120 : wl2;
    #pragma unroll
    for(int n=0;n<10;n++) W[n/5][n%5]=ldsr128(WAD(n/5,n%5));
    #pragma unroll
    for(int k=0;k<13;k++){
      if(k+HPF<14) hf[k+HPF]=ld16(rs,ho+((k+HPF==13&&kh)?12:k+HPF)*1024);
      SCHED();
      #pragma unroll
      for(int n=0;n<5;n++){ acc[n]=mfma16(W[k%3][n],hf[k],acc[n]); SCHED();
        if(k<11) W[(k+2)%3][n]=ldsr128(WAD(k+2,n)); else if(k==11) W[1][n]=ldsr128(wl3+(65+n)*1024);
        SCHED(); }
      if(k==5 && !kh) signal(smb+C_G1,lane);
    }
    const u32 pb=smb+m*5120+lane*16;
    if(!kh){
      #pragma unroll
      for(int n=0;n<5;n++) acc[n]=mfma16(W[1][n],hf[13],acc[n]);
      ldswait(smb+C_G1,4u);
      #pragma unroll
      for(int n=0;n<5;n++) *(LAS f4*)(pb+n*1024)=acc[n];
      signal(smb+C_P+m*4,lane);
    } else {
      if(w==6 && !adone){ pollw(rs,fao,tag); aload(); WAITV(0); signal(smb+C_AT,lane); }
      if(!early){ ldswait(smb+C_AT,1u); addax(); }
      ldswait(smb+C_P+m*4,1u);
      #pragma unroll
      for(int n=0;n<5;n++){ const f4 o=(acc[n]+ax[n])+*(LAS f4*)(pb+n*1024); *(LAS u2*)(tb+r*176+n*32+q*8)=u2{pkbf(o[0],o[1]),pkbf(o[2],o[3])}; }
      CFENCE();
      #pragma unroll
      for(int i=0;i<3;i++) if(i<2||lane<32) __builtin_amdgcn_raw_buffer_store_b128(ldsr128(to[i]),__builtin_amdgcn_make_buffer_rsrc(p.out,0,2048*1280,0x00020000),oo[i],0,16);
    }
  }
}
extern "C" void kernel_launch(void* const* d_in,const int* in_sizes,int n_in,void* d_out,int out_size,void* d_ws,size_t ws_size,hipStream_t stream){
  Params p;
  p.x=(const u16*)d_in[0]; p.lar=(const u16*)d_in[1]; p.aim=(const u16*)d_in[2]; p.cre=(const u16*)d_in[3]; p.cim=(const u16*)d_in[4];
  p.ldt=(const u16*)d_in[5]; p.dsk=(const u16*)d_in[6]; p.win=(const u16*)d_in[8]; p.wsp=(const u16*)d_in[9]; p.bsp=(const u16*)d_in[10]; p.wout=(const u16*)d_in[11];
  p.out=(u16*)d_out; p.ws=(char*)d_ws;
  (void)hipFuncSetAttribute((const void*)fused,hipFuncAttributeMaxDynamicSharedMemorySize,LDS_TOTAL);
  fused<<<256,512,LDS_TOTAL,stream>>>(p);
}
```

```cpp
#include <hip/hip_runtime.h>
#include <utility>
#pragma clang diagnostic ignored "-Wint-to-pointer-cast"
typedef unsigned short u16;
typedef unsigned int u32;
typedef unsigned long long u64;
typedef __attribute__((ext_vector_type(8))) __bf16 bf8;
typedef __attribute__((ext_vector_type(8))) _Float16 hf8;
typedef __attribute__((ext_vector_type(2))) __bf16 bf2;
typedef __attribute__((ext_vector_type(2))) _Float16 hf2;
typedef __attribute__((ext_vector_type(2))) float f2;
typedef __attribute__((ext_vector_type(4))) float f4;
typedef __attribute__((ext_vector_type(16))) float f16v;
typedef __attribute__((ext_vector_type(4))) u32 u4;
typedef __attribute__((ext_vector_type(2))) u32 u2;
typedef __attribute__((ext_vector_type(4))) short s4;
#define LAS __attribute__((address_space(3)))
#define GAS __attribute__((address_space(1)))
#define DI __device__ __forceinline__

constexpr u32 OFF_Z2F = 0;
constexpr u32 OFF_Z1T = OFF_Z2F + 64*54*1024;
constexpr u32 OFF_XNT = OFF_Z1T + 128*27*1024;
constexpr u32 OFF_WOF = OFF_XNT + 640*4096;
constexpr u32 OFF_HF  = OFF_WOF + 8*27*5*1024;
constexpr u32 OFF_AT  = OFF_HF + 128*27*1024;
constexpr u32 OFF_ST  = OFF_AT + 640*4096;
constexpr u32 OFF_RS  = OFF_ST + 16*14*2048+8192;
constexpr u32 OFF_EP  = OFF_RS + 2048*16;
constexpr u32 OFF_F2  = OFF_EP + 256;
constexpr u32 OFF_F1  = OFF_F2 + 256*128;
constexpr u32 OFF_FX  = OFF_F1 + 256*128;
constexpr u32 OFF_FW  = OFF_FX + 256*128;
constexpr u32 OFF_FH  = OFF_FW + 32*128;
constexpr u32 OFF_FA  = OFF_FH + 256*128;
constexpr u32 WS_END  = OFF_FA + 256*128;

struct Params {
  const u16 *x, *lar, *aim, *cre, *cim, *ldt, *dsk, *win, *wsp, *bsp, *wout;
  u16* out; char* ws;
};
#define STAMP(id,wv) do{}while(0)
#define RST(i) do{}while(0)
template<class F,int... I> DI void sfor_impl(F&& f, std::integer_sequence<int,I...>){ (f(std::integral_constant<int,I>{}),...); }
template<int N,class F> DI void sfor(F&& f){ sfor_impl(f, std::make_integer_sequence<int,N>{}); }

DI float bflo(u32 v){ return __uint_as_float(v<<16); }
DI float bfhi(u32 v){ return __uint_as_float(v&0xffff0000u); }
DI float bf1(u16 v){ return __uint_as_float(((u32)v)<<16); }
DI u32 pkbf(float a,float b){ f2 v={a,b}; return __builtin_bit_cast(u32,__builtin_convertvector(v,bf2)); }
DI u32 pkhf(float a,float b){ f2 v={a,b}; return __builtin_bit_cast(u32,__builtin_convertvector(v,hf2)); }
DI float rbf(float a){ return bflo(pkbf(a,a)); }
DI float hlo(u32 v){ return (float)__builtin_bit_cast(hf2,v)[0]; }
DI float hhi(u32 v){ return (float)__builtin_bit_cast(hf2,v)[1]; }
DI float dot2(u32 a,u32 b,float c){ return __builtin_amdgcn_fdot2_f32_bf16(__builtin_bit_cast(bf2,a),__builtin_bit_cast(bf2,b),c,false); }
DI f4 mfma16(u4 a,u4 b,f4 c){ return __builtin_amdgcn_mfma_f32_16x16x32_bf16(__builtin_bit_cast(bf8,a),__builtin_bit_cast(bf8,b),c,0,0,0); }
DI f16v mfma32h(u4 a,u4 b,f16v c){ return __builtin_amdgcn_mfma_f32_32x32x16_f16(__builtin_bit_cast(hf8,a),__builtin_bit_cast(hf8,b),c,0,0,0); }
DI u4 ldsr128(u32 a){ return *(LAS u4*)a; }
DI u2 ldsr64(u32 a){ return *(LAS u2*)a; }
DI void ldsw128(u32 a,u4 v){ *(LAS u4*)a=v; }
DI u2 ldstr(u32 a){ return __builtin_bit_cast(u2,__builtin_amdgcn_ds_read_tr16_b64_v4i16((LAS s4*)a)); }
DI u32 uni(u32 v){ return __builtin_amdgcn_readfirstlane(v); }

template<int OFF> DI void gload(u4& dst,u32 voff,const char* base){ asm volatile("global_load_dwordx4 %0, %1, %2 offset:%3 sc1" : "=v"(dst) : "v"(voff), "s"(base), "n"(OFF)); }
template<int OFF> DI void gload_t(u4& dst,u32 voff,const char* base){ asm volatile("global_load_dwordx4 %0, %1, %2 offset:%3 sc1" : "+v"(dst) : "v"(voff), "s"(base), "n"(OFF)); }
DI void mfma4(f4& c0,f4& c1,f4& c2,f4& c3,const u4& a,const u4 (&b)[4]){
  asm volatile("v_mfma_f32_16x16x32_bf16 %0, %4, %5, %0\n\tv_mfma_f32_16x16x32_bf16 %1, %4, %6, %1\n\tv_mfma_f32_16x16x32_bf16 %2, %4, %7, %2\n\tv_mfma_f32_16x16x32_bf16 %3, %4, %8, %3"
    : "+v"(c0),"+v"(c1),"+v"(c2),"+v"(c3) : "v"(a),"v"(b[0]),"v"(b[1]),"v"(b[2]),"v"(b[3])); }
DI void mfma4z(f4& c0,f4& c1,f4& c2,f4& c3,const u4& a,const u4 (&b)[4]){
  asm volatile("v_mfma_f32_16x16x32_bf16 %0, %4, %5, 0\n\tv_mfma_f32_16x16x32_bf16 %1, %4, %6, 0\n\tv_mfma_f32_16x16x32_bf16 %2, %4, %7, 0\n\tv_mfma_f32_16x16x32_bf16 %3, %4, %8, 0"
    : "=&v"(c0),"=&v"(c1),"=&v"(c2),"=&v"(c3) : "v"(a),"v"(b[0]),"v"(b[1]),"v"(b[2]),"v"(b[3])); }
template<int CNT> DI void waitB(u4 (&B)[7]){ asm volatile("s_waitcnt vmcnt(%7)" : "+v"(B[0]),"+v"(B[1]),"+v"(B[2]),"+v"(B[3]),"+v"(B[4]),"+v"(B[5]),"+v"(B[6]) : "n"(CNT) : "memory"); }
DI void waitA(u4 (&A)[4]){ asm volatile("s_waitcnt lgkmcnt(0)" : "+v"(A[0]),"+v"(A[1]),"+v"(A[2]),"+v"(A[3]) :: "memory"); }
#define WAITV(n) asm volatile("s_waitcnt vmcnt(%0)" :: "n"(n) : "memory")
#define WAITL0() asm volatile("s_waitcnt lgkmcnt(0)" ::: "memory")
#define SCHED() __builtin_amdgcn_sched_barrier(0)
DI void bar(){ asm volatile("s_waitcnt lgkmcnt(0)\n\ts_barrier" ::: "memory"); }
DI f2 unpk(u32 v){ return f2{bflo(v),bfhi(v)}; }
DI u32 pkbf2(f2 v){ return __builtin_bit_cast(u32,__builtin_convertvector(v,bf2)); }
DI u32 pkhf2(f2 v){ return __builtin_bit_cast(u32,__builtin_convertvector(v,hf2)); }
DI f2 fma2(f2 a,f2 b,f2 c){ return __builtin_elementwise_fma(a,b,c); }
DI f2 gelu2(f2 x){
  f2 u=x*fma2(x*x,f2{-0.10294324f,-0.10294324f},f2{-2.30220820f,-2.30220820f});
  f2 d=f2{__builtin_amdgcn_exp2f(u[0]),__builtin_amdgcn_exp2f(u[1])}+f2{1.f,1.f};
  return x*f2{__builtin_amdgcn_rcpf(d[0]),__builtin_amdgcn_rcpf(d[1])};
}
DI void ln_fin(float mean,float var,float& m,float& rinv){
  m=rbf(mean); float v=rbf(__builtin_fmaxf(var,0.f));
  float s=rbf(__builtin_amdgcn_sqrtf(rbf(v+1e-5f))); float r=__builtin_amdgcn_rcpf(s); rinv=__builtin_fmaf(__builtin_fmaf(-s,r,1.f),r,r);
}
typedef __amdgpu_buffer_rsrc_t rsrc_t;
DI void st16(rsrc_t rs,u32 off,u4 v){ __builtin_amdgcn_raw_buffer_store_b128(v,rs,off,0,16); }
DI void st8(rsrc_t rs,u32 off,u2 v){ __builtin_amdgcn_raw_buffer_store_b64(v,rs,off,0,16); }
DI void st4(rsrc_t rs,u32 off,u32 v){ __builtin_amdgcn_raw_buffer_store_b32(v,rs,off,0,16); }
DI u4 ld16(rsrc_t rs,u32 off){ return __builtin_amdgcn_raw_buffer_load_b128(rs,off,0,16); }
DI u2 ld8(rsrc_t rs,u32 off){ return __builtin_amdgcn_raw_buffer_load_b64(rs,off,0,16); }
DI u32 ld4(rsrc_t rs,u32 off){ return __builtin_amdgcn_raw_buffer_load_b32(rs,off,0,16); }
constexpr int SPIN=1<<17;
constexpr u32 TAGINC=0x9E3779B9u;
template<int SL=2> DI void pollw(rsrc_t rs,u32 off,u32 tag){
  #pragma unroll 1
  for(int s=0;s<SPIN;s++){ u32 v=ld4(rs,off); asm volatile("" ::: "memory"); if(__all(v==tag)) break; __builtin_amdgcn_s_sleep(SL); }
}

DI f16v mfma32b(u4 a,u4 b,f16v c){ return __builtin_amdgcn_mfma_f32_32x32x16_bf16(__builtin_bit_cast(bf8,a),__builtin_bit_cast(bf8,b),c,0,0,0); }
DI u4 mksrd(const void* b){ u64 a=(u64)b; return u4{(u32)a,(u32)(a>>32)&0xffffu,0xffffffffu,0x00020000u}; }
template<int IMM> DI void setm0(u32 base){ asm volatile("s_add_u32 m0, %0, %1\n\ts_nop 0" :: "s"(base), "n"(IMM) : "scc","memory"); }
template<int OFF> DI void dma(u4 srd,u32 voff,u32 soff){ asm volatile("buffer_load_dwordx4 %0, %1, %2 offen offset:%3 sc1 lds" :: "v"(voff), "s"(srd), "s"(soff), "n"(OFF) : "memory"); }
DI void touch(u4 srd,u32 voff,u32 soff){ asm volatile("buffer_load_dword %0, %1, %2 offen sc1 lds" :: "v"(voff), "s"(srd), "s"(soff) : "memory"); }
template<int OFF> DI void gload8(u2& dst,u32 voff,const char* base){ asm volatile("global_load_dwordx2 %0, %1, %2 offset:%3 sc1" : "=v"(dst) : "v"(voff), "s"(base), "n"(OFF)); }
DI void gload2(u32& dst,u32 voff,const char* base){ asm volatile("global_load_ushort %0, %1, %2" : "=v"(dst) : "v"(voff), "s"(base)); }
DI u32 arrive(u32 a,int){ u32 o; asm volatile("s_mov_b64 exec, 1\n\tds_add_rtn_u32 %0, %1, %2\n\ts_mov_b64 exec, -1\n\ts_waitcnt lgkmcnt(0)" : "=&v"(o) : "v"(a), "v"(1u) : "memory"); return uni(o); }
DI void ldswait(u32 a,u32 n){ while(__hip_atomic_load((LAS u32*)a,__ATOMIC_RELAXED,__HIP_MEMORY_SCOPE_WORKGROUP)<n) __builtin_amdgcn_s_sleep(1); asm volatile("" ::: "memory"); }
DI float addf(float a,float b){ float o; asm("v_add_f32 %0, %1, %2" : "=v"(o) : "v"(a), "v"(b)); return o; }
DI void tie1(u32& g){ asm volatile("" : "+v"(g) :: "memory"); }
DI u32 opq(u32 v){ asm volatile("" : "+v"(v)); return v; }

constexpr u32 STG=32768;
constexpr u32 RING=24576, RB=62848, CONV=0, KP=2208, UOF=16768, CVS=UOF+4160, S7O=4*KP, S9O=S7O+2112, KZO=S9O+2112, MISC=RB+4*RING, JNK=MISC+2048, PM=163840-128, LDS_TOTAL=163840;
constexpr u32 ATL=2*CVS;
constexpr u32 C_FULL=PM, C_G1=PM+4, C_Z2=PM+8, C_Z1=PM+12, C_RED=PM+16, C_H=PM+20, C_FREE=PM+24, C_CV=PM+28, C_Z1R=PM+32, C_TAG=PM+40, C_KP=PM+48, C_DONE=PM+64, C_ZQ=PM+68, C_CS=PM+72, C_K2=PM+80, C_P=PM+96, C_AT=PM+112, C_WR=PM+116;
constexpr u32 CODE_BYTES=37888;
#define LA 3
#define TC_S 7
#define G2PRIO 2
#define XMAX 9
#define GS_K 35
#define WNT ""
#define TS1 6
#define HPRIO 3
#define CPRIO 3
#define POLLM 1
#define HPF 5
#define CVSKIP 0
#define BROT 0
#define STAG 0u
#define FXDLY 4
#define HSH 0
#define ZW 1
#define HZZ 14
#define ZPRE 1
#define PDLY 8
#define RS_S 7
#define KROT ((((j+rt)&3)*5)>>1)
DI void signal(u32 a,int){ asm volatile("s_mov_b64 exec, 1\n\tds_add_u32 %0, %1\n\ts_mov_b64 exec, -1" :: "v"(a), "v"(1u) : "memory"); }
template<int OFF> DI void gload4(u32& dst,u32 voff,const char* base){ asm volatile("global_load_dword %0, %1, %2 offset:%3 sc1" : "=v"(dst) : "v"(voff), "s"(base), "n"(OFF)); }
template<int CNT> DI void waitG(u4& g){ asm volatile("s_waitcnt vmcnt(%1)" : "+v"(g) : "n"(CNT) : "memory"); }

DI f2 cmulp(f2 a,f2 b,f2 bs){ return fma2(f2{a[1],a[1]},bs,f2{a[0],a[0]}*b); }
DI float mulf(float a,float b){ float o; asm("v_mul_f32 %0, %1, %2" : "=v"(o) : "v"(a),"v"(b)); return o; }
DI float fmaf1(float a,float b,float c){ float o; asm("v_fma_f32 %0, %1, %2, %3" : "=v"(o) : "v"(a),"v"(b),"v"(c)); return o; }
DI u32 ldsv(u32 a){ return __hip_atomic_load((LAS u32*)a,__ATOMIC_RELAXED,__HIP_MEMORY_SCOPE_WORKGROUP); }
DI void ldswait4(u32 a,int lane,u32 n){ while(__any(ldsv(a+(lane&3)*4)<n)) __builtin_amdgcn_s_sleep(1); asm volatile("" ::: "memory"); }
DI u32 gettag(u32 a){ ldswait(a+4,1u); return uni(ldsv(a)); }
DI f2 cpow_dir(float lr,float rv,float pw){ float e=__builtin_amdgcn_exp2f(lr*pw); float f=__builtin_amdgcn_fractf(rv*pw);
  return f2{e*__builtin_amdgcn_cosf(f),e*__builtin_amdgcn_sinf(f)}; }
DI u32 dup(u32 a){ return __builtin_amdgcn_perm(a,a,0x01000100u); }
DI u32 cat(u32 lo,u32 hi){ return __builtin_amdgcn_perm(hi,lo,0x05040100u); }
DI u32 lds16(u32 a){ return *(LAS u16*)a; }

template<class CB> DI float kgen_tab(u32 cb,int lane,const u32 (&pr)[6],float (&kp)[11],CB&& mid){
  const float dt=rbf(__expf(bflo(pr[4])));
  const float cr[2]={bflo(pr[2]),bfhi(pr[2])}, cm[2]={bflo(pr[3]),bfhi(pr[3])};
  float mx=fmaxf(fmaxf(fabsf(cr[0]),fabsf(cm[0])),fmaxf(fabsf(cr[1]),fabsf(cm[1])));
  u32 eb=__float_as_uint(mx)&0x7f800000u;
  #define DPPMAX(c) eb=max(eb,(u32)__builtin_amdgcn_update_dpp(0,(int)eb,c,0xf,0xf,false))
  DPPMAX(0x128); DPPMAX(0x124); DPPMAX(0x122); DPPMAX(0x121);
  eb=max(max((u32)__builtin_amdgcn_readlane(eb,0),(u32)__builtin_amdgcn_readlane(eb,16)),max((u32)__builtin_amdgcn_readlane(eb,32),(u32)__builtin_amdgcn_readlane(eb,48))); eb=eb<0x01000000u?0x3f800000u:(eb>0x7e000000u?0x7e000000u:eb);
  const float ksc=2.f*dt*__uint_as_float(eb), isc=__uint_as_float(0x7f000000u-eb);
  const int r=lane&31,h=lane>>5;
  const u32 TP=272, TA=cb, TB=cb+22*TP;
  f16v acc={};
  f2 an[2][4];
  #pragma unroll
  for(int c=0;c<2;c++){ const float l_a=c?bfhi(pr[0]):bflo(pr[0]), i_m=c?bfhi(pr[1]):bflo(pr[1]);
    const float lr=(-rbf(__expf(l_a))*dt)*1.44269504f, rv=(i_m*dt)*0.15915494f;
    an[c][0]=cpow_dir(lr,rv,1.f); an[c][1]=cpow_dir(lr,-rv,8.f); an[c][2]=cpow_dir(lr,-rv,16.f); an[c][3]=cpow_dir(lr,rv,256.f); }
  u32 hold[54]; const u32 wt=TA+lane*4;
  struct P2 { f2 v[2]; };
  auto mul=[&](const P2& x,int k){ P2 o;
    #pragma unroll
    for(int c=0;c<2;c++) o.v[c]=cmulp(x.v[c],an[c][k],f2{-an[c][k][1],an[c][k][0]});
    return o; };
  auto put=[&](int row,const P2& x){ *(LAS u32*)(wt+row*TP)=pkhf2(x.v[0]); hold[row]=pkhf2(x.v[1]); };
  P2 e={{f2{1.f,0.f},f2{1.f,0.f}}}, o={{an[0][1],an[1][1]}}, bk[8], t;
  bk[0].v[0]=f2{cr[0]*isc,cm[0]*isc}; bk[0].v[1]=f2{cr[1]*isc,cm[1]*isc}; put(0,bk[0]); t=bk[0];
  constexpr int tr[8]={8,10,11,16,17,20,21,18};
  #pragma unroll
  for(int k=0;k<16;k++){ put(22+2*k,e); put(23+2*k,o);
    if(k<15){ if(k) e=mul(e,2); else { e.v[0]=an[0][2]; e.v[1]=an[1][2]; } o=mul(o,2); }
    if(k<8){ t=mul(t,3); put(tr[k],t); }
    if(k<7){ bk[k+1]=mul(bk[k],0); put(k+1,bk[k+1]); }
    if(k==8){ put(9,mul(bk[2],3)); put(13,mul(bk[6],3)); t=mul(bk[4],3); put(12,t); }
    if(k==9||k==10){ t=mul(t,3); put(5+k,t); }
    SCHED(); }
  const int ra=(r>21||r==19)?0:r;
  #pragma unroll
  for(int c=0;c<2;c++){
    asm volatile("" ::: "memory");
    u4 fa[8],fb[8];
    #pragma unroll
    for(int ks=0;ks<8;ks++){ fa[ks]=ldsr128(TA+ra*TP+ks*32+h*16); fb[ks]=ldsr128(TB+r*TP+ks*32+h*16); }
    asm volatile("" ::: "memory");
    if(c==0){
      #pragma unroll
      for(int i=0;i<54;i++) if(i!=19) *(LAS u32*)(wt+i*TP)=hold[i]; }
    #pragma unroll
    for(int ks=0;ks<8;ks++) acc=mfma32h(fa[ks],fb[ks],acc);
  }
  asm volatile("" ::: "memory");
  #pragma unroll
  for(int i=0;i<11;i++) kp[i]=acc[i];
  return ksc;
}
DI void kgen_exp(u32 cb,int lane,const float (&acc)[11],const u32 (&ga)[11]){
  const int r=lane&31,h=lane>>5;
  { const u32 a4=cb+r*16+h*8, a5=cb+r*16+h*1024, az=cb+KZO+r*2+h*128;
    #define PK(addr,base,reg) do{ *(LAS _Float16*)((addr)+2*(base))=(_Float16)acc[reg]; }while(0)
    PK(a4,0,0);PK(a4,1,1);PK(a4,2,2);PK(a4,3,3); PK(a4,256,4);PK(a4,258,5);PK(a4,512,6);PK(a4,768,7);
    PK(a5,1024,8); PK(az,255,10); PK(az,127,8); PK(az,159,9);
    #undef PK
    if(lane<16) ldsw128(cb+KZO+lane*16,u4{0,0,0,0});
  }
  { const u32 g0=cb+2*(1024-8*lane);
    u32 f2_=lds16(g0+(lane?8:0)), a2=lds16(g0), m2=lds16(g0-8), b2=lds16(g0-16);
    u32 e[11];
    #pragma unroll
    for(int j=0;j<11;j++) e[j]=lds16(ga[j]);
    u32 k01=*(LAS u32*)cb, k23=*(LAS u32*)(cb+4), kl=lds16(cb+2048);
    WAITL0();
    const u32 wo=cb+96+lane*16;
    { u32 aa=dup(a2),mm=dup(m2),bb=dup(b2),am=cat(a2,m2),fa=cat(f2_,a2),mb=cat(m2,b2);
      ldsw128(wo,u4{aa,am,mm,bb}); ldsw128(wo+KP,u4{aa,aa,mm,mb}); ldsw128(wo+2*KP,u4{aa,aa,am,mm}); ldsw128(wo+3*KP,u4{fa,aa,aa,mm}); }
    { u32 o0=cat(e[0],e[1]),o1=cat(e[2],e[3]),o2=cat(e[4],e[5]),o3=cat(e[6],e[7]),o4=cat(e[8],e[9]);
      u32 p1=cat(e[1],e[2]),d0=cat(e[3],e[4]),d1=cat(e[5],e[6]),d2=cat(e[7],e[8]),d3=cat(e[9],e[10]);
      ldsw128(wo+1024,u4{d0,d1,d2,d3}); ldsw128(wo+1024+KP,u4{o1,o2,o3,o4}); ldsw128(wo+1024+2*KP,u4{p1,d0,d1,d2}); ldsw128(wo+1024+3*KP,u4{o0,o1,o2,o3}); }
    if(lane<10){ if(lane){k01=0;k23=0;} if(lane!=9) kl=0;
      const u32 d0=k01&0xffffu, o1=__builtin_amdgcn_alignbit(k01,k01,16), o0=__builtin_amdgcn_alignbit(k23,k23,16), p1=(k23&0xffffu)|(k01&0xffff0000u), kk=dup(kl), zk=kl<<16;
      const u32 w2=lane<4? wo+2048 : wo-160;
      ldsw128(w2,u4{d0,0,kk,kk}); ldsw128(w2+KP,u4{o1,0,zk,kk}); ldsw128(w2+2*KP,u4{p1,d0,0,kk}); ldsw128(w2+3*KP,u4{o0,o1,0,zk}); }
  }
}
DI f4 mfma16h(u4 a,u4 b,f4 c){ return __builtin_amdgcn_mfma_f32_16x16x32_f16(__builtin_bit_cast(hf8,a),__builtin_bit_cast(hf8,b),c,0,0,0); }
DI u4 lds2x64(u32 a){ const u2 lo=ldsr64(a), hi=ldsr64(a+8); return u4{lo[0],lo[1],hi[0],hi[1]}; }
DI void conv_win(u32 cb,int lane,u4 (&sw)[4]){
  float P[25]; P[0]=0.f;
  #pragma unroll
  for(int c=0;c<4;c++){ const int X=2*lane-2+c; const u4 v=ldsr128(cb+UOF+(X&3)*1040+((X>>2)+1)*16);
    #pragma unroll
    for(int e=0;e<8;e++){ const int i=8*c+e-4; if(i>=0&&i<24) P[i+1]=P[i]+((e&1)?hhi(v[e>>1]):hlo(v[e>>1])); } }
  #pragma unroll
  for(int k=0;k<8;k++){ sw[k>>2][k&3]=pkhf(P[2*k+8]-P[2*k+1],P[2*k+9]-P[2*k+2]); sw[2+(k>>2)][k&3]=pkhf(P[2*k+9]-P[2*k],P[2*k+10]-P[2*k+1]); }
}
DI void conv_help(u32 cb,int lane,const u4 (&sw)[4]){
  const int r=lane&15,q=lane>>4; f4 y[2][2]={};
  ldsw128(cb+S7O+48+lane*32,sw[0]); ldsw128(cb+S7O+64+lane*32,sw[1]); ldsw128(cb+S9O+48+lane*32,sw[2]); ldsw128(cb+S9O+64+lane*32,sw[3]);
  if(lane<3){ ldsw128(cb+S7O+lane*16,u4{0,0,0,0}); ldsw128(cb+S9O+lane*16,u4{0,0,0,0}); }
  const u32 ka=cb+(r&3)*KP+16*q-8*(r>>2)+64, ub=cb+UOF+q*1040+(r+1)*16;
  u4 a[2][4][2],bb[2][4]; const u32 kg=ka+64, ug=ub+272;
  auto rdn=[&](int g){
    #pragma unroll
    for(int i=0;i<4;i++){ a[g&1][i][0]=lds2x64(kg+(3-g)*256+i*64+32); a[g&1][i][1]=lds2x64(kg+(3-g)*256+i*64); bb[g&1][i]=ldsr128(ug+(3-g)*64+i*16); } };
  auto mmn=[&](int g){
    #pragma unroll
    for(int i=0;i<4;i++){ y[0][1]=mfma16h(a[g&1][i][0],bb[g&1][i],y[0][1]); y[1][1]=mfma16h(a[g&1][i][1],bb[g&1][i],y[1][1]); } };
  if(HSH==4){ rdn(0); rdn(1); SCHED(); mmn(0); SCHED(); rdn(2); SCHED(); mmn(1); SCHED(); rdn(3); SCHED(); mmn(2); SCHED(); }
  if(HSH==2){ rdn(2); rdn(3); SCHED(); mmn(2); SCHED(); }
  const u4 a0=lds2x64(ka+32), a1=lds2x64(ka), b0=ldsr128(ub), b1=ldsr128(ub+256), a2=lds2x64(ka-32), b2=ldsr128(ub-16), b3=ldsr128(ub+240);
  const u32 sa=cb+(((r>>2)&1)?S9O:S7O)+2*(64*q+24-8*(r>>2)+4*(r&3)), kz=cb+KZO+8*(r-2*q+23); u4 bz[4],fa[4][2];
  #pragma unroll
  for(int ks=0;ks<4;ks++) bz[ks]=lds2x64(kz+(3-ks)*64);
  SCHED(); if(HSH) mmn(3); SCHED();
  #pragma unroll
  for(int i=0;i<8;i++){ const u2 lo=ldstr(sa+(i>>1)*512+(i&1)*32+64), hi=ldstr(sa+(i>>1)*512+(i&1)*32); fa[i>>1][i&1]=u4{lo[0],lo[1],hi[0],hi[1]}; }
  SCHED();
  y[0][0]=mfma16h(a2,b2,y[0][0]); y[0][1]=mfma16h(a2,b3,y[0][1]);
  y[0][0]=mfma16h(a0,b0,y[0][0]); y[1][0]=mfma16h(a1,b0,y[1][0]); y[0][1]=mfma16h(a0,b1,y[0][1]); y[1][1]=mfma16h(a1,b1,y[1][1]);
  #pragma unroll
  for(int ks=0;ks<4;ks++)
    #pragma unroll
    for(int m=0;m<2;m++){ y[m][1]=mfma16h(fa[ks][m],bz[ks],y[m][1]); if(ks<2) y[m][0]=mfma16h(fa[ks][m],bz[ks+2],y[m][0]); }
  #pragma unroll
  for(int i=0;i<4;i++) *(LAS f4*)(cb+S7O+i*1024+lane*16)=y[i&1][i>>1];
}
#define RDA(S,i,m) asm volatile("ds_read2_b64 %0, %1 offset0:%2 offset1:%3" : "=v"(AF[S][i][m]) : "v"(ka), "n"((1-(i))*128+(1-(m))*4), "n"((1-(i))*128+(1-(m))*4+1))
#define RDB(S,i,ad,o) asm volatile("ds_read_b128 %0, %1 offset:%2" : "=v"(BF[S][i]) : "v"(ad), "n"(o))
#define TIEA(S,i) "+v"(AF[S][i][0]),"+v"(AF[S][i][1])
#define CV_M(S,i,n) do{ y[0][n]=mfma16h(AF[S][i][0],BF[S][(n)-(i)],y[0][n]); y[1][n]=mfma16h(AF[S][i][1],BF[S][(n)-(i)],y[1][n]); SCHED(); }while(0)
#define CV_U0() u32 u0=u1-256; u0=((int)u0<(int)uz)?uz:u0
template<class CB> DI void conv_wave(CB&& mid,rsrc_t rs,int d,u32 cb,u32 flag,u32 turn,u32 need,int lane,float ksc,float dsk){
  const int r=lane&15,q=lane>>4; u32 ka=cb+(r&3)*KP+16*q-8*(r>>2)+1088; const u32 uz=cb+UOF+q*1040; u32 u1=uz+(r+17)*16; u4 AF[2][2][2]; u4 BF[2][4]; f4 y[2][4]={};
  #define CV_WT(S) asm volatile("s_waitcnt lgkmcnt(0)" : TIEA(S,0),TIEA(S,1),"+v"(BF[S][0]),"+v"(BF[S][1]),"+v"(BF[S][2]),"+v"(BF[S][3]))
  #define CV_STEP(C,N) do{ CV_U0(); CV_M(C,0,0); RDA(N,0,0); RDB(N,0,u0,0); RDA(N,0,1); SCHED(); CV_M(C,0,1); RDB(N,1,u1,0); RDA(N,1,0); RDB(N,2,u1,256); SCHED(); CV_M(C,0,2); RDA(N,1,1); RDB(N,3,u1,512); \
      ka-=64; u1-=16; SCHED(); CV_M(C,0,3); CV_M(C,1,1); CV_M(C,1,2); if(dl<8-2*HSH) CV_M(C,1,3); CV_WT(N); }while(0)
  { CV_U0(); RDA(0,0,0); RDA(0,0,1); RDA(0,1,0); RDA(0,1,1); RDB(0,0,u0,0); RDB(0,1,u1,0); RDB(0,2,u1,256); RDB(0,3,u1,512); ka-=64; u1-=16; CV_WT(0); }
  #pragma unroll 1
  for(int dl=0;dl<8;dl++){ const u32 tv=ldsv(turn); CV_STEP(0,1); if(uni(tv)>=need) __builtin_amdgcn_s_setprio(3); if(dl==7){ ka+=64; u1+=16; } CV_STEP(1,0); }
  mid();
  asm volatile("s_nop 15" : "+v"(y[0][0]),"+v"(y[0][1]),"+v"(y[0][2]),"+v"(y[0][3]),"+v"(y[1][0]),"+v"(y[1][1]),"+v"(y[1][2]),"+v"(y[1][3]));
  __builtin_amdgcn_s_setprio(3);
  u2 uv[2][4];
  #pragma unroll
  for(int n=0;n<4;n++)
    #pragma unroll
    for(int m=0;m<2;m++) uv[m][n]=ldsr64(cb+UOF+(2*m+(q>>1))*1040+(16*n+r+1)*16+8*(q&1));
  const u32 sc=cb+UOF+r*64+q*8;
  auto fin=[&](int n){
    #pragma unroll
    for(int m=0;m<2;m++){ u2 o;
      #pragma unroll
      for(int e=0;e<2;e++) o[e]=pkbf(fmaf1(hlo(uv[m][n][e]),dsk,mulf(y[m][n][2*e],ksc)),fmaf1(hhi(uv[m][n][e]),dsk,mulf(y[m][n][2*e+1],ksc)));
      *(LAS u2*)(sc+n*1024+m*32)=o; } };
  ldswait(flag,2u);
  { f4 hp[4];
    #pragma unroll
    for(int i=0;i<4;i++) hp[i]=*(LAS f4*)(cb+S7O+i*1024+lane*16);
    fin(0); fin(1);
    #pragma unroll
    for(int i=0;i<4;i++)
      #pragma unroll
      for(int e=0;e<4;e++) y[i&1][2+(i>>1)][e]=addf(y[i&1][2+(i>>1)][e],hp[i][e]);
    fin(2); fin(3); }
  u4 ov[4];
  #pragma unroll
  for(int n=0;n<4;n++) ov[n]=ldsr128(cb+UOF+n*1024+lane*16);
  #pragma unroll
  for(int n=0;n<4;n++) st16(rs,OFF_AT+d*4096+n*1024+lane*16,ov[n]);
  WAITV(0);
}

#define NB 3
constexpr int g2ops(int h){ return h<0? 7 : (h+NB<16?7:0)+((h>=3&&h<=10)?4:0)+(h==HZZ-3?1:0)+(h==HZZ?8:0); }
constexpr int g2cnt(int h){ int c=0; for(int i=h-NB+1;i<h;i++) c+=g2ops(i); return c; }
struct Frag { u4 x[4], w[4][2]; };
__global__ __launch_bounds__(512,2) void fused(Params p){
  extern __shared__ __attribute__((aligned(16))) char sm[];
  const u32 smb=(u32)(size_t)(LAS char*)sm;
  u64 pc0; asm volatile("s_getpc_b64 %0" : "=s"(pc0));
  const int b=blockIdx.x, xc=b&7, idx=b>>3;
  const int tid=threadIdx.x, w=uni(tid>>6); int lane=tid&63;
  auto cpf=[&](u32 o){ u4 sc=mksrd((const void*)(pc0&~127ull)); sc[2]=CODE_BYTES; setm0<JNK>(smb); touch(sc,lane*128,o); };
  if(idx>=28) cpf(w*8192);
  SCHED();
  const int rt=4*(xc>>1)+(idx&3), j=7*(xc&1)+(idx>>2), mi=w&3, g1=w>>2;
  u32 vx0,vx1,vw1,vw2,k0=KROT*128,k0w=KROT*(64*3456); const u32 mw=smb+w*4096;
  { const u32 lr=lane>>3,c=lane&7; vx0=(rt*128+16*w+lr)*1280+((c^(lr>>1))<<4); vx1=(vx0^64u)+9216;
    vw1=(8*w+lr)*3456+((c^((lr&2)<<1))<<4)+j*128; vw2=vw1+1024+(j==13?0:14*128); }
  asm volatile("" : "+v"(vx0),"+v"(vx1),"+v"(vw1),"+v"(vw2),"+s"(k0),"+s"(k0w));
  setm0<0>(mw);
  SCHED();
  const u4 srdX=mksrd(p.x), srdW=mksrd((const char*)p.win-3072);
  if(idx<28){ dma<0>(srdX,vx0,k0); dma<1024>(srdX,vx1,k0); dma<2048>(srdW,vw2,k0w); dma<3072>(srdW,vw1,k0w); }
  SCHED();
  const rsrc_t rs=__builtin_amdgcn_make_buffer_rsrc(p.ws,0,WS_END,0x00020000);
  const u4 srdS=mksrd(p.ws);
  const int rtB=8*(xc>>1)+(idx&7), ct=4*(xc&1)+(idx>>3);
  const bool hask=idx<28 && idx+28*(w&3)<80, hasc=hask && w>=4;
  const int dch=80*xc+(hask? idx+28*(w&3) : 0);
  const u32 cb=smb+CONV+(w&3)*CVS;
  u32 tag; float ksc=0.f, dsk=0.f;
  STAMP(0,0);
  u32 zp[2][8]={};
  auto z1st=[&](){ const int r=lane&31,h=lane>>5;
    #pragma unroll
    for(int n=0;n<2;n++) if(2*j+n<27){ const u32 oo=OFF_Z1T+((rt*8+2*mi+(r>>4))*27+2*j+n)*1024+(h*16+(r&15))*16;
      st16(rs,oo,u4{zp[n][0],zp[n][1],zp[n][4],zp[n][5]}); st16(rs,oo+512,u4{zp[n][2],zp[n][3],zp[n][6],zp[n][7]}); } };
  auto f1=[&](){ WAITV(0); if(arrive(smb+C_Z1,lane)==3u && lane==0) st4(rs,OFF_F1+(rt*16+j)*128,tag); };
  auto pre01=[&](){
    const u4 sa=mksrd((const char*)p.wsp+(size_t)rtB*64*4096+mi*1024-4096); const u32 v0=(lane>>3)*4096+(((lane&7)^(lane>>4))<<4);
    #pragma unroll 1
    for(int i=0;i<4;i++){ setm0<RB+8192>(smb+mi*RING+i*4096); const u32 g=(i&1)*4+2*mi, k=(i>>1)*128+4096;
      dma<0>(sa,v0,(g&7)*32768+k); dma<1024>(sa,v0^64u,(g&7)*32768+32768-1024+k); dma<2048>(sa,v0,((g+2)&7)*32768-2048+k); dma<3072>(sa,v0^64u,((g+2)&7)*32768+32768-3072+k); } };
  if(idx>=28){
    const int a=xc*4+idx-28, r=lane&15, q=lane>>4;
    const u32 ep=ld4(rs,OFF_EP);
    u4 xr[10]; const char* xs=(const char*)p.x+(size_t)(64*a+8*w+(lane>>3))*1280+(lane&7)*16;
    #pragma unroll
    for(int i=0;i<10;i++) xr[i]=*(const u4*)(xs+i*128);
    if(tid<32) *(LAS u32*)(smb+PM+tid*4)=0u;
    tag=uni(ep)+TAGINC;
    float s0=0.f,s1=0.f,q0=0.f,q1=0.f;
    #pragma unroll
    for(int i=0;i<10;i++){ s0=dot2(xr[i][0],0x3f803f80u,s0); q0=dot2(xr[i][0],xr[i][0],q0); s1=dot2(xr[i][1],0x3f803f80u,s1); q1=dot2(xr[i][1],xr[i][1],q1);
      s0=dot2(xr[i][2],0x3f803f80u,s0); q0=dot2(xr[i][2],xr[i][2],q0); s1=dot2(xr[i][3],0x3f803f80u,s1); q1=dot2(xr[i][3],xr[i][3],q1); }
    s0+=s1; q0+=q1;
    #define DPPADD(v,c) v+=__int_as_float(__builtin_amdgcn_update_dpp(0,__float_as_int(v),c,0xf,0xf,false))
    DPPADD(s0,0xB1); DPPADD(q0,0xB1); DPPADD(s0,0x4E); DPPADD(q0,0x4E); DPPADD(s0,0x141); DPPADD(q0,0x141);
    float m1,ri; { float mean=s0*(1.f/640.f); ln_fin(mean,q0*(1.f/640.f)-mean*mean,m1,ri); }
    if((lane&7)==0) st16(rs,OFF_RS+(64*a+8*w+(lane>>3))*16,u4{__float_as_uint(m1),tag,__float_as_uint(ri),tag});
    STAMP(5,0);
    if(a<27){ const u4 srdO=mksrd((const char*)p.wout+a*(32*1280)+w*5120); const u32 mo=smb+w*5120; setm0<0>(mo);
      dma<0>(srdO,lane*16,0); dma<1024>(srdO,lane*16,0); dma<2048>(srdO,lane*16,0); dma<3072>(srdO,lane*16,0); setm0<4096>(mo); dma<0>(srdO,lane*16,4096); }
    #pragma unroll
    for(int i=0;i<10;i++){ u4 o;
      #pragma unroll
      for(int e=0;e<4;e++) o[e]=pkhf2(fma2(unpk(xr[i][e]),f2{ri,ri},f2{-m1*ri,-m1*ri}));
      ldsw128(smb+RB+(8*w+(lane>>3))*1296+(lane&7)*16+i*128,o); }
    WAITV(0); bar();
    #pragma unroll
    for(int i=0;i<10;i++){ const int c=w*10+i; const u32 ad=smb+RB+(32*((r>>1)&1)+8*q+(r>>2))*1296+c*16+(r&1)*8; u2 lo=ldstr(ad), hi=ldstr(ad+4*1296);
      const u32 sc=smb+40960+w*2048+(i&1)*1024; ldsw128(sc+(r&7)*128+((((r>>3)*4+q)^(r&7))<<4),u4{lo[0],lo[1],hi[0],hi[1]});
      st16(rs,OFF_XNT+a*81920+c*1024+lane*16,ldsr128(sc+(lane>>3)*128+(((lane&7)^(lane>>3))<<4))); }
    WAITV(0);
    if(arrive(smb+C_Z1,lane)==7u && lane==0) st4(rs,OFF_FX+a*128,tag);
    STAMP(6,7);
    if(a<27){
      #pragma unroll
      for(int i=0;i<5;i++){ u32 ad=smb+(8*q+(r>>2))*1280+(w*5+i)*32+(r&3)*8; u2 lo=ldstr(ad), hi=ldstr(ad+4*1280);
        st16(rs,OFF_WOF+(u32)((w*27+a)*5+i)*1024+lane*16,u4{lo[0],lo[1],hi[0],hi[1]}); } }
    WAITV(0); bar();
    if(tid==0) st4(rs,OFF_FW+a*128,tag);
    if(!g1){ pre01(); WAITV(0); }
    STAMP(31,0);
  } else {
    const int r=lane&31, h=lane>>5;
    u32 epv;
    u32 kx[10]; { u32 k=KROT;
      #pragma unroll
      for(int i=0;i<10;i++){ kx[i]=k*128; k=k==9?0:k+1; } }
    auto issue1=[&](auto sc,int k){ constexpr int s=sc.value;
      if(k==0){ setm0<(s&3)*STG>(mw); dma<0>(srdX,vx0,kx[s]); } if(k==1) dma<1024>(srdX,vx1,kx[s]); if(k==2) dma<2048>(srdW,vw2,kx[s]*1728); if(k==3) dma<3072>(srdW,vw1,kx[s]*1728); };
    auto issue=[&](auto sc){ issue1(sc,0); issue1(sc,1); issue1(sc,2); issue1(sc,3); };
    u32 pr[6];
    gload4<0>(epv,0u,p.ws+OFF_EP);
    gload4<0>(pr[0],lane*4,(const char*)(p.lar+dch*128)); gload4<0>(pr[1],lane*4,(const char*)(p.aim+dch*128)); gload4<0>(pr[2],lane*4,(const char*)(p.cre+dch*128)); gload4<0>(pr[3],lane*4,(const char*)(p.cim+dch*128));
    gload2(pr[4],0u,(const char*)(p.ldt+dch)); gload2(pr[5],0u,(const char*)(p.dsk+dch));
    asm volatile("s_barrier" ::: "memory");
    issue(std::integral_constant<int,1>{});
    if(tid<32) *(LAS u32*)(smb+PM+tid*4)=0u;
    u32 ax[2][4],aw[2][4][2];
    { u32 row=32*mi+r, a0=smb+(row>>4)*4096+(row&15)*128+((h^((r>>1)&7))<<4);
      u32 qq=(lane&15)>>2, w0=smb+2048+g1*1024+h*4096+qq*128+8*(lane&1)+(((2*((lane>>4)&1)+((lane>>1)&1))^((qq&2)<<1))<<4);
      #pragma unroll
      for(int i=0;i<2;i++)
        #pragma unroll
        for(int k=0;k<4;k++){ const u32 kk=(k+mi)&3, wo=i*2*STG; ax[i][k]=opq((a0^(kk*32u))+wo); aw[i][k][0]=opq(w0+kk*8192+wo); aw[i][k][1]=opq((w0^64u)+kk*8192+wo); } }
    u4 sel; { const u32 o=((lane&15)==((lane>>4)&1))?0x3f803f80u:0u; sel=u4{o,o,o,o}; asm volatile("" : "+v"(sel)); }
    u32 ga[11];
    #pragma unroll
    for(int i=0;i<11;i++) ga[i]=opq(cb+2u*(u32)rbf((float)(512-8*lane+3-i)));
    Frag F[2];
    asm volatile("s_waitcnt vmcnt(4)" : "+v"(epv),"+v"(pr[0]),"+v"(pr[1]),"+v"(pr[2]),"+v"(pr[3]),"+v"(pr[4]),"+v"(pr[5]) :: "memory");
    bar();
    tag=uni(epv)+TAGINC; dsk=bflo(pr[5]);
    #pragma unroll
    for(int k=0;k<4;k++){
      #pragma unroll
      for(int n=0;n<2;n++){ u2 lo=ldstr(aw[0][k][n]), hi=ldstr(aw[0][k][n]+512); F[0].w[k][n]=u4{lo[0],lo[1],hi[0],hi[1]}; }
      F[0].x[k]=ldsr128(ax[0][k]); }
    issue(std::integral_constant<int,2>{});
    STAMP(1,0);
    f16v acc[2]={}; f4 cacc[2]={}; u4 G;
    const u32 go=OFF_RS+rt*2048+mi*512, cp=smb+MISC+g1*1024;
    sfor<10>([&](auto ic){
      constexpr int s=ic.value; Frag& cur=F[s&1]; Frag& nxt=F[(s&1)^1];
      if constexpr(s<9){ WAITV(s==8? 3 : s==7? 7 : 4); asm volatile("s_barrier" ::: "memory"); }
      auto rw=[&](int k,int n){ if constexpr(s<9){ const int i=((s+1)>>1)&1; const u32 so=((s+1)&1)*STG; u2 lo=ldstr(aw[i][k][n]+so), hi=ldstr(aw[i][k][n]+so+512); nxt.w[k][n]=u4{lo[0],lo[1],hi[0],hi[1]}; } };
      auto rx=[&](int k){ if constexpr(s<9){ const int i=((s+1)>>1)&1; const u32 so=((s+1)&1)*STG; nxt.x[k]=ldsr128(ax[i][k]+so); } };
      #pragma unroll
      for(int k=0;k<4;k++){
        acc[0]=mfma32b(cur.w[k][0],cur.x[k],acc[0]); SCHED();
        if constexpr(s+3<=9) issue1(std::integral_constant<int,s+3>{},k);
        rw(k,0); rx(k);
        SCHED();
        acc[1]=mfma32b(cur.w[k][1],cur.x[k],acc[1]); SCHED();
        rw(k,1);
        if(k==0){ cacc[0]=mfma16(cur.w[0][0],sel,cacc[0]); cacc[1]=mfma16(cur.w[0][1],sel,cacc[1]); }
        if constexpr(s==6){ if(k==3){ gload<0>(G,r*16,p.ws+go); setm0<JNK>(smb);
            if(g1){ const u4 sp=mksrd((const char*)p.wsp+(size_t)(xc>>1)*512*4096); const u32 lb=(((xc&1)*28+idx)*4+mi)*74, l0=lb+lane, l1=lb+64+(lane<10?lane:9);
              touch(sp,(l0<16384?l0:16383)*128,0u); touch(sp,(l1<16384?l1:16383)*128,0u); }
            else { touch(srdS,0u,go); touch(srdS,0u,go); } } }
        if constexpr(s==8){ if(k==3) signal(smb+C_DONE,lane); }
        if constexpr(s==9){ if(k==1){
          if((lane&14)==0){ *(LAS f4*)(cp+mi*256+(lane&1)*64+(lane>>4)*16)=cacc[0]; *(LAS f4*)(cp+mi*256+128+(lane&1)*64+(lane>>4)*16)=cacc[1]; }
          signal(smb+C_CS+g1*4,lane); } }
        SCHED();
      }
    });
    RST(0);
    if(!g1) __builtin_amdgcn_s_setprio(2);
    asm volatile("s_waitcnt vmcnt(2)" : "+v"(G) :: "memory");
    ldswait(smb+C_CS+g1*4,4u);
    const float cs0=*(LAS float*)(cp+h*512+r*4)+*(LAS float*)(cp+h*512+256+r*4), cs1=*(LAS float*)(cp+h*512+128+r*4)+*(LAS float*)(cp+h*512+384+r*4);
    RST(2);
    #pragma unroll 1
    for(int sp=0;sp<SPIN && !__all(G[1]==tag&&G[3]==tag);sp++)asm volatile("global_load_dwordx4 %0, %1, %2 sc1\n\ts_waitcnt vmcnt(0)" : "=&v"(G) : "v"(r*16), "s"(p.ws+go) : "memory");
    RST(3);
    const float nm=-__uint_as_float(G[0]), ri1=__uint_as_float(G[2]);
    acc[0]=__builtin_amdgcn_mfma_f32_32x32x2f32(cs0,nm,acc[0],0,0,0); acc[1]=__builtin_amdgcn_mfma_f32_32x32x2f32(cs1,nm,acc[1],0,0,0);
    #pragma unroll
    for(int e=0;e<16;e++) zp[e>>3][e&7]=pkbf2(gelu2(f2{acc[e>>3][2*(e&7)],acc[e>>3][2*(e&7)+1]}*ri1));
    if(g1){ ldswait(smb+C_DONE,8u); RST(4); }
    else {
      float S=0.f,Q=0.f;
      if(j<13){
        #pragma unroll
        for(int e=0;e<8;e++){ S=dot2(zp[0][e],0x3f803f80u,S); Q=dot2(zp[0][e],zp[0][e],Q); } }
      #pragma unroll
      for(int e=0;e<8;e++){ S=dot2(zp[1][e],0x3f803f80u,S); Q=dot2(zp[1][e],zp[1][e],Q); }
      #define XSUM32(v) do{ auto t_=__builtin_amdgcn_permlane32_swap(__float_as_uint(v),__float_as_uint(v),false,false); v=__uint_as_float(t_[0])+__uint_as_float(t_[1]); }while(0)
      XSUM32(S); XSUM32(Q);
      const u32 sto=OFF_ST+rt*(14*2048)+(32*mi+r)*16;
      if(h==0) st16(rs,sto+j*2048,u4{__float_as_uint(S),tag,__float_as_uint(Q),tag});
      RST(6);
    }
    float kp[11]; u4 g[7]={};
    const u32 gof=OFF_ST+rt*(14*2048)+(32*mi+r)*16+h*(7*2048);
    auto samp=[&](){ asm volatile("" ::: "memory");
      #pragma unroll
      for(int i=0;i<7;i++) g[i]=ld16(rs,gof+i*2048);
      asm volatile("" ::: "memory"); };
    if(hasc) ksc=kgen_tab(cb,lane,pr,kp,[](){}); else if(!g1){ pre01(); samp(); }
    if(g1){
      if(hasc){
        RST(6);
        kgen_exp(cb,lane,kp,ga);
        signal(smb+C_KP+mi*4,lane);
        RST(8);
        RST(9);
      }
      if(hasc) ldswait(smb+C_FULL,1u); else if(w<7){ z1st(); f1(); }
      if(ZPRE && hasc) z1st();
    } else {
      RST(1);
      float S,Q;
      #pragma unroll 1
      for(int sp=0;sp<SPIN;sp++){ bool ok=true;
        #pragma unroll
        for(int i=0;i<7;i++) ok=ok&&g[i][1]==tag&&g[i][3]==tag;
        if(__all(ok)) break;
        __builtin_amdgcn_s_sleep(4); samp(); }
      RST(7);
      S=0.f; Q=0.f;
      #pragma unroll
      for(int i=0;i<7;i++){ S+=__uint_as_float(g[i][0]); Q+=__uint_as_float(g[i][2]); }
      XSUM32(S); XSUM32(Q);
      float mean=S*(1.f/864.f), m2,r2; ln_fin(mean,Q*(1.f/864.f)-mean*mean,m2,r2); m2*=-r2;
      ldswait(smb+C_DONE,8u);
      const u32 tb=smb+RB+mi*RING, ad=tb+(8*(lane>>4)+((lane&15)>>2))*80+(lane&3)*8;
      const u32 zo=OFF_Z2F+((rt*4+mi)*54+(j==13?-2:2+4*j))*1024+lane*16;
      #pragma unroll
      for(int n=0;n<2;n++){
        #pragma unroll
        for(int g2=0;g2<4;g2++){ u2 o;
          #pragma unroll
          for(int e=0;e<2;e++) o[e]=pkbf2(fma2(unpk(zp[n][2*g2+e]),f2{r2,r2},f2{m2,m2}));
          *(LAS u2*)(tb+r*80+g2*16+h*8)=o; }
        u2 l0=ldstr(ad), h0=ldstr(ad+320), l1=ldstr(ad+32), h1=ldstr(ad+32+320);
        if(j<13||n){ st16(rs,zo+n*2048,u4{l0[0],l0[1],h0[0],h0[1]}); st16(rs,zo+n*2048+1024,u4{l1[0],l1[1],h1[0],h1[1]}); } }
      RST(8);
      WAITV(0);
      if(arrive(smb+C_Z2,lane)==3u && lane==0) st4(rs,OFF_F2+(rt*16+j)*128,tag);
      RST(9);
    }
  }
  const int ft0=ct<7?7*ct:47;
  bool adone=true; const u32 fao=OFF_FA+((lane<28?lane:0)*8+ct)*128;
  auto aload=[&](){ const u32 vo=(lane>>3)*4096+(lane&7)*16;
    #pragma unroll 1
    for(int i=0;i<10;i++){ setm0<ATL>(smb+i*1024); dma<0>(srdS,vo,OFF_AT+(u32)(80*ct+8*i)*4096+rtB*128); } };
  if(w>=4){
    if(hasc){
      conv_wave([&](){ if(ZPRE) f1(); else z1st(); },rs,dch,cb,smb+C_KP+mi*4,smb+C_ZQ,STAG,lane,ksc,dsk);
      __builtin_amdgcn_s_setprio(0);
      if(!ZPRE) f1();
      if(arrive(smb+C_CV,lane)==(idx<24?2u:1u) && lane==0) st4(rs,OFF_FA+b*128,tag);
    }
    if(w==7){
      if(idx<28){
        __builtin_amdgcn_s_sleep(FXDLY);
        pollw<16>(rs,OFF_FX+(lane&31)*128,tag);
        u4 sw[3][4];
        #pragma unroll
        for(int c=0;c<3;c++)
          #pragma unroll
          for(int i=0;i<4;i++) sw[c][i]=ld16(rs,OFF_XNT+((lane>>3)+8*i)*81920+(80*xc+idx+((c<2||idx<24)?28*c:0))*128+(lane&7)*16);
        #pragma unroll
        for(int c=0;c<3;c++){
          #pragma unroll
          for(int i=0;i<4;i++){ const int x=lane+64*i; ldsw128(smb+c*CVS+UOF+(x&3)*1040+((x>>2)+1)*16,sw[c][i]); }
          if(lane<4) ldsw128(smb+c*CVS+UOF+lane*1040,u4{0,0,0,0}); }
        signal(smb+C_FULL,lane);
        RST(4);
        #pragma unroll 1
        for(int c=0;c<3;c++){
          #pragma unroll
          for(int i=0;i<4;i++){ sw[0][i]=sw[1][i]; sw[1][i]=sw[2][i]; }
          conv_win(smb+c*CVS,lane,sw[2]); }
        RST(5);
        __builtin_amdgcn_s_setprio(HPRIO);
        #pragma unroll 1
        for(int c=0;c<(idx<24?3:2);c++){ ldswait(smb+C_KP+c*4,1u); conv_help(smb+c*CVS,lane,sw[0]); signal(smb+C_KP+c*4,lane);
          #pragma unroll
          for(int i=0;i<4;i++){ sw[0][i]=sw[1][i]; sw[1][i]=sw[2][i]; } } }
      __builtin_amdgcn_s_setprio(0);
      if(idx<28){ z1st(); f1(); }
      pollw<16>(rs,OFF_FW+(lane<27?lane:0)*128,tag); signal(smb+C_Z1R,lane);
      RST(6);
    }
    RST(10);
    ldswait(smb+C_Z1R,5u);
    u4 wq[24];
    #pragma unroll
    for(int i=0;i<24;i++) wq[i]=ld16(rs,OFF_WOF+ct*(135*1024)+40960+mi*RING+((i==23&&mi==3)?22:i)*1024+lane*16);
    if(w<6){
      #pragma unroll 1
      for(u32 i=0;i<20480;i+=4096){ setm0<0>(cb+i); const u32 o=OFF_WOF+ct*(135*1024)+mi*20480+i; dma<0>(srdS,lane*16,o); dma<1024>(srdS,lane*16,o); dma<2048>(srdS,lane*16,o); dma<3072>(srdS,lane*16,o); } }
    if(w==6){ adone=__all(ld4(rs,fao)==tag); if(adone) aload(); }
    ldswait(smb+C_FREE,4u);
    #pragma unroll
    for(int i=0;i<24;i++) ldsw128(smb+RB+mi*RING+i*1024+lane*16,wq[i]);
    if(w<6) WAITV(0);
    signal(smb+C_WR,lane);
    if(!adone){ adone=__all(ld4(rs,fao)==tag); if(adone) aload(); }
    if(w==7 && b==255){
      #pragma unroll 1
      for(int sp=0;sp<SPIN;sp++){ const u32 o=OFF_FA+lane*128; u32 v0=ld4(rs,o),v1=ld4(rs,o+8192),v2=ld4(rs,o+16384),v3=ld4(rs,lane<32? o+24576 : OFF_FW+(lane-32)*128); asm volatile("" ::: "memory");
        if(__all(v0==tag&&v1==tag&&v2==tag&&v3==tag)) break; __builtin_amdgcn_s_sleep(4); }
      if(lane==0) st4(rs,OFF_EP,tag); }
  }
  int w2=w; asm volatile("" : "+s"(w2));
  if(w2<4){
    __builtin_amdgcn_s_setprio(G2PRIO);
    int r=lane&15, q=lane>>4;
    const u32 rb=smb+RB+w*RING;
    u4 srdA=mksrd((const char*)p.wsp+(size_t)rtB*64*4096+w*1024-4096+256);
    const char* zb=p.ws+OFF_Z2F+(u32)(16*w*54+ft0+3)*1024;
    u32 voA[2]; { u32 lr=lane>>3,c=lane&7; voA[0]=lr*4096+((c^(lr>>1))<<4); voA[1]=voA[0]^64u; }
    u32 sofa[8];
    #pragma unroll
    for(int g=0;g<8;g++) sofa[g]=((g+2*w)&7)*32768+4096-(g&3)*1024;
    const u32 voB=lane*16;
    u32 la[2]; la[0]=rb+r*128+((q^(r>>1))<<4); la[1]=la[0]^64u;
    f4 acc[4][7]; u4 B[NB][7]; u4 A[2][4]; u2 z1[7];
    auto issueA=[&](auto sc,auto gc){ constexpr int s=sc.value,g=gc.value; if constexpr((g&3)==0) setm0<((s+1)%3)*8192+g*1024>(rb); dma<(g&3)*1024>(srdA,voA[g&1],sofa[g]);
      if constexpr(g==7){ u64 a=((u64)srdA[1]<<32|srdA[0])+128; srdA[0]=(u32)a; srdA[1]=(u32)(a>>32); } };

    u32 bsr,fv;
    auto fof=[&](){ const u32 l=voB>>4; return OFF_F1+((rtB>>1)*16+(((ft0>>1)+(l&3))>>1))*128; };
    #pragma unroll
    for(int m=0;m<4;m++){ A[0][m]=ldsr128(la[0]+m*2048+8192); A[1][m]=ldsr128(la[1]+m*2048+8192); }
    sfor<8>([&](auto g){ issueA(std::integral_constant<int,2>{},g); });
    waitA(A[0]); waitA(A[1]);
    sfor<8>([&](auto g){ issueA(std::integral_constant<int,3>{},g); });
    RST(2);
    { const int hf=(ft0>>1)+q; pollw(rs,OFF_F2+((4*w+(lane&3))*16+(hf==0?13:(hf-1)>>1))*128,tag); }
    RST(10);
    sfor<NB>([&](auto hh){ const char* s=zb+(u32)hh.value*(54*1024); sfor<7>([&](auto n){ gload<(n.value-3)*1024>(B[hh][n],voB,s); }); });
    auto redw=[&](auto nc){ constexpr int n=nc.value; *(LAS f4*)(rb+n*1024+voB)=acc[1][n]; *(LAS f4*)(rb+(7+n)*1024+voB)=acc[2][n]; *(LAS f4*)(rb+(14+n)*1024+voB)=acc[3][n]; };
    const char* z1b=p.ws+OFF_Z1T+(u32)((rtB*4+w)*27+(ft0>>1))*1024;
    sfor<16>([&](auto hc){
      constexpr int h=hc.value, S=h%NB, P=h&1, HZ=HZZ, cnt=g2cnt(h);
      waitB<cnt>(B[S]); waitA(A[P]);
      if constexpr(h==HZ){ tie1(fv); if(!__all(fv==tag)) pollw(rs,fof(),tag); }
      if constexpr(h+1<16 && h>0){
        #pragma unroll
        for(int m=0;m<4;m++) A[P^1][m]=ldsr128(la[(h+1)&1]+m*2048+((((h+1)>>1)+1)%3)*8192);
      }
      if constexpr(h==HZ-3) gload4<0>(fv,fof(),p.ws);
      const char* s=zb+(u32)(h+NB)*(54*1024);
      sfor<7>([&](auto n){
        if constexpr(h==0) mfma4z(acc[0][n],acc[1][n],acc[2][n],acc[3][n],B[S][n],A[P]); else mfma4(acc[0][n],acc[1][n],acc[2][n],acc[3][n],B[S][n],A[P]);
        if constexpr(h>=3 && h<=10 && (n.value&1)==0) issueA(std::integral_constant<int,(h-1)/2+3>{},std::integral_constant<int,((h-1)&1)*4+n.value/2>{});
        if constexpr(h+NB<16) gload_t<(n.value-3)*1024>(B[S][n],voB,s);
        if constexpr(h==HZ){ gload8<(n.value>>1)*1024>(z1[n],voB,z1b+((n.value&1)? ((ft0&1)?1024:8) : (ft0&1)*8));
          if constexpr(n.value==6) { gload2(bsr,(voB>>3)&30u,(const char*)(p.bsp+(rtB*4+w)*16)); } }
        if constexpr(h==13 && n.value==0) signal(smb+C_Z1R,lane);
        if constexpr(h==15){ SCHED(); if constexpr(n.value>=2) redw(std::integral_constant<int,n.value-2>{}); SCHED(); }
      });
    });
    asm volatile("s_nop 15\n\ts_nop 3" ::: "memory");
    SCHED();
    STAMP(10,0);
    lane=__builtin_amdgcn_mbcnt_hi(~0u,__builtin_amdgcn_mbcnt_lo(~0u,0u)); asm volatile("" : "+v"(lane)); r=lane&15; q=lane>>4;
    redw(std::integral_constant<int,5>{}); redw(std::integral_constant<int,6>{});
    signal(smb+C_RED,lane);
    ldswait(smb+C_RED,4u);
    #pragma unroll
    for(int m=1;m<4;m++){ u32 src=smb+RB+((w-m)&3)*RING+(m-1)*7168+lane*16;
      #pragma unroll
      for(int n=0;n<7;n++) acc[0][n]+=*(LAS f4*)(src+n*1024); }
    asm volatile("s_waitcnt vmcnt(0) lgkmcnt(0)" : "+v"(z1[0]),"+v"(z1[1]),"+v"(z1[2]),"+v"(z1[3]),"+v"(z1[4]),"+v"(z1[5]),"+v"(z1[6]),"+v"(bsr) :: "memory");
    const float bs=bflo(bsr);
    signal(smb+C_FREE,lane);
    u2 o[7];
    #pragma unroll
    for(int n=0;n<7;n++)
      #pragma unroll
      for(int e=0;e<2;e++)o[n][e]=pkbf2(unpk(z1[n][e])*(f2{acc[0][n][2*e],acc[0][n][2*e+1]}+bs));
    const u32 ho=OFF_HF+(u32)((rtB*4+w)*54+ft0)*512+(q>>1)*256+r*16;
    #pragma unroll
    for(int n=0;n<6;n+=2){ auto sx=__builtin_amdgcn_permlane16_swap(o[n][0],o[n+1][0],false,false); auto sy=__builtin_amdgcn_permlane16_swap(o[n][1],o[n+1][1],false,false);
      if(ct<7||n>=2) st16(rs,ho+(n+(q&1))*512,u4{sx[0],sy[0],sx[1],sy[1]}); }
    st8(rs,ho+6*512+(q&1)*8,o[6]);
    STAMP(11,0);
    WAITV(0);
    if(arrive(smb+C_H,lane)==3u && lane==0) st4(rs,OFF_FH+(rtB*8+ct)*128,tag);
    STAMP(12,0);
    __builtin_amdgcn_s_setprio(0);
  }
  { const int r=lane&15, q=lane>>4, m=w&3, kh=w>>2;
    u2 xv[5]={};
    if(kh){
      #pragma unroll
      for(int n=0;n<5;n++) xv[n]=*(const u2*)(p.x+(rtB*64+m*16+r)*640+80*ct+16*n+4*q);
    }
    f4 acc[5]={}; u4 W[3][5];
    const u32 wl2=smb+lane*16+RB-40960+kh*14*5120, wl0=kh? wl2 : smb+lane*16, wl1=kh? wl2 : smb+lane*16+CVS-20480;
    #define WAD(k,n) (((k)<4? wl0 : (k)<8? wl1 : wl2)+((k)*5+(n))*1024)
    f4 ax[5]={};
    auto addax=[&](){
      #pragma unroll
      for(int n=0;n<5;n++){ u2 av=ldstr(smb+ATL+(16*n+4*q+(r>>2))*128+(16*m+4*(r&3))*2); ax[n]=f4{bflo(av[0]),bfhi(av[0]),bflo(av[1]),bfhi(av[1])}+f4{bflo(xv[n][0]),bfhi(xv[n][0]),bflo(xv[n][1]),bfhi(xv[n][1])}; } };
    if(w==6 && adone){ WAITV(0); signal(smb+C_AT,lane); }
    u32 oo[3],to[3]; const u32 tb=smb+CVS+m*3072;
    #pragma unroll
    for(int i=0;i<3;i++){ const int it=lane+64*i, rr=(it*205)>>11, cc=it-rr*10; oo[i]=opq((rtB*64+m*16+rr)*1280+ct*160+cc*16); to[i]=opq(tb+rr*176+cc*16); }
    if(m==0){ ldswait(smb+C_H,4u); pollw(rs,OFF_FH+(rtB*8+kh*4+(lane&3))*128,tag); signal(smb+C_TAG+kh*4,lane); } else ldswait(smb+C_TAG+kh*4,1u);
    STAMP(16,0);
    u4 hf[14];
    const u32 ho=OFF_HF+(u32)((rtB*4+m)*27+kh*14)*1024+lane*16;
    #pragma unroll
    for(int k=0;k<HPF;k++) hf[k]=ld16(rs,ho+k*1024);
    const bool early=kh && uni(ldsv(smb+C_AT))!=0u;
    if(early) addax();
    ldswait(smb+C_WR,4u);
    STAMP(17,0);
    const u32 wl3=kh? wl2-5120 : wl2;
    #pragma unroll
    for(int n=0;n<10;n++) W[n/5][n%5]=ldsr128(WAD(n/5,n%5));
    #pragma unroll
    for(int k=0;k<13;k++){
      if(k+HPF<14) hf[k+HPF]=ld16(rs,ho+((k+HPF==13&&kh)?12:k+HPF)*1024);
      SCHED();
      #pragma unroll
      for(int n=0;n<5;n++){ acc[n]=mfma16(W[k%3][n],hf[k],acc[n]); SCHED();
        if(k<11) W[(k+2)%3][n]=ldsr128(WAD(k+2,n)); else if(k==11) W[1][n]=ldsr128(wl3+(65+n)*1024);
        SCHED(); }
      if(k==5 && !kh) signal(smb+C_G1,lane);
    }
    const u32 pb=smb+m*5120+lane*16;
    if(!kh){
      #pragma unroll
      for(int n=0;n<5;n++) acc[n]=mfma16(W[1][n],hf[13],acc[n]);
      STAMP(18,0);
      ldswait(smb+C_G1,4u);
      #pragma unroll
      for(int n=0;n<5;n++) *(LAS f4*)(pb+n*1024)=acc[n];
      signal(smb+C_P+m*4,lane);
    } else {
      if(w==6 && !adone){ pollw(rs,fao,tag); aload(); WAITV(0); signal(smb+C_AT,lane); }
      if(!early){ ldswait(smb+C_AT,1u); addax(); }
      RST(7);
      ldswait(smb+C_P+m*4,1u);
      #pragma unroll
      for(int n=0;n<5;n++){ const f4 o=(acc[n]+ax[n])+*(LAS f4*)(pb+n*1024); *(LAS u2*)(tb+r*176+n*32+q*8)=u2{pkbf(o[0],o[1]),pkbf(o[2],o[3])}; }
      #pragma unroll
      for(int i=0;i<3;i++) if(i<2||lane<32) __builtin_amdgcn_raw_buffer_store_b128(ldsr128(to[i]),__builtin_amdgcn_make_buffer_rsrc(p.out,0,2048*1280,0x00020000),oo[i],0,16);
      STAMP(19,4);
    }
  }
}
extern "C" void kernel_launch(void* const* d_in,const int* in_sizes,int n_in,void* d_out,int out_size,void* d_ws,size_t ws_size,hipStream_t stream){
  Params p;
  p.x=(const u16*)d_in[0]; p.lar=(const u16*)d_in[1]; p.aim=(const u16*)d_in[2]; p.cre=(const u16*)d_in[3]; p.cim=(const u16*)d_in[4];
  p.ldt=(const u16*)d_in[5]; p.dsk=(const u16*)d_in[6]; p.win=(const u16*)d_in[8]; p.wsp=(const u16*)d_in[9]; p.bsp=(const u16*)d_in[10]; p.wout=(const u16*)d_in[11];
  p.out=(u16*)d_out; p.ws=(char*)d_ws;
  (void)hipFuncSetAttribute((const void*)fused,hipFuncAttributeMaxDynamicSharedMemorySize,LDS_TOTAL);
  fused<<<256,512,LDS_TOTAL,stream>>>(p);
}
```

```cpp
#include <hip/hip_runtime.h>
#include <utility>
#pragma clang diagnostic ignored "-Wint-to-pointer-cast"
typedef unsigned short u16;
typedef unsigned int u32;
typedef unsigned long long u64;
typedef __attribute__((ext_vector_type(8))) __bf16 bfx8;
typedef __attribute__((ext_vector_type(8))) _Float16 hfx8;
typedef __attribute__((ext_vector_type(2))) __bf16 bf2;
typedef __attribute__((ext_vector_type(2))) _Float16 hf2;
typedef __attribute__((ext_vector_type(2))) float f2;
typedef __attribute__((ext_vector_type(4))) float f4;
typedef __attribute__((ext_vector_type(16))) float f16v;
typedef __attribute__((ext_vector_type(4))) u32 u4;
typedef __attribute__((ext_vector_type(2))) u32 u2;
typedef __attribute__((ext_vector_type(4))) short s4;
#define LAS __attribute__((address_space(3)))
#define DI __device__ __forceinline__

constexpr u32 OFF_Z2F = 0;
constexpr u32 OFF_Z1T = OFF_Z2F + 64*54*1024;
constexpr u32 OFF_XNT = OFF_Z1T + 128*27*1024;
constexpr u32 OFF_WOF = OFF_XNT + 640*4096;
constexpr u32 OFF_HF  = OFF_WOF + 8*27*5*1024;
constexpr u32 OFF_AT  = OFF_HF + 128*27*1024;
constexpr u32 OFF_ST  = OFF_AT + 640*4096;
constexpr u32 OFF_RS  = OFF_ST + 16*14*2048+8192;
constexpr u32 OFF_EP  = OFF_RS + 2048*16;
constexpr u32 OFF_F2  = OFF_EP + 256;
constexpr u32 OFF_F1  = OFF_F2 + 256*128;
constexpr u32 OFF_FX  = OFF_F1 + 256*128;
constexpr u32 OFF_FW  = OFF_FX + 32*128;
constexpr u32 OFF_FH  = OFF_FW + 32*128;
constexpr u32 OFF_FA  = OFF_FH + 256*128;
constexpr u32 WS_END  = OFF_FA + 256*128;

struct Params {
  const u16 *x, *lar, *aim, *cre, *cim, *ldt, *dsk, *win, *wsp, *bsp, *wout;
  u16* out; char* ws;
};
template<class F,int... I> DI void sfor_impl(F&& f, std::integer_sequence<int,I...>){ (f(std::integral_constant<int,I>{}),...); }
template<int N,class F> DI void sfor(F&& f){ sfor_impl(f, std::make_integer_sequence<int,N>{}); }

DI float bflo(u32 v){ return __uint_as_float(v<<16); }
DI float bfhi(u32 v){ return __uint_as_float(v&0xffff0000u); }
DI u32 pkbf(float a,float b){ f2 v={a,b}; return __builtin_bit_cast(u32,__builtin_convertvector(v,bf2)); }
DI u32 pkhf(float a,float b){ f2 v={a,b}; return __builtin_bit_cast(u32,__builtin_convertvector(v,hf2)); }
DI float rbf(float a){ return bflo(pkbf(a,a)); }
DI float hlo(u32 v){ return (float)__builtin_bit_cast(hf2,v)[0]; }
DI float hhi(u32 v){ return (float)__builtin_bit_cast(hf2,v)[1]; }
DI float dot2(u32 a,u32 b,float c){ return __builtin_amdgcn_fdot2_f32_bf16(__builtin_bit_cast(bf2,a),__builtin_bit_cast(bf2,b),c,false); }
DI f4 mfma16(u4 a,u4 b,f4 c){ return __builtin_amdgcn_mfma_f32_16x16x32_bf16(__builtin_bit_cast(bfx8,a),__builtin_bit_cast(bfx8,b),c,0,0,0); }
DI f16v mfma32h(u4 a,u4 b,f16v c){ return __builtin_amdgcn_mfma_f32_32x32x16_f16(__builtin_bit_cast(hfx8,a),__builtin_bit_cast(hfx8,b),c,0,0,0); }
DI u4 ldsr128(u32 a){ return *(LAS u4*)a; }
DI u2 ldsr64(u32 a){ return *(LAS u2*)a; }
DI void ldsw128(u32 a,u4 v){ *(LAS u4*)a=v; }
DI u2 ldstr(u32 a){ return __builtin_bit_cast(u2,__builtin_amdgcn_ds_read_tr16_b64_v4i16((LAS s4*)a)); }
DI u32 uni(u32 v){ return __builtin_amdgcn_readfirstlane(v); }

template<int OFF> DI void gload(u4& dst,u32 voff,const char* base){ asm volatile("global_load_dwordx4 %0, %1, %2 offset:%3 sc1" : "=v"(dst) : "v"(voff), "s"(base), "n"(OFF)); }
template<int OFF> DI void gload_t(u4& dst,u32 voff,const char* base){ asm volatile("global_load_dwordx4 %0, %1, %2 offset:%3 sc1" : "+v"(dst) : "v"(voff), "s"(base), "n"(OFF)); }
DI void mfma4(f4& c0,f4& c1,f4& c2,f4& c3,const u4& a,const u4 (&b)[4]){
  asm volatile("v_mfma_f32_16x16x32_bf16 %0, %4, %5, %0\n\tv_mfma_f32_16x16x32_bf16 %1, %4, %6, %1\n\tv_mfma_f32_16x16x32_bf16 %2, %4, %7, %2\n\tv_mfma_f32_16x16x32_bf16 %3, %4, %8, %3"
    : "+v"(c0),"+v"(c1),"+v"(c2),"+v"(c3) : "v"(a),"v"(b[0]),"v"(b[1]),"v"(b[2]),"v"(b[3])); }
DI void mfma4z(f4& c0,f4& c1,f4& c2,f4& c3,const u4& a,const u4 (&b)[4]){
  asm volatile("v_mfma_f32_16x16x32_bf16 %0, %4, %5, 0\n\tv_mfma_f32_16x16x32_bf16 %1, %4, %6, 0\n\tv_mfma_f32_16x16x32_bf16 %2, %4, %7, 0\n\tv_mfma_f32_16x16x32_bf16 %3, %4, %8, 0"
    : "=&v"(c0),"=&v"(c1),"=&v"(c2),"=&v"(c3) : "v"(a),"v"(b[0]),"v"(b[1]),"v"(b[2]),"v"(b[3])); }
template<int CNT> DI void waitB(u4 (&B)[7]){ asm volatile("s_waitcnt vmcnt(%7)" : "+v"(B[0]),"+v"(B[1]),"+v"(B[2]),"+v"(B[3]),"+v"(B[4]),"+v"(B[5]),"+v"(B[6]) : "n"(CNT) : "memory"); }
DI void waitA(u4 (&A)[4]){ asm volatile("s_waitcnt lgkmcnt(0)" : "+v"(A[0]),"+v"(A[1]),"+v"(A[2]),"+v"(A[3]) :: "memory"); }
#define WAITV(n) asm volatile("s_waitcnt vmcnt(%0)" :: "n"(n) : "memory")
#define WAITL0() asm volatile("s_waitcnt lgkmcnt(0)" ::: "memory")
#define CFENCE() asm volatile("" ::: "memory")
#define SCHED() __builtin_amdgcn_sched_barrier(0)
DI void bar(){ asm volatile("s_waitcnt lgkmcnt(0)\n\ts_barrier" ::: "memory"); }
DI f2 unpk(u32 v){ return f2{bflo(v),bfhi(v)}; }
DI u32 pkbf2(f2 v){ return __builtin_bit_cast(u32,__builtin_convertvector(v,bf2)); }
DI u32 pkhf2(f2 v){ return __builtin_bit_cast(u32,__builtin_convertvector(v,hf2)); }
DI f2 fma2(f2 a,f2 b,f2 c){ return __builtin_elementwise_fma(a,b,c); }
DI f2 gelu2(f2 x){
  f2 u=x*fma2(x*x,f2{-0.10294324f,-0.10294324f},f2{-2.30220820f,-2.30220820f});
  f2 d=f2{__builtin_amdgcn_exp2f(u[0]),__builtin_amdgcn_exp2f(u[1])}+f2{1.f,1.f};
  return x*f2{__builtin_amdgcn_rcpf(d[0]),__builtin_amdgcn_rcpf(d[1])};
}
DI void ln_fin(float mean,float var,float& m,float& rinv){
  m=rbf(mean); float v=rbf(__builtin_fmaxf(var,0.f));
  float s=rbf(__builtin_amdgcn_sqrtf(rbf(v+1e-5f))); float r=__builtin_amdgcn_rcpf(s); rinv=__builtin_fmaf(__builtin_fmaf(-s,r,1.f),r,r);
}
typedef __amdgpu_buffer_rsrc_t rsrc_t;
DI void st16(rsrc_t rs,u32 off,u4 v){ __builtin_amdgcn_raw_buffer_store_b128(v,rs,off,0,16); }
DI void st8(rsrc_t rs,u32 off,u2 v){ __builtin_amdgcn_raw_buffer_store_b64(v,rs,off,0,16); }
DI void st4(rsrc_t rs,u32 off,u32 v){ __builtin_amdgcn_raw_buffer_store_b32(v,rs,off,0,16); }
DI u4 ld16(rsrc_t rs,u32 off){ return __builtin_amdgcn_raw_buffer_load_b128(rs,off,0,16); }
DI u32 ld4(rsrc_t rs,u32 off){ return __builtin_amdgcn_raw_buffer_load_b32(rs,off,0,16); }
constexpr int SPIN=1<<17;
constexpr u32 TAGINC=0x9E3779B9u;
template<int SL=2> DI void pollw(rsrc_t rs,u32 off,u32 tag){
  #pragma unroll 1
  for(int s=0;s<SPIN;s++){ u32 v=ld4(rs,off); CFENCE(); if(__all(v==tag)) break; __builtin_amdgcn_s_sleep(SL); }
}

DI f16v mfma32b(u4 a,u4 b,f16v c){ return __builtin_amdgcn_mfma_f32_32x32x16_bf16(__builtin_bit_cast(bfx8,a),__builtin_bit_cast(bfx8,b),c,0,0,0); }
DI u4 mksrd(const void* b){ u64 a=(u64)b; return u4{(u32)a,(u32)(a>>32)&0xffffu,0xffffffffu,0x00020000u}; }
template<int IMM> DI void setm0(u32 base){ asm volatile("s_add_u32 m0, %0, %1\n\ts_nop 0" :: "s"(base), "n"(IMM) : "scc","memory"); }
template<int OFF> DI void dma(u4 srd,u32 voff,u32 soff){ asm volatile("buffer_load_dwordx4 %0, %1, %2 offen offset:%3 sc1 lds" :: "v"(voff), "s"(srd), "s"(soff), "n"(OFF) : "memory"); }
DI void touch(u4 srd,u32 voff,u32 soff){ asm volatile("buffer_load_dword %0, %1, %2 offen sc1 lds" :: "v"(voff), "s"(srd), "s"(soff) : "memory"); }
template<int OFF> DI void gload8(u2& dst,u32 voff,const char* base){ asm volatile("global_load_dwordx2 %0, %1, %2 offset:%3 sc1" : "=v"(dst) : "v"(voff), "s"(base), "n"(OFF)); }
DI void gload2(u32& dst,u32 voff,const char* base){ asm volatile("global_load_ushort %0, %1, %2" : "=v"(dst) : "v"(voff), "s"(base)); }
DI u32 arrive(u32 a,int){ u32 o; asm volatile("s_mov_b64 exec, 1\n\tds_add_rtn_u32 %0, %1, %2\n\ts_mov_b64 exec, -1\n\ts_waitcnt lgkmcnt(0)" : "=&v"(o) : "v"(a), "v"(1u) : "memory"); return uni(o); }
template<int SL=1> DI void ldswait(u32 a,u32 n){ while(__hip_atomic_load((LAS u32*)a,__ATOMIC_RELAXED,__HIP_MEMORY_SCOPE_WORKGROUP)<n) __builtin_amdgcn_s_sleep(SL); CFENCE(); }
DI void tie1(u32& g){ asm volatile("" : "+v"(g) :: "memory"); }
DI u32 opq(u32 v){ asm volatile("" : "+v"(v)); return v; }

constexpr u32 STG=32768;
constexpr u32 RING=24576, RB=62848, CONV=0, KP=2208, UOF=16768, CVS=UOF+4160, S7O=4*KP, S9O=S7O+2112, KZO=S9O+2112, MISC=RB+4*RING, JNK=MISC+2048, PM=163840-128, LDS_TOTAL=163840;
constexpr u32 ATL=2*CVS;
constexpr u32 C_FULL=PM, C_G1=PM+4, C_Z2=PM+8, C_Z1=PM+12, C_RED=PM+16, C_H=PM+20, C_FREE=PM+24, C_CV=PM+28, C_Z1R=PM+32, C_TAG=PM+40, C_KP=PM+48, C_DONE=PM+64, C_CS=PM+72, C_P=PM+96, C_AT=PM+112, C_WR=PM+116;
constexpr u32 CODE_BYTES=37888;
constexpr int HPF=5;
DI void signal(u32 a,int){ asm volatile("s_mov_b64 exec, 1\n\tds_add_u32 %0, %1\n\ts_mov_b64 exec, -1" :: "v"(a), "v"(1u) : "memory"); }
template<int OFF> DI void gload4(u32& dst,u32 voff,const char* base){ asm volatile("global_load_dword %0, %1, %2 offset:%3 sc1" : "=v"(dst) : "v"(voff), "s"(base), "n"(OFF)); }

DI f2 cmulp(f2 a,f2 b,f2 bs){ return fma2(f2{a[1],a[1]},bs,f2{a[0],a[0]}*b); }
DI u32 ldsv(u32 a){ return __hip_atomic_load((LAS u32*)a,__ATOMIC_RELAXED,__HIP_MEMORY_SCOPE_WORKGROUP); }
DI f2 cpow_dir(float lr,float rv,float pw){ float e=__builtin_amdgcn_exp2f(lr*pw); float f=__builtin_amdgcn_fractf(rv*pw);
  return f2{e*__builtin_amdgcn_cosf(f),e*__builtin_amdgcn_sinf(f)}; }
DI u32 dup(u32 a){ return __builtin_amdgcn_perm(a,a,0x01000100u); }
DI u32 cat(u32 lo,u32 hi){ return __builtin_amdgcn_perm(hi,lo,0x05040100u); }
DI u32 lds16(u32 a){ return *(LAS u16*)a; }

DI float kgen_tab(u32 cb,int lane,const u32 (&pr)[6],float (&kp)[11]){
  const float dt=rbf(__expf(bflo(pr[4])));
  const float cr[2]={bflo(pr[2]),bfhi(pr[2])}, cm[2]={bflo(pr[3]),bfhi(pr[3])};
  float mx=fmaxf(fmaxf(fabsf(cr[0]),fabsf(cm[0])),fmaxf(fabsf(cr[1]),fabsf(cm[1])));
  u32 eb=__float_as_uint(mx)&0x7f800000u;
  #define DPPMAX(c) eb=max(eb,(u32)__builtin_amdgcn_update_dpp(0,(int)eb,c,0xf,0xf,false))
  DPPMAX(0x128); DPPMAX(0x124); DPPMAX(0x122); DPPMAX(0x121);
  eb=max(max((u32)__builtin_amdgcn_readlane(eb,0),(u32)__builtin_amdgcn_readlane(eb,16)),max((u32)__builtin_amdgcn_readlane(eb,32),(u32)__builtin_amdgcn_readlane(eb,48))); eb=eb<0x01000000u?0x3f800000u:(eb>0x7e000000u?0x7e000000u:eb);
  const float ksc=2.f*dt*__uint_as_float(eb), isc=__uint_as_float(0x7f000000u-eb);
  const int r=lane&31,h=lane>>5;
  const u32 TP=272, TA=cb, TB=cb+22*TP;
  f16v acc={};
  f2 an[2][4];
  #pragma unroll
  for(int c=0;c<2;c++){ const float l_a=c?bfhi(pr[0]):bflo(pr[0]), i_m=c?bfhi(pr[1]):bflo(pr[1]);
    const float lr=(-rbf(__expf(l_a))*dt)*1.44269504f, rv=(i_m*dt)*0.15915494f;
    an[c][0]=cpow_dir(lr,rv,1.f); an[c][1]=cpow_dir(lr,-rv,8.f); an[c][2]=cpow_dir(lr,-rv,16.f); an[c][3]=cpow_dir(lr,rv,256.f); }
  u32 hold[54]; const u32 wt=TA+lane*4;
  struct P2 { f2 v[2]; };
  auto mul=[&](const P2& x,int k){ P2 o;
    #pragma unroll
    for(int c=0;c<2;c++) o.v[c]=cmulp(x.v[c],an[c][k],f2{-an[c][k][1],an[c][k][0]});
    return o; };
  auto put=[&](int row,const P2& x){ *(LAS u32*)(wt+row*TP)=pkhf2(x.v[0]); hold[row]=pkhf2(x.v[1]); };
  P2 e={{f2{1.f,0.f},f2{1.f,0.f}}}, o={{an[0][1],an[1][1]}}, bk[8], t;
  bk[0].v[0]=f2{cr[0]*isc,cm[0]*isc}; bk[0].v[1]=f2{cr[1]*isc,cm[1]*isc}; put(0,bk[0]); t=bk[0];
  constexpr int tr[8]={8,10,11,16,17,20,21,18};
  #pragma unroll
  for(int k=0;k<16;k++){ put(22+2*k,e); put(23+2*k,o);
    if(k<15){ if(k) e=mul(e,2); else { e.v[0]=an[0][2]; e.v[1]=an[1][2]; } o=mul(o,2); }
    if(k<8){ t=mul(t,3); put(tr[k],t); }
    if(k<7){ bk[k+1]=mul(bk[k],0); put(k+1,bk[k+1]); }
    if(k==8){ put(9,mul(bk[2],3)); put(13,mul(bk[6],3)); t=mul(bk[4],3); put(12,t); }
    if(k==9||k==10){ t=mul(t,3); put(5+k,t); }
    SCHED(); }
  const int ra=(r>21||r==19)?0:r;
  #pragma unroll
  for(int c=0;c<2;c++){
    CFENCE();
    u4 fa[8],fb[8];
    #pragma unroll
    for(int ks=0;ks<8;ks++){ fa[ks]=ldsr128(TA+ra*TP+ks*32+h*16); fb[ks]=ldsr128(TB+r*TP+ks*32+h*16); }
    CFENCE();
    if(c==0){
      #pragma unroll
      for(int i=0;i<54;i++) if(i!=19) *(LAS u32*)(wt+i*TP)=hold[i]; }
    #pragma unroll
    for(int ks=0;ks<8;ks++) acc=mfma32h(fa[ks],fb[ks],acc);
  }
  CFENCE();
  #pragma unroll
  for(int i=0;i<11;i++) kp[i]=acc[i];
  return ksc;
}
DI void kgen_exp(u32 cb,int lane,const float (&acc)[11],const u32 (&ga)[11]){
  const int r=lane&31,h=lane>>5;
  { const u32 a4=cb+r*16+h*8, a5=cb+r*16+h*1024, az=cb+KZO+r*2+h*128;
    #define PK(addr,base,reg) do{ *(LAS _Float16*)((addr)+2*(base))=(_Float16)acc[reg]; }while(0)
    PK(a4,0,0);PK(a4,1,1);PK(a4,2,2);PK(a4,3,3); PK(a4,256,4);PK(a4,258,5);PK(a4,512,6);PK(a4,768,7);
    PK(a5,1024,8); PK(az,255,10); PK(az,127,8); PK(az,159,9);
    #undef PK
    CFENCE();
    if(lane<16) ldsw128(cb+KZO+lane*16,u4{0,0,0,0});
    CFENCE();
  }
  { const u32 g0=cb+2*(1024-8*lane);
    u32 f2_=lds16(g0+(lane?8:0)), a2=lds16(g0), m2=lds16(g0-8), b2=lds16(g0-16);
    u32 e[11];
    #pragma unroll
    for(int j=0;j<11;j++) e[j]=lds16(ga[j]);
    u32 k01=*(LAS u32*)cb, k23=*(LAS u32*)(cb+4), kl=lds16(cb+2048);
    WAITL0();
    const u32 wo=cb+96+lane*16;
    { u32 aa=dup(a2),mm=dup(m2),bb=dup(b2),am=cat(a2,m2),fa=cat(f2_,a2),mb=cat(m2,b2);
      ldsw128(wo,u4{aa,am,mm,bb}); ldsw128(wo+KP,u4{aa,aa,mm,mb}); ldsw128(wo+2*KP,u4{aa,aa,am,mm}); ldsw128(wo+3*KP,u4{fa,aa,aa,mm}); }
    { u32 o0=cat(e[0],e[1]),o1=cat(e[2],e[3]),o2=cat(e[4],e[5]),o3=cat(e[6],e[7]),o4=cat(e[8],e[9]);
      u32 p1=cat(e[1],e[2]),d0=cat(e[3],e[4]),d1=cat(e[5],e[6]),d2=cat(e[7],e[8]),d3=cat(e[9],e[10]);
      ldsw128(wo+1024,u4{d0,d1,d2,d3}); ldsw128(wo+1024+KP,u4{o1,o2,o3,o4}); ldsw128(wo+1024+2*KP,u4{p1,d0,d1,d2}); ldsw128(wo+1024+3*KP,u4{o0,o1,o2,o3}); }
    if(lane<10){ if(lane){k01=0;k23=0;} if(lane!=9) kl=0;
      const u32 d0=k01&0xffffu, o1=__builtin_amdgcn_alignbit(k01,k01,16), o0=__builtin_amdgcn_alignbit(k23,k23,16), p1=(k23&0xffffu)|(k01&0xffff0000u), kk=dup(kl), zk=kl<<16;
      const u32 w2=lane<4? wo+2048 : wo-160;
      ldsw128(w2,u4{d0,0,kk,kk}); ldsw128(w2+KP,u4{o1,0,zk,kk}); ldsw128(w2+2*KP,u4{p1,d0,0,kk}); ldsw128(w2+3*KP,u4{o0,o1,0,zk}); }
  }
}
DI f4 mfma16h(u4 a,u4 b,f4 c){ return __builtin_amdgcn_mfma_f32_16x16x32_f16(__builtin_bit_cast(hfx8,a),__builtin_bit_cast(hfx8,b),c,0,0,0); }
DI u4 lds2x64(u32 a){ const u2 lo=ldsr64(a), hi=ldsr64(a+8); return u4{lo[0],lo[1],hi[0],hi[1]}; }
DI void conv_win(u32 cb,int lane,u4 (&sw)[4]){
  float P[25]; P[0]=0.f;
  #pragma unroll
  for(int c=0;c<4;c++){ const int X=2*lane-2+c; const u4 v=ldsr128(cb+UOF+(X&3)*1040+((X>>2)+1)*16);
    #pragma unroll
    for(int e=0;e<8;e++){ const int i=8*c+e-4; if(i>=0&&i<24) P[i+1]=P[i]+((e&1)?hhi(v[e>>1]):hlo(v[e>>1])); } }
  #pragma unroll
  for(int k=0;k<8;k++){ sw[k>>2][k&3]=pkhf(P[2*k+8]-P[2*k+1],P[2*k+9]-P[2*k+2]); sw[2+(k>>2)][k&3]=pkhf(P[2*k+9]-P[2*k],P[2*k+10]-P[2*k+1]); }
}
DI void conv_help(u32 cb,int lane,const u4 (&sw)[4]){
  const int r=lane&15,q=lane>>4; f4 y[2][2]={};
  ldsw128(cb+S7O+48+lane*32,sw[0]); ldsw128(cb+S7O+64+lane*32,sw[1]); ldsw128(cb+S9O+48+lane*32,sw[2]); ldsw128(cb+S9O+64+lane*32,sw[3]);
  if(lane<3){ ldsw128(cb+S7O+lane*16,u4{0,0,0,0}); ldsw128(cb+S9O+lane*16,u4{0,0,0,0}); }
  CFENCE();
  const u32 ka=cb+(r&3)*KP+16*q-8*(r>>2)+64, ub=cb+UOF+q*1040+(r+1)*16;
  const u4 a0=lds2x64(ka+32), a1=lds2x64(ka), b0=ldsr128(ub), b1=ldsr128(ub+256), a2=lds2x64(ka-32), b2=ldsr128(ub-16), b3=ldsr128(ub+240);
  const u32 sa=cb+(((r>>2)&1)?S9O:S7O)+2*(64*q+24-8*(r>>2)+4*(r&3)), kz=cb+KZO+8*(r-2*q+23); u4 bz[4],fa[4][2];
  #pragma unroll
  for(int ks=0;ks<4;ks++) bz[ks]=lds2x64(kz+(3-ks)*64);
  #pragma unroll
  for(int i=0;i<8;i++){ const u2 lo=ldstr(sa+(i>>1)*512+(i&1)*32+64), hi=ldstr(sa+(i>>1)*512+(i&1)*32); fa[i>>1][i&1]=u4{lo[0],lo[1],hi[0],hi[1]}; }
  SCHED();
  y[0][0]=mfma16h(a2,b2,y[0][0]); y[0][1]=mfma16h(a2,b3,y[0][1]);
  y[0][0]=mfma16h(a0,b0,y[0][0]); y[1][0]=mfma16h(a1,b0,y[1][0]); y[0][1]=mfma16h(a0,b1,y[0][1]); y[1][1]=mfma16h(a1,b1,y[1][1]);
  #pragma unroll
  for(int ks=0;ks<4;ks++)
    #pragma unroll
    for(int m=0;m<2;m++){ y[m][1]=mfma16h(fa[ks][m],bz[ks],y[m][1]); if(ks<2) y[m][0]=mfma16h(fa[ks][m],bz[ks+2],y[m][0]); }
  CFENCE();
  #pragma unroll
  for(int i=0;i<4;i++) *(LAS f4*)(cb+S7O+i*1024+lane*16)=y[i&1][i>>1];
}
#define RDA(S,i,m) asm volatile("ds_read2_b64 %0, %1 offset0:%2 offset1:%3" : "=v"(AF[S][i][m]) : "v"(ka), "n"((1-(i))*128+(1-(m))*4), "n"((1-(i))*128+(1-(m))*4+1))
#define RDB(S,i,ad,o) asm volatile("ds_read_b128 %0, %1 offset:%2" : "=v"(BF[S][i]) : "v"(ad), "n"(o))
#define TIEA(S,i) "+v"(AF[S][i][0]),"+v"(AF[S][i][1])
#define CV_M(S,i,n) do{ y[0][n]=mfma16h(AF[S][i][0],BF[S][(n)-(i)],y[0][n]); y[1][n]=mfma16h(AF[S][i][1],BF[S][(n)-(i)],y[1][n]); SCHED(); }while(0)
#define CV_U0() u32 u0=u1-256; u0=((int)u0<(int)uz)?uz:u0
template<class CB> DI void conv_wave(CB&& mid,rsrc_t rs,int d,u32 cb,u32 flag,int lane,float ksc,float dsk){
  const int r=lane&15,q=lane>>4; u32 ka=cb+(r&3)*KP+16*q-8*(r>>2)+1088; const u32 uz=cb+UOF+q*1040; u32 u1=uz+(r+17)*16; u4 AF[2][2][2]; u4 BF[2][4]; f4 y[2][4]={};
  #define CV_WT(S) asm volatile("s_waitcnt lgkmcnt(0)" : TIEA(S,0),TIEA(S,1),"+v"(BF[S][0]),"+v"(BF[S][1]),"+v"(BF[S][2]),"+v"(BF[S][3]))
  #define CV_STEP(C,N) do{ CV_U0(); CV_M(C,0,0); RDA(N,0,0); RDB(N,0,u0,0); RDA(N,0,1); SCHED(); CV_M(C,0,1); RDB(N,1,u1,0); RDA(N,1,0); RDB(N,2,u1,256); SCHED(); CV_M(C,0,2); RDA(N,1,1); RDB(N,3,u1,512); \
      ka-=64; u1-=16; SCHED(); CV_M(C,0,3); CV_M(C,1,1); CV_M(C,1,2); CV_M(C,1,3); CV_WT(N); }while(0)
  { CV_U0(); RDA(0,0,0); RDA(0,0,1); RDA(0,1,0); RDA(0,1,1); RDB(0,0,u0,0); RDB(0,1,u1,0); RDB(0,2,u1,256); RDB(0,3,u1,512); ka-=64; u1-=16; CV_WT(0); }
  #pragma unroll 1
  for(int dl=0;dl<8;dl++){ CV_STEP(0,1); if(dl==7){ ka+=64; u1+=16; } CV_STEP(1,0); }
  mid();
  u2 uv[2][4];
  #pragma unroll
  for(int n=0;n<4;n++)
    #pragma unroll
    for(int m=0;m<2;m++) uv[m][n]=ldsr64(cb+UOF+(2*m+(q>>1))*1040+(16*n+r+1)*16+8*(q&1));
  const u32 sc=cb+UOF+r*64+q*8;
  auto fin=[&](int n){
    #pragma unroll
    for(int m=0;m<2;m++){ u2 o;
      #pragma unroll
      for(int e=0;e<2;e++) o[e]=pkbf2(fma2(f2{hlo(uv[m][n][e]),hhi(uv[m][n][e])},f2{dsk,dsk},f2{y[m][n][2*e],y[m][n][2*e+1]}*ksc));
      *(LAS u2*)(sc+n*1024+m*32)=o; } };
  ldswait(flag,2u);
  { f4 hp[4];
    #pragma unroll
    for(int i=0;i<4;i++) hp[i]=*(LAS f4*)(cb+S7O+i*1024+lane*16);
    fin(0); fin(1);
    #pragma unroll
    for(int i=0;i<4;i++) y[i&1][2+(i>>1)]+=hp[i];
    fin(2); fin(3); }
  CFENCE();
  u4 ov[4];
  #pragma unroll
  for(int n=0;n<4;n++) ov[n]=ldsr128(cb+UOF+n*1024+lane*16);
  #pragma unroll
  for(int n=0;n<4;n++) st16(rs,OFF_AT+d*4096+n*1024+lane*16,ov[n]);
  WAITV(0);
}

constexpr int NB=3, HZZ=14;
constexpr int g2ops(int h){ return h<0? 7 : (h+NB<16?7:0)+((h>=3&&h<=10)?4:0)+(h==HZZ-3?1:0)+(h==HZZ?8:0); }
constexpr int g2cnt(int h){ int c=0; for(int i=h-NB+1;i<h;i++) c+=g2ops(i); return c; }
struct Frag { u4 x[4], w[4][2]; };
__global__ __launch_bounds__(512,2) void fused(Params p){
  extern __shared__ __attribute__((aligned(16))) char sm[];
  const u32 smb=(u32)(size_t)(LAS char*)sm;
  u64 pc0; asm volatile("s_getpc_b64 %0" : "=s"(pc0));
  const int b=blockIdx.x, xc=b&7, idx=b>>3;
  const int tid=threadIdx.x, w=uni(tid>>6); int lane=tid&63;
  auto cpf=[&](u32 o){ u4 sc=mksrd((const void*)(pc0&~127ull)); sc[2]=CODE_BYTES; setm0<JNK>(smb); touch(sc,lane*128,o); };
  if(idx>=28) cpf(w*8192);
  SCHED();
  const int rt=4*(xc>>1)+(idx&3), j=7*(xc&1)+(idx>>2), mi=w&3, g1=w>>2;
  const u32 kr0=(((j+rt)&3)*5)>>1;
  u32 vx0,vx1,vw1,vw2,k0=kr0*128,k0w=kr0*(64*3456); const u32 mw=smb+w*4096;
  { const u32 lr=lane>>3,c=lane&7; vx0=(rt*128+16*w+lr)*1280+((c^(lr>>1))<<4); vx1=(vx0^64u)+9216;
    vw1=(8*w+lr)*3456+((c^((lr&2)<<1))<<4)+j*128; vw2=vw1+1024+(j==13?0:14*128); }
  asm volatile("" : "+v"(vx0),"+v"(vx1),"+v"(vw1),"+v"(vw2),"+s"(k0),"+s"(k0w));
  setm0<0>(mw);
  SCHED();
  const u4 srdX=mksrd(p.x), srdW=mksrd((const char*)p.win-3072);
  if(idx<28){ dma<0>(srdX,vx0,k0); dma<1024>(srdX,vx1,k0); dma<2048>(srdW,vw2,k0w); dma<3072>(srdW,vw1,k0w); }
  SCHED();
  const rsrc_t rs=__builtin_amdgcn_make_buffer_rsrc(p.ws,0,WS_END,0x00020000);
  const u4 srdS=mksrd(p.ws);
  const int rtB=8*(xc>>1)+(idx&7), ct=4*(xc&1)+(idx>>3);
  const bool hasc=idx<28 && idx+28*(w&3)<80 && w>=4;
  const int dch=80*xc+(hasc? idx+28*(w&3) : 0);
  const u32 cb=smb+CONV+(w&3)*CVS;
  u32 tag; float ksc=0.f, dsk=0.f;
  u32 zp[2][8]={};
  auto z1st=[&](){ const int r=lane&31,h=lane>>5;
    #pragma unroll
    for(int n=0;n<2;n++) if(2*j+n<27){ const u32 oo=OFF_Z1T+((rt*8+2*mi+(r>>4))*27+2*j+n)*1024+(h*16+(r&15))*16;
      st16(rs,oo,u4{zp[n][0],zp[n][1],zp[n][4],zp[n][5]}); st16(rs,oo+512,u4{zp[n][2],zp[n][3],zp[n][6],zp[n][7]}); } };
  auto f1=[&](){ WAITV(0); if(arrive(smb+C_Z1,lane)==3u && lane==0) st4(rs,OFF_F1+(rt*16+j)*128,tag); };
  auto pre01=[&](){
    const u4 sa=mksrd((const char*)p.wsp+(size_t)rtB*64*4096+mi*1024-4096); const u32 v0=(lane>>3)*4096+(((lane&7)^(lane>>4))<<4);
    #pragma unroll 1
    for(int i=0;i<4;i++){ setm0<RB+8192>(smb+mi*RING+i*4096); const u32 g=(i&1)*4+2*mi, k=(i>>1)*128+4096;
      dma<0>(sa,v0,(g&7)*32768+k); dma<1024>(sa,v0^64u,(g&7)*32768+32768-1024+k); dma<2048>(sa,v0,((g+2)&7)*32768-2048+k); dma<3072>(sa,v0^64u,((g+2)&7)*32768+32768-3072+k); } };
  if(idx>=28){
    const int a=xc*4+idx-28, r=lane&15, q=lane>>4;
    const u32 ep=ld4(rs,OFF_EP);
    u4 xr[10]; const char* xs=(const char*)p.x+(size_t)(64*a+8*w+(lane>>3))*1280+(lane&7)*16;
    #pragma unroll
    for(int i=0;i<10;i++) xr[i]=*(const u4*)(xs+i*128);
    if(tid<32) *(LAS u32*)(smb+PM+tid*4)=0u;
    tag=uni(ep)+TAGINC;
    float s0=0.f,s1=0.f,q0=0.f,q1=0.f;
    #pragma unroll
    for(int i=0;i<10;i++){ s0=dot2(xr[i][0],0x3f803f80u,s0); q0=dot2(xr[i][0],xr[i][0],q0); s1=dot2(xr[i][1],0x3f803f80u,s1); q1=dot2(xr[i][1],xr[i][1],q1);
      s0=dot2(xr[i][2],0x3f803f80u,s0); q0=dot2(xr[i][2],xr[i][2],q0); s1=dot2(xr[i][3],0x3f803f80u,s1); q1=dot2(xr[i][3],xr[i][3],q1); }
    s0+=s1; q0+=q1;
    #define DPPADD(v,c) v+=__int_as_float(__builtin_amdgcn_update_dpp(0,__float_as_int(v),c,0xf,0xf,false))
    DPPADD(s0,0xB1); DPPADD(q0,0xB1); DPPADD(s0,0x4E); DPPADD(q0,0x4E); DPPADD(s0,0x141); DPPADD(q0,0x141);
    float m1,ri; { float mean=s0*(1.f/640.f); ln_fin(mean,q0*(1.f/640.f)-mean*mean,m1,ri); }
    if((lane&7)==0) st16(rs,OFF_RS+(64*a+8*w+(lane>>3))*16,u4{__float_as_uint(m1),tag,__float_as_uint(ri),tag});
    if(a<27){ const u4 srdO=mksrd((const char*)p.wout+a*(32*1280)+w*5120); const u32 mo=smb+w*5120; setm0<0>(mo);
      dma<0>(srdO,lane*16,0); dma<1024>(srdO,lane*16,0); dma<2048>(srdO,lane*16,0); dma<3072>(srdO,lane*16,0); setm0<4096>(mo); dma<0>(srdO,lane*16,4096); }
    #pragma unroll
    for(int i=0;i<10;i++){ u4 o;
      #pragma unroll
      for(int e=0;e<4;e++) o[e]=pkhf2(fma2(unpk(xr[i][e]),f2{ri,ri},f2{-m1*ri,-m1*ri}));
      ldsw128(smb+RB+(8*w+(lane>>3))*1296+(lane&7)*16+i*128,o); }
    WAITV(0); bar();
    #pragma unroll
    for(int i=0;i<10;i++){ const int c=w*10+i; const u32 ad=smb+RB+(32*((r>>1)&1)+8*q+(r>>2))*1296+c*16+(r&1)*8; u2 lo=ldstr(ad), hi=ldstr(ad+4*1296);
      const u32 sc=smb+40960+w*2048+(i&1)*1024; ldsw128(sc+(r&7)*128+((((r>>3)*4+q)^(r&7))<<4),u4{lo[0],lo[1],hi[0],hi[1]});
      CFENCE(); const u4 o=ldsr128(sc+(lane>>3)*128+(((lane&7)^(lane>>3))<<4)); CFENCE();
      st16(rs,OFF_XNT+a*81920+c*1024+lane*16,o); }
    WAITV(0);
    if(arrive(smb+C_Z1,lane)==7u && lane==0) st4(rs,OFF_FX+a*128,tag);
    if(a<27){
      #pragma unroll
      for(int i=0;i<5;i++){ u32 ad=smb+(8*q+(r>>2))*1280+(w*5+i)*32+(r&3)*8; u2 lo=ldstr(ad), hi=ldstr(ad+4*1280);
        st16(rs,OFF_WOF+(u32)((w*27+a)*5+i)*1024+lane*16,u4{lo[0],lo[1],hi[0],hi[1]}); } }
    WAITV(0); bar();
    if(tid==0) st4(rs,OFF_FW+a*128,tag);
    if(!g1){ pre01(); WAITV(0); }
  } else {
    const int r=lane&31, h=lane>>5;
    u32 epv;
    u32 kx[10]; { u32 k=kr0;
      #pragma unroll
      for(int i=0;i<10;i++){ kx[i]=k*128; k=k==9?0:k+1; } }
    auto issue1=[&](auto sc,int k){ constexpr int s=sc.value;
      if(k==0){ setm0<(s&3)*STG>(mw); dma<0>(srdX,vx0,kx[s]); } if(k==1) dma<1024>(srdX,vx1,kx[s]); if(k==2) dma<2048>(srdW,vw2,kx[s]*1728); if(k==3) dma<3072>(srdW,vw1,kx[s]*1728); };
    auto issue=[&](auto sc){ issue1(sc,0); issue1(sc,1); issue1(sc,2); issue1(sc,3); };
    u32 pr[6];
    gload4<0>(epv,0u,p.ws+OFF_EP);
    gload4<0>(pr[0],lane*4,(const char*)(p.lar+dch*128)); gload4<0>(pr[1],lane*4,(const char*)(p.aim+dch*128)); gload4<0>(pr[2],lane*4,(const char*)(p.cre+dch*128)); gload4<0>(pr[3],lane*4,(const char*)(p.cim+dch*128));
    gload2(pr[4],0u,(const char*)(p.ldt+dch)); gload2(pr[5],0u,(const char*)(p.dsk+dch));
    asm volatile("s_barrier" ::: "memory");
    issue(std::integral_constant<int,1>{});
    if(tid<32) *(LAS u32*)(smb+PM+tid*4)=0u;
    u32 ax[2][4],aw[2][4][2];
    { u32 row=32*mi+r, a0=smb+(row>>4)*4096+(row&15)*128+((h^((r>>1)&7))<<4);
      u32 qq=(lane&15)>>2, w0=smb+2048+g1*1024+h*4096+qq*128+8*(lane&1)+(((2*((lane>>4)&1)+((lane>>1)&1))^((qq&2)<<1))<<4);
      #pragma unroll
      for(int i=0;i<2;i++)
        #pragma unroll
        for(int k=0;k<4;k++){ const u32 kk=(k+mi)&3, wo=i*2*STG; ax[i][k]=opq((a0^(kk*32u))+wo); aw[i][k][0]=opq(w0+kk*8192+wo); aw[i][k][1]=opq((w0^64u)+kk*8192+wo); } }
    u4 sel; { const u32 o=((lane&15)==((lane>>4)&1))?0x3f803f80u:0u; sel=u4{o,o,o,o}; asm volatile("" : "+v"(sel)); }
    u32 ga[11];
    #pragma unroll
    for(int i=0;i<11;i++) ga[i]=opq(cb+2u*(u32)rbf((float)(512-8*lane+3-i)));
    Frag F[2];
    asm volatile("s_waitcnt vmcnt(4)" : "+v"(epv),"+v"(pr[0]),"+v"(pr[1]),"+v"(pr[2]),"+v"(pr[3]),"+v"(pr[4]),"+v"(pr[5]) :: "memory");
    bar();
    tag=uni(epv)+TAGINC; dsk=bflo(pr[5]);
    #pragma unroll
    for(int k=0;k<4;k++){
      #pragma unroll
      for(int n=0;n<2;n++){ u2 lo=ldstr(aw[0][k][n]), hi=ldstr(aw[0][k][n]+512); F[0].w[k][n]=u4{lo[0],lo[1],hi[0],hi[1]}; }
      F[0].x[k]=ldsr128(ax[0][k]); }
    issue(std::integral_constant<int,2>{});
    f16v acc[2]={}; f4 cacc[2]={}; u4 G;
    const u32 go=OFF_RS+rt*2048+mi*512, cp=smb+MISC+g1*1024;
    sfor<10>([&](auto ic){
      constexpr int s=ic.value; Frag& cur=F[s&1]; Frag& nxt=F[(s&1)^1];
      if constexpr(s<9){ WAITV(s==8? 3 : s==7? 7 : 4); asm volatile("s_barrier" ::: "memory"); }
      auto rw=[&](int k,int n){ if constexpr(s<9){ const int i=((s+1)>>1)&1; const u32 so=((s+1)&1)*STG; u2 lo=ldstr(aw[i][k][n]+so), hi=ldstr(aw[i][k][n]+so+512); nxt.w[k][n]=u4{lo[0],lo[1],hi[0],hi[1]}; } };
      auto rx=[&](int k){ if constexpr(s<9){ const int i=((s+1)>>1)&1; const u32 so=((s+1)&1)*STG; nxt.x[k]=ldsr128(ax[i][k]+so); } };
      #pragma unroll
      for(int k=0;k<4;k++){
        acc[0]=mfma32b(cur.w[k][0],cur.x[k],acc[0]); SCHED();
        if constexpr(s+3<=9) issue1(std::integral_constant<int,s+3>{},k);
        rw(k,0); rx(k);
        SCHED();
        acc[1]=mfma32b(cur.w[k][1],cur.x[k],acc[1]); SCHED();
        rw(k,1);
        if(k==0){ cacc[0]=mfma16(cur.w[0][0],sel,cacc[0]); cacc[1]=mfma16(cur.w[0][1],sel,cacc[1]); }
        if constexpr(s==6){ if(k==3){ gload<0>(G,r*16,p.ws+go); setm0<JNK>(smb);
            if(g1){ const u4 sp=mksrd((const char*)p.wsp+(size_t)(xc>>1)*512*4096); const u32 lb=(((xc&1)*28+idx)*4+mi)*74, l0=lb+lane, l1=lb+64+(lane<10?lane:9);
              touch(sp,(l0<16384?l0:16383)*128,0u); touch(sp,(l1<16384?l1:16383)*128,0u); }
            else { touch(srdS,0u,go); touch(srdS,0u,go); } } }
        if constexpr(s==8){ if(k==3) signal(smb+C_DONE,lane); }
        if constexpr(s==9){ if(k==1){
          if((lane&14)==0){ *(LAS f4*)(cp+mi*256+(lane&1)*64+(lane>>4)*16)=cacc[0]; *(LAS f4*)(cp+mi*256+128+(lane&1)*64+(lane>>4)*16)=cacc[1]; }
          signal(smb+C_CS+g1*4,lane); } }
        SCHED();
      }
    });
    if(!g1) __builtin_amdgcn_s_setprio(2);
    asm volatile("s_waitcnt vmcnt(2)" : "+v"(G) :: "memory");
    ldswait(smb+C_CS+g1*4,4u);
    const float cs0=*(LAS float*)(cp+h*512+r*4)+*(LAS float*)(cp+h*512+256+r*4), cs1=*(LAS float*)(cp+h*512+128+r*4)+*(LAS float*)(cp+h*512+384+r*4);
    #pragma unroll 1
    for(int sp=0;sp<SPIN && !__all(G[1]==tag&&G[3]==tag);sp++)asm volatile("global_load_dwordx4 %0, %1, %2 sc1\n\ts_waitcnt vmcnt(0)" : "=&v"(G) : "v"(r*16), "s"(p.ws+go) : "memory");
    const float nm=-__uint_as_float(G[0]), ri1=__uint_as_float(G[2]);
    acc[0]=__builtin_amdgcn_mfma_f32_32x32x2f32(cs0,nm,acc[0],0,0,0); acc[1]=__builtin_amdgcn_mfma_f32_32x32x2f32(cs1,nm,acc[1],0,0,0);
    #pragma unroll
    for(int e=0;e<16;e++) zp[e>>3][e&7]=pkbf2(gelu2(f2{acc[e>>3][2*(e&7)],acc[e>>3][2*(e&7)+1]}*ri1));
    if(g1){ ldswait(smb+C_DONE,8u); }
    else {
      float S=0.f,Q=0.f;
      if(j<13){
        #pragma unroll
        for(int e=0;e<8;e++){ S=dot2(zp[0][e],0x3f803f80u,S); Q=dot2(zp[0][e],zp[0][e],Q); } }
      #pragma unroll
      for(int e=0;e<8;e++){ S=dot2(zp[1][e],0x3f803f80u,S); Q=dot2(zp[1][e],zp[1][e],Q); }
      #define XSUM32(v) do{ auto t_=__builtin_amdgcn_permlane32_swap(__float_as_uint(v),__float_as_uint(v),false,false); v=__uint_as_float(t_[0])+__uint_as_float(t_[1]); }while(0)
      XSUM32(S); XSUM32(Q);
      const u32 sto=OFF_ST+rt*(14*2048)+(32*mi+r)*16;
      if(h==0) st16(rs,sto+j*2048,u4{__float_as_uint(S),tag,__float_as_uint(Q),tag});
    }
    float kp[11]; u4 g[7]={};
    const u32 gof=OFF_ST+rt*(14*2048)+(32*mi+r)*16+h*(7*2048);
    auto samp=[&](){ CFENCE();
      #pragma unroll
      for(int i=0;i<7;i++) g[i]=ld16(rs,gof+i*2048);
      CFENCE(); };
    if(hasc) ksc=kgen_tab(cb,lane,pr,kp); else if(!g1){ pre01(); samp(); }
    if(g1){
      if(hasc){
        kgen_exp(cb,lane,kp,ga);
        signal(smb+C_KP+mi*4,lane);
      }
      if(hasc) ldswait(smb+C_FULL,1u); else if(w<7){ z1st(); f1(); }
      if(hasc) z1st();
    } else {
      float S,Q;
      #pragma unroll 1
      for(int sp=0;sp<SPIN;sp++){ bool ok=true;
        #pragma unroll
        for(int i=0;i<7;i++) ok=ok&&g[i][1]==tag&&g[i][3]==tag;
        if(__all(ok)) break;
        __builtin_amdgcn_s_sleep(4); samp(); }
      S=0.f; Q=0.f;
      #pragma unroll
      for(int i=0;i<7;i++){ S+=__uint_as_float(g[i][0]); Q+=__uint_as_float(g[i][2]); }
      XSUM32(S); XSUM32(Q);
      float mean=S*(1.f/864.f), m2,r2; ln_fin(mean,Q*(1.f/864.f)-mean*mean,m2,r2); m2*=-r2;
      ldswait(smb+C_DONE,8u);
      const u32 tb=smb+RB+mi*RING, ad=tb+(8*(lane>>4)+((lane&15)>>2))*80+(lane&3)*8;
      const u32 zo=OFF_Z2F+((rt*4+mi)*54+(j==13?-2:2+4*j))*1024+lane*16;
      #pragma unroll
      for(int n=0;n<2;n++){
        #pragma unroll
        for(int g2=0;g2<4;g2++){ u2 o;
          #pragma unroll
          for(int e=0;e<2;e++) o[e]=pkbf2(fma2(unpk(zp[n][2*g2+e]),f2{r2,r2},f2{m2,m2}));
          *(LAS u2*)(tb+r*80+g2*16+h*8)=o; }
        CFENCE();
        u2 l0=ldstr(ad), h0=ldstr(ad+320), l1=ldstr(ad+32), h1=ldstr(ad+32+320);
        CFENCE();
        if(j<13||n){ st16(rs,zo+n*2048,u4{l0[0],l0[1],h0[0],h0[1]}); st16(rs,zo+n*2048+1024,u4{l1[0],l1[1],h1[0],h1[1]}); } }
      WAITV(0);
      if(arrive(smb+C_Z2,lane)==3u && lane==0) st4(rs,OFF_F2+(rt*16+j)*128,tag);
    }
  }
  const int ft0=ct<7?7*ct:47;
  bool adone=true; const u32 fao=OFF_FA+((lane<28?lane:0)*8+ct)*128;
  auto aload=[&](){ const u32 vo=(lane>>3)*4096+(lane&7)*16;
    #pragma unroll 1
    for(int i=0;i<10;i++){ setm0<ATL>(smb+i*1024); dma<0>(srdS,vo,OFF_AT+(u32)(80*ct+8*i)*4096+rtB*128); } };
  if(w>=4){
    if(hasc){
      __builtin_amdgcn_s_setprio(3);
      conv_wave(f1,rs,dch,cb,smb+C_KP+mi*4,lane,ksc,dsk);
      __builtin_amdgcn_s_setprio(0);
      if(arrive(smb+C_CV,lane)==(idx<24?2u:1u) && lane==0) st4(rs,OFF_FA+b*128,tag);
    }
    if(w==7){
      if(idx<28){
        pollw<16>(rs,OFF_FX+(lane&31)*128,tag);
        u4 sw[3][4];
        #pragma unroll
        for(int c=0;c<3;c++)
          #pragma unroll
          for(int i=0;i<4;i++) sw[c][i]=ld16(rs,OFF_XNT+((lane>>3)+8*i)*81920+(80*xc+idx+((c<2||idx<24)?28*c:0))*128+(lane&7)*16);
        #pragma unroll
        for(int c=0;c<3;c++){
          #pragma unroll
          for(int i=0;i<4;i++){ const int x=lane+64*i; ldsw128(smb+c*CVS+UOF+(x&3)*1040+((x>>2)+1)*16,sw[c][i]); }
          if(lane<4) ldsw128(smb+c*CVS+UOF+lane*1040,u4{0,0,0,0}); }
        signal(smb+C_FULL,lane);
        #pragma unroll 1
        for(int c=0;c<3;c++){
          #pragma unroll
          for(int i=0;i<4;i++){ sw[0][i]=sw[1][i]; sw[1][i]=sw[2][i]; }
          conv_win(smb+c*CVS,lane,sw[2]); }
        __builtin_amdgcn_s_setprio(3);
        #pragma unroll 1
        for(int c=0;c<(idx<24?3:2);c++){ ldswait<4>(smb+C_KP+c*4,1u); conv_help(smb+c*CVS,lane,sw[0]); signal(smb+C_KP+c*4,lane);
          #pragma unroll
          for(int i=0;i<4;i++){ sw[0][i]=sw[1][i]; sw[1][i]=sw[2][i]; } } }
      __builtin_amdgcn_s_setprio(0);
      if(idx<28){ z1st(); f1(); }
      pollw<16>(rs,OFF_FW+(lane<27?lane:0)*128,tag); signal(smb+C_Z1R,lane);
    }
    ldswait<6>(smb+C_Z1R,5u);
    u4 wq[24];
    #pragma unroll
    for(int i=0;i<24;i++) wq[i]=ld16(rs,OFF_WOF+ct*(135*1024)+40960+mi*RING+((i==23&&mi==3)?22:i)*1024+lane*16);
    if(w<6){
      #pragma unroll 1
      for(u32 i=0;i<20480;i+=4096){ setm0<0>(cb+i); const u32 o=OFF_WOF+ct*(135*1024)+mi*20480+i; dma<0>(srdS,lane*16,o); dma<1024>(srdS,lane*16,o); dma<2048>(srdS,lane*16,o); dma<3072>(srdS,lane*16,o); } }
    if(w==6){ adone=__all(ld4(rs,fao)==tag); if(adone) aload(); }
    ldswait(smb+C_FREE,4u);
    #pragma unroll
    for(int i=0;i<24;i++) ldsw128(smb+RB+mi*RING+i*1024+lane*16,wq[i]);
    if(w<6) WAITV(0);
    signal(smb+C_WR,lane);
    if(!adone){ adone=__all(ld4(rs,fao)==tag); if(adone) aload(); }
    if(w==7 && b==255){
      #pragma unroll 1
      for(int sp=0;sp<SPIN;sp++){ const u32 o=OFF_FA+lane*128; u32 v0=ld4(rs,o),v1=ld4(rs,o+8192),v2=ld4(rs,o+16384),v3=ld4(rs,lane<32? o+24576 : OFF_FW+(lane-32)*128); CFENCE();
        if(__all(v0==tag&&v1==tag&&v2==tag&&v3==tag)) break; __builtin_amdgcn_s_sleep(4); }
      if(lane==0) st4(rs,OFF_EP,tag); }
  }
  int w2=w; asm volatile("" : "+s"(w2));
  if(w2<4){
    __builtin_amdgcn_s_setprio(2);
    int r=lane&15, q=lane>>4;
    const u32 rb=smb+RB+w*RING;
    u4 srdA=mksrd((const char*)p.wsp+(size_t)rtB*64*4096+w*1024-4096+256);
    const char* zb=p.ws+OFF_Z2F+(u32)(16*w*54+ft0+3)*1024;
    u32 voA[2]; { u32 lr=lane>>3,c=lane&7; voA[0]=lr*4096+((c^(lr>>1))<<4); voA[1]=voA[0]^64u; }
    u32 sofa[8];
    #pragma unroll
    for(int g=0;g<8;g++) sofa[g]=((g+2*w)&7)*32768+4096-(g&3)*1024;
    const u32 voB=lane*16;
    u32 la[2]; la[0]=rb+r*128+((q^(r>>1))<<4); la[1]=la[0]^64u;
    f4 acc[4][7]; u4 B[NB][7]; u4 A[2][4]; u2 z1[7];
    auto issueA=[&](auto sc,auto gc){ constexpr int s=sc.value,g=gc.value; if constexpr((g&3)==0) setm0<((s+1)%3)*8192+g*1024>(rb); dma<(g&3)*1024>(srdA,voA[g&1],sofa[g]);
      if constexpr(g==7){ u64 a=((u64)srdA[1]<<32|srdA[0])+128; srdA[0]=(u32)a; srdA[1]=(u32)(a>>32); } };

    u32 bsr,fv;
    auto fof=[&](){ const u32 l=voB>>4; return OFF_F1+((rtB>>1)*16+(((ft0>>1)+(l&3))>>1))*128; };
    #pragma unroll
    for(int m=0;m<4;m++){ A[0][m]=ldsr128(la[0]+m*2048+8192); A[1][m]=ldsr128(la[1]+m*2048+8192); }
    sfor<8>([&](auto g){ issueA(std::integral_constant<int,2>{},g); });
    waitA(A[0]); waitA(A[1]);
    sfor<8>([&](auto g){ issueA(std::integral_constant<int,3>{},g); });
    { const int hf=(ft0>>1)+q; pollw(rs,OFF_F2+((4*w+(lane&3))*16+(hf==0?13:(hf-1)>>1))*128,tag); }
    sfor<NB>([&](auto hh){ const char* s=zb+(u32)hh.value*(54*1024); sfor<7>([&](auto n){ gload<(n.value-3)*1024>(B[hh][n],voB,s); }); });
    auto redw=[&](auto nc){ constexpr int n=nc.value; *(LAS f4*)(rb+n*1024+voB)=acc[1][n]; *(LAS f4*)(rb+(7+n)*1024+voB)=acc[2][n]; *(LAS f4*)(rb+(14+n)*1024+voB)=acc[3][n]; };
    const char* z1b=p.ws+OFF_Z1T+(u32)((rtB*4+w)*27+(ft0>>1))*1024;
    sfor<16>([&](auto hc){
      constexpr int h=hc.value, S=h%NB, P=h&1, HZ=HZZ, cnt=g2cnt(h);
      waitB<cnt>(B[S]); waitA(A[P]);
      if constexpr(h==HZ){ tie1(fv); if(!__all(fv==tag)) pollw(rs,fof(),tag); }
      if constexpr(h+1<16 && h>0){
        #pragma unroll
        for(int m=0;m<4;m++) A[P^1][m]=ldsr128(la[(h+1)&1]+m*2048+((((h+1)>>1)+1)%3)*8192);
      }
      if constexpr(h==HZ-3) gload4<0>(fv,fof(),p.ws);
      const char* s=zb+(u32)(h+NB)*(54*1024);
      sfor<7>([&](auto n){
        if constexpr(h==0) mfma4z(acc[0][n],acc[1][n],acc[2][n],acc[3][n],B[S][n],A[P]); else mfma4(acc[0][n],acc[1][n],acc[2][n],acc[3][n],B[S][n],A[P]);
        if constexpr(h>=3 && h<=10 && (n.value&1)==0) issueA(std::integral_constant<int,(h-1)/2+3>{},std::integral_constant<int,((h-1)&1)*4+n.value/2>{});
        if constexpr(h+NB<16) gload_t<(n.value-3)*1024>(B[S][n],voB,s);
        if constexpr(h==HZ){ gload8<(n.value>>1)*1024>(z1[n],voB,z1b+((n.value&1)? ((ft0&1)?1024:8) : (ft0&1)*8));
          if constexpr(n.value==6) { gload2(bsr,(voB>>3)&30u,(const char*)(p.bsp+(rtB*4+w)*16)); } }
        if constexpr(h==13 && n.value==0) signal(smb+C_Z1R,lane);
        if constexpr(h==15){ SCHED(); if constexpr(n.value>=2) redw(std::integral_constant<int,n.value-2>{}); SCHED(); }
      });
    });
    asm volatile("s_nop 15\n\ts_nop 3" ::: "memory");
    SCHED();
    lane=__builtin_amdgcn_mbcnt_hi(~0u,__builtin_amdgcn_mbcnt_lo(~0u,0u)); asm volatile("" : "+v"(lane)); r=lane&15; q=lane>>4;
    redw(std::integral_constant<int,5>{}); redw(std::integral_constant<int,6>{});
    signal(smb+C_RED,lane);
    ldswait(smb+C_RED,4u);
    #pragma unroll
    for(int m=1;m<4;m++){ u32 src=smb+RB+((w-m)&3)*RING+(m-1)*7168+lane*16;
      #pragma unroll
      for(int n=0;n<7;n++) acc[0][n]+=*(LAS f4*)(src+n*1024); }
    asm volatile("s_waitcnt vmcnt(0) lgkmcnt(0)" : "+v"(z1[0]),"+v"(z1[1]),"+v"(z1[2]),"+v"(z1[3]),"+v"(z1[4]),"+v"(z1[5]),"+v"(z1[6]),"+v"(bsr) :: "memory");
    const float bs=bflo(bsr);
    signal(smb+C_FREE,lane);
    u2 o[7];
    #pragma unroll
    for(int n=0;n<7;n++)
      #pragma unroll
      for(int e=0;e<2;e++)o[n][e]=pkbf2(unpk(z1[n][e])*(f2{acc[0][n][2*e],acc[0][n][2*e+1]}+bs));
    const u32 ho=OFF_HF+(u32)((rtB*4+w)*54+ft0)*512+(q>>1)*256+r*16;
    #pragma unroll
    for(int n=0;n<6;n+=2){ auto sx=__builtin_amdgcn_permlane16_swap(o[n][0],o[n+1][0],false,false); auto sy=__builtin_amdgcn_permlane16_swap(o[n][1],o[n+1][1],false,false);
      if(ct<7||n>=2) st16(rs,ho+(n+(q&1))*512,u4{sx[0],sy[0],sx[1],sy[1]}); }
    st8(rs,ho+6*512+(q&1)*8,o[6]);
    WAITV(0);
    if(arrive(smb+C_H,lane)==3u && lane==0) st4(rs,OFF_FH+(rtB*8+ct)*128,tag);
    __builtin_amdgcn_s_setprio(0);
  }
  { const int r=lane&15, q=lane>>4, m=w&3, kh=w>>2;
    u2 xv[5]={};
    if(kh){
      #pragma unroll
      for(int n=0;n<5;n++) xv[n]=*(const u2*)(p.x+(rtB*64+m*16+r)*640+80*ct+16*n+4*q);
    }
    f4 acc[5]={}; u4 W[3][5];
    const u32 wl2=smb+lane*16+RB-40960+kh*14*5120, wl0=kh? wl2 : smb+lane*16, wl1=kh? wl2 : smb+lane*16+CVS-20480;
    #define WAD(k,n) (((k)<4? wl0 : (k)<8? wl1 : wl2)+((k)*5+(n))*1024)
    f4 ax[5]={};
    auto addax=[&](){
      #pragma unroll
      for(int n=0;n<5;n++){ u2 av=ldstr(smb+ATL+(16*n+4*q+(r>>2))*128+(16*m+4*(r&3))*2); ax[n]=f4{bflo(av[0]),bfhi(av[0]),bflo(av[1]),bfhi(av[1])}+f4{bflo(xv[n][0]),bfhi(xv[n][0]),bflo(xv[n][1]),bfhi(xv[n][1])}; } };
    if(w==6 && adone){ WAITV(0); signal(smb+C_AT,lane); }
    u32 oo[3],to[3]; const u32 tb=smb+CVS+m*3072;
    #pragma unroll
    for(int i=0;i<3;i++){ const int it=lane+64*i, rr=(it*205)>>11, cc=it-rr*10; oo[i]=opq((rtB*64+m*16+rr)*1280+ct*160+cc*16); to[i]=opq(tb+rr*176+cc*16); }
    if(m==0){ ldswait(smb+C_H,4u); pollw(rs,OFF_FH+(rtB*8+kh*4+(lane&3))*128,tag); signal(smb+C_TAG+kh*4,lane); } else ldswait(smb+C_TAG+kh*4,1u);
    u4 hf[14];
    const u32 ho=OFF_HF+(u32)((rtB*4+m)*27+kh*14)*1024+lane*16;
    #pragma unroll
    for(int k=0;k<HPF;k++) hf[k]=ld16(rs,ho+k*1024);
    const bool early=kh && uni(ldsv(smb+C_AT))!=0u;
    CFENCE();
    if(early) addax();
    ldswait(smb+C_WR,4u);
    const u32 wl3=kh? wl2-5120 : wl2;
    #pragma unroll
    for(int n=0;n<10;n++) W[n/5][n%5]=ldsr128(WAD(n/5,n%5));
    #pragma unroll
    for(int k=0;k<13;k++){
      if(k+HPF<14) hf[k+HPF]=ld16(rs,ho+((k+HPF==13&&kh)?12:k+HPF)*1024);
      SCHED();
      #pragma unroll
      for(int n=0;n<5;n++){ acc[n]=mfma16(W[k%3][n],hf[k],acc[n]); SCHED();
        if(k<11) W[(k+2)%3][n]=ldsr128(WAD(k+2,n)); else if(k==11) W[1][n]=ldsr128(wl3+(65+n)*1024);
        SCHED(); }
      if(k==5 && !kh) signal(smb+C_G1,lane);
    }
    const u32 pb=smb+m*5120+lane*16;
    if(!kh){
      #pragma unroll
      for(int n=0;n<5;n++) acc[n]=mfma16(W[1][n],hf[13],acc[n]);
      ldswait(smb+C_G1,4u);
      #pragma unroll
      for(int n=0;n<5;n++) *(LAS f4*)(pb+n*1024)=acc[n];
      signal(smb+C_P+m*4,lane);
    } else {
      if(w==6 && !adone){ pollw(rs,fao,tag); aload(); WAITV(0); signal(smb+C_AT,lane); }
      if(!early){ ldswait(smb+C_AT,1u); addax(); }
      ldswait(smb+C_P+m*4,1u);
      #pragma unroll
      for(int n=0;n<5;n++){ const f4 o=(acc[n]+ax[n])+*(LAS f4*)(pb+n*1024); *(LAS u2*)(tb+r*176+n*32+q*8)=u2{pkbf(o[0],o[1]),pkbf(o[2],o[3])}; }
      CFENCE();
      #pragma unroll
      for(int i=0;i<3;i++) if(i<2||lane<32) __builtin_amdgcn_raw_buffer_store_b128(ldsr128(to[i]),__builtin_amdgcn_make_buffer_rsrc(p.out,0,2048*1280,0x00020000),oo[i],0,16);
    }
  }
}
extern "C" void kernel_launch(void* const* d_in,const int* in_sizes,int n_in,void* d_out,int out_size,void* d_ws,size_t ws_size,hipStream_t stream){
  Params p;
  p.x=(const u16*)d_in[0]; p.lar=(const u16*)d_in[1]; p.aim=(const u16*)d_in[2]; p.cre=(const u16*)d_in[3]; p.cim=(const u16*)d_in[4];
  p.ldt=(const u16*)d_in[5]; p.dsk=(const u16*)d_in[6]; p.win=(const u16*)d_in[8]; p.wsp=(const u16*)d_in[9]; p.bsp=(const u16*)d_in[10]; p.wout=(const u16*)d_in[11];
  p.out=(u16*)d_out; p.ws=(char*)d_ws;
  (void)hipFuncSetAttribute((const void*)fused,hipFuncAttributeMaxDynamicSharedMemorySize,LDS_TOTAL);
  fused<<<256,512,LDS_TOTAL,stream>>>(p);
}
```

```cpp
#define DIAG 99
#include <hip/hip_runtime.h>
#include <utility>
#pragma clang diagnostic ignored "-Wint-to-pointer-cast"
typedef unsigned short u16;
typedef unsigned int u32;
typedef unsigned long long u64;
typedef __attribute__((ext_vector_type(8))) __bf16 bf8;
typedef __attribute__((ext_vector_type(8))) _Float16 hf8;
typedef __attribute__((ext_vector_type(2))) __bf16 bf2;
typedef __attribute__((ext_vector_type(2))) _Float16 hf2;
typedef __attribute__((ext_vector_type(2))) float f2;
typedef __attribute__((ext_vector_type(4))) float f4;
typedef __attribute__((ext_vector_type(16))) float f16v;
typedef __attribute__((ext_vector_type(4))) u32 u4;
typedef __attribute__((ext_vector_type(2))) u32 u2;
typedef __attribute__((ext_vector_type(4))) short s4;
#define LAS __attribute__((address_space(3)))
#define GAS __attribute__((address_space(1)))
#define DI __device__ __forceinline__

constexpr u32 OFF_Z2F = 0;
constexpr u32 OFF_Z1T = OFF_Z2F + 64*54*1024;
constexpr u32 OFF_XNT = OFF_Z1T + 128*27*1024;
constexpr u32 OFF_WOF = OFF_XNT + 640*4096;
constexpr u32 OFF_HF  = OFF_WOF + 8*27*5*1024;
constexpr u32 OFF_AT  = OFF_HF + 128*27*1024;
constexpr u32 OFF_ST  = OFF_AT + 640*4096;
constexpr u32 OFF_RS  = OFF_ST + 16*14*2048+8192;
constexpr u32 OFF_EP  = OFF_RS + 2048*16;
constexpr u32 OFF_F2  = OFF_EP + 256;
constexpr u32 OFF_F1  = OFF_F2 + 256*128;
constexpr u32 OFF_FX  = OFF_F1 + 256*128;
constexpr u32 OFF_FW  = OFF_FX + 256*128;
constexpr u32 OFF_FH  = OFF_FW + 32*128;
constexpr u32 OFF_FA  = OFF_FH + 256*128;
constexpr u32 WS_END  = OFF_FA + 256*128;

struct Params {
  const u16 *x, *lar, *aim, *cre, *cim, *ldt, *dsk, *win, *wsp, *bsp, *wout;
  u16* out; char* ws;
};
#define STAMP(id,wv) do{ if((id)==DIAG) dg1=__builtin_amdgcn_s_memrealtime(); }while(0)
#define RST(i) do{ if((i)+20==DIAG) dg1=__builtin_amdgcn_s_memrealtime(); }while(0)
template<class F,int... I> DI void sfor_impl(F&& f, std::integer_sequence<int,I...>){ (f(std::integral_constant<int,I>{}),...); }
template<int N,class F> DI void sfor(F&& f){ sfor_impl(f, std::make_integer_sequence<int,N>{}); }

DI float bflo(u32 v){ return __uint_as_float(v<<16); }
DI float bfhi(u32 v){ return __uint_as_float(v&0xffff0000u); }
DI float bf1(u16 v){ return __uint_as_float(((u32)v)<<16); }
DI u32 pkbf(float a,float b){ f2 v={a,b}; return __builtin_bit_cast(u32,__builtin_convertvector(v,bf2)); }
DI u32 pkhf(float a,float b){ f2 v={a,b}; return __builtin_bit_cast(u32,__builtin_convertvector(v,hf2)); }
DI float rbf(float a){ return bflo(pkbf(a,a)); }
DI float hlo(u32 v){ return (float)__builtin_bit_cast(hf2,v)[0]; }
DI float hhi(u32 v){ return (float)__builtin_bit_cast(hf2,v)[1]; }
DI float dot2(u32 a,u32 b,float c){ return __builtin_amdgcn_fdot2_f32_bf16(__builtin_bit_cast(bf2,a),__builtin_bit_cast(bf2,b),c,false); }
DI f4 mfma16(u4 a,u4 b,f4 c){ return __builtin_amdgcn_mfma_f32_16x16x32_bf16(__builtin_bit_cast(bf8,a),__builtin_bit_cast(bf8,b),c,0,0,0); }
DI f16v mfma32h(u4 a,u4 b,f16v c){ return __builtin_amdgcn_mfma_f32_32x32x16_f16(__builtin_bit_cast(hf8,a),__builtin_bit_cast(hf8,b),c,0,0,0); }
DI u4 ldsr128(u32 a){ return *(LAS u4*)a; }
DI u2 ldsr64(u32 a){ return *(LAS u2*)a; }
DI void ldsw128(u32 a,u4 v){ *(LAS u4*)a=v; }
DI u2 ldstr(u32 a){ return __builtin_bit_cast(u2,__builtin_amdgcn_ds_read_tr16_b64_v4i16((LAS s4*)a)); }
DI u32 uni(u32 v){ return __builtin_amdgcn_readfirstlane(v); }

template<int OFF> DI void gload(u4& dst,u32 voff,const char* base){ asm volatile("global_load_dwordx4 %0, %1, %2 offset:%3 sc1" : "=v"(dst) : "v"(voff), "s"(base), "n"(OFF)); }
template<int OFF> DI void gload_t(u4& dst,u32 voff,const char* base){ asm volatile("global_load_dwordx4 %0, %1, %2 offset:%3 sc1" : "+v"(dst) : "v"(voff), "s"(base), "n"(OFF)); }
DI void mfma4(f4& c0,f4& c1,f4& c2,f4& c3,const u4& a,const u4 (&b)[4]){
  asm volatile("v_mfma_f32_16x16x32_bf16 %0, %4, %5, %0\n\tv_mfma_f32_16x16x32_bf16 %1, %4, %6, %1\n\tv_mfma_f32_16x16x32_bf16 %2, %4, %7, %2\n\tv_mfma_f32_16x16x32_bf16 %3, %4, %8, %3"
    : "+v"(c0),"+v"(c1),"+v"(c2),"+v"(c3) : "v"(a),"v"(b[0]),"v"(b[1]),"v"(b[2]),"v"(b[3])); }
DI void mfma4z(f4& c0,f4& c1,f4& c2,f4& c3,const u4& a,const u4 (&b)[4]){
  asm volatile("v_mfma_f32_16x16x32_bf16 %0, %4, %5, 0\n\tv_mfma_f32_16x16x32_bf16 %1, %4, %6, 0\n\tv_mfma_f32_16x16x32_bf16 %2, %4, %7, 0\n\tv_mfma_f32_16x16x32_bf16 %3, %4, %8, 0"
    : "=&v"(c0),"=&v"(c1),"=&v"(c2),"=&v"(c3) : "v"(a),"v"(b[0]),"v"(b[1]),"v"(b[2]),"v"(b[3])); }
template<int CNT> DI void waitB(u4 (&B)[7]){ asm volatile("s_waitcnt vmcnt(%7)" : "+v"(B[0]),"+v"(B[1]),"+v"(B[2]),"+v"(B[3]),"+v"(B[4]),"+v"(B[5]),"+v"(B[6]) : "n"(CNT) : "memory"); }
DI void waitA(u4 (&A)[4]){ asm volatile("s_waitcnt lgkmcnt(0)" : "+v"(A[0]),"+v"(A[1]),"+v"(A[2]),"+v"(A[3]) :: "memory"); }
#define WAITV(n) asm volatile("s_waitcnt vmcnt(%0)" :: "n"(n) : "memory")
#define WAITL0() asm volatile("s_waitcnt lgkmcnt(0)" ::: "memory")
#define SCHED() __builtin_amdgcn_sched_barrier(0)
DI void bar(){ asm volatile("s_waitcnt lgkmcnt(0)\n\ts_barrier" ::: "memory"); }
DI f2 unpk(u32 v){ return f2{bflo(v),bfhi(v)}; }
DI u32 pkbf2(f2 v){ return __builtin_bit_cast(u32,__builtin_convertvector(v,bf2)); }
DI u32 pkhf2(f2 v){ return __builtin_bit_cast(u32,__builtin_convertvector(v,hf2)); }
DI f2 fma2(f2 a,f2 b,f2 c){ return __builtin_elementwise_fma(a,b,c); }
DI f2 gelu2(f2 x){
  f2 u=x*fma2(x*x,f2{-0.10294324f,-0.10294324f},f2{-2.30220820f,-2.30220820f});
  f2 d=f2{__builtin_amdgcn_exp2f(u[0]),__builtin_amdgcn_exp2f(u[1])}+f2{1.f,1.f};
  return x*f2{__builtin_amdgcn_rcpf(d[0]),__builtin_amdgcn_rcpf(d[1])};
}
DI void ln_fin(float mean,float var,float& m,float& rinv){
  m=rbf(mean); float v=rbf(__builtin_fmaxf(var,0.f));
  float s=rbf(__builtin_amdgcn_sqrtf(rbf(v+1e-5f))); float r=__builtin_amdgcn_rcpf(s); rinv=__builtin_fmaf(__builtin_fmaf(-s,r,1.f),r,r);
}
typedef __amdgpu_buffer_rsrc_t rsrc_t;
DI void st16(rsrc_t rs,u32 off,u4 v){ __builtin_amdgcn_raw_buffer_store_b128(v,rs,off,0,16); }
DI void st8(rsrc_t rs,u32 off,u2 v){ __builtin_amdgcn_raw_buffer_store_b64(v,rs,off,0,16); }
DI void st4(rsrc_t rs,u32 off,u32 v){ __builtin_amdgcn_raw_buffer_store_b32(v,rs,off,0,16); }
DI u4 ld16(rsrc_t rs,u32 off){ return __builtin_amdgcn_raw_buffer_load_b128(rs,off,0,16); }
DI u2 ld8(rsrc_t rs,u32 off){ return __builtin_amdgcn_raw_buffer_load_b64(rs,off,0,16); }
DI u32 ld4(rsrc_t rs,u32 off){ return __builtin_amdgcn_raw_buffer_load_b32(rs,off,0,16); }
constexpr int SPIN=1<<17;
constexpr u32 TAGINC=0x9E3779B9u;
template<int SL=2> DI void pollw(rsrc_t rs,u32 off,u32 tag){
  #pragma unroll 1
  for(int s=0;s<SPIN;s++){ u32 v=ld4(rs,off); asm volatile("" ::: "memory"); if(__all(v==tag)) break; __builtin_amdgcn_s_sleep(SL); }
}

DI f16v mfma32b(u4 a,u4 b,f16v c){ return __builtin_amdgcn_mfma_f32_32x32x16_bf16(__builtin_bit_cast(bf8,a),__builtin_bit_cast(bf8,b),c,0,0,0); }
DI u4 mksrd(const void* b){ u64 a=(u64)b; return u4{(u32)a,(u32)(a>>32)&0xffffu,0xffffffffu,0x00020000u}; }
template<int IMM> DI void setm0(u32 base){ asm volatile("s_add_u32 m0, %0, %1\n\ts_nop 0" :: "s"(base), "n"(IMM) : "scc","memory"); }
template<int OFF> DI void dma(u4 srd,u32 voff,u32 soff){ asm volatile("buffer_load_dwordx4 %0, %1, %2 offen offset:%3 sc1 lds" :: "v"(voff), "s"(srd), "s"(soff), "n"(OFF) : "memory"); }
DI void touch(u4 srd,u32 voff,u32 soff){ asm volatile("buffer_load_dword %0, %1, %2 offen sc1 lds" :: "v"(voff), "s"(srd), "s"(soff) : "memory"); }
template<int OFF> DI void gload8(u2& dst,u32 voff,const char* base){ asm volatile("global_load_dwordx2 %0, %1, %2 offset:%3 sc1" : "=v"(dst) : "v"(voff), "s"(base), "n"(OFF)); }
DI void gload2(u32& dst,u32 voff,const char* base){ asm volatile("global_load_ushort %0, %1, %2" : "=v"(dst) : "v"(voff), "s"(base)); }
DI u32 arrive(u32 a,int){ u32 o; asm volatile("s_mov_b64 exec, 1\n\tds_add_rtn_u32 %0, %1, %2\n\ts_mov_b64 exec, -1\n\ts_waitcnt lgkmcnt(0)" : "=&v"(o) : "v"(a), "v"(1u) : "memory"); return uni(o); }
DI void ldswait(u32 a,u32 n){ while(__hip_atomic_load((LAS u32*)a,__ATOMIC_RELAXED,__HIP_MEMORY_SCOPE_WORKGROUP)<n) __builtin_amdgcn_s_sleep(1); asm volatile("" ::: "memory"); }
DI float addf(float a,float b){ float o; asm("v_add_f32 %0, %1, %2" : "=v"(o) : "v"(a), "v"(b)); return o; }
DI void tie1(u32& g){ asm volatile("" : "+v"(g) :: "memory"); }
DI u32 opq(u32 v){ asm volatile("" : "+v"(v)); return v; }

constexpr u32 STG=32768;
constexpr u32 RING=24576, RB=62848, CONV=0, KP=2208, UOF=16768, CVS=UOF+4160, S7O=4*KP, S9O=S7O+2112, KZO=S9O+2112, MISC=RB+4*RING, JNK=MISC+2048, PM=163840-128, LDS_TOTAL=163840;
constexpr u32 ATL=2*CVS;
constexpr u32 C_FULL=PM, C_G1=PM+4, C_Z2=PM+8, C_Z1=PM+12, C_RED=PM+16, C_H=PM+20, C_FREE=PM+24, C_CV=PM+28, C_Z1R=PM+32, C_TAG=PM+40, C_KP=PM+48, C_DONE=PM+64, C_ZQ=PM+68, C_CS=PM+72, C_K2=PM+80, C_P=PM+96, C_AT=PM+112, C_WR=PM+116;
constexpr u32 CODE_BYTES=37888;
#define LA 3
#define TC_S 7
#define G2PRIO 2
#define XMAX 9
#define GS_K 35
#define WNT ""
#define TS1 6
#define HPRIO 3
#define CPRIO 3
#define POLLM 1
#define HPF 5
#define CVSKIP 0
#define BROT 0
#define STAG 0u
#define FXDLY 4
#define HSH 0
#define ZW 1
#define HZZ 14
#define ZPRE 1
#define PDLY 8
#define RS_S 7
#define KROT ((((j+rt)&3)*5)>>1)
DI void signal(u32 a,int){ asm volatile("s_mov_b64 exec, 1\n\tds_add_u32 %0, %1\n\ts_mov_b64 exec, -1" :: "v"(a), "v"(1u) : "memory"); }
template<int OFF> DI void gload4(u32& dst,u32 voff,const char* base){ asm volatile("global_load_dword %0, %1, %2 offset:%3 sc1" : "=v"(dst) : "v"(voff), "s"(base), "n"(OFF)); }
template<int CNT> DI void waitG(u4& g){ asm volatile("s_waitcnt vmcnt(%1)" : "+v"(g) : "n"(CNT) : "memory"); }

DI f2 cmulp(f2 a,f2 b,f2 bs){ return fma2(f2{a[1],a[1]},bs,f2{a[0],a[0]}*b); }
DI float mulf(float a,float b){ float o; asm("v_mul_f32 %0, %1, %2" : "=v"(o) : "v"(a),"v"(b)); return o; }
DI float fmaf1(float a,float b,float c){ float o; asm("v_fma_f32 %0, %1, %2, %3" : "=v"(o) : "v"(a),"v"(b),"v"(c)); return o; }
DI u32 ldsv(u32 a){ return __hip_atomic_load((LAS u32*)a,__ATOMIC_RELAXED,__HIP_MEMORY_SCOPE_WORKGROUP); }
DI void ldswait4(u32 a,int lane,u32 n){ while(__any(ldsv(a+(lane&3)*4)<n)) __builtin_amdgcn_s_sleep(1); asm volatile("" ::: "memory"); }
DI u32 gettag(u32 a){ ldswait(a+4,1u); return uni(ldsv(a)); }
DI f2 cpow_dir(float lr,float rv,float pw){ float e=__builtin_amdgcn_exp2f(lr*pw); float f=__builtin_amdgcn_fractf(rv*pw);
  return f2{e*__builtin_amdgcn_cosf(f),e*__builtin_amdgcn_sinf(f)}; }
DI u32 dup(u32 a){ return __builtin_amdgcn_perm(a,a,0x01000100u); }
DI u32 cat(u32 lo,u32 hi){ return __builtin_amdgcn_perm(hi,lo,0x05040100u); }
DI u32 lds16(u32 a){ return *(LAS u16*)a; }

template<class CB> DI float kgen_tab(u32 cb,int lane,const u32 (&pr)[6],float (&kp)[11],CB&& mid){
  const float dt=rbf(__expf(bflo(pr[4])));
  const float cr[2]={bflo(pr[2]),bfhi(pr[2])}, cm[2]={bflo(pr[3]),bfhi(pr[3])};
  float mx=fmaxf(fmaxf(fabsf(cr[0]),fabsf(cm[0])),fmaxf(fabsf(cr[1]),fabsf(cm[1])));
  u32 eb=__float_as_uint(mx)&0x7f800000u;
  #define DPPMAX(c) eb=max(eb,(u32)__builtin_amdgcn_update_dpp(0,(int)eb,c,0xf,0xf,false))
  DPPMAX(0x128); DPPMAX(0x124); DPPMAX(0x122); DPPMAX(0x121);
  eb=max(max((u32)__builtin_amdgcn_readlane(eb,0),(u32)__builtin_amdgcn_readlane(eb,16)),max((u32)__builtin_amdgcn_readlane(eb,32),(u32)__builtin_amdgcn_readlane(eb,48))); eb=eb<0x01000000u?0x3f800000u:(eb>0x7e000000u?0x7e000000u:eb);
  const float ksc=2.f*dt*__uint_as_float(eb), isc=__uint_as_float(0x7f000000u-eb);
  const int r=lane&31,h=lane>>5;
  const u32 TP=272, TA=cb, TB=cb+22*TP;
  f16v acc={};
  f2 an[2][4];
  #pragma unroll
  for(int c=0;c<2;c++){ const float l_a=c?bfhi(pr[0]):bflo(pr[0]), i_m=c?bfhi(pr[1]):bflo(pr[1]);
    const float lr=(-rbf(__expf(l_a))*dt)*1.44269504f, rv=(i_m*dt)*0.15915494f;
    an[c][0]=cpow_dir(lr,rv,1.f); an[c][1]=cpow_dir(lr,-rv,8.f); an[c][2]=cpow_dir(lr,-rv,16.f); an[c][3]=cpow_dir(lr,rv,256.f); }
  u32 hold[54]; const u32 wt=TA+lane*4;
  struct P2 { f2 v[2]; };
  auto mul=[&](const P2& x,int k){ P2 o;
    #pragma unroll
    for(int c=0;c<2;c++) o.v[c]=cmulp(x.v[c],an[c][k],f2{-an[c][k][1],an[c][k][0]});
    return o; };
  auto put=[&](int row,const P2& x){ *(LAS u32*)(wt+row*TP)=pkhf2(x.v[0]); hold[row]=pkhf2(x.v[1]); };
  P2 e={{f2{1.f,0.f},f2{1.f,0.f}}}, o={{an[0][1],an[1][1]}}, bk[8], t;
  bk[0].v[0]=f2{cr[0]*isc,cm[0]*isc}; bk[0].v[1]=f2{cr[1]*isc,cm[1]*isc}; put(0,bk[0]); t=bk[0];
  constexpr int tr[8]={8,10,11,16,17,20,21,18};
  #pragma unroll
  for(int k=0;k<16;k++){ put(22+2*k,e); put(23+2*k,o);
    if(k<15){ if(k) e=mul(e,2); else { e.v[0]=an[0][2]; e.v[1]=an[1][2]; } o=mul(o,2); }
    if(k<8){ t=mul(t,3); put(tr[k],t); }
    if(k<7){ bk[k+1]=mul(bk[k],0); put(k+1,bk[k+1]); }
    if(k==8){ put(9,mul(bk[2],3)); put(13,mul(bk[6],3)); t=mul(bk[4],3); put(12,t); }
    if(k==9||k==10){ t=mul(t,3); put(5+k,t); }
    SCHED(); }
  const int ra=(r>21||r==19)?0:r;
  #pragma unroll
  for(int c=0;c<2;c++){
    asm volatile("" ::: "memory");
    u4 fa[8],fb[8];
    #pragma unroll
    for(int ks=0;ks<8;ks++){ fa[ks]=ldsr128(TA+ra*TP+ks*32+h*16); fb[ks]=ldsr128(TB+r*TP+ks*32+h*16); }
    asm volatile("" ::: "memory");
    if(c==0){
      #pragma unroll
      for(int i=0;i<54;i++) if(i!=19) *(LAS u32*)(wt+i*TP)=hold[i]; }
    #pragma unroll
    for(int ks=0;ks<8;ks++) acc=mfma32h(fa[ks],fb[ks],acc);
  }
  asm volatile("" ::: "memory");
  #pragma unroll
  for(int i=0;i<11;i++) kp[i]=acc[i];
  return ksc;
}
DI void kgen_exp(u32 cb,int lane,const float (&acc)[11],const u32 (&ga)[11]){
  const int r=lane&31,h=lane>>5;
  { const u32 a4=cb+r*16+h*8, a5=cb+r*16+h*1024, az=cb+KZO+r*2+h*128;
    #define PK(addr,base,reg) do{ *(LAS _Float16*)((addr)+2*(base))=(_Float16)acc[reg]; }while(0)
    PK(a4,0,0);PK(a4,1,1);PK(a4,2,2);PK(a4,3,3); PK(a4,256,4);PK(a4,258,5);PK(a4,512,6);PK(a4,768,7);
    PK(a5,1024,8); PK(az,255,10); PK(az,127,8); PK(az,159,9);
    #undef PK
    if(lane<16) ldsw128(cb+KZO+lane*16,u4{0,0,0,0});
  }
  { const u32 g0=cb+2*(1024-8*lane);
    u32 f2_=lds16(g0+(lane?8:0)), a2=lds16(g0), m2=lds16(g0-8), b2=lds16(g0-16);
    u32 e[11];
    #pragma unroll
    for(int j=0;j<11;j++) e[j]=lds16(ga[j]);
    u32 k01=*(LAS u32*)cb, k23=*(LAS u32*)(cb+4), kl=lds16(cb+2048);
    WAITL0();
    const u32 wo=cb+96+lane*16;
    { u32 aa=dup(a2),mm=dup(m2),bb=dup(b2),am=cat(a2,m2),fa=cat(f2_,a2),mb=cat(m2,b2);
      ldsw128(wo,u4{aa,am,mm,bb}); ldsw128(wo+KP,u4{aa,aa,mm,mb}); ldsw128(wo+2*KP,u4{aa,aa,am,mm}); ldsw128(wo+3*KP,u4{fa,aa,aa,mm}); }
    { u32 o0=cat(e[0],e[1]),o1=cat(e[2],e[3]),o2=cat(e[4],e[5]),o3=cat(e[6],e[7]),o4=cat(e[8],e[9]);
      u32 p1=cat(e[1],e[2]),d0=cat(e[3],e[4]),d1=cat(e[5],e[6]),d2=cat(e[7],e[8]),d3=cat(e[9],e[10]);
      ldsw128(wo+1024,u4{d0,d1,d2,d3}); ldsw128(wo+1024+KP,u4{o1,o2,o3,o4}); ldsw128(wo+1024+2*KP,u4{p1,d0,d1,d2}); ldsw128(wo+1024+3*KP,u4{o0,o1,o2,o3}); }
    if(lane<10){ if(lane){k01=0;k23=0;} if(lane!=9) kl=0;
      const u32 d0=k01&0xffffu, o1=__builtin_amdgcn_alignbit(k01,k01,16), o0=__builtin_amdgcn_alignbit(k23,k23,16), p1=(k23&0xffffu)|(k01&0xffff0000u), kk=dup(kl), zk=kl<<16;
      const u32 w2=lane<4? wo+2048 : wo-160;
      ldsw128(w2,u4{d0,0,kk,kk}); ldsw128(w2+KP,u4{o1,0,zk,kk}); ldsw128(w2+2*KP,u4{p1,d0,0,kk}); ldsw128(w2+3*KP,u4{o0,o1,0,zk}); }
  }
}
DI f4 mfma16h(u4 a,u4 b,f4 c){ return __builtin_amdgcn_mfma_f32_16x16x32_f16(__builtin_bit_cast(hf8,a),__builtin_bit_cast(hf8,b),c,0,0,0); }
DI u4 lds2x64(u32 a){ const u2 lo=ldsr64(a), hi=ldsr64(a+8); return u4{lo[0],lo[1],hi[0],hi[1]}; }
DI void conv_win(u32 cb,int lane,u4 (&sw)[4]){
  float P[25]; P[0]=0.f;
  #pragma unroll
  for(int c=0;c<4;c++){ const int X=2*lane-2+c; const u4 v=ldsr128(cb+UOF+(X&3)*1040+((X>>2)+1)*16);
    #pragma unroll
    for(int e=0;e<8;e++){ const int i=8*c+e-4; if(i>=0&&i<24) P[i+1]=P[i]+((e&1)?hhi(v[e>>1]):hlo(v[e>>1])); } }
  #pragma unroll
  for(int k=0;k<8;k++){ sw[k>>2][k&3]=pkhf(P[2*k+8]-P[2*k+1],P[2*k+9]-P[2*k+2]); sw[2+(k>>2)][k&3]=pkhf(P[2*k+9]-P[2*k],P[2*k+10]-P[2*k+1]); }
}
DI void conv_help(u32 cb,int lane,const u4 (&sw)[4]){
  const int r=lane&15,q=lane>>4; f4 y[2][2]={};
  ldsw128(cb+S7O+48+lane*32,sw[0]); ldsw128(cb+S7O+64+lane*32,sw[1]); ldsw128(cb+S9O+48+lane*32,sw[2]); ldsw128(cb+S9O+64+lane*32,sw[3]);
  if(lane<3){ ldsw128(cb+S7O+lane*16,u4{0,0,0,0}); ldsw128(cb+S9O+lane*16,u4{0,0,0,0}); }
  const u32 ka=cb+(r&3)*KP+16*q-8*(r>>2)+64, ub=cb+UOF+q*1040+(r+1)*16;
  u4 a[2][4][2],bb[2][4]; const u32 kg=ka+64, ug=ub+272;
  auto rdn=[&](int g){
    #pragma unroll
    for(int i=0;i<4;i++){ a[g&1][i][0]=lds2x64(kg+(3-g)*256+i*64+32); a[g&1][i][1]=lds2x64(kg+(3-g)*256+i*64); bb[g&1][i]=ldsr128(ug+(3-g)*64+i*16); } };
  auto mmn=[&](int g){
    #pragma unroll
    for(int i=0;i<4;i++){ y[0][1]=mfma16h(a[g&1][i][0],bb[g&1][i],y[0][1]); y[1][1]=mfma16h(a[g&1][i][1],bb[g&1][i],y[1][1]); } };
  if(HSH==4){ rdn(0); rdn(1); SCHED(); mmn(0); SCHED(); rdn(2); SCHED(); mmn(1); SCHED(); rdn(3); SCHED(); mmn(2); SCHED(); }
  if(HSH==2){ rdn(2); rdn(3); SCHED(); mmn(2); SCHED(); }
  const u4 a0=lds2x64(ka+32), a1=lds2x64(ka), b0=ldsr128(ub), b1=ldsr128(ub+256), a2=lds2x64(ka-32), b2=ldsr128(ub-16), b3=ldsr128(ub+240);
  const u32 sa=cb+(((r>>2)&1)?S9O:S7O)+2*(64*q+24-8*(r>>2)+4*(r&3)), kz=cb+KZO+8*(r-2*q+23); u4 bz[4],fa[4][2];
  #pragma unroll
  for(int ks=0;ks<4;ks++) bz[ks]=lds2x64(kz+(3-ks)*64);
  SCHED(); if(HSH) mmn(3); SCHED();
  #pragma unroll
  for(int i=0;i<8;i++){ const u2 lo=ldstr(sa+(i>>1)*512+(i&1)*32+64), hi=ldstr(sa+(i>>1)*512+(i&1)*32); fa[i>>1][i&1]=u4{lo[0],lo[1],hi[0],hi[1]}; }
  SCHED();
  y[0][0]=mfma16h(a2,b2,y[0][0]); y[0][1]=mfma16h(a2,b3,y[0][1]);
  y[0][0]=mfma16h(a0,b0,y[0][0]); y[1][0]=mfma16h(a1,b0,y[1][0]); y[0][1]=mfma16h(a0,b1,y[0][1]); y[1][1]=mfma16h(a1,b1,y[1][1]);
  #pragma unroll
  for(int ks=0;ks<4;ks++)
    #pragma unroll
    for(int m=0;m<2;m++){ y[m][1]=mfma16h(fa[ks][m],bz[ks],y[m][1]); if(ks<2) y[m][0]=mfma16h(fa[ks][m],bz[ks+2],y[m][0]); }
  #pragma unroll
  for(int i=0;i<4;i++) *(LAS f4*)(cb+S7O+i*1024+lane*16)=y[i&1][i>>1];
}
#define RDA(S,i,m) asm volatile("ds_read2_b64 %0, %1 offset0:%2 offset1:%3" : "=v"(AF[S][i][m]) : "v"(ka), "n"((1-(i))*128+(1-(m))*4), "n"((1-(i))*128+(1-(m))*4+1))
#define RDB(S,i,ad,o) asm volatile("ds_read_b128 %0, %1 offset:%2" : "=v"(BF[S][i]) : "v"(ad), "n"(o))
#define TIEA(S,i) "+v"(AF[S][i][0]),"+v"(AF[S][i][1])
#define CV_M(S,i,n) do{ y[0][n]=mfma16h(AF[S][i][0],BF[S][(n)-(i)],y[0][n]); y[1][n]=mfma16h(AF[S][i][1],BF[S][(n)-(i)],y[1][n]); SCHED(); }while(0)
#define CV_U0() u32 u0=u1-256; u0=((int)u0<(int)uz)?uz:u0
template<class CB> DI void conv_wave(CB&& mid,rsrc_t rs,int d,u32 cb,u32 flag,u32 turn,u32 need,int lane,float ksc,float dsk){
  const int r=lane&15,q=lane>>4; u32 ka=cb+(r&3)*KP+16*q-8*(r>>2)+1088; const u32 uz=cb+UOF+q*1040; u32 u1=uz+(r+17)*16; u4 AF[2][2][2]; u4 BF[2][4]; f4 y[2][4]={};
  #define CV_WT(S) asm volatile("s_waitcnt lgkmcnt(0)" : TIEA(S,0),TIEA(S,1),"+v"(BF[S][0]),"+v"(BF[S][1]),"+v"(BF[S][2]),"+v"(BF[S][3]))
  #define CV_STEP(C,N) do{ CV_U0(); CV_M(C,0,0); RDA(N,0,0); RDB(N,0,u0,0); RDA(N,0,1); SCHED(); CV_M(C,0,1); RDB(N,1,u1,0); RDA(N,1,0); RDB(N,2,u1,256); SCHED(); CV_M(C,0,2); RDA(N,1,1); RDB(N,3,u1,512); \
      ka-=64; u1-=16; SCHED(); CV_M(C,0,3); CV_M(C,1,1); CV_M(C,1,2); if(dl<8-2*HSH) CV_M(C,1,3); CV_WT(N); }while(0)
  { CV_U0(); RDA(0,0,0); RDA(0,0,1); RDA(0,1,0); RDA(0,1,1); RDB(0,0,u0,0); RDB(0,1,u1,0); RDB(0,2,u1,256); RDB(0,3,u1,512); ka-=64; u1-=16; CV_WT(0); }
  #pragma unroll 1
  for(int dl=0;dl<8;dl++){ const u32 tv=ldsv(turn); CV_STEP(0,1); if(uni(tv)>=need) __builtin_amdgcn_s_setprio(3); if(dl==7){ ka+=64; u1+=16; } CV_STEP(1,0); }
  mid();
  asm volatile("s_nop 15" : "+v"(y[0][0]),"+v"(y[0][1]),"+v"(y[0][2]),"+v"(y[0][3]),"+v"(y[1][0]),"+v"(y[1][1]),"+v"(y[1][2]),"+v"(y[1][3]));
  __builtin_amdgcn_s_setprio(3);
  u2 uv[2][4];
  #pragma unroll
  for(int n=0;n<4;n++)
    #pragma unroll
    for(int m=0;m<2;m++) uv[m][n]=ldsr64(cb+UOF+(2*m+(q>>1))*1040+(16*n+r+1)*16+8*(q&1));
  const u32 sc=cb+UOF+r*64+q*8;
  auto fin=[&](int n){
    #pragma unroll
    for(int m=0;m<2;m++){ u2 o;
      #pragma unroll
      for(int e=0;e<2;e++) o[e]=pkbf(fmaf1(hlo(uv[m][n][e]),dsk,mulf(y[m][n][2*e],ksc)),fmaf1(hhi(uv[m][n][e]),dsk,mulf(y[m][n][2*e+1],ksc)));
      *(LAS u2*)(sc+n*1024+m*32)=o; } };
  ldswait(flag,2u);
  { f4 hp[4];
    #pragma unroll
    for(int i=0;i<4;i++) hp[i]=*(LAS f4*)(cb+S7O+i*1024+lane*16);
    fin(0); fin(1);
    #pragma unroll
    for(int i=0;i<4;i++)
      #pragma unroll
      for(int e=0;e<4;e++) y[i&1][2+(i>>1)][e]=addf(y[i&1][2+(i>>1)][e],hp[i][e]);
    fin(2); fin(3); }
  u4 ov[4];
  #pragma unroll
  for(int n=0;n<4;n++) ov[n]=ldsr128(cb+UOF+n*1024+lane*16);
  #pragma unroll
  for(int n=0;n<4;n++) st16(rs,OFF_AT+d*4096+n*1024+lane*16,ov[n]);
  WAITV(0);
}

#define NB 3
constexpr int g2ops(int h){ return h<0? 7 : (h+NB<16?7:0)+((h>=3&&h<=10)?4:0)+(h==HZZ-3?1:0)+(h==HZZ?8:0); }
constexpr int g2cnt(int h){ int c=0; for(int i=h-NB+1;i<h;i++) c+=g2ops(i); return c; }
struct Frag { u4 x[4], w[4][2]; };
__global__ __launch_bounds__(512,2) void fused(Params p){
  extern __shared__ __attribute__((aligned(16))) char sm[];
  const u32 smb=(u32)(size_t)(LAS char*)sm;
  u64 dg0; asm volatile("s_memrealtime %0\n\ts_waitcnt lgkmcnt(0)" : "=s"(dg0) :: "memory"); u64 dg1=0;
  u64 pc0; asm volatile("s_getpc_b64 %0" : "=s"(pc0));
  const int b=blockIdx.x, xc=b&7, idx=b>>3;
  const int tid=threadIdx.x, w=uni(tid>>6); int lane=tid&63;
  auto cpf=[&](u32 o){ u4 sc=mksrd((const void*)(pc0&~127ull)); sc[2]=CODE_BYTES; setm0<JNK>(smb); touch(sc,lane*128,o); };
  if(idx>=28) cpf(w*8192);
  SCHED();
  const int rt=4*(xc>>1)+(idx&3), j=7*(xc&1)+(idx>>2), mi=w&3, g1=w>>2;
  u32 vx0,vx1,vw1,vw2,k0=KROT*128,k0w=KROT*(64*3456); const u32 mw=smb+w*4096;
  { const u32 lr=lane>>3,c=lane&7; vx0=(rt*128+16*w+lr)*1280+((c^(lr>>1))<<4); vx1=(vx0^64u)+9216;
    vw1=(8*w+lr)*3456+((c^((lr&2)<<1))<<4)+j*128; vw2=vw1+1024+(j==13?0:14*128); }
  asm volatile("" : "+v"(vx0),"+v"(vx1),"+v"(vw1),"+v"(vw2),"+s"(k0),"+s"(k0w));
  setm0<0>(mw);
  SCHED();
  const u4 srdX=mksrd(p.x), srdW=mksrd((const char*)p.win-3072);
  if(idx<28){ dma<0>(srdX,vx0,k0); dma<1024>(srdX,vx1,k0); dma<2048>(srdW,vw2,k0w); dma<3072>(srdW,vw1,k0w); }
  SCHED();
  const rsrc_t rs=__builtin_amdgcn_make_buffer_rsrc(p.ws,0,WS_END,0x00020000);
  const u4 srdS=mksrd(p.ws);
  const int rtB=8*(xc>>1)+(idx&7), ct=4*(xc&1)+(idx>>3);
  const bool hask=idx<28 && idx+28*(w&3)<80, hasc=hask && w>=4;
  const int dch=80*xc+(hask? idx+28*(w&3) : 0);
  const u32 cb=smb+CONV+(w&3)*CVS;
  u32 tag; float ksc=0.f, dsk=0.f;
  u32 zp[2][8]={};
  auto z1st=[&](){ const int r=lane&31,h=lane>>5;
    #pragma unroll
    for(int n=0;n<2;n++) if(2*j+n<27){ const u32 oo=OFF_Z1T+((rt*8+2*mi+(r>>4))*27+2*j+n)*1024+(h*16+(r&15))*16;
      st16(rs,oo,u4{zp[n][0],zp[n][1],zp[n][4],zp[n][5]}); st16(rs,oo+512,u4{zp[n][2],zp[n][3],zp[n][6],zp[n][7]}); } };
  auto f1=[&](){ WAITV(0); if(arrive(smb+C_Z1,lane)==3u && lane==0) st4(rs,OFF_F1+(rt*16+j)*128,tag); };
  auto pre01=[&](){
    const u4 sa=mksrd((const char*)p.wsp+(size_t)rtB*64*4096+mi*1024-4096); const u32 v0=(lane>>3)*4096+(((lane&7)^(lane>>4))<<4);
    #pragma unroll 1
    for(int i=0;i<4;i++){ setm0<RB+8192>(smb+mi*RING+i*4096); const u32 g=(i&1)*4+2*mi, k=(i>>1)*128+4096;
      dma<0>(sa,v0,(g&7)*32768+k); dma<1024>(sa,v0^64u,(g&7)*32768+32768-1024+k); dma<2048>(sa,v0,((g+2)&7)*32768-2048+k); dma<3072>(sa,v0^64u,((g+2)&7)*32768+32768-3072+k); } };
  if(idx>=28){
    const int a=xc*4+idx-28, r=lane&15, q=lane>>4;
    const u32 ep=ld4(rs,OFF_EP);
    u4 xr[10]; const char* xs=(const char*)p.x+(size_t)(64*a+8*w+(lane>>3))*1280+(lane&7)*16;
    #pragma unroll
    for(int i=0;i<10;i++) xr[i]=*(const u4*)(xs+i*128);
    if(tid<32) *(LAS u32*)(smb+PM+tid*4)=0u;
    tag=uni(ep)+TAGINC;
    float s0=0.f,s1=0.f,q0=0.f,q1=0.f;
    #pragma unroll
    for(int i=0;i<10;i++){ s0=dot2(xr[i][0],0x3f803f80u,s0); q0=dot2(xr[i][0],xr[i][0],q0); s1=dot2(xr[i][1],0x3f803f80u,s1); q1=dot2(xr[i][1],xr[i][1],q1);
      s0=dot2(xr[i][2],0x3f803f80u,s0); q0=dot2(xr[i][2],xr[i][2],q0); s1=dot2(xr[i][3],0x3f803f80u,s1); q1=dot2(xr[i][3],xr[i][3],q1); }
    s0+=s1; q0+=q1;
    #define DPPADD(v,c) v+=__int_as_float(__builtin_amdgcn_update_dpp(0,__float_as_int(v),c,0xf,0xf,false))
    DPPADD(s0,0xB1); DPPADD(q0,0xB1); DPPADD(s0,0x4E); DPPADD(q0,0x4E); DPPADD(s0,0x141); DPPADD(q0,0x141);
    float m1,ri; { float mean=s0*(1.f/640.f); ln_fin(mean,q0*(1.f/640.f)-mean*mean,m1,ri); }
    if((lane&7)==0) st16(rs,OFF_RS+(64*a+8*w+(lane>>3))*16,u4{__float_as_uint(m1),tag,__float_as_uint(ri),tag});
    STAMP(5,0);
    if(a<27){ const u4 srdO=mksrd((const char*)p.wout+a*(32*1280)+w*5120); const u32 mo=smb+w*5120; setm0<0>(mo);
      dma<0>(srdO,lane*16,0); dma<1024>(srdO,lane*16,0); dma<2048>(srdO,lane*16,0); dma<3072>(srdO,lane*16,0); setm0<4096>(mo); dma<0>(srdO,lane*16,4096); }
    #pragma unroll
    for(int i=0;i<10;i++){ u4 o;
      #pragma unroll
      for(int e=0;e<4;e++) o[e]=pkhf2(fma2(unpk(xr[i][e]),f2{ri,ri},f2{-m1*ri,-m1*ri}));
      ldsw128(smb+RB+(8*w+(lane>>3))*1296+(lane&7)*16+i*128,o); }
    WAITV(0); bar();
    #pragma unroll
    for(int i=0;i<10;i++){ const int c=w*10+i; const u32 ad=smb+RB+(32*((r>>1)&1)+8*q+(r>>2))*1296+c*16+(r&1)*8; u2 lo=ldstr(ad), hi=ldstr(ad+4*1296);
      const u32 sc=smb+40960+w*2048+(i&1)*1024; ldsw128(sc+(r&7)*128+((((r>>3)*4+q)^(r&7))<<4),u4{lo[0],lo[1],hi[0],hi[1]});
      st16(rs,OFF_XNT+a*81920+c*1024+lane*16,ldsr128(sc+(lane>>3)*128+(((lane&7)^(lane>>3))<<4))); }
    WAITV(0);
    if(arrive(smb+C_Z1,lane)==7u && lane==0) st4(rs,OFF_FX+a*128,tag);
    STAMP(6,7);
    if(a<27){
      #pragma unroll
      for(int i=0;i<5;i++){ u32 ad=smb+(8*q+(r>>2))*1280+(w*5+i)*32+(r&3)*8; u2 lo=ldstr(ad), hi=ldstr(ad+4*1280);
        st16(rs,OFF_WOF+(u32)((w*27+a)*5+i)*1024+lane*16,u4{lo[0],lo[1],hi[0],hi[1]}); } }
    WAITV(0); bar();
    if(tid==0) st4(rs,OFF_FW+a*128,tag);
    if(!g1){ pre01(); WAITV(0); }
    STAMP(31,0);
  } else {
    const int r=lane&31, h=lane>>5;
    u32 epv;
    u32 kx[10]; { u32 k=KROT;
      #pragma unroll
      for(int i=0;i<10;i++){ kx[i]=k*128; k=k==9?0:k+1; } }
    auto issue1=[&](auto sc,int k){ constexpr int s=sc.value;
      if(k==0){ setm0<(s&3)*STG>(mw); dma<0>(srdX,vx0,kx[s]); } if(k==1) dma<1024>(srdX,vx1,kx[s]); if(k==2) dma<2048>(srdW,vw2,kx[s]*1728); if(k==3) dma<3072>(srdW,vw1,kx[s]*1728); };
    auto issue=[&](auto sc){ issue1(sc,0); issue1(sc,1); issue1(sc,2); issue1(sc,3); };
    u32 pr[6];
    gload4<0>(epv,0u,p.ws+OFF_EP);
    gload4<0>(pr[0],lane*4,(const char*)(p.lar+dch*128)); gload4<0>(pr[1],lane*4,(const char*)(p.aim+dch*128)); gload4<0>(pr[2],lane*4,(const char*)(p.cre+dch*128)); gload4<0>(pr[3],lane*4,(const char*)(p.cim+dch*128));
    gload2(pr[4],0u,(const char*)(p.ldt+dch)); gload2(pr[5],0u,(const char*)(p.dsk+dch));
    asm volatile("s_barrier" ::: "memory");
    issue(std::integral_constant<int,1>{});
    if(tid<32) *(LAS u32*)(smb+PM+tid*4)=0u;
    u32 ax[2][4],aw[2][4][2];
    { u32 row=32*mi+r, a0=smb+(row>>4)*4096+(row&15)*128+((h^((r>>1)&7))<<4);
      u32 qq=(lane&15)>>2, w0=smb+2048+g1*1024+h*4096+qq*128+8*(lane&1)+(((2*((lane>>4)&1)+((lane>>1)&1))^((qq&2)<<1))<<4);
      #pragma unroll
      for(int i=0;i<2;i++)
        #pragma unroll
        for(int k=0;k<4;k++){ const u32 kk=(k+mi)&3, wo=i*2*STG; ax[i][k]=opq((a0^(kk*32u))+wo); aw[i][k][0]=opq(w0+kk*8192+wo); aw[i][k][1]=opq((w0^64u)+kk*8192+wo); } }
    u4 sel; { const u32 o=((lane&15)==((lane>>4)&1))?0x3f803f80u:0u; sel=u4{o,o,o,o}; asm volatile("" : "+v"(sel)); }
    u32 ga[11];
    #pragma unroll
    for(int i=0;i<11;i++) ga[i]=opq(cb+2u*(u32)rbf((float)(512-8*lane+3-i)));
    Frag F[2];
    asm volatile("s_waitcnt vmcnt(4)" : "+v"(epv),"+v"(pr[0]),"+v"(pr[1]),"+v"(pr[2]),"+v"(pr[3]),"+v"(pr[4]),"+v"(pr[5]) :: "memory");
    bar();
    tag=uni(epv)+TAGINC; dsk=bflo(pr[5]);
    #pragma unroll
    for(int k=0;k<4;k++){
      #pragma unroll
      for(int n=0;n<2;n++){ u2 lo=ldstr(aw[0][k][n]), hi=ldstr(aw[0][k][n]+512); F[0].w[k][n]=u4{lo[0],lo[1],hi[0],hi[1]}; }
      F[0].x[k]=ldsr128(ax[0][k]); }
    issue(std::integral_constant<int,2>{});
    STAMP(1,0);
    f16v acc[2]={}; f4 cacc[2]={}; u4 G;
    const u32 go=OFF_RS+rt*2048+mi*512, cp=smb+MISC+g1*1024;
    sfor<10>([&](auto ic){
      constexpr int s=ic.value; Frag& cur=F[s&1]; Frag& nxt=F[(s&1)^1];
      if constexpr(s<9){ WAITV(s==8? 3 : s==7? 7 : 4); asm volatile("s_barrier" ::: "memory"); }
      auto rw=[&](int k,int n){ if constexpr(s<9){ const int i=((s+1)>>1)&1; const u32 so=((s+1)&1)*STG; u2 lo=ldstr(aw[i][k][n]+so), hi=ldstr(aw[i][k][n]+so+512); nxt.w[k][n]=u4{lo[0],lo[1],hi[0],hi[1]}; } };
      auto rx=[&](int k){ if constexpr(s<9){ const int i=((s+1)>>1)&1; const u32 so=((s+1)&1)*STG; nxt.x[k]=ldsr128(ax[i][k]+so); } };
      #pragma unroll
      for(int k=0;k<4;k++){
        acc[0]=mfma32b(cur.w[k][0],cur.x[k],acc[0]); SCHED();
        if constexpr(s+3<=9) issue1(std::integral_constant<int,s+3>{},k);
        rw(k,0); rx(k);
        SCHED();
        acc[1]=mfma32b(cur.w[k][1],cur.x[k],acc[1]); SCHED();
        rw(k,1);
        if(k==0){ cacc[0]=mfma16(cur.w[0][0],sel,cacc[0]); cacc[1]=mfma16(cur.w[0][1],sel,cacc[1]); }
        if constexpr(s==6){ if(k==3){ gload<0>(G,r*16,p.ws+go); setm0<JNK>(smb);
            if(g1){ const u4 sp=mksrd((const char*)p.wsp+(size_t)(xc>>1)*512*4096); const u32 lb=(((xc&1)*28+idx)*4+mi)*74, l0=lb+lane, l1=lb+64+(lane<10?lane:9);
              touch(sp,(l0<16384?l0:16383)*128,0u); touch(sp,(l1<16384?l1:16383)*128,0u); }
            else { touch(srdS,0u,go); touch(srdS,0u,go); } } }
        if constexpr(s==8){ if(k==3) signal(smb+C_DONE,lane); }
        if constexpr(s==9){ if(k==1){
          if((lane&14)==0){ *(LAS f4*)(cp+mi*256+(lane&1)*64+(lane>>4)*16)=cacc[0]; *(LAS f4*)(cp+mi*256+128+(lane&1)*64+(lane>>4)*16)=cacc[1]; }
          signal(smb+C_CS+g1*4,lane); } }
        SCHED();
      }
    });
    RST(0);
    if(!g1) __builtin_amdgcn_s_setprio(2);
    asm volatile("s_waitcnt vmcnt(2)" : "+v"(G) :: "memory");
    ldswait(smb+C_CS+g1*4,4u);
    const float cs0=*(LAS float*)(cp+h*512+r*4)+*(LAS float*)(cp+h*512+256+r*4), cs1=*(LAS float*)(cp+h*512+128+r*4)+*(LAS float*)(cp+h*512+384+r*4);
    RST(2);
    #pragma unroll 1
    for(int sp=0;sp<SPIN && !__all(G[1]==tag&&G[3]==tag);sp++)asm volatile("global_load_dwordx4 %0, %1, %2 sc1\n\ts_waitcnt vmcnt(0)" : "=&v"(G) : "v"(r*16), "s"(p.ws+go) : "memory");
    RST(3);
    const float nm=-__uint_as_float(G[0]), ri1=__uint_as_float(G[2]);
    acc[0]=__builtin_amdgcn_mfma_f32_32x32x2f32(cs0,nm,acc[0],0,0,0); acc[1]=__builtin_amdgcn_mfma_f32_32x32x2f32(cs1,nm,acc[1],0,0,0);
    #pragma unroll
    for(int e=0;e<16;e++) zp[e>>3][e&7]=pkbf2(gelu2(f2{acc[e>>3][2*(e&7)],acc[e>>3][2*(e&7)+1]}*ri1));
    if(g1){ ldswait(smb+C_DONE,8u); RST(4); }
    else {
      float S=0.f,Q=0.f;
      if(j<13){
        #pragma unroll
        for(int e=0;e<8;e++){ S=dot2(zp[0][e],0x3f803f80u,S); Q=dot2(zp[0][e],zp[0][e],Q); } }
      #pragma unroll
      for(int e=0;e<8;e++){ S=dot2(zp[1][e],0x3f803f80u,S); Q=dot2(zp[1][e],zp[1][e],Q); }
      #define XSUM32(v) do{ auto t_=__builtin_amdgcn_permlane32_swap(__float_as_uint(v),__float_as_uint(v),false,false); v=__uint_as_float(t_[0])+__uint_as_float(t_[1]); }while(0)
      XSUM32(S); XSUM32(Q);
      const u32 sto=OFF_ST+rt*(14*2048)+(32*mi+r)*16;
      if(h==0) st16(rs,sto+j*2048,u4{__float_as_uint(S),tag,__float_as_uint(Q),tag});
      RST(6);
    }
    float kp[11]; u4 g[7]={};
    const u32 gof=OFF_ST+rt*(14*2048)+(32*mi+r)*16+h*(7*2048);
    auto samp=[&](){ asm volatile("" ::: "memory");
      #pragma unroll
      for(int i=0;i<7;i++) g[i]=ld16(rs,gof+i*2048);
      asm volatile("" ::: "memory"); };
    if(hasc) ksc=kgen_tab(cb,lane,pr,kp,[](){}); else if(!g1){ pre01(); samp(); }
    if(g1){
      if(hasc){
        RST(6);
        kgen_exp(cb,lane,kp,ga);
        signal(smb+C_KP+mi*4,lane);
        RST(8);
        RST(9);
      }
      if(hasc) ldswait(smb+C_FULL,1u); else if(w<7){ z1st(); f1(); }
      if(ZPRE && hasc) z1st();
    } else {
      RST(1);
      float S,Q;
      #pragma unroll 1
      for(int sp=0;sp<SPIN;sp++){ bool ok=true;
        #pragma unroll
        for(int i=0;i<7;i++) ok=ok&&g[i][1]==tag&&g[i][3]==tag;
        if(__all(ok)) break;
        __builtin_amdgcn_s_sleep(4); samp(); }
      RST(7);
      S=0.f; Q=0.f;
      #pragma unroll
      for(int i=0;i<7;i++){ S+=__uint_as_float(g[i][0]); Q+=__uint_as_float(g[i][2]); }
      XSUM32(S); XSUM32(Q);
      float mean=S*(1.f/864.f), m2,r2; ln_fin(mean,Q*(1.f/864.f)-mean*mean,m2,r2); m2*=-r2;
      ldswait(smb+C_DONE,8u);
      const u32 tb=smb+RB+mi*RING, ad=tb+(8*(lane>>4)+((lane&15)>>2))*80+(lane&3)*8;
      const u32 zo=OFF_Z2F+((rt*4+mi)*54+(j==13?-2:2+4*j))*1024+lane*16;
      #pragma unroll
      for(int n=0;n<2;n++){
        #pragma unroll
        for(int g2=0;g2<4;g2++){ u2 o;
          #pragma unroll
          for(int e=0;e<2;e++) o[e]=pkbf2(fma2(unpk(zp[n][2*g2+e]),f2{r2,r2},f2{m2,m2}));
          *(LAS u2*)(tb+r*80+g2*16+h*8)=o; }
        u2 l0=ldstr(ad), h0=ldstr(ad+320), l1=ldstr(ad+32), h1=ldstr(ad+32+320);
        if(j<13||n){ st16(rs,zo+n*2048,u4{l0[0],l0[1],h0[0],h0[1]}); st16(rs,zo+n*2048+1024,u4{l1[0],l1[1],h1[0],h1[1]}); } }
      RST(8);
      WAITV(0);
      if(arrive(smb+C_Z2,lane)==3u && lane==0) st4(rs,OFF_F2+(rt*16+j)*128,tag);
      RST(9);
    }
  }
  const int ft0=ct<7?7*ct:47;
  bool adone=true; const u32 fao=OFF_FA+((lane<28?lane:0)*8+ct)*128;
  auto aload=[&](){ const u32 vo=(lane>>3)*4096+(lane&7)*16;
    #pragma unroll 1
    for(int i=0;i<10;i++){ setm0<ATL>(smb+i*1024); dma<0>(srdS,vo,OFF_AT+(u32)(80*ct+8*i)*4096+rtB*128); } };
  if(w>=4){
    if(hasc){
      conv_wave([&](){ if(ZPRE) f1(); else z1st(); },rs,dch,cb,smb+C_KP+mi*4,smb+C_ZQ,STAG,lane,ksc,dsk);
      __builtin_amdgcn_s_setprio(0);
      if(!ZPRE) f1();
      if(arrive(smb+C_CV,lane)==(idx<24?2u:1u) && lane==0) st4(rs,OFF_FA+b*128,tag);
    }
    if(w==7){
      if(idx<28){
        __builtin_amdgcn_s_sleep(FXDLY);
        pollw<16>(rs,OFF_FX+(lane&31)*128,tag);
        u4 sw[3][4];
        #pragma unroll
        for(int c=0;c<3;c++)
          #pragma unroll
          for(int i=0;i<4;i++) sw[c][i]=ld16(rs,OFF_XNT+((lane>>3)+8*i)*81920+(80*xc+idx+((c<2||idx<24)?28*c:0))*128+(lane&7)*16);
        #pragma unroll
        for(int c=0;c<3;c++){
          #pragma unroll
          for(int i=0;i<4;i++){ const int x=lane+64*i; ldsw128(smb+c*CVS+UOF+(x&3)*1040+((x>>2)+1)*16,sw[c][i]); }
          if(lane<4) ldsw128(smb+c*CVS+UOF+lane*1040,u4{0,0,0,0}); }
        signal(smb+C_FULL,lane);
        RST(4);
        #pragma unroll 1
        for(int c=0;c<3;c++){
          #pragma unroll
          for(int i=0;i<4;i++){ sw[0][i]=sw[1][i]; sw[1][i]=sw[2][i]; }
          conv_win(smb+c*CVS,lane,sw[2]); }
        RST(5);
        __builtin_amdgcn_s_setprio(HPRIO);
        #pragma unroll 1
        for(int c=0;c<(idx<24?3:2);c++){ ldswait(smb+C_KP+c*4,1u); conv_help(smb+c*CVS,lane,sw[0]); signal(smb+C_KP+c*4,lane);
          #pragma unroll
          for(int i=0;i<4;i++){ sw[0][i]=sw[1][i]; sw[1][i]=sw[2][i]; } } }
      __builtin_amdgcn_s_setprio(0);
      if(idx<28){ z1st(); f1(); }
      pollw<16>(rs,OFF_FW+(lane<27?lane:0)*128,tag); signal(smb+C_Z1R,lane);
      RST(6);
    }
    RST(10);
    ldswait(smb+C_Z1R,5u);
    u4 wq[24];
    #pragma unroll
    for(int i=0;i<24;i++) wq[i]=ld16(rs,OFF_WOF+ct*(135*1024)+40960+mi*RING+((i==23&&mi==3)?22:i)*1024+lane*16);
    if(w<6){
      #pragma unroll 1
      for(u32 i=0;i<20480;i+=4096){ setm0<0>(cb+i); const u32 o=OFF_WOF+ct*(135*1024)+mi*20480+i; dma<0>(srdS,lane*16,o); dma<1024>(srdS,lane*16,o); dma<2048>(srdS,lane*16,o); dma<3072>(srdS,lane*16,o); } }
    if(w==6){ adone=__all(ld4(rs,fao)==tag); if(adone) aload(); }
    ldswait(smb+C_FREE,4u);
    #pragma unroll
    for(int i=0;i<24;i++) ldsw128(smb+RB+mi*RING+i*1024+lane*16,wq[i]);
    if(w<6) WAITV(0);
    signal(smb+C_WR,lane);
    if(!adone){ adone=__all(ld4(rs,fao)==tag); if(adone) aload(); }
    if(w==7 && b==255){
      #pragma unroll 1
      for(int sp=0;sp<SPIN;sp++){ const u32 o=OFF_FA+lane*128; u32 v0=ld4(rs,o),v1=ld4(rs,o+8192),v2=ld4(rs,o+16384),v3=ld4(rs,lane<32? o+24576 : OFF_FW+(lane-32)*128); asm volatile("" ::: "memory");
        if(__all(v0==tag&&v1==tag&&v2==tag&&v3==tag)) break; __builtin_amdgcn_s_sleep(4); }
      if(lane==0) st4(rs,OFF_EP,tag); }
  }
  int w2=w; asm volatile("" : "+s"(w2));
  if(w2<4){
    __builtin_amdgcn_s_setprio(G2PRIO);
    int r=lane&15, q=lane>>4;
    const u32 rb=smb+RB+w*RING;
    u4 srdA=mksrd((const char*)p.wsp+(size_t)rtB*64*4096+w*1024-4096+256);
    const char* zb=p.ws+OFF_Z2F+(u32)(16*w*54+ft0+3)*1024;
    u32 voA[2]; { u32 lr=lane>>3,c=lane&7; voA[0]=lr*4096+((c^(lr>>1))<<4); voA[1]=voA[0]^64u; }
    u32 sofa[8];
    #pragma unroll
    for(int g=0;g<8;g++) sofa[g]=((g+2*w)&7)*32768+4096-(g&3)*1024;
    const u32 voB=lane*16;
    u32 la[2]; la[0]=rb+r*128+((q^(r>>1))<<4); la[1]=la[0]^64u;
    f4 acc[4][7]; u4 B[NB][7]; u4 A[2][4]; u2 z1[7];
    auto issueA=[&](auto sc,auto gc){ constexpr int s=sc.value,g=gc.value; if constexpr((g&3)==0) setm0<((s+1)%3)*8192+g*1024>(rb); dma<(g&3)*1024>(srdA,voA[g&1],sofa[g]);
      if constexpr(g==7){ u64 a=((u64)srdA[1]<<32|srdA[0])+128; srdA[0]=(u32)a; srdA[1]=(u32)(a>>32); } };

    u32 bsr,fv;
    auto fof=[&](){ const u32 l=voB>>4; return OFF_F1+((rtB>>1)*16+(((ft0>>1)+(l&3))>>1))*128; };
    #pragma unroll
    for(int m=0;m<4;m++){ A[0][m]=ldsr128(la[0]+m*2048+8192); A[1][m]=ldsr128(la[1]+m*2048+8192); }
    sfor<8>([&](auto g){ issueA(std::integral_constant<int,2>{},g); });
    waitA(A[0]); waitA(A[1]);
    sfor<8>([&](auto g){ issueA(std::integral_constant<int,3>{},g); });
    RST(2);
    { const int hf=(ft0>>1)+q; pollw(rs,OFF_F2+((4*w+(lane&3))*16+(hf==0?13:(hf-1)>>1))*128,tag); }
    RST(10);
    sfor<NB>([&](auto hh){ const char* s=zb+(u32)hh.value*(54*1024); sfor<7>([&](auto n){ gload<(n.value-3)*1024>(B[hh][n],voB,s); }); });
    auto redw=[&](auto nc){ constexpr int n=nc.value; *(LAS f4*)(rb+n*1024+voB)=acc[1][n]; *(LAS f4*)(rb+(7+n)*1024+voB)=acc[2][n]; *(LAS f4*)(rb+(14+n)*1024+voB)=acc[3][n]; };
    const char* z1b=p.ws+OFF_Z1T+(u32)((rtB*4+w)*27+(ft0>>1))*1024;
    sfor<16>([&](auto hc){
      constexpr int h=hc.value, S=h%NB, P=h&1, HZ=HZZ, cnt=g2cnt(h);
      waitB<cnt>(B[S]); waitA(A[P]);
      if constexpr(h==HZ){ tie1(fv); if(!__all(fv==tag)) pollw(rs,fof(),tag); }
      if constexpr(h+1<16 && h>0){
        #pragma unroll
        for(int m=0;m<4;m++) A[P^1][m]=ldsr128(la[(h+1)&1]+m*2048+((((h+1)>>1)+1)%3)*8192);
      }
      if constexpr(h==HZ-3) gload4<0>(fv,fof(),p.ws);
      const char* s=zb+(u32)(h+NB)*(54*1024);
      sfor<7>([&](auto n){
        if constexpr(h==0) mfma4z(acc[0][n],acc[1][n],acc[2][n],acc[3][n],B[S][n],A[P]); else mfma4(acc[0][n],acc[1][n],acc[2][n],acc[3][n],B[S][n],A[P]);
        if constexpr(h>=3 && h<=10 && (n.value&1)==0) issueA(std::integral_constant<int,(h-1)/2+3>{},std::integral_constant<int,((h-1)&1)*4+n.value/2>{});
        if constexpr(h+NB<16) gload_t<(n.value-3)*1024>(B[S][n],voB,s);
        if constexpr(h==HZ){ gload8<(n.value>>1)*1024>(z1[n],voB,z1b+((n.value&1)? ((ft0&1)?1024:8) : (ft0&1)*8));
          if constexpr(n.value==6) { gload2(bsr,(voB>>3)&30u,(const char*)(p.bsp+(rtB*4+w)*16)); } }
        if constexpr(h==13 && n.value==0) signal(smb+C_Z1R,lane);
        if constexpr(h==15){ SCHED(); if constexpr(n.value>=2) redw(std::integral_constant<int,n.value-2>{}); SCHED(); }
      });
    });
    asm volatile("s_nop 15\n\ts_nop 3" ::: "memory");
    SCHED();
    STAMP(10,0);
    lane=__builtin_amdgcn_mbcnt_hi(~0u,__builtin_amdgcn_mbcnt_lo(~0u,0u)); asm volatile("" : "+v"(lane)); r=lane&15; q=lane>>4;
    redw(std::integral_constant<int,5>{}); redw(std::integral_constant<int,6>{});
    signal(smb+C_RED,lane);
    ldswait(smb+C_RED,4u);
    #pragma unroll
    for(int m=1;m<4;m++){ u32 src=smb+RB+((w-m)&3)*RING+(m-1)*7168+lane*16;
      #pragma unroll
      for(int n=0;n<7;n++) acc[0][n]+=*(LAS f4*)(src+n*1024); }
    asm volatile("s_waitcnt vmcnt(0) lgkmcnt(0)" : "+v"(z1[0]),"+v"(z1[1]),"+v"(z1[2]),"+v"(z1[3]),"+v"(z1[4]),"+v"(z1[5]),"+v"(z1[6]),"+v"(bsr) :: "memory");
    const float bs=bflo(bsr);
    signal(smb+C_FREE,lane);
    u2 o[7];
    #pragma unroll
    for(int n=0;n<7;n++)
      #pragma unroll
      for(int e=0;e<2;e++)o[n][e]=pkbf2(unpk(z1[n][e])*(f2{acc[0][n][2*e],acc[0][n][2*e+1]}+bs));
    const u32 ho=OFF_HF+(u32)((rtB*4+w)*54+ft0)*512+(q>>1)*256+r*16;
    #pragma unroll
    for(int n=0;n<6;n+=2){ auto sx=__builtin_amdgcn_permlane16_swap(o[n][0],o[n+1][0],false,false); auto sy=__builtin_amdgcn_permlane16_swap(o[n][1],o[n+1][1],false,false);
      if(ct<7||n>=2) st16(rs,ho+(n+(q&1))*512,u4{sx[0],sy[0],sx[1],sy[1]}); }
    st8(rs,ho+6*512+(q&1)*8,o[6]);
    STAMP(11,0);
    WAITV(0);
    if(arrive(smb+C_H,lane)==3u && lane==0) st4(rs,OFF_FH+(rtB*8+ct)*128,tag);
    STAMP(12,0);
    __builtin_amdgcn_s_setprio(0);
  }
  { const int r=lane&15, q=lane>>4, m=w&3, kh=w>>2;
    u2 xv[5]={};
    if(kh){
      #pragma unroll
      for(int n=0;n<5;n++) xv[n]=*(const u2*)(p.x+(rtB*64+m*16+r)*640+80*ct+16*n+4*q);
    }
    f4 acc[5]={}; u4 W[3][5];
    const u32 wl2=smb+lane*16+RB-40960+kh*14*5120, wl0=kh? wl2 : smb+lane*16, wl1=kh? wl2 : smb+lane*16+CVS-20480;
    #define WAD(k,n) (((k)<4? wl0 : (k)<8? wl1 : wl2)+((k)*5+(n))*1024)
    f4 ax[5]={};
    auto addax=[&](){
      #pragma unroll
      for(int n=0;n<5;n++){ u2 av=ldstr(smb+ATL+(16*n+4*q+(r>>2))*128+(16*m+4*(r&3))*2); ax[n]=f4{bflo(av[0]),bfhi(av[0]),bflo(av[1]),bfhi(av[1])}+f4{bflo(xv[n][0]),bfhi(xv[n][0]),bflo(xv[n][1]),bfhi(xv[n][1])}; } };
    if(w==6 && adone){ WAITV(0); signal(smb+C_AT,lane); }
    u32 oo[3],to[3]; const u32 tb=smb+CVS+m*3072;
    #pragma unroll
    for(int i=0;i<3;i++){ const int it=lane+64*i, rr=(it*205)>>11, cc=it-rr*10; oo[i]=opq((rtB*64+m*16+rr)*1280+ct*160+cc*16); to[i]=opq(tb+rr*176+cc*16); }
    if(m==0){ ldswait(smb+C_H,4u); pollw(rs,OFF_FH+(rtB*8+kh*4+(lane&3))*128,tag); signal(smb+C_TAG+kh*4,lane); } else ldswait(smb+C_TAG+kh*4,1u);
    STAMP(16,0);
    u4 hf[14];
    const u32 ho=OFF_HF+(u32)((rtB*4+m)*27+kh*14)*1024+lane*16;
    #pragma unroll
    for(int k=0;k<HPF;k++) hf[k]=ld16(rs,ho+k*1024);
    const bool early=kh && uni(ldsv(smb+C_AT))!=0u;
    if(early) addax();
    ldswait(smb+C_WR,4u);
    STAMP(17,0);
    const u32 wl3=kh? wl2-5120 : wl2;
    #pragma unroll
    for(int n=0;n<10;n++) W[n/5][n%5]=ldsr128(WAD(n/5,n%5));
    #pragma unroll
    for(int k=0;k<13;k++){
      if(k+HPF<14) hf[k+HPF]=ld16(rs,ho+((k+HPF==13&&kh)?12:k+HPF)*1024);
      SCHED();
      #pragma unroll
      for(int n=0;n<5;n++){ acc[n]=mfma16(W[k%3][n],hf[k],acc[n]); SCHED();
        if(k<11) W[(k+2)%3][n]=ldsr128(WAD(k+2,n)); else if(k==11) W[1][n]=ldsr128(wl3+(65+n)*1024);
        SCHED(); }
      if(k==5 && !kh) signal(smb+C_G1,lane);
    }
    const u32 pb=smb+m*5120+lane*16;
    if(!kh){
      #pragma unroll
      for(int n=0;n<5;n++) acc[n]=mfma16(W[1][n],hf[13],acc[n]);
      STAMP(18,0);
      ldswait(smb+C_G1,4u);
      #pragma unroll
      for(int n=0;n<5;n++) *(LAS f4*)(pb+n*1024)=acc[n];
      signal(smb+C_P+m*4,lane);
    } else {
      if(w==6 && !adone){ pollw(rs,fao,tag); aload(); WAITV(0); signal(smb+C_AT,lane); }
      if(!early){ ldswait(smb+C_AT,1u); addax(); }
      RST(7);
      ldswait(smb+C_P+m*4,1u);
      #pragma unroll
      for(int n=0;n<5;n++){ const f4 o=(acc[n]+ax[n])+*(LAS f4*)(pb+n*1024); *(LAS u2*)(tb+r*176+n*32+q*8)=u2{pkbf(o[0],o[1]),pkbf(o[2],o[3])}; }
      #pragma unroll
      for(int i=0;i<3;i++) if(i<2||lane<32) __builtin_amdgcn_raw_buffer_store_b128(ldsr128(to[i]),__builtin_amdgcn_make_buffer_rsrc(p.out,0,2048*1280,0x00020000),oo[i],0,16);
    }
    bar(); STAMP(19,0);
    if(w==0 && dg1){ const u64 e=__builtin_amdgcn_s_memrealtime()+2*(dg1-dg0); while(__builtin_amdgcn_s_memrealtime()<e) __builtin_amdgcn_s_sleep(1); }
  }
}
extern "C" void kernel_launch(void* const* d_in,const int* in_sizes,int n_in,void* d_out,int out_size,void* d_ws,size_t ws_size,hipStream_t stream){
  Params p;
  p.x=(const u16*)d_in[0]; p.lar=(const u16*)d_in[1]; p.aim=(const u16*)d_in[2]; p.cre=(const u16*)d_in[3]; p.cim=(const u16*)d_in[4];
  p.ldt=(const u16*)d_in[5]; p.dsk=(const u16*)d_in[6]; p.win=(const u16*)d_in[8]; p.wsp=(const u16*)d_in[9]; p.bsp=(const u16*)d_in[10]; p.wout=(const u16*)d_in[11];
  p.out=(u16*)d_out; p.ws=(char*)d_ws;
  (void)hipFuncSetAttribute((const void*)fused,hipFuncAttributeMaxDynamicSharedMemorySize,LDS_TOTAL);
  fused<<<256,512,LDS_TOTAL,stream>>>(p);
}
```

```cpp
#include <hip/hip_runtime.h>
#include <utility>
#pragma clang diagnostic ignored "-Wint-to-pointer-cast"
typedef unsigned short u16;
typedef unsigned int u32;
typedef unsigned long long u64;
typedef __attribute__((ext_vector_type(8))) __bf16 bfx8;
typedef __attribute__((ext_vector_type(8))) _Float16 hfx8;
typedef __attribute__((ext_vector_type(2))) __bf16 bf2;
typedef __attribute__((ext_vector_type(2))) _Float16 hf2;
typedef __attribute__((ext_vector_type(2))) float f2;
typedef __attribute__((ext_vector_type(4))) float f4;
typedef __attribute__((ext_vector_type(16))) float f16v;
typedef __attribute__((ext_vector_type(4))) u32 u4;
typedef __attribute__((ext_vector_type(2))) u32 u2;
typedef __attribute__((ext_vector_type(4))) short s4;
#define LAS __attribute__((address_space(3)))
#define DI __device__ __forceinline__

constexpr u32 OFF_Z2F = 0;
constexpr u32 OFF_Z1T = OFF_Z2F + 64*54*1024;
constexpr u32 OFF_XNT = OFF_Z1T + 128*27*1024;
constexpr u32 OFF_WOF = OFF_XNT + 640*4096;
constexpr u32 OFF_HF  = OFF_WOF + 8*27*5*1024;
constexpr u32 OFF_AT  = OFF_HF + 128*27*1024;
constexpr u32 OFF_ST  = OFF_AT + 640*4096;
constexpr u32 OFF_RS  = OFF_ST + 16*14*2048+8192;
constexpr u32 OFF_EP  = OFF_RS + 2048*16;
constexpr u32 OFF_F2  = OFF_EP + 256;
constexpr u32 OFF_F1  = OFF_F2 + 256*128;
constexpr u32 OFF_FX  = OFF_F1 + 256*128;
constexpr u32 OFF_FW  = OFF_FX + 32*128;
constexpr u32 OFF_FH  = OFF_FW + 32*128;
constexpr u32 OFF_FA  = OFF_FH + 256*128;
constexpr u32 WS_END  = OFF_FA + 256*128;

struct Params {
  const u16 *x, *lar, *aim, *cre, *cim, *ldt, *dsk, *win, *wsp, *bsp, *wout;
  u16* out; char* ws;
};
template<class F,int... I> DI void sfor_impl(F&& f, std::integer_sequence<int,I...>){ (f(std::integral_constant<int,I>{}),...); }
template<int N,class F> DI void sfor(F&& f){ sfor_impl(f, std::make_integer_sequence<int,N>{}); }

DI float bflo(u32 v){ return __uint_as_float(v<<16); }
DI float bfhi(u32 v){ return __uint_as_float(v&0xffff0000u); }
DI u32 pkbf(float a,float b){ f2 v={a,b}; return __builtin_bit_cast(u32,__builtin_convertvector(v,bf2)); }
DI u32 pkhf(float a,float b){ f2 v={a,b}; return __builtin_bit_cast(u32,__builtin_convertvector(v,hf2)); }
DI float rbf(float a){ return bflo(pkbf(a,a)); }
DI float hlo(u32 v){ return (float)__builtin_bit_cast(hf2,v)[0]; }
DI float hhi(u32 v){ return (float)__builtin_bit_cast(hf2,v)[1]; }
DI float dot2(u32 a,u32 b,float c){ return __builtin_amdgcn_fdot2_f32_bf16(__builtin_bit_cast(bf2,a),__builtin_bit_cast(bf2,b),c,false); }
DI f4 mfma16(u4 a,u4 b,f4 c){ return __builtin_amdgcn_mfma_f32_16x16x32_bf16(__builtin_bit_cast(bfx8,a),__builtin_bit_cast(bfx8,b),c,0,0,0); }
DI f16v mfma32h(u4 a,u4 b,f16v c){ return __builtin_amdgcn_mfma_f32_32x32x16_f16(__builtin_bit_cast(hfx8,a),__builtin_bit_cast(hfx8,b),c,0,0,0); }
DI u4 ldsr128(u32 a){ return *(LAS u4*)a; }
DI u2 ldsr64(u32 a){ return *(LAS u2*)a; }
DI void ldsw128(u32 a,u4 v){ *(LAS u4*)a=v; }
DI u2 ldstr(u32 a){ return __builtin_bit_cast(u2,__builtin_amdgcn_ds_read_tr16_b64_v4i16((LAS s4*)a)); }
DI u32 uni(u32 v){ return __builtin_amdgcn_readfirstlane(v); }

template<int OFF> DI void gload(u4& dst,u32 voff,const char* base){ asm volatile("global_load_dwordx4 %0, %1, %2 offset:%3 sc1" : "=v"(dst) : "v"(voff), "s"(base), "n"(OFF)); }
template<int OFF> DI void gload_t(u4& dst,u32 voff,const char* base){ asm volatile("global_load_dwordx4 %0, %1, %2 offset:%3 sc1" : "+v"(dst) : "v"(voff), "s"(base), "n"(OFF)); }
DI void mfma4(f4& c0,f4& c1,f4& c2,f4& c3,const u4& a,const u4 (&b)[4]){
  asm volatile("v_mfma_f32_16x16x32_bf16 %0, %4, %5, %0\n\tv_mfma_f32_16x16x32_bf16 %1, %4, %6, %1\n\tv_mfma_f32_16x16x32_bf16 %2, %4, %7, %2\n\tv_mfma_f32_16x16x32_bf16 %3, %4, %8, %3"
    : "+v"(c0),"+v"(c1),"+v"(c2),"+v"(c3) : "v"(a),"v"(b[0]),"v"(b[1]),"v"(b[2]),"v"(b[3])); }
DI void mfma4z(f4& c0,f4& c1,f4& c2,f4& c3,const u4& a,const u4 (&b)[4]){
  asm volatile("v_mfma_f32_16x16x32_bf16 %0, %4, %5, 0\n\tv_mfma_f32_16x16x32_bf16 %1, %4, %6, 0\n\tv_mfma_f32_16x16x32_bf16 %2, %4, %7, 0\n\tv_mfma_f32_16x16x32_bf16 %3, %4, %8, 0"
    : "=&v"(c0),"=&v"(c1),"=&v"(c2),"=&v"(c3) : "v"(a),"v"(b[0]),"v"(b[1]),"v"(b[2]),"v"(b[3])); }
template<int CNT> DI void waitB(u4 (&B)[7]){ asm volatile("s_waitcnt vmcnt(%7)" : "+v"(B[0]),"+v"(B[1]),"+v"(B[2]),"+v"(B[3]),"+v"(B[4]),"+v"(B[5]),"+v"(B[6]) : "n"(CNT) : "memory"); }
DI void waitA(u4 (&A)[4]){ asm volatile("s_waitcnt lgkmcnt(0)" : "+v"(A[0]),"+v"(A[1]),"+v"(A[2]),"+v"(A[3]) :: "memory"); }
#define WAITV(n) asm volatile("s_waitcnt vmcnt(%0)" :: "n"(n) : "memory")
#define WAITL0() asm volatile("s_waitcnt lgkmcnt(0)" ::: "memory")
#define CFENCE() asm volatile("" ::: "memory")
#define SCHED() __builtin_amdgcn_sched_barrier(0)
DI void bar(){ asm volatile("s_waitcnt lgkmcnt(0)\n\ts_barrier" ::: "memory"); }
DI f2 unpk(u32 v){ return f2{bflo(v),bfhi(v)}; }
DI u32 pkbf2(f2 v){ return __builtin_bit_cast(u32,__builtin_convertvector(v,bf2)); }
DI u32 pkhf2(f2 v){ return __builtin_bit_cast(u32,__builtin_convertvector(v,hf2)); }
DI f2 fma2(f2 a,f2 b,f2 c){ return __builtin_elementwise_fma(a,b,c); }
DI f2 gelu2(f2 x){
  f2 u=x*fma2(x*x,f2{-0.10294324f,-0.10294324f},f2{-2.30220820f,-2.30220820f});
  f2 d=f2{__builtin_amdgcn_exp2f(u[0]),__builtin_amdgcn_exp2f(u[1])}+f2{1.f,1.f};
  return x*f2{__builtin_amdgcn_rcpf(d[0]),__builtin_amdgcn_rcpf(d[1])};
}
DI void ln_fin(float mean,float var,float& m,float& rinv){
  m=rbf(mean); float v=rbf(__builtin_fmaxf(var,0.f));
  float s=rbf(__builtin_amdgcn_sqrtf(rbf(v+1e-5f))); float r=__builtin_amdgcn_rcpf(s); rinv=__builtin_fmaf(__builtin_fmaf(-s,r,1.f),r,r);
}
typedef __amdgpu_buffer_rsrc_t rsrc_t;
DI void st16(rsrc_t rs,u32 off,u4 v){ __builtin_amdgcn_raw_buffer_store_b128(v,rs,off,0,16); }
DI void st8(rsrc_t rs,u32 off,u2 v){ __builtin_amdgcn_raw_buffer_store_b64(v,rs,off,0,16); }
DI void st4(rsrc_t rs,u32 off,u32 v){ __builtin_amdgcn_raw_buffer_store_b32(v,rs,off,0,16); }
DI u4 ld16(rsrc_t rs,u32 off){ return __builtin_amdgcn_raw_buffer_load_b128(rs,off,0,16); }
DI u32 ld4(rsrc_t rs,u32 off){ return __builtin_amdgcn_raw_buffer_load_b32(rs,off,0,16); }
constexpr int SPIN=1<<17;
constexpr u32 TAGINC=0x9E3779B9u;
template<int SL=2> DI void pollw(rsrc_t rs,u32 off,u32 tag){
  #pragma unroll 1
  for(int s=0;s<SPIN;s++){ u32 v=ld4(rs,off); CFENCE(); if(__all(v==tag)) break; __builtin_amdgcn_s_sleep(SL); }
}

DI f16v mfma32b(u4 a,u4 b,f16v c){ return __builtin_amdgcn_mfma_f32_32x32x16_bf16(__builtin_bit_cast(bfx8,a),__builtin_bit_cast(bfx8,b),c,0,0,0); }
DI u4 mksrd(const void* b){ u64 a=(u64)b; return u4{(u32)a,(u32)(a>>32)&0xffffu,0xffffffffu,0x00020000u}; }
template<int IMM> DI void setm0(u32 base){ asm volatile("s_add_u32 m0, %0, %1\n\ts_nop 0" :: "s"(base), "n"(IMM) : "scc","memory"); }
template<int OFF> DI void dma(u4 srd,u32 voff,u32 soff){ asm volatile("buffer_load_dwordx4 %0, %1, %2 offen offset:%3 sc1 lds" :: "v"(voff), "s"(srd), "s"(soff), "n"(OFF) : "memory"); }
DI void touch(u4 srd,u32 voff,u32 soff){ asm volatile("buffer_load_dword %0, %1, %2 offen sc1 lds" :: "v"(voff), "s"(srd), "s"(soff) : "memory"); }
template<int OFF> DI void gload8(u2& dst,u32 voff,const char* base){ asm volatile("global_load_dwordx2 %0, %1, %2 offset:%3 sc1" : "=v"(dst) : "v"(voff), "s"(base), "n"(OFF)); }
DI void gload2(u32& dst,u32 voff,const char* base){ asm volatile("global_load_ushort %0, %1, %2" : "=v"(dst) : "v"(voff), "s"(base)); }
DI u32 arrive(u32 a,int){ u32 o; asm volatile("s_mov_b64 exec, 1\n\tds_add_rtn_u32 %0, %1, %2\n\ts_mov_b64 exec, -1\n\ts_waitcnt lgkmcnt(0)" : "=&v"(o) : "v"(a), "v"(1u) : "memory"); return uni(o); }
template<int SL=1> DI void ldswait(u32 a,u32 n){ while(__hip_atomic_load((LAS u32*)a,__ATOMIC_RELAXED,__HIP_MEMORY_SCOPE_WORKGROUP)<n) __builtin_amdgcn_s_sleep(SL); CFENCE(); }
DI void tie1(u32& g){ asm volatile("" : "+v"(g) :: "memory"); }
DI u32 opq(u32 v){ asm volatile("" : "+v"(v)); return v; }

constexpr u32 STG=32768;
constexpr u32 RING=24576, RB=62848, CONV=0, KP=2208, UOF=16768, CVS=UOF+4160, S7O=4*KP, S9O=S7O+2112, KZO=S9O+2112, MISC=RB+4*RING, JNK=MISC+2048, PM=163840-128, LDS_TOTAL=163840;
constexpr u32 ATL=2*CVS;
constexpr u32 C_FULL=PM, C_G1=PM+4, C_Z2=PM+8, C_Z1=PM+12, C_RED=PM+16, C_H=PM+20, C_FREE=PM+24, C_CV=PM+28, C_Z1R=PM+32, C_TAG=PM+40, C_KP=PM+48, C_DONE=PM+64, C_CS=PM+72, C_P=PM+96, C_AT=PM+112, C_WR=PM+116;
constexpr u32 CODE_BYTES=37888;
constexpr int HPF=5;
DI void signal(u32 a,int){ asm volatile("s_mov_b64 exec, 1\n\tds_add_u32 %0, %1\n\ts_mov_b64 exec, -1" :: "v"(a), "v"(1u) : "memory"); }
template<int OFF> DI void gload4(u32& dst,u32 voff,const char* base){ asm volatile("global_load_dword %0, %1, %2 offset:%3 sc1" : "=v"(dst) : "v"(voff), "s"(base), "n"(OFF)); }

DI f2 cmulp(f2 a,f2 b,f2 bs){ return fma2(f2{a[1],a[1]},bs,f2{a[0],a[0]}*b); }
DI u32 ldsv(u32 a){ return __hip_atomic_load((LAS u32*)a,__ATOMIC_RELAXED,__HIP_MEMORY_SCOPE_WORKGROUP); }
DI f2 cpow_dir(float lr,float rv,float pw){ float e=__builtin_amdgcn_exp2f(lr*pw); float f=__builtin_amdgcn_fractf(rv*pw);
  return f2{e*__builtin_amdgcn_cosf(f),e*__builtin_amdgcn_sinf(f)}; }
DI u32 dup(u32 a){ return __builtin_amdgcn_perm(a,a,0x01000100u); }
DI u32 cat(u32 lo,u32 hi){ return __builtin_amdgcn_perm(hi,lo,0x05040100u); }
DI u32 lds16(u32 a){ return *(LAS u16*)a; }

DI float kgen_tab(u32 cb,int lane,const u32 (&pr)[6],float (&kp)[11]){
  const float dt=rbf(__expf(bflo(pr[4])));
  const float cr[2]={bflo(pr[2]),bfhi(pr[2])}, cm[2]={bflo(pr[3]),bfhi(pr[3])};
  float mx=fmaxf(fmaxf(fabsf(cr[0]),fabsf(cm[0])),fmaxf(fabsf(cr[1]),fabsf(cm[1])));
  u32 eb=__float_as_uint(mx)&0x7f800000u;
  #define DPPMAX(c) eb=max(eb,(u32)__builtin_amdgcn_update_dpp(0,(int)eb,c,0xf,0xf,false))
  DPPMAX(0x128); DPPMAX(0x124); DPPMAX(0x122); DPPMAX(0x121);
  eb=max(max((u32)__builtin_amdgcn_readlane(eb,0),(u32)__builtin_amdgcn_readlane(eb,16)),max((u32)__builtin_amdgcn_readlane(eb,32),(u32)__builtin_amdgcn_readlane(eb,48))); eb=eb<0x01000000u?0x3f800000u:(eb>0x7e000000u?0x7e000000u:eb);
  const float ksc=2.f*dt*__uint_as_float(eb), isc=__uint_as_float(0x7f000000u-eb);
  const int r=lane&31,h=lane>>5;
  const u32 TP=272, TA=cb, TB=cb+22*TP;
  f16v acc={};
  f2 an[2][4];
  #pragma unroll
  for(int c=0;c<2;c++){ const float l_a=c?bfhi(pr[0]):bflo(pr[0]), i_m=c?bfhi(pr[1]):bflo(pr[1]);
    const float lr=(-rbf(__expf(l_a))*dt)*1.44269504f, rv=(i_m*dt)*0.15915494f;
    an[c][0]=cpow_dir(lr,rv,1.f); an[c][1]=cpow_dir(lr,-rv,8.f); an[c][2]=cpow_dir(lr,-rv,16.f); an[c][3]=cpow_dir(lr,rv,256.f); }
  u32 hold[54]; const u32 wt=TA+lane*4;
  struct P2 { f2 v[2]; };
  auto mul=[&](const P2& x,int k){ P2 o;
    #pragma unroll
    for(int c=0;c<2;c++) o.v[c]=cmulp(x.v[c],an[c][k],f2{-an[c][k][1],an[c][k][0]});
    return o; };
  auto put=[&](int row,const P2& x){ *(LAS u32*)(wt+row*TP)=pkhf2(x.v[0]); hold[row]=pkhf2(x.v[1]); };
  P2 e={{f2{1.f,0.f},f2{1.f,0.f}}}, o={{an[0][1],an[1][1]}}, bk[8], t;
  bk[0].v[0]=f2{cr[0]*isc,cm[0]*isc}; bk[0].v[1]=f2{cr[1]*isc,cm[1]*isc}; put(0,bk[0]); t=bk[0];
  constexpr int tr[8]={8,10,11,16,17,20,21,18};
  #pragma unroll
  for(int k=0;k<16;k++){ put(22+2*k,e); put(23+2*k,o);
    if(k<15){ if(k) e=mul(e,2); else { e.v[0]=an[0][2]; e.v[1]=an[1][2]; } o=mul(o,2); }
    if(k<8){ t=mul(t,3); put(tr[k],t); }
    if(k<7){ bk[k+1]=mul(bk[k],0); put(k+1,bk[k+1]); }
    if(k==8){ put(9,mul(bk[2],3)); put(13,mul(bk[6],3)); t=mul(bk[4],3); put(12,t); }
    if(k==9||k==10){ t=mul(t,3); put(5+k,t); }
    SCHED(); }
  const int ra=(r>21||r==19)?0:r;
  #pragma unroll
  for(int c=0;c<2;c++){
    CFENCE();
    u4 fa[8],fb[8];
    #pragma unroll
    for(int ks=0;ks<8;ks++){ fa[ks]=ldsr128(TA+ra*TP+ks*32+h*16); fb[ks]=ldsr128(TB+r*TP+ks*32+h*16); }
    CFENCE();
    if(c==0){
      #pragma unroll
      for(int i=0;i<54;i++) if(i!=19) *(LAS u32*)(wt+i*TP)=hold[i]; }
    #pragma unroll
    for(int ks=0;ks<8;ks++) acc=mfma32h(fa[ks],fb[ks],acc);
  }
  CFENCE();
  #pragma unroll
  for(int i=0;i<11;i++) kp[i]=acc[i];
  return ksc;
}
DI void kgen_exp(u32 cb,int lane,const float (&acc)[11],const u32 (&ga)[11]){
  const int r=lane&31,h=lane>>5;
  { const u32 a4=cb+r*16+h*8, a5=cb+r*16+h*1024, az=cb+KZO+r*2+h*128;
    #define PK(addr,base,reg) do{ *(LAS _Float16*)((addr)+2*(base))=(_Float16)acc[reg]; }while(0)
    PK(a4,0,0);PK(a4,1,1);PK(a4,2,2);PK(a4,3,3); PK(a4,256,4);PK(a4,258,5);PK(a4,512,6);PK(a4,768,7);
    PK(a5,1024,8); PK(az,255,10); PK(az,127,8); PK(az,159,9);
    #undef PK
    CFENCE();
    if(lane<16) ldsw128(cb+KZO+lane*16,u4{0,0,0,0});
    CFENCE();
  }
  { const u32 g0=cb+2*(1024-8*lane);
    u32 f2_=lds16(g0+(lane?8:0)), a2=lds16(g0), m2=lds16(g0-8), b2=lds16(g0-16);
    u32 e[11];
    #pragma unroll
    for(int j=0;j<11;j++) e[j]=lds16(ga[j]);
    u32 k01=*(LAS u32*)cb, k23=*(LAS u32*)(cb+4), kl=lds16(cb+2048);
    WAITL0();
    const u32 wo=cb+96+lane*16;
    { u32 aa=dup(a2),mm=dup(m2),bb=dup(b2),am=cat(a2,m2),fa=cat(f2_,a2),mb=cat(m2,b2);
      ldsw128(wo,u4{aa,am,mm,bb}); ldsw128(wo+KP,u4{aa,aa,mm,mb}); ldsw128(wo+2*KP,u4{aa,aa,am,mm}); ldsw128(wo+3*KP,u4{fa,aa,aa,mm}); }
    { u32 o0=cat(e[0],e[1]),o1=cat(e[2],e[3]),o2=cat(e[4],e[5]),o3=cat(e[6],e[7]),o4=cat(e[8],e[9]);
      u32 p1=cat(e[1],e[2]),d0=cat(e[3],e[4]),d1=cat(e[5],e[6]),d2=cat(e[7],e[8]),d3=cat(e[9],e[10]);
      ldsw128(wo+1024,u4{d0,d1,d2,d3}); ldsw128(wo+1024+KP,u4{o1,o2,o3,o4}); ldsw128(wo+1024+2*KP,u4{p1,d0,d1,d2}); ldsw128(wo+1024+3*KP,u4{o0,o1,o2,o3}); }
    if(lane<10){ if(lane){k01=0;k23=0;} if(lane!=9) kl=0;
      const u32 d0=k01&0xffffu, o1=__builtin_amdgcn_alignbit(k01,k01,16), o0=__builtin_amdgcn_alignbit(k23,k23,16), p1=(k23&0xffffu)|(k01&0xffff0000u), kk=dup(kl), zk=kl<<16;
      const u32 w2=lane<4? wo+2048 : wo-160;
      ldsw128(w2,u4{d0,0,kk,kk}); ldsw128(w2+KP,u4{o1,0,zk,kk}); ldsw128(w2+2*KP,u4{p1,d0,0,kk}); ldsw128(w2+3*KP,u4{o0,o1,0,zk}); }
  }
}
DI f4 mfma16h(u4 a,u4 b,f4 c){ return __builtin_amdgcn_mfma_f32_16x16x32_f16(__builtin_bit_cast(hfx8,a),__builtin_bit_cast(hfx8,b),c,0,0,0); }
DI u4 lds2x64(u32 a){ const u2 lo=ldsr64(a), hi=ldsr64(a+8); return u4{lo[0],lo[1],hi[0],hi[1]}; }
DI void conv_win(u32 cb,int lane,u4 (&sw)[4]){
  float P[25]; P[0]=0.f;
  #pragma unroll
  for(int c=0;c<4;c++){ const int X=2*lane-2+c; const u4 v=ldsr128(cb+UOF+(X&3)*1040+((X>>2)+1)*16);
    #pragma unroll
    for(int e=0;e<8;e++){ const int i=8*c+e-4; if(i>=0&&i<24) P[i+1]=P[i]+((e&1)?hhi(v[e>>1]):hlo(v[e>>1])); } }
  #pragma unroll
  for(int k=0;k<8;k++){ sw[k>>2][k&3]=pkhf(P[2*k+8]-P[2*k+1],P[2*k+9]-P[2*k+2]); sw[2+(k>>2)][k&3]=pkhf(P[2*k+9]-P[2*k],P[2*k+10]-P[2*k+1]); }
}
DI void conv_help(u32 cb,int lane,const u4 (&sw)[4]){
  const int r=lane&15,q=lane>>4; f4 y[2][2]={};
  ldsw128(cb+S7O+48+lane*32,sw[0]); ldsw128(cb+S7O+64+lane*32,sw[1]); ldsw128(cb+S9O+48+lane*32,sw[2]); ldsw128(cb+S9O+64+lane*32,sw[3]);
  if(lane<3){ ldsw128(cb+S7O+lane*16,u4{0,0,0,0}); ldsw128(cb+S9O+lane*16,u4{0,0,0,0}); }
  CFENCE();
  const u32 ka=cb+(r&3)*KP+16*q-8*(r>>2)+64, ub=cb+UOF+q*1040+(r+1)*16;
  const u4 a0=lds2x64(ka+32), a1=lds2x64(ka), b0=ldsr128(ub), b1=ldsr128(ub+256), a2=lds2x64(ka-32), b2=ldsr128(ub-16), b3=ldsr128(ub+240);
  const u32 sa=cb+(((r>>2)&1)?S9O:S7O)+2*(64*q+24-8*(r>>2)+4*(r&3)), kz=cb+KZO+8*(r-2*q+23); u4 bz[4],fa[4][2];
  #pragma unroll
  for(int ks=0;ks<4;ks++) bz[ks]=lds2x64(kz+(3-ks)*64);
  #pragma unroll
  for(int i=0;i<8;i++){ const u2 lo=ldstr(sa+(i>>1)*512+(i&1)*32+64), hi=ldstr(sa+(i>>1)*512+(i&1)*32); fa[i>>1][i&1]=u4{lo[0],lo[1],hi[0],hi[1]}; }
  SCHED();
  y[0][0]=mfma16h(a2,b2,y[0][0]); y[0][1]=mfma16h(a2,b3,y[0][1]);
  y[0][0]=mfma16h(a0,b0,y[0][0]); y[1][0]=mfma16h(a1,b0,y[1][0]); y[0][1]=mfma16h(a0,b1,y[0][1]); y[1][1]=mfma16h(a1,b1,y[1][1]);
  #pragma unroll
  for(int ks=0;ks<4;ks++)
    #pragma unroll
    for(int m=0;m<2;m++){ y[m][1]=mfma16h(fa[ks][m],bz[ks],y[m][1]); if(ks<2) y[m][0]=mfma16h(fa[ks][m],bz[ks+2],y[m][0]); }
  CFENCE();
  #pragma unroll
  for(int i=0;i<4;i++) *(LAS f4*)(cb+S7O+i*1024+lane*16)=y[i&1][i>>1];
}
#define RDA(S,i,m) asm volatile("ds_read2_b64 %0, %1 offset0:%2 offset1:%3" : "=v"(AF[S][i][m]) : "v"(ka), "n"((1-(i))*128+(1-(m))*4), "n"((1-(i))*128+(1-(m))*4+1))
#define RDB(S,i,ad,o) asm volatile("ds_read_b128 %0, %1 offset:%2" : "=v"(BF[S][i]) : "v"(ad), "n"(o))
#define TIEA(S,i) "+v"(AF[S][i][0]),"+v"(AF[S][i][1])
#define CV_M(S,i,n) do{ y[0][n]=mfma16h(AF[S][i][0],BF[S][(n)-(i)],y[0][n]); y[1][n]=mfma16h(AF[S][i][1],BF[S][(n)-(i)],y[1][n]); SCHED(); }while(0)
#define CV_U0() u32 u0=u1-256; u0=((int)u0<(int)uz)?uz:u0
template<class CB> DI void conv_wave(CB&& mid,rsrc_t rs,int d,u32 cb,u32 flag,int lane,float ksc,float dsk){
  const int r=lane&15,q=lane>>4; u32 ka=cb+(r&3)*KP+16*q-8*(r>>2)+1088; const u32 uz=cb+UOF+q*1040; u32 u1=uz+(r+17)*16; u4 AF[2][2][2]; u4 BF[2][4]; f4 y[2][4]={};
  #define CV_WT(S) asm volatile("s_waitcnt lgkmcnt(0)" : TIEA(S,0),TIEA(S,1),"+v"(BF[S][0]),"+v"(BF[S][1]),"+v"(BF[S][2]),"+v"(BF[S][3]))
  #define CV_STEP(C,N) do{ CV_U0(); CV_M(C,0,0); RDA(N,0,0); RDB(N,0,u0,0); RDA(N,0,1); SCHED(); CV_M(C,0,1); RDB(N,1,u1,0); RDA(N,1,0); RDB(N,2,u1,256); SCHED(); CV_M(C,0,2); RDA(N,1,1); RDB(N,3,u1,512); \
      ka-=64; u1-=16; SCHED(); CV_M(C,0,3); CV_M(C,1,1); CV_M(C,1,2); CV_M(C,1,3); CV_WT(N); }while(0)
  { CV_U0(); RDA(0,0,0); RDA(0,0,1); RDA(0,1,0); RDA(0,1,1); RDB(0,0,u0,0); RDB(0,1,u1,0); RDB(0,2,u1,256); RDB(0,3,u1,512); ka-=64; u1-=16; CV_WT(0); }
  #pragma unroll 1
  for(int dl=0;dl<8;dl++){ CV_STEP(0,1); if(dl==7){ ka+=64; u1+=16; } CV_STEP(1,0); }
  mid();
  u2 uv[2][4];
  #pragma unroll
  for(int n=0;n<4;n++)
    #pragma unroll
    for(int m=0;m<2;m++) uv[m][n]=ldsr64(cb+UOF+(2*m+(q>>1))*1040+(16*n+r+1)*16+8*(q&1));
  const u32 sc=cb+UOF+r*64+q*8;
  auto fin=[&](int n){
    #pragma unroll
    for(int m=0;m<2;m++){ u2 o;
      #pragma unroll
      for(int e=0;e<2;e++) o[e]=pkbf2(fma2(f2{hlo(uv[m][n][e]),hhi(uv[m][n][e])},f2{dsk,dsk},f2{y[m][n][2*e],y[m][n][2*e+1]}*ksc));
      *(LAS u2*)(sc+n*1024+m*32)=o; } };
  ldswait(flag,2u);
  { f4 hp[4];
    #pragma unroll
    for(int i=0;i<4;i++) hp[i]=*(LAS f4*)(cb+S7O+i*1024+lane*16);
    fin(0); fin(1);
    #pragma unroll
    for(int i=0;i<4;i++) y[i&1][2+(i>>1)]+=hp[i];
    fin(2); fin(3); }
  CFENCE();
  u4 ov[4];
  #pragma unroll
  for(int n=0;n<4;n++) ov[n]=ldsr128(cb+UOF+n*1024+lane*16);
  #pragma unroll
  for(int n=0;n<4;n++) st16(rs,OFF_AT+d*4096+n*1024+lane*16,ov[n]);
  WAITV(0);
}

constexpr int NB=3, HZZ=14;
constexpr int g2ops(int h){ return h<0? 7 : (h+NB<16?7:0)+((h>=3&&h<=10)?4:0)+(h==HZZ-3?1:0)+(h==HZZ?8:0); }
constexpr int g2cnt(int h){ int c=0; for(int i=h-NB+1;i<h;i++) c+=g2ops(i); return c; }
struct Frag { u4 x[4], w[4][2]; };
__global__ __launch_bounds__(512,2) void fused(Params p){
  extern __shared__ __attribute__((aligned(16))) char sm[];
  const u32 smb=(u32)(size_t)(LAS char*)sm;
  u64 pc0; asm volatile("s_getpc_b64 %0" : "=s"(pc0));
  const int b=blockIdx.x, xc=b&7, idx=b>>3;
  const int tid=threadIdx.x, w=uni(tid>>6); int lane=tid&63;
  auto cpf=[&](u32 o){ u4 sc=mksrd((const void*)(pc0&~127ull)); sc[2]=CODE_BYTES; setm0<JNK>(smb); touch(sc,lane*128,o); };
  if(idx>=28) cpf(w*8192);
  SCHED();
  const int rt=4*(xc>>1)+(idx&3), j=7*(xc&1)+(idx>>2), mi=w&3, g1=w>>2;
  const u32 kr0=(((j+rt)&3)*5)>>1;
  u32 vx0,vx1,vw1,vw2,k0=kr0*128,k0w=kr0*(64*3456); const u32 mw=smb+w*4096;
  { const u32 lr=lane>>3,c=lane&7; vx0=(rt*128+16*w+lr)*1280+((c^(lr>>1))<<4); vx1=(vx0^64u)+9216;
    vw1=(8*w+lr)*3456+((c^((lr&2)<<1))<<4)+j*128; vw2=vw1+1024+(j==13?0:14*128); }
  asm volatile("" : "+v"(vx0),"+v"(vx1),"+v"(vw1),"+v"(vw2),"+s"(k0),"+s"(k0w));
  setm0<0>(mw);
  SCHED();
  const u4 srdX=mksrd(p.x), srdW=mksrd((const char*)p.win-3072);
  if(idx<28){ dma<0>(srdX,vx0,k0); dma<1024>(srdX,vx1,k0); dma<2048>(srdW,vw2,k0w); dma<3072>(srdW,vw1,k0w); }
  SCHED();
  const rsrc_t rs=__builtin_amdgcn_make_buffer_rsrc(p.ws,0,WS_END,0x00020000);
  const u4 srdS=mksrd(p.ws);
  const int rtB=8*(xc>>1)+(idx&7), ct=4*(xc&1)+(idx>>3);
  const bool hasc=idx<28 && idx+28*(w&3)<80 && w>=4;
  const int dch=80*xc+(hasc? idx+28*(w&3) : 0);
  const u32 cb=smb+CONV+(w&3)*CVS;
  u32 tag; float ksc=0.f, dsk=0.f;
  u32 zp[2][8]={};
  auto z1st=[&](){ const int r=lane&31,h=lane>>5;
    #pragma unroll
    for(int n=0;n<2;n++) if(2*j+n<27){ const u32 oo=OFF_Z1T+((rt*8+2*mi+(r>>4))*27+2*j+n)*1024+(h*16+(r&15))*16;
      st16(rs,oo,u4{zp[n][0],zp[n][1],zp[n][4],zp[n][5]}); st16(rs,oo+512,u4{zp[n][2],zp[n][3],zp[n][6],zp[n][7]}); } };
  auto f1=[&](){ WAITV(0); if(arrive(smb+C_Z1,lane)==3u && lane==0) st4(rs,OFF_F1+(rt*16+j)*128,tag); };
  auto pre01=[&](){
    const u4 sa=mksrd((const char*)p.wsp+(size_t)rtB*64*4096+mi*1024-4096); const u32 v0=(lane>>3)*4096+(((lane&7)^(lane>>4))<<4);
    #pragma unroll 1
    for(int i=0;i<4;i++){ setm0<RB+8192>(smb+mi*RING+i*4096); const u32 g=(i&1)*4+2*mi, k=(i>>1)*128+4096;
      dma<0>(sa,v0,(g&7)*32768+k); dma<1024>(sa,v0^64u,(g&7)*32768+32768-1024+k); dma<2048>(sa,v0,((g+2)&7)*32768-2048+k); dma<3072>(sa,v0^64u,((g+2)&7)*32768+32768-3072+k); } };
  if(idx>=28){
    const int a=xc*4+idx-28, r=lane&15, q=lane>>4;
    const u32 ep=ld4(rs,OFF_EP);
    u4 xr[10]; const char* xs=(const char*)p.x+(size_t)(64*a+8*w+(lane>>3))*1280+(lane&7)*16;
    #pragma unroll
    for(int i=0;i<10;i++) xr[i]=*(const u4*)(xs+i*128);
    { const u32 l=(a*8+w)*19+(lane<19?lane:18); setm0<JNK>(smb); touch(srdS,(l<3584? OFF_ST/128+l : OFF_F2/128+(l-3584<1088?l-3584:1087))*128,0u); }
    if(tid<32) *(LAS u32*)(smb+PM+tid*4)=0u;
    tag=uni(ep)+TAGINC;
    float s0=0.f,s1=0.f,q0=0.f,q1=0.f;
    #pragma unroll
    for(int i=0;i<10;i++){ s0=dot2(xr[i][0],0x3f803f80u,s0); q0=dot2(xr[i][0],xr[i][0],q0); s1=dot2(xr[i][1],0x3f803f80u,s1); q1=dot2(xr[i][1],xr[i][1],q1);
      s0=dot2(xr[i][2],0x3f803f80u,s0); q0=dot2(xr[i][2],xr[i][2],q0); s1=dot2(xr[i][3],0x3f803f80u,s1); q1=dot2(xr[i][3],xr[i][3],q1); }
    s0+=s1; q0+=q1;
    #define DPPADD(v,c) v+=__int_as_float(__builtin_amdgcn_update_dpp(0,__float_as_int(v),c,0xf,0xf,false))
    DPPADD(s0,0xB1); DPPADD(q0,0xB1); DPPADD(s0,0x4E); DPPADD(q0,0x4E); DPPADD(s0,0x141); DPPADD(q0,0x141);
    float m1,ri; { float mean=s0*(1.f/640.f); ln_fin(mean,q0*(1.f/640.f)-mean*mean,m1,ri); }
    if((lane&7)==0) st16(rs,OFF_RS+(64*a+8*w+(lane>>3))*16,u4{__float_as_uint(m1),tag,__float_as_uint(ri),tag});
    if(a<27){ const u4 srdO=mksrd((const char*)p.wout+a*(32*1280)+w*5120); const u32 mo=smb+w*5120; setm0<0>(mo);
      dma<0>(srdO,lane*16,0); dma<1024>(srdO,lane*16,0); dma<2048>(srdO,lane*16,0); dma<3072>(srdO,lane*16,0); setm0<4096>(mo); dma<0>(srdO,lane*16,4096); }
    #pragma unroll
    for(int i=0;i<10;i++){ u4 o;
      #pragma unroll
      for(int e=0;e<4;e++) o[e]=pkhf2(fma2(unpk(xr[i][e]),f2{ri,ri},f2{-m1*ri,-m1*ri}));
      ldsw128(smb+RB+(8*w+(lane>>3))*1296+(lane&7)*16+i*128,o); }
    WAITV(0); bar();
    #pragma unroll
    for(int i=0;i<10;i++){ const int c=w*10+i; const u32 ad=smb+RB+(32*((r>>1)&1)+8*q+(r>>2))*1296+c*16+(r&1)*8; u2 lo=ldstr(ad), hi=ldstr(ad+4*1296);
      const u32 sc=smb+40960+w*2048+(i&1)*1024; ldsw128(sc+(r&7)*128+((((r>>3)*4+q)^(r&7))<<4),u4{lo[0],lo[1],hi[0],hi[1]});
      CFENCE(); const u4 o=ldsr128(sc+(lane>>3)*128+(((lane&7)^(lane>>3))<<4)); CFENCE();
      st16(rs,OFF_XNT+a*81920+c*1024+lane*16,o); }
    WAITV(0);
    if(arrive(smb+C_Z1,lane)==7u && lane==0) st4(rs,OFF_FX+a*128,tag);
    if(a<27){
      #pragma unroll
      for(int i=0;i<5;i++){ u32 ad=smb+(8*q+(r>>2))*1280+(w*5+i)*32+(r&3)*8; u2 lo=ldstr(ad), hi=ldstr(ad+4*1280);
        st16(rs,OFF_WOF+(u32)((w*27+a)*5+i)*1024+lane*16,u4{lo[0],lo[1],hi[0],hi[1]}); } }
    WAITV(0); bar();
    if(tid==0) st4(rs,OFF_FW+a*128,tag);
    if(!g1){ pre01(); WAITV(0); }
    else { const u4 so=mksrd((const char*)p.out+(size_t)(a*4+mi)*20480); setm0<JNK>(smb); touch(so,lane*128,0u); touch(so,lane*128,8192u); touch(so,(lane&31)*128,16384u); }
  } else {
    const int r=lane&31, h=lane>>5;
    u32 epv;
    u32 kx[10]; { u32 k=kr0;
      #pragma unroll
      for(int i=0;i<10;i++){ kx[i]=k*128; k=k==9?0:k+1; } }
    auto issue1=[&](auto sc,int k){ constexpr int s=sc.value;
      if(k==0){ setm0<(s&3)*STG>(mw); dma<0>(srdX,vx0,kx[s]); } if(k==1) dma<1024>(srdX,vx1,kx[s]); if(k==2) dma<2048>(srdW,vw2,kx[s]*1728); if(k==3) dma<3072>(srdW,vw1,kx[s]*1728); };
    auto issue=[&](auto sc){ issue1(sc,0); issue1(sc,1); issue1(sc,2); issue1(sc,3); };
    u32 pr[6];
    gload4<0>(epv,0u,p.ws+OFF_EP);
    gload4<0>(pr[0],lane*4,(const char*)(p.lar+dch*128)); gload4<0>(pr[1],lane*4,(const char*)(p.aim+dch*128)); gload4<0>(pr[2],lane*4,(const char*)(p.cre+dch*128)); gload4<0>(pr[3],lane*4,(const char*)(p.cim+dch*128));
    gload2(pr[4],0u,(const char*)(p.ldt+dch)); gload2(pr[5],0u,(const char*)(p.dsk+dch));
    asm volatile("s_barrier" ::: "memory");
    issue(std::integral_constant<int,1>{});
    if(tid<32) *(LAS u32*)(smb+PM+tid*4)=0u;
    u32 ax[2][4],aw[2][4][2];
    { u32 row=32*mi+r, a0=smb+(row>>4)*4096+(row&15)*128+((h^((r>>1)&7))<<4);
      u32 qq=(lane&15)>>2, w0=smb+2048+g1*1024+h*4096+qq*128+8*(lane&1)+(((2*((lane>>4)&1)+((lane>>1)&1))^((qq&2)<<1))<<4);
      #pragma unroll
      for(int i=0;i<2;i++)
        #pragma unroll
        for(int k=0;k<4;k++){ const u32 kk=(k+mi)&3, wo=i*2*STG; ax[i][k]=opq((a0^(kk*32u))+wo); aw[i][k][0]=opq(w0+kk*8192+wo); aw[i][k][1]=opq((w0^64u)+kk*8192+wo); } }
    u4 sel; { const u32 o=((lane&15)==((lane>>4)&1))?0x3f803f80u:0u; sel=u4{o,o,o,o}; asm volatile("" : "+v"(sel)); }
    u32 ga[11];
    #pragma unroll
    for(int i=0;i<11;i++) ga[i]=opq(cb+2u*(u32)rbf((float)(512-8*lane+3-i)));
    Frag F[2];
    asm volatile("s_waitcnt vmcnt(4)" : "+v"(epv),"+v"(pr[0]),"+v"(pr[1]),"+v"(pr[2]),"+v"(pr[3]),"+v"(pr[4]),"+v"(pr[5]) :: "memory");
    bar();
    tag=uni(epv)+TAGINC; dsk=bflo(pr[5]);
    #pragma unroll
    for(int k=0;k<4;k++){
      #pragma unroll
      for(int n=0;n<2;n++){ u2 lo=ldstr(aw[0][k][n]), hi=ldstr(aw[0][k][n]+512); F[0].w[k][n]=u4{lo[0],lo[1],hi[0],hi[1]}; }
      F[0].x[k]=ldsr128(ax[0][k]); }
    issue(std::integral_constant<int,2>{});
    f16v acc[2]={}; f4 cacc[2]={}; u4 G;
    const u32 go=OFF_RS+rt*2048+mi*512, cp=smb+MISC+g1*1024;
    sfor<10>([&](auto ic){
      constexpr int s=ic.value; Frag& cur=F[s&1]; Frag& nxt=F[(s&1)^1];
      if constexpr(s<9){ WAITV(s==8? 3 : s==7? 7 : 4); asm volatile("s_barrier" ::: "memory"); }
      auto rw=[&](int k,int n){ if constexpr(s<9){ const int i=((s+1)>>1)&1; const u32 so=((s+1)&1)*STG; u2 lo=ldstr(aw[i][k][n]+so), hi=ldstr(aw[i][k][n]+so+512); nxt.w[k][n]=u4{lo[0],lo[1],hi[0],hi[1]}; } };
      auto rx=[&](int k){ if constexpr(s<9){ const int i=((s+1)>>1)&1; const u32 so=((s+1)&1)*STG; nxt.x[k]=ldsr128(ax[i][k]+so); } };
      #pragma unroll
      for(int k=0;k<4;k++){
        acc[0]=mfma32b(cur.w[k][0],cur.x[k],acc[0]); SCHED();
        if constexpr(s+3<=9) issue1(std::integral_constant<int,s+3>{},k);
        rw(k,0); rx(k);
        SCHED();
        acc[1]=mfma32b(cur.w[k][1],cur.x[k],acc[1]); SCHED();
        rw(k,1);
        if(k==0){ cacc[0]=mfma16(cur.w[0][0],sel,cacc[0]); cacc[1]=mfma16(cur.w[0][1],sel,cacc[1]); }
        if constexpr(s==6){ if(k==3){ gload<0>(G,r*16,p.ws+go); setm0<JNK>(smb);
            if(g1){ const u4 sp=mksrd((const char*)p.wsp+(size_t)(xc>>1)*512*4096); const u32 lb=(((xc&1)*28+idx)*4+mi)*74, l0=lb+lane, l1=lb+64+(lane<10?lane:9);
              touch(sp,(l0<16384?l0:16383)*128,0u); touch(sp,(l1<16384?l1:16383)*128,0u); }
            else { touch(mksrd(p.out+rtB*64*640),0u,0u); touch(mksrd(p.bsp+rtB*64),0u,0u); } } }
        if constexpr(s==8){ if(k==3) signal(smb+C_DONE,lane); }
        if constexpr(s==9){ if(k==1){
          if((lane&14)==0){ *(LAS f4*)(cp+mi*256+(lane&1)*64+(lane>>4)*16)=cacc[0]; *(LAS f4*)(cp+mi*256+128+(lane&1)*64+(lane>>4)*16)=cacc[1]; }
          signal(smb+C_CS+g1*4,lane); } }
        SCHED();
      }
    });
    if(!g1) __builtin_amdgcn_s_setprio(2);
    asm volatile("s_waitcnt vmcnt(2)" : "+v"(G) :: "memory");
    ldswait(smb+C_CS+g1*4,4u);
    const float cs0=*(LAS float*)(cp+h*512+r*4)+*(LAS float*)(cp+h*512+256+r*4), cs1=*(LAS float*)(cp+h*512+128+r*4)+*(LAS float*)(cp+h*512+384+r*4);
    #pragma unroll 1
    for(int sp=0;sp<SPIN && !__all(G[1]==tag&&G[3]==tag);sp++)asm volatile("global_load_dwordx4 %0, %1, %2 sc1\n\ts_waitcnt vmcnt(0)" : "=&v"(G) : "v"(r*16), "s"(p.ws+go) : "memory");
    const float nm=-__uint_as_float(G[0]), ri1=__uint_as_float(G[2]);
    acc[0]=__builtin_amdgcn_mfma_f32_32x32x2f32(cs0,nm,acc[0],0,0,0); acc[1]=__builtin_amdgcn_mfma_f32_32x32x2f32(cs1,nm,acc[1],0,0,0);
    #pragma unroll
    for(int e=0;e<16;e++) zp[e>>3][e&7]=pkbf2(gelu2(f2{acc[e>>3][2*(e&7)],acc[e>>3][2*(e&7)+1]}*ri1));
    if(g1){ ldswait(smb+C_DONE,8u); }
    else {
      float S=0.f,Q=0.f;
      if(j<13){
        #pragma unroll
        for(int e=0;e<8;e++){ S=dot2(zp[0][e],0x3f803f80u,S); Q=dot2(zp[0][e],zp[0][e],Q); } }
      #pragma unroll
      for(int e=0;e<8;e++){ S=dot2(zp[1][e],0x3f803f80u,S); Q=dot2(zp[1][e],zp[1][e],Q); }
      #define XSUM32(v) do{ auto t_=__builtin_amdgcn_permlane32_swap(__float_as_uint(v),__float_as_uint(v),false,false); v=__uint_as_float(t_[0])+__uint_as_float(t_[1]); }while(0)
      XSUM32(S); XSUM32(Q);
      const u32 sto=OFF_ST+rt*(14*2048)+(32*mi+r)*16;
      if(h==0) st16(rs,sto+j*2048,u4{__float_as_uint(S),tag,__float_as_uint(Q),tag});
    }
    float kp[11]; u4 g[7]={};
    const u32 gof=OFF_ST+rt*(14*2048)+(32*mi+r)*16+h*(7*2048);
    auto samp=[&](){ CFENCE();
      #pragma unroll
      for(int i=0;i<7;i++) g[i]=ld16(rs,gof+i*2048);
      CFENCE(); };
    if(hasc) ksc=kgen_tab(cb,lane,pr,kp); else if(!g1){ pre01(); samp(); }
    if(g1){
      if(hasc){
        kgen_exp(cb,lane,kp,ga);
        signal(smb+C_KP+mi*4,lane);
      }
      if(hasc) ldswait(smb+C_FULL,1u); else if(w<7){ z1st(); f1(); }
      if(hasc) z1st();
    } else {
      float S,Q;
      #pragma unroll 1
      for(int sp=0;sp<SPIN;sp++){ bool ok=true;
        #pragma unroll
        for(int i=0;i<7;i++) ok=ok&&g[i][1]==tag&&g[i][3]==tag;
        if(__all(ok)) break;
        __builtin_amdgcn_s_sleep(4); samp(); }
      S=0.f; Q=0.f;
      #pragma unroll
      for(int i=0;i<7;i++){ S+=__uint_as_float(g[i][0]); Q+=__uint_as_float(g[i][2]); }
      XSUM32(S); XSUM32(Q);
      float mean=S*(1.f/864.f), m2,r2; ln_fin(mean,Q*(1.f/864.f)-mean*mean,m2,r2); m2*=-r2;
      ldswait(smb+C_DONE,8u);
      const u32 tb=smb+RB+mi*RING, ad=tb+(8*(lane>>4)+((lane&15)>>2))*80+(lane&3)*8;
      const u32 zo=OFF_Z2F+((rt*4+mi)*54+(j==13?-2:2+4*j))*1024+lane*16;
      #pragma unroll
      for(int n=0;n<2;n++){
        #pragma unroll
        for(int g2=0;g2<4;g2++){ u2 o;
          #pragma unroll
          for(int e=0;e<2;e++) o[e]=pkbf2(fma2(unpk(zp[n][2*g2+e]),f2{r2,r2},f2{m2,m2}));
          *(LAS u2*)(tb+r*80+g2*16+h*8)=o; }
        CFENCE();
        u2 l0=ldstr(ad), h0=ldstr(ad+320), l1=ldstr(ad+32), h1=ldstr(ad+32+320);
        CFENCE();
        if(j<13||n){ st16(rs,zo+n*2048,u4{l0[0],l0[1],h0[0],h0[1]}); st16(rs,zo+n*2048+1024,u4{l1[0],l1[1],h1[0],h1[1]}); } }
      WAITV(0);
      if(arrive(smb+C_Z2,lane)==3u && lane==0) st4(rs,OFF_F2+(rt*16+j)*128,tag);
    }
  }
  const int ft0=ct<7?7*ct:47;
  bool adone=true; const u32 fao=OFF_FA+((lane<28?lane:0)*8+ct)*128;
  auto aload=[&](){ const u32 vo=(lane>>3)*4096+(lane&7)*16;
    #pragma unroll 1
    for(int i=0;i<10;i++){ setm0<ATL>(smb+i*1024); dma<0>(srdS,vo,OFF_AT+(u32)(80*ct+8*i)*4096+rtB*128); } };
  if(w>=4){
    if(hasc){
      __builtin_amdgcn_s_setprio(3);
      conv_wave(f1,rs,dch,cb,smb+C_KP+mi*4,lane,ksc,dsk);
      __builtin_amdgcn_s_setprio(0);
      if(arrive(smb+C_CV,lane)==(idx<24?2u:1u) && lane==0) st4(rs,OFF_FA+b*128,tag);
    }
    if(w==7){
      if(idx<28){
        pollw<16>(rs,OFF_FX+(lane&31)*128,tag);
        u4 sw[3][4];
        #pragma unroll
        for(int c=0;c<3;c++)
          #pragma unroll
          for(int i=0;i<4;i++) sw[c][i]=ld16(rs,OFF_XNT+((lane>>3)+8*i)*81920+(80*xc+idx+((c<2||idx<24)?28*c:0))*128+(lane&7)*16);
        #pragma unroll
        for(int c=0;c<3;c++){
          #pragma unroll
          for(int i=0;i<4;i++){ const int x=lane+64*i; ldsw128(smb+c*CVS+UOF+(x&3)*1040+((x>>2)+1)*16,sw[c][i]); }
          if(lane<4) ldsw128(smb+c*CVS+UOF+lane*1040,u4{0,0,0,0}); }
        signal(smb+C_FULL,lane);
        #pragma unroll 1
        for(int c=0;c<3;c++){
          #pragma unroll
          for(int i=0;i<4;i++){ sw[0][i]=sw[1][i]; sw[1][i]=sw[2][i]; }
          conv_win(smb+c*CVS,lane,sw[2]); }
        __builtin_amdgcn_s_setprio(3);
        #pragma unroll 1
        for(int c=0;c<(idx<24?3:2);c++){ ldswait<4>(smb+C_KP+c*4,1u); conv_help(smb+c*CVS,lane,sw[0]); signal(smb+C_KP+c*4,lane);
          #pragma unroll
          for(int i=0;i<4;i++){ sw[0][i]=sw[1][i]; sw[1][i]=sw[2][i]; } } }
      __builtin_amdgcn_s_setprio(0);
      if(idx<28){ z1st(); f1(); }
      pollw<16>(rs,OFF_FW+(lane<27?lane:0)*128,tag); signal(smb+C_Z1R,lane);
    }
    ldswait<6>(smb+C_Z1R,5u);
    u4 wq[24];
    #pragma unroll
    for(int i=0;i<24;i++) wq[i]=ld16(rs,OFF_WOF+ct*(135*1024)+40960+mi*RING+((i==23&&mi==3)?22:i)*1024+lane*16);
    if(w<6){
      #pragma unroll 1
      for(u32 i=0;i<20480;i+=4096){ setm0<0>(cb+i); const u32 o=OFF_WOF+ct*(135*1024)+mi*20480+i; dma<0>(srdS,lane*16,o); dma<1024>(srdS,lane*16,o); dma<2048>(srdS,lane*16,o); dma<3072>(srdS,lane*16,o); } }
    if(w==6){ adone=__all(ld4(rs,fao)==tag); if(adone) aload(); }
    ldswait(smb+C_FREE,4u);
    #pragma unroll
    for(int i=0;i<24;i++) ldsw128(smb+RB+mi*RING+i*1024+lane*16,wq[i]);
    if(w<6) WAITV(0);
    signal(smb+C_WR,lane);
    if(!adone){ adone=__all(ld4(rs,fao)==tag); if(adone) aload(); }
    if(w==7 && b==255){
      #pragma unroll 1
      for(int sp=0;sp<SPIN;sp++){ const u32 o=OFF_FA+lane*128; u32 v0=ld4(rs,o),v1=ld4(rs,o+8192),v2=ld4(rs,o+16384),v3=ld4(rs,lane<32? o+24576 : OFF_FW+(lane-32)*128); CFENCE();
        if(__all(v0==tag&&v1==tag&&v2==tag&&v3==tag)) break; __builtin_amdgcn_s_sleep(4); }
      if(lane==0) st4(rs,OFF_EP,tag); }
  }
  int w2=w; asm volatile("" : "+s"(w2));
  if(w2<4){
    __builtin_amdgcn_s_setprio(2);
    int r=lane&15, q=lane>>4;
    const u32 rb=smb+RB+w*RING;
    u4 srdA=mksrd((const char*)p.wsp+(size_t)rtB*64*4096+w*1024-4096+256);
    const char* zb=p.ws+OFF_Z2F+(u32)(16*w*54+ft0+3)*1024;
    u32 voA[2]; { u32 lr=lane>>3,c=lane&7; voA[0]=lr*4096+((c^(lr>>1))<<4); voA[1]=voA[0]^64u; }
    u32 sofa[8];
    #pragma unroll
    for(int g=0;g<8;g++) sofa[g]=((g+2*w)&7)*32768+4096-(g&3)*1024;
    const u32 voB=lane*16;
    u32 la[2]; la[0]=rb+r*128+((q^(r>>1))<<4); la[1]=la[0]^64u;
    f4 acc[4][7]; u4 B[NB][7]; u4 A[2][4]; u2 z1[7];
    auto issueA=[&](auto sc,auto gc){ constexpr int s=sc.value,g=gc.value; if constexpr((g&3)==0) setm0<((s+1)%3)*8192+g*1024>(rb); dma<(g&3)*1024>(srdA,voA[g&1],sofa[g]);
      if constexpr(g==7){ u64 a=((u64)srdA[1]<<32|srdA[0])+128; srdA[0]=(u32)a; srdA[1]=(u32)(a>>32); } };

    u32 bsr,fv;
    auto fof=[&](){ const u32 l=voB>>4; return OFF_F1+((rtB>>1)*16+(((ft0>>1)+(l&3))>>1))*128; };
    #pragma unroll
    for(int m=0;m<4;m++){ A[0][m]=ldsr128(la[0]+m*2048+8192); A[1][m]=ldsr128(la[1]+m*2048+8192); }
    sfor<8>([&](auto g){ issueA(std::integral_constant<int,2>{},g); });
    waitA(A[0]); waitA(A[1]);
    sfor<8>([&](auto g){ issueA(std::integral_constant<int,3>{},g); });
    { const int hf=(ft0>>1)+q; pollw(rs,OFF_F2+((4*w+(lane&3))*16+(hf==0?13:(hf-1)>>1))*128,tag); }
    sfor<NB>([&](auto hh){ const char* s=zb+(u32)hh.value*(54*1024); sfor<7>([&](auto n){ gload<(n.value-3)*1024>(B[hh][n],voB,s); }); });
    auto redw=[&](auto nc){ constexpr int n=nc.value; *(LAS f4*)(rb+n*1024+voB)=acc[1][n]; *(LAS f4*)(rb+(7+n)*1024+voB)=acc[2][n]; *(LAS f4*)(rb+(14+n)*1024+voB)=acc[3][n]; };
    const char* z1b=p.ws+OFF_Z1T+(u32)((rtB*4+w)*27+(ft0>>1))*1024;
    sfor<16>([&](auto hc){
      constexpr int h=hc.value, S=h%NB, P=h&1, HZ=HZZ, cnt=g2cnt(h);
      waitB<cnt>(B[S]); waitA(A[P]);
      if constexpr(h==HZ){ tie1(fv); if(!__all(fv==tag)) pollw(rs,fof(),tag); }
      if constexpr(h+1<16 && h>0){
        #pragma unroll
        for(int m=0;m<4;m++) A[P^1][m]=ldsr128(la[(h+1)&1]+m*2048+((((h+1)>>1)+1)%3)*8192);
      }
      if constexpr(h==HZ-3) gload4<0>(fv,fof(),p.ws);
      const char* s=zb+(u32)(h+NB)*(54*1024);
      sfor<7>([&](auto n){
        if constexpr(h==0) mfma4z(acc[0][n],acc[1][n],acc[2][n],acc[3][n],B[S][n],A[P]); else mfma4(acc[0][n],acc[1][n],acc[2][n],acc[3][n],B[S][n],A[P]);
        if constexpr(h>=3 && h<=10 && (n.value&1)==0) issueA(std::integral_constant<int,(h-1)/2+3>{},std::integral_constant<int,((h-1)&1)*4+n.value/2>{});
        if constexpr(h+NB<16) gload_t<(n.value-3)*1024>(B[S][n],voB,s);
        if constexpr(h==HZ){ gload8<(n.value>>1)*1024>(z1[n],voB,z1b+((n.value&1)? ((ft0&1)?1024:8) : (ft0&1)*8));
          if constexpr(n.value==6) { gload2(bsr,(voB>>3)&30u,(const char*)(p.bsp+(rtB*4+w)*16)); } }
        if constexpr(h==13 && n.value==0) signal(smb+C_Z1R,lane);
        if constexpr(h==15){ SCHED(); if constexpr(n.value>=2) redw(std::integral_constant<int,n.value-2>{}); SCHED(); }
      });
    });
    asm volatile("s_nop 15\n\ts_nop 3" ::: "memory");
    SCHED();
    lane=__builtin_amdgcn_mbcnt_hi(~0u,__builtin_amdgcn_mbcnt_lo(~0u,0u)); asm volatile("" : "+v"(lane)); r=lane&15; q=lane>>4;
    redw(std::integral_constant<int,5>{}); redw(std::integral_constant<int,6>{});
    signal(smb+C_RED,lane);
    ldswait(smb+C_RED,4u);
    #pragma unroll
    for(int m=1;m<4;m++){ u32 src=smb+RB+((w-m)&3)*RING+(m-1)*7168+lane*16;
      #pragma unroll
      for(int n=0;n<7;n++) acc[0][n]+=*(LAS f4*)(src+n*1024); }
    asm volatile("s_waitcnt vmcnt(0) lgkmcnt(0)" : "+v"(z1[0]),"+v"(z1[1]),"+v"(z1[2]),"+v"(z1[3]),"+v"(z1[4]),"+v"(z1[5]),"+v"(z1[6]),"+v"(bsr) :: "memory");
    const float bs=bflo(bsr);
    signal(smb+C_FREE,lane);
    u2 o[7];
    #pragma unroll
    for(int n=0;n<7;n++)
      #pragma unroll
      for(int e=0;e<2;e++)o[n][e]=pkbf2(unpk(z1[n][e])*(f2{acc[0][n][2*e],acc[0][n][2*e+1]}+bs));
    const u32 ho=OFF_HF+(u32)((rtB*4+w)*54+ft0)*512+(q>>1)*256+r*16;
    #pragma unroll
    for(int n=0;n<6;n+=2){ auto sx=__builtin_amdgcn_permlane16_swap(o[n][0],o[n+1][0],false,false); auto sy=__builtin_amdgcn_permlane16_swap(o[n][1],o[n+1][1],false,false);
      if(ct<7||n>=2) st16(rs,ho+(n+(q&1))*512,u4{sx[0],sy[0],sx[1],sy[1]}); }
    st8(rs,ho+6*512+(q&1)*8,o[6]);
    WAITV(0);
    if(arrive(smb+C_H,lane)==3u && lane==0) st4(rs,OFF_FH+(rtB*8+ct)*128,tag);
    __builtin_amdgcn_s_setprio(0);
  }
  { const int r=lane&15, q=lane>>4, m=w&3, kh=w>>2;
    u2 xv[5]={};
    if(kh){
      #pragma unroll
      for(int n=0;n<5;n++) xv[n]=*(const u2*)(p.x+(rtB*64+m*16+r)*640+80*ct+16*n+4*q);
    }
    f4 acc[5]={}; u4 W[3][5];
    const u32 wl2=smb+lane*16+RB-40960+kh*14*5120, wl0=kh? wl2 : smb+lane*16, wl1=kh? wl2 : smb+lane*16+CVS-20480;
    #define WAD(k,n) (((k)<4? wl0 : (k)<8? wl1 : wl2)+((k)*5+(n))*1024)
    f4 ax[5]={};
    auto addax=[&](){
      #pragma unroll
      for(int n=0;n<5;n++){ u2 av=ldstr(smb+ATL+(16*n+4*q+(r>>2))*128+(16*m+4*(r&3))*2); ax[n]=f4{bflo(av[0]),bfhi(av[0]),bflo(av[1]),bfhi(av[1])}+f4{bflo(xv[n][0]),bfhi(xv[n][0]),bflo(xv[n][1]),bfhi(xv[n][1])}; } };
    if(w==6 && adone){ WAITV(0); signal(smb+C_AT,lane); }
    u32 oo[3],to[3]; const u32 tb=smb+CVS+m*3072;
    #pragma unroll
    for(int i=0;i<3;i++){ const int it=lane+64*i, rr=(it*205)>>11, cc=it-rr*10; oo[i]=opq((rtB*64+m*16+rr)*1280+ct*160+cc*16); to[i]=opq(tb+rr*176+cc*16); }
    if(m==0){ ldswait(smb+C_H,4u); pollw(rs,OFF_FH+(rtB*8+kh*4+(lane&3))*128,tag); signal(smb+C_TAG+kh*4,lane); } else ldswait(smb+C_TAG+kh*4,1u);
    u4 hf[14];
    const u32 ho=OFF_HF+(u32)((rtB*4+m)*27+kh*14)*1024+lane*16;
    #pragma unroll
    for(int k=0;k<HPF;k++) hf[k]=ld16(rs,ho+k*1024);
    const bool early=kh && uni(ldsv(smb+C_AT))!=0u;
    CFENCE();
    if(early) addax();
    ldswait(smb+C_WR,4u);
    const u32 wl3=kh? wl2-5120 : wl2;
    #pragma unroll
    for(int n=0;n<10;n++) W[n/5][n%5]=ldsr128(WAD(n/5,n%5));
    #pragma unroll
    for(int k=0;k<13;k++){
      if(k+HPF<14) hf[k+HPF]=ld16(rs,ho+((k+HPF==13&&kh)?12:k+HPF)*1024);
      SCHED();
      #pragma unroll
      for(int n=0;n<5;n++){ acc[n]=mfma16(W[k%3][n],hf[k],acc[n]); SCHED();
        if(k<11) W[(k+2)%3][n]=ldsr128(WAD(k+2,n)); else if(k==11) W[1][n]=ldsr128(wl3+(65+n)*1024);
        SCHED(); }
      if(k==5 && !kh) signal(smb+C_G1,lane);
    }
    const u32 pb=smb+m*5120+lane*16;
    if(!kh){
      #pragma unroll
      for(int n=0;n<5;n++) acc[n]=mfma16(W[1][n],hf[13],acc[n]);
      ldswait(smb+C_G1,4u);
      #pragma unroll
      for(int n=0;n<5;n++) *(LAS f4*)(pb+n*1024)=acc[n];
      signal(smb+C_P+m*4,lane);
    } else {
      if(w==6 && !adone){ pollw(rs,fao,tag); aload(); WAITV(0); signal(smb+C_AT,lane); }
      if(!early){ ldswait(smb+C_AT,1u); addax(); }
      ldswait(smb+C_P+m*4,1u);
      #pragma unroll
      for(int n=0;n<5;n++){ const f4 o=(acc[n]+ax[n])+*(LAS f4*)(pb+n*1024); *(LAS u2*)(tb+r*176+n*32+q*8)=u2{pkbf(o[0],o[1]),pkbf(o[2],o[3])}; }
      CFENCE();
      #pragma unroll
      for(int i=0;i<3;i++) if(i<2||lane<32) __builtin_amdgcn_raw_buffer_store_b128(ldsr128(to[i]),__builtin_amdgcn_make_buffer_rsrc(p.out,0,2048*1280,0x00020000),oo[i],0,16);
    }
  }
}
extern "C" void kernel_launch(void* const* d_in,const int* in_sizes,int n_in,void* d_out,int out_size,void* d_ws,size_t ws_size,hipStream_t stream){
  Params p;
  p.x=(const u16*)d_in[0]; p.lar=(const u16*)d_in[1]; p.aim=(const u16*)d_in[2]; p.cre=(const u16*)d_in[3]; p.cim=(const u16*)d_in[4];
  p.ldt=(const u16*)d_in[5]; p.dsk=(const u16*)d_in[6]; p.win=(const u16*)d_in[8]; p.wsp=(const u16*)d_in[9]; p.bsp=(const u16*)d_in[10]; p.wout=(const u16*)d_in[11];
  p.out=(u16*)d_out; p.ws=(char*)d_ws;
  (void)hipFuncSetAttribute((const void*)fused,hipFuncAttributeMaxDynamicSharedMemorySize,LDS_TOTAL);
  fused<<<256,512,LDS_TOTAL,stream>>>(p);
}
```

```cpp
#include <hip/hip_runtime.h>
#include <utility>
#pragma clang diagnostic ignored "-Wint-to-pointer-cast"
typedef unsigned short u16;
typedef unsigned int u32;
typedef unsigned long long u64;
typedef __attribute__((ext_vector_type(8))) __bf16 bfx8;
typedef __attribute__((ext_vector_type(8))) _Float16 hfx8;
typedef __attribute__((ext_vector_type(2))) __bf16 bf2;
typedef __attribute__((ext_vector_type(2))) _Float16 hf2;
typedef __attribute__((ext_vector_type(2))) float f2;
typedef __attribute__((ext_vector_type(4))) float f4;
typedef __attribute__((ext_vector_type(16))) float f16v;
typedef __attribute__((ext_vector_type(4))) u32 u4;
typedef __attribute__((ext_vector_type(2))) u32 u2;
typedef __attribute__((ext_vector_type(4))) short s4;
#define LAS __attribute__((address_space(3)))
#define DI __device__ __forceinline__

constexpr u32 OFF_Z2F = 0;
constexpr u32 OFF_Z1T = OFF_Z2F + 64*54*1024;
constexpr u32 OFF_XNT = OFF_Z1T + 128*27*1024;
constexpr u32 OFF_WOF = OFF_XNT + 640*4096;
constexpr u32 OFF_HF  = OFF_WOF + 8*27*5*1024;
constexpr u32 OFF_AT  = OFF_HF + 128*27*1024;
constexpr u32 OFF_ST  = OFF_AT + 640*4096;
constexpr u32 OFF_RS  = OFF_ST + 16*14*2048+8192;
constexpr u32 OFF_EP  = OFF_RS + 2048*16;
constexpr u32 OFF_F2  = OFF_EP + 256;
constexpr u32 OFF_F1  = OFF_F2 + 256*128;
constexpr u32 OFF_FX  = OFF_F1 + 256*128;
constexpr u32 OFF_FW  = OFF_FX + 32*128;
constexpr u32 OFF_FH  = OFF_FW + 32*128;
constexpr u32 OFF_FA  = OFF_FH + 256*128;
constexpr u32 WS_END  = OFF_FA + 256*128;

struct Params {
  const u16 *x, *lar, *aim, *cre, *cim, *ldt, *dsk, *win, *wsp, *bsp, *wout;
  u16* out; char* ws;
};
template<class F,int... I> DI void sfor_impl(F&& f, std::integer_sequence<int,I...>){ (f(std::integral_constant<int,I>{}),...); }
template<int N,class F> DI void sfor(F&& f){ sfor_impl(f, std::make_integer_sequence<int,N>{}); }

DI float bflo(u32 v){ return __uint_as_float(v<<16); }
DI float bfhi(u32 v){ return __uint_as_float(v&0xffff0000u); }
DI u32 pkbf(float a,float b){ f2 v={a,b}; return __builtin_bit_cast(u32,__builtin_convertvector(v,bf2)); }
DI u32 pkhf(float a,float b){ f2 v={a,b}; return __builtin_bit_cast(u32,__builtin_convertvector(v,hf2)); }
DI float rbf(float a){ return bflo(pkbf(a,a)); }
DI float hlo(u32 v){ return (float)__builtin_bit_cast(hf2,v)[0]; }
DI float hhi(u32 v){ return (float)__builtin_bit_cast(hf2,v)[1]; }
DI float dot2(u32 a,u32 b,float c){ return __builtin_amdgcn_fdot2_f32_bf16(__builtin_bit_cast(bf2,a),__builtin_bit_cast(bf2,b),c,false); }
DI f4 mfma16(u4 a,u4 b,f4 c){ return __builtin_amdgcn_mfma_f32_16x16x32_bf16(__builtin_bit_cast(bfx8,a),__builtin_bit_cast(bfx8,b),c,0,0,0); }
DI f16v mfma32h(u4 a,u4 b,f16v c){ return __builtin_amdgcn_mfma_f32_32x32x16_f16(__builtin_bit_cast(hfx8,a),__builtin_bit_cast(hfx8,b),c,0,0,0); }
DI u4 ldsr128(u32 a){ return *(LAS u4*)a; }
DI u2 ldsr64(u32 a){ return *(LAS u2*)a; }
DI void ldsw128(u32 a,u4 v){ *(LAS u4*)a=v; }
DI u2 ldstr(u32 a){ return __builtin_bit_cast(u2,__builtin_amdgcn_ds_read_tr16_b64_v4i16((LAS s4*)a)); }
DI u32 uni(u32 v){ return __builtin_amdgcn_readfirstlane(v); }

template<int OFF> DI void gload(u4& dst,u32 voff,const char* base){ asm volatile("global_load_dwordx4 %0, %1, %2 offset:%3 sc1" : "=v"(dst) : "v"(voff), "s"(base), "n"(OFF)); }
template<int OFF> DI void gload_t(u4& dst,u32 voff,const char* base){ asm volatile("global_load_dwordx4 %0, %1, %2 offset:%3 sc1" : "+v"(dst) : "v"(voff), "s"(base), "n"(OFF)); }
DI void mfma4(f4& c0,f4& c1,f4& c2,f4& c3,const u4& a,const u4 (&b)[4]){
  asm volatile("v_mfma_f32_16x16x32_bf16 %0, %4, %5, %0\n\tv_mfma_f32_16x16x32_bf16 %1, %4, %6, %1\n\tv_mfma_f32_16x16x32_bf16 %2, %4, %7, %2\n\tv_mfma_f32_16x16x32_bf16 %3, %4, %8, %3"
    : "+v"(c0),"+v"(c1),"+v"(c2),"+v"(c3) : "v"(a),"v"(b[0]),"v"(b[1]),"v"(b[2]),"v"(b[3])); }
DI void mfma4z(f4& c0,f4& c1,f4& c2,f4& c3,const u4& a,const u4 (&b)[4]){
  asm volatile("v_mfma_f32_16x16x32_bf16 %0, %4, %5, 0\n\tv_mfma_f32_16x16x32_bf16 %1, %4, %6, 0\n\tv_mfma_f32_16x16x32_bf16 %2, %4, %7, 0\n\tv_mfma_f32_16x16x32_bf16 %3, %4, %8, 0"
    : "=&v"(c0),"=&v"(c1),"=&v"(c2),"=&v"(c3) : "v"(a),"v"(b[0]),"v"(b[1]),"v"(b[2]),"v"(b[3])); }
template<int CNT> DI void waitB(u4 (&B)[7]){ asm volatile("s_waitcnt vmcnt(%7)" : "+v"(B[0]),"+v"(B[1]),"+v"(B[2]),"+v"(B[3]),"+v"(B[4]),"+v"(B[5]),"+v"(B[6]) : "n"(CNT) : "memory"); }
DI void waitA(u4 (&A)[4]){ asm volatile("s_waitcnt lgkmcnt(0)" : "+v"(A[0]),"+v"(A[1]),"+v"(A[2]),"+v"(A[3]) :: "memory"); }
#define WAITV(n) asm volatile("s_waitcnt vmcnt(%0)" :: "n"(n) : "memory")
#define WAITL0() asm volatile("s_waitcnt lgkmcnt(0)" ::: "memory")
#define CFENCE() asm volatile("" ::: "memory")
#define SCHED() __builtin_amdgcn_sched_barrier(0)
DI void bar(){ asm volatile("s_waitcnt lgkmcnt(0)\n\ts_barrier" ::: "memory"); }
DI f2 unpk(u32 v){ return f2{bflo(v),bfhi(v)}; }
DI u32 pkbf2(f2 v){ return __builtin_bit_cast(u32,__builtin_convertvector(v,bf2)); }
DI u32 pkhf2(f2 v){ return __builtin_bit_cast(u32,__builtin_convertvector(v,hf2)); }
DI f2 fma2(f2 a,f2 b,f2 c){ return __builtin_elementwise_fma(a,b,c); }
DI f2 gelu2(f2 x){
  f2 u=x*fma2(x*x,f2{-0.10294324f,-0.10294324f},f2{-2.30220820f,-2.30220820f});
  f2 d=f2{__builtin_amdgcn_exp2f(u[0]),__builtin_amdgcn_exp2f(u[1])}+f2{1.f,1.f};
  return x*f2{__builtin_amdgcn_rcpf(d[0]),__builtin_amdgcn_rcpf(d[1])};
}
DI void ln_fin(float mean,float var,float& m,float& rinv){
  m=rbf(mean); float v=rbf(__builtin_fmaxf(var,0.f));
  float s=rbf(__builtin_amdgcn_sqrtf(rbf(v+1e-5f))); float r=__builtin_amdgcn_rcpf(s); rinv=__builtin_fmaf(__builtin_fmaf(-s,r,1.f),r,r);
}
typedef __amdgpu_buffer_rsrc_t rsrc_t;
DI void st16(rsrc_t rs,u32 off,u4 v){ __builtin_amdgcn_raw_buffer_store_b128(v,rs,off,0,16); }
DI void st8(rsrc_t rs,u32 off,u2 v){ __builtin_amdgcn_raw_buffer_store_b64(v,rs,off,0,16); }
DI void st4(rsrc_t rs,u32 off,u32 v){ __builtin_amdgcn_raw_buffer_store_b32(v,rs,off,0,16); }
DI u4 ld16(rsrc_t rs,u32 off){ return __builtin_amdgcn_raw_buffer_load_b128(rs,off,0,16); }
DI u32 ld4(rsrc_t rs,u32 off){ return __builtin_amdgcn_raw_buffer_load_b32(rs,off,0,16); }
constexpr int SPIN=1<<17;
constexpr u32 TAGINC=0x9E3779B9u;
template<int SL=2> DI void pollw(rsrc_t rs,u32 off,u32 tag){
  #pragma unroll 1
  for(int s=0;s<SPIN;s++){ u32 v=ld4(rs,off); CFENCE(); if(__all(v==tag)) break; __builtin_amdgcn_s_sleep(SL); }
}

DI f16v mfma32b(u4 a,u4 b,f16v c){ return __builtin_amdgcn_mfma_f32_32x32x16_bf16(__builtin_bit_cast(bfx8,a),__builtin_bit_cast(bfx8,b),c,0,0,0); }
DI u4 mksrd(const void* b){ u64 a=(u64)b; return u4{(u32)a,(u32)(a>>32)&0xffffu,0xffffffffu,0x00020000u}; }
template<int IMM> DI void setm0(u32 base){ asm volatile("s_add_u32 m0, %0, %1\n\ts_nop 0" :: "s"(base), "n"(IMM) : "scc","memory"); }
template<int OFF> DI void dma(u4 srd,u32 voff,u32 soff){ asm volatile("buffer_load_dwordx4 %0, %1, %2 offen offset:%3 sc1 lds" :: "v"(voff), "s"(srd), "s"(soff), "n"(OFF) : "memory"); }
DI void touch(u4 srd,u32 voff,u32 soff){ asm volatile("buffer_load_dword %0, %1, %2 offen sc1 lds" :: "v"(voff), "s"(srd), "s"(soff) : "memory"); }
template<int OFF> DI void gload8(u2& dst,u32 voff,const char* base){ asm volatile("global_load_dwordx2 %0, %1, %2 offset:%3 sc1" : "=v"(dst) : "v"(voff), "s"(base), "n"(OFF)); }
DI void gload2(u32& dst,u32 voff,const char* base){ asm volatile("global_load_ushort %0, %1, %2" : "=v"(dst) : "v"(voff), "s"(base)); }
DI u32 arrive(u32 a,int){ u32 o; asm volatile("s_mov_b64 exec, 1\n\tds_add_rtn_u32 %0, %1, %2\n\ts_mov_b64 exec, -1\n\ts_waitcnt lgkmcnt(0)" : "=&v"(o) : "v"(a), "v"(1u) : "memory"); return uni(o); }
template<int SL=1> DI void ldswait(u32 a,u32 n){ while(__hip_atomic_load((LAS u32*)a,__ATOMIC_RELAXED,__HIP_MEMORY_SCOPE_WORKGROUP)<n) __builtin_amdgcn_s_sleep(SL); CFENCE(); }
DI void tie1(u32& g){ asm volatile("" : "+v"(g) :: "memory"); }
DI u32 opq(u32 v){ asm volatile("" : "+v"(v)); return v; }

constexpr u32 STG=32768;
constexpr u32 RING=24576, RB=62848, CONV=0, KP=2208, UOF=16768, CVS=UOF+4160, S7O=4*KP, S9O=S7O+2112, KZO=S9O+2112, MISC=RB+4*RING, JNK=MISC+2048, PM=163840-128, LDS_TOTAL=163840;
constexpr u32 ATL=2*CVS;
constexpr u32 C_FULL=PM, C_G1=PM+4, C_Z2=PM+8, C_Z1=PM+12, C_RED=PM+16, C_H=PM+20, C_FREE=PM+24, C_CV=PM+28, C_Z1R=PM+32, C_TAG=PM+40, C_KP=PM+48, C_DONE=PM+64, C_CS=PM+72, C_P=PM+96, C_AT=PM+112, C_WR=PM+116;
constexpr u32 CODE_BYTES=37888;
constexpr int HPF=5;
DI void signal(u32 a,int){ asm volatile("s_mov_b64 exec, 1\n\tds_add_u32 %0, %1\n\ts_mov_b64 exec, -1" :: "v"(a), "v"(1u) : "memory"); }
template<int OFF> DI void gload4(u32& dst,u32 voff,const char* base){ asm volatile("global_load_dword %0, %1, %2 offset:%3 sc1" : "=v"(dst) : "v"(voff), "s"(base), "n"(OFF)); }

DI f2 cmulp(f2 a,f2 b,f2 bs){ return fma2(f2{a[1],a[1]},bs,f2{a[0],a[0]}*b); }
DI u32 ldsv(u32 a){ return __hip_atomic_load((LAS u32*)a,__ATOMIC_RELAXED,__HIP_MEMORY_SCOPE_WORKGROUP); }
DI f2 cpow_dir(float lr,float rv,float pw){ float e=__builtin_amdgcn_exp2f(lr*pw); float f=__builtin_amdgcn_fractf(rv*pw);
  return f2{e*__builtin_amdgcn_cosf(f),e*__builtin_amdgcn_sinf(f)}; }
DI u32 dup(u32 a){ return __builtin_amdgcn_perm(a,a,0x01000100u); }
DI u32 cat(u32 lo,u32 hi){ return __builtin_amdgcn_perm(hi,lo,0x05040100u); }
DI u32 lds16(u32 a){ return *(LAS u16*)a; }

DI float kgen_tab(u32 cb,int lane,const u32 (&pr)[6],float (&kp)[11]){
  const float dt=rbf(__expf(bflo(pr[4])));
  const float cr[2]={bflo(pr[2]),bfhi(pr[2])}, cm[2]={bflo(pr[3]),bfhi(pr[3])};
  float mx=fmaxf(fmaxf(fabsf(cr[0]),fabsf(cm[0])),fmaxf(fabsf(cr[1]),fabsf(cm[1])));
  u32 eb=__float_as_uint(mx)&0x7f800000u;
  #define DPPMAX(c) eb=max(eb,(u32)__builtin_amdgcn_update_dpp(0,(int)eb,c,0xf,0xf,false))
  DPPMAX(0x128); DPPMAX(0x124); DPPMAX(0x122); DPPMAX(0x121);
  eb=max(max((u32)__builtin_amdgcn_readlane(eb,0),(u32)__builtin_amdgcn_readlane(eb,16)),max((u32)__builtin_amdgcn_readlane(eb,32),(u32)__builtin_amdgcn_readlane(eb,48))); eb=eb<0x01000000u?0x3f800000u:(eb>0x7e000000u?0x7e000000u:eb);
  const float ksc=2.f*dt*__uint_as_float(eb), isc=__uint_as_float(0x7f000000u-eb);
  const int r=lane&31,h=lane>>5;
  const u32 TP=272, TA=cb, TB=cb+22*TP;
  f16v acc={};
  f2 an[2][4];
  #pragma unroll
  for(int c=0;c<2;c++){ const float l_a=c?bfhi(pr[0]):bflo(pr[0]), i_m=c?bfhi(pr[1]):bflo(pr[1]);
    const float lr=(-rbf(__expf(l_a))*dt)*1.44269504f, rv=(i_m*dt)*0.15915494f;
    an[c][0]=cpow_dir(lr,rv,1.f); an[c][1]=cpow_dir(lr,-rv,8.f); an[c][2]=cpow_dir(lr,-rv,16.f); an[c][3]=cpow_dir(lr,rv,256.f); }
  u32 hold[54]; const u32 wt=TA+lane*4;
  struct P2 { f2 v[2]; };
  auto mul=[&](const P2& x,int k){ P2 o;
    #pragma unroll
    for(int c=0;c<2;c++) o.v[c]=cmulp(x.v[c],an[c][k],f2{-an[c][k][1],an[c][k][0]});
    return o; };
  auto put=[&](int row,const P2& x){ *(LAS u32*)(wt+row*TP)=pkhf2(x.v[0]); hold[row]=pkhf2(x.v[1]); };
  P2 e={{f2{1.f,0.f},f2{1.f,0.f}}}, o={{an[0][1],an[1][1]}}, bk[8], t;
  bk[0].v[0]=f2{cr[0]*isc,cm[0]*isc}; bk[0].v[1]=f2{cr[1]*isc,cm[1]*isc}; put(0,bk[0]); t=bk[0];
  constexpr int tr[8]={8,10,11,16,17,20,21,18};
  #pragma unroll
  for(int k=0;k<16;k++){ put(22+2*k,e); put(23+2*k,o);
    if(k<15){ if(k) e=mul(e,2); else { e.v[0]=an[0][2]; e.v[1]=an[1][2]; } o=mul(o,2); }
    if(k<8){ t=mul(t,3); put(tr[k],t); }
    if(k<7){ bk[k+1]=mul(bk[k],0); put(k+1,bk[k+1]); }
    if(k==8){ put(9,mul(bk[2],3)); put(13,mul(bk[6],3)); t=mul(bk[4],3); put(12,t); }
    if(k==9||k==10){ t=mul(t,3); put(5+k,t); }
    SCHED(); }
  const int ra=(r>21||r==19)?0:r;
  #pragma unroll
  for(int c=0;c<2;c++){
    CFENCE();
    u4 fa[8],fb[8];
    #pragma unroll
    for(int ks=0;ks<8;ks++){ fa[ks]=ldsr128(TA+ra*TP+ks*32+h*16); fb[ks]=ldsr128(TB+r*TP+ks*32+h*16); }
    CFENCE();
    if(c==0){
      #pragma unroll
      for(int i=0;i<54;i++) if(i!=19) *(LAS u32*)(wt+i*TP)=hold[i]; }
    #pragma unroll
    for(int ks=0;ks<8;ks++) acc=mfma32h(fa[ks],fb[ks],acc);
  }
  CFENCE();
  #pragma unroll
  for(int i=0;i<11;i++) kp[i]=acc[i];
  return ksc;
}
DI void kgen_exp(u32 cb,int lane,const float (&acc)[11],const u32 (&ga)[11]){
  const int r=lane&31,h=lane>>5;
  { const u32 a4=cb+r*16+h*8, a5=cb+r*16+h*1024, az=cb+KZO+r*2+h*128;
    #define PK(addr,base,reg) do{ *(LAS _Float16*)((addr)+2*(base))=(_Float16)acc[reg]; }while(0)
    PK(a4,0,0);PK(a4,1,1);PK(a4,2,2);PK(a4,3,3); PK(a4,256,4);PK(a4,258,5);PK(a4,512,6);PK(a4,768,7);
    PK(a5,1024,8); PK(az,255,10); PK(az,127,8); PK(az,159,9);
    #undef PK
    CFENCE();
    if(lane<16) ldsw128(cb+KZO+lane*16,u4{0,0,0,0});
    CFENCE();
  }
  { const u32 g0=cb+2*(1024-8*lane);
    u32 f2_=lds16(g0+(lane?8:0)), a2=lds16(g0), m2=lds16(g0-8), b2=lds16(g0-16);
    u32 e[11];
    #pragma unroll
    for(int j=0;j<11;j++) e[j]=lds16(ga[j]);
    u32 k01=*(LAS u32*)cb, k23=*(LAS u32*)(cb+4), kl=lds16(cb+2048);
    WAITL0();
    const u32 wo=cb+96+lane*16;
    { u32 aa=dup(a2),mm=dup(m2),bb=dup(b2),am=cat(a2,m2),fa=cat(f2_,a2),mb=cat(m2,b2);
      ldsw128(wo,u4{aa,am,mm,bb}); ldsw128(wo+KP,u4{aa,aa,mm,mb}); ldsw128(wo+2*KP,u4{aa,aa,am,mm}); ldsw128(wo+3*KP,u4{fa,aa,aa,mm}); }
    { u32 o0=cat(e[0],e[1]),o1=cat(e[2],e[3]),o2=cat(e[4],e[5]),o3=cat(e[6],e[7]),o4=cat(e[8],e[9]);
      u32 p1=cat(e[1],e[2]),d0=cat(e[3],e[4]),d1=cat(e[5],e[6]),d2=cat(e[7],e[8]),d3=cat(e[9],e[10]);
      ldsw128(wo+1024,u4{d0,d1,d2,d3}); ldsw128(wo+1024+KP,u4{o1,o2,o3,o4}); ldsw128(wo+1024+2*KP,u4{p1,d0,d1,d2}); ldsw128(wo+1024+3*KP,u4{o0,o1,o2,o3}); }
    if(lane<10){ if(lane){k01=0;k23=0;} if(lane!=9) kl=0;
      const u32 d0=k01&0xffffu, o1=__builtin_amdgcn_alignbit(k01,k01,16), o0=__builtin_amdgcn_alignbit(k23,k23,16), p1=(k23&0xffffu)|(k01&0xffff0000u), kk=dup(kl), zk=kl<<16;
      const u32 w2=lane<4? wo+2048 : wo-160;
      ldsw128(w2,u4{d0,0,kk,kk}); ldsw128(w2+KP,u4{o1,0,zk,kk}); ldsw128(w2+2*KP,u4{p1,d0,0,kk}); ldsw128(w2+3*KP,u4{o0,o1,0,zk}); }
  }
}
DI f4 mfma16h(u4 a,u4 b,f4 c){ return __builtin_amdgcn_mfma_f32_16x16x32_f16(__builtin_bit_cast(hfx8,a),__builtin_bit_cast(hfx8,b),c,0,0,0); }
DI u4 lds2x64(u32 a){ const u2 lo=ldsr64(a), hi=ldsr64(a+8); return u4{lo[0],lo[1],hi[0],hi[1]}; }
DI void conv_win(u32 cb,int lane,u4 (&sw)[4]){
  float P[25]; P[0]=0.f;
  #pragma unroll
  for(int c=0;c<4;c++){ const int X=2*lane-2+c; const u4 v=ldsr128(cb+UOF+(X&3)*1040+((X>>2)+1)*16);
    #pragma unroll
    for(int e=0;e<8;e++){ const int i=8*c+e-4; if(i>=0&&i<24) P[i+1]=P[i]+((e&1)?hhi(v[e>>1]):hlo(v[e>>1])); } }
  #pragma unroll
  for(int k=0;k<8;k++){ sw[k>>2][k&3]=pkhf(P[2*k+8]-P[2*k+1],P[2*k+9]-P[2*k+2]); sw[2+(k>>2)][k&3]=pkhf(P[2*k+9]-P[2*k],P[2*k+10]-P[2*k+1]); }
}
DI void conv_help(u32 cb,int lane,const u4 (&sw)[4]){
  const int r=lane&15,q=lane>>4; f4 y[2][2]={};
  ldsw128(cb+S7O+48+lane*32,sw[0]); ldsw128(cb+S7O+64+lane*32,sw[1]); ldsw128(cb+S9O+48+lane*32,sw[2]); ldsw128(cb+S9O+64+lane*32,sw[3]);
  if(lane<3){ ldsw128(cb+S7O+lane*16,u4{0,0,0,0}); ldsw128(cb+S9O+lane*16,u4{0,0,0,0}); }
  CFENCE();
  const u32 ka=cb+(r&3)*KP+16*q-8*(r>>2)+64, ub=cb+UOF+q*1040+(r+1)*16;
  const u4 a0=lds2x64(ka+32), a1=lds2x64(ka), b0=ldsr128(ub), b1=ldsr128(ub+256), a2=lds2x64(ka-32), b2=ldsr128(ub-16), b3=ldsr128(ub+240);
  const u32 sa=cb+(((r>>2)&1)?S9O:S7O)+2*(64*q+24-8*(r>>2)+4*(r&3)), kz=cb+KZO+8*(r-2*q+23); u4 bz[4],fa[4][2];
  #pragma unroll
  for(int ks=0;ks<4;ks++) bz[ks]=lds2x64(kz+(3-ks)*64);
  #pragma unroll
  for(int i=0;i<8;i++){ const u2 lo=ldstr(sa+(i>>1)*512+(i&1)*32+64), hi=ldstr(sa+(i>>1)*512+(i&1)*32); fa[i>>1][i&1]=u4{lo[0],lo[1],hi[0],hi[1]}; }
  SCHED();
  y[0][0]=mfma16h(a2,b2,y[0][0]); y[0][1]=mfma16h(a2,b3,y[0][1]);
  y[0][0]=mfma16h(a0,b0,y[0][0]); y[1][0]=mfma16h(a1,b0,y[1][0]); y[0][1]=mfma16h(a0,b1,y[0][1]); y[1][1]=mfma16h(a1,b1,y[1][1]);
  #pragma unroll
  for(int ks=0;ks<4;ks++)
    #pragma unroll
    for(int m=0;m<2;m++){ y[m][1]=mfma16h(fa[ks][m],bz[ks],y[m][1]); if(ks<2) y[m][0]=mfma16h(fa[ks][m],bz[ks+2],y[m][0]); }
  CFENCE();
  #pragma unroll
  for(int i=0;i<4;i++) *(LAS f4*)(cb+S7O+i*1024+lane*16)=y[i&1][i>>1];
}
#define RDA(S,i,m) asm volatile("ds_read2_b64 %0, %1 offset0:%2 offset1:%3" : "=v"(AF[S][i][m]) : "v"(ka), "n"((1-(i))*128+(1-(m))*4), "n"((1-(i))*128+(1-(m))*4+1))
#define RDB(S,i,ad,o) asm volatile("ds_read_b128 %0, %1 offset:%2" : "=v"(BF[S][i]) : "v"(ad), "n"(o))
#define TIEA(S,i) "+v"(AF[S][i][0]),"+v"(AF[S][i][1])
#define CV_M(S,i,n) do{ y[0][n]=mfma16h(AF[S][i][0],BF[S][(n)-(i)],y[0][n]); y[1][n]=mfma16h(AF[S][i][1],BF[S][(n)-(i)],y[1][n]); SCHED(); }while(0)
#define CV_U0() u32 u0=u1-256; u0=((int)u0<(int)uz)?uz:u0
template<class CB> DI void conv_wave(CB&& mid,rsrc_t rs,int d,u32 cb,u32 flag,int lane,float ksc,float dsk){
  const int r=lane&15,q=lane>>4; u32 ka=cb+(r&3)*KP+16*q-8*(r>>2)+1088; const u32 uz=cb+UOF+q*1040; u32 u1=uz+(r+17)*16; u4 AF[2][2][2]; u4 BF[2][4]; f4 y[2][4]={};
  #define CV_WT(S) asm volatile("s_waitcnt lgkmcnt(0)" : TIEA(S,0),TIEA(S,1),"+v"(BF[S][0]),"+v"(BF[S][1]),"+v"(BF[S][2]),"+v"(BF[S][3]))
  #define CV_STEP(C,N) do{ CV_U0(); CV_M(C,0,0); RDA(N,0,0); RDB(N,0,u0,0); RDA(N,0,1); SCHED(); CV_M(C,0,1); RDB(N,1,u1,0); RDA(N,1,0); RDB(N,2,u1,256); SCHED(); CV_M(C,0,2); RDA(N,1,1); RDB(N,3,u1,512); \
      ka-=64; u1-=16; SCHED(); CV_M(C,0,3); CV_M(C,1,1); CV_M(C,1,2); CV_M(C,1,3); CV_WT(N); }while(0)
  { CV_U0(); RDA(0,0,0); RDA(0,0,1); RDA(0,1,0); RDA(0,1,1); RDB(0,0,u0,0); RDB(0,1,u1,0); RDB(0,2,u1,256); RDB(0,3,u1,512); ka-=64; u1-=16; CV_WT(0); }
  #pragma unroll 1
  for(int dl=0;dl<8;dl++){ CV_STEP(0,1); if(dl==7){ ka+=64; u1+=16; } CV_STEP(1,0); }
  mid();
  u2 uv[2][4];
  #pragma unroll
  for(int n=0;n<4;n++)
    #pragma unroll
    for(int m=0;m<2;m++) uv[m][n]=ldsr64(cb+UOF+(2*m+(q>>1))*1040+(16*n+r+1)*16+8*(q&1));
  const u32 sc=cb+UOF+r*64+q*8;
  auto fin=[&](int n){
    #pragma unroll
    for(int m=0;m<2;m++){ u2 o;
      #pragma unroll
      for(int e=0;e<2;e++) o[e]=pkbf2(fma2(f2{hlo(uv[m][n][e]),hhi(uv[m][n][e])},f2{dsk,dsk},f2{y[m][n][2*e],y[m][n][2*e+1]}*ksc));
      *(LAS u2*)(sc+n*1024+m*32)=o; } };
  ldswait(flag,2u);
  { f4 hp[4];
    #pragma unroll
    for(int i=0;i<4;i++) hp[i]=*(LAS f4*)(cb+S7O+i*1024+lane*16);
    fin(0); fin(1);
    #pragma unroll
    for(int i=0;i<4;i++) y[i&1][2+(i>>1)]+=hp[i];
    fin(2); fin(3); }
  CFENCE();
  u4 ov[4];
  #pragma unroll
  for(int n=0;n<4;n++) ov[n]=ldsr128(cb+UOF+n*1024+lane*16);
  #pragma unroll
  for(int n=0;n<4;n++) st16(rs,OFF_AT+d*4096+n*1024+lane*16,ov[n]);
  WAITV(0);
}

constexpr int NB=3, HZZ=14;
constexpr int g2ops(int h){ return h<0? 7 : (h+NB<16?7:0)+((h>=3&&h<=10)?4:0)+(h==HZZ-3?1:0)+(h==HZZ?8:0); }
constexpr int g2cnt(int h){ int c=0; for(int i=h-NB+1;i<h;i++) c+=g2ops(i); return c; }
struct Frag { u4 x[4], w[4][2]; };
__global__ __launch_bounds__(512,2) void fused(Params p){
  extern __shared__ __attribute__((aligned(16))) char sm[];
  const u32 smb=(u32)(size_t)(LAS char*)sm;
  u64 pc0; asm volatile("s_getpc_b64 %0" : "=s"(pc0));
  const int b=blockIdx.x, xc=b&7, idx=b>>3;
  const int tid=threadIdx.x, w=uni(tid>>6); int lane=tid&63;
  auto cpf=[&](u32 o){ u4 sc=mksrd((const void*)(pc0&~127ull)); sc[2]=CODE_BYTES; setm0<JNK>(smb); touch(sc,lane*128,o); };
  if(idx>=28) cpf(w*8192);
  SCHED();
  const int rt=4*(xc>>1)+(idx&3), j=7*(xc&1)+(idx>>2), mi=w&3, g1=w>>2;
  const u32 kr0=(((j+rt)&3)*5)>>1;
  u32 vx0,vx1,vw1,vw2,k0=kr0*128,k0w=kr0*(64*3456),k1=k0+128,k1w=k0w+64*3456; const u32 mw=smb+w*4096;
  { const u32 lr=lane>>3,c=lane&7; vx0=(rt*128+16*w+lr)*1280+((c^(lr>>1))<<4); vx1=(vx0^64u)+9216;
    vw1=(8*w+lr)*3456+((c^((lr&2)<<1))<<4)+j*128; vw2=vw1+1024+(j==13?0:14*128); }
  asm volatile("" : "+v"(vx0),"+v"(vx1),"+v"(vw1),"+v"(vw2),"+s"(k0),"+s"(k0w),"+s"(k1),"+s"(k1w));
  setm0<0>(mw);
  SCHED();
  const u4 srdX=mksrd(p.x), srdW=mksrd((const char*)p.win-3072);
  if(idx<28){ dma<0>(srdX,vx0,k0); dma<1024>(srdX,vx1,k0); dma<2048>(srdW,vw2,k0w); dma<3072>(srdW,vw1,k0w);
    asm volatile("s_barrier" ::: "memory");
    setm0<STG>(mw); dma<0>(srdX,vx0,k1); dma<1024>(srdX,vx1,k1); dma<2048>(srdW,vw2,k1w); dma<3072>(srdW,vw1,k1w); }
  SCHED();
  const rsrc_t rs=__builtin_amdgcn_make_buffer_rsrc(p.ws,0,WS_END,0x00020000);
  const u4 srdS=mksrd(p.ws);
  const int rtB=8*(xc>>1)+(idx&7), ct=4*(xc&1)+(idx>>3);
  const bool hasc=idx<28 && idx+28*(w&3)<80 && w>=4;
  const int dch=80*xc+(hasc? idx+28*(w&3) : 0);
  const u32 cb=smb+CONV+(w&3)*CVS;
  u32 tag; float ksc=0.f, dsk=0.f;
  u32 zp[2][8]={};
  auto z1st=[&](){ const int r=lane&31,h=lane>>5;
    #pragma unroll
    for(int n=0;n<2;n++) if(2*j+n<27){ const u32 oo=OFF_Z1T+((rt*8+2*mi+(r>>4))*27+2*j+n)*1024+(h*16+(r&15))*16;
      st16(rs,oo,u4{zp[n][0],zp[n][1],zp[n][4],zp[n][5]}); st16(rs,oo+512,u4{zp[n][2],zp[n][3],zp[n][6],zp[n][7]}); } };
  auto f1=[&](){ WAITV(0); if(arrive(smb+C_Z1,lane)==3u && lane==0) st4(rs,OFF_F1+(rt*16+j)*128,tag); };
  auto pre01=[&](){
    const u4 sa=mksrd((const char*)p.wsp+(size_t)rtB*64*4096+mi*1024-4096); const u32 v0=(lane>>3)*4096+(((lane&7)^(lane>>4))<<4);
    #pragma unroll 1
    for(int i=0;i<4;i++){ setm0<RB+8192>(smb+mi*RING+i*4096); const u32 g=(i&1)*4+2*mi, k=(i>>1)*128+4096;
      dma<0>(sa,v0,(g&7)*32768+k); dma<1024>(sa,v0^64u,(g&7)*32768+32768-1024+k); dma<2048>(sa,v0,((g+2)&7)*32768-2048+k); dma<3072>(sa,v0^64u,((g+2)&7)*32768+32768-3072+k); } };
  if(idx>=28){
    const int a=xc*4+idx-28, r=lane&15, q=lane>>4;
    const u32 ep=ld4(rs,OFF_EP);
    u4 xr[10]; const char* xs=(const char*)p.x+(size_t)(64*a+8*w+(lane>>3))*1280+(lane&7)*16;
    #pragma unroll
    for(int i=0;i<10;i++) xr[i]=*(const u4*)(xs+i*128);
    { const u32 l=(a*8+w)*19+(lane<19?lane:18); setm0<JNK>(smb); touch(srdS,(l<3584? OFF_ST/128+l : OFF_F2/128+(l-3584<1088?l-3584:1087))*128,0u); }
    if(tid<32) *(LAS u32*)(smb+PM+tid*4)=0u;
    tag=uni(ep)+TAGINC;
    float s0=0.f,s1=0.f,q0=0.f,q1=0.f;
    #pragma unroll
    for(int i=0;i<10;i++){ s0=dot2(xr[i][0],0x3f803f80u,s0); q0=dot2(xr[i][0],xr[i][0],q0); s1=dot2(xr[i][1],0x3f803f80u,s1); q1=dot2(xr[i][1],xr[i][1],q1);
      s0=dot2(xr[i][2],0x3f803f80u,s0); q0=dot2(xr[i][2],xr[i][2],q0); s1=dot2(xr[i][3],0x3f803f80u,s1); q1=dot2(xr[i][3],xr[i][3],q1); }
    s0+=s1; q0+=q1;
    #define DPPADD(v,c) v+=__int_as_float(__builtin_amdgcn_update_dpp(0,__float_as_int(v),c,0xf,0xf,false))
    DPPADD(s0,0xB1); DPPADD(q0,0xB1); DPPADD(s0,0x4E); DPPADD(q0,0x4E); DPPADD(s0,0x141); DPPADD(q0,0x141);
    float m1,ri; { float mean=s0*(1.f/640.f); ln_fin(mean,q0*(1.f/640.f)-mean*mean,m1,ri); }
    if((lane&7)==0) st16(rs,OFF_RS+(64*a+8*w+(lane>>3))*16,u4{__float_as_uint(m1),tag,__float_as_uint(ri),tag});
    if(a<27){ const u4 srdO=mksrd((const char*)p.wout+a*(32*1280)+w*5120); const u32 mo=smb+w*5120; setm0<0>(mo);
      dma<0>(srdO,lane*16,0); dma<1024>(srdO,lane*16,0); dma<2048>(srdO,lane*16,0); dma<3072>(srdO,lane*16,0); setm0<4096>(mo); dma<0>(srdO,lane*16,4096); }
    #pragma unroll
    for(int i=0;i<10;i++){ u4 o;
      #pragma unroll
      for(int e=0;e<4;e++) o[e]=pkhf2(fma2(unpk(xr[i][e]),f2{ri,ri},f2{-m1*ri,-m1*ri}));
      ldsw128(smb+RB+(8*w+(lane>>3))*1296+(lane&7)*16+i*128,o); }
    WAITV(0); bar();
    #pragma unroll
    for(int i=0;i<10;i++){ const int c=w*10+i; const u32 ad=smb+RB+(32*((r>>1)&1)+8*q+(r>>2))*1296+c*16+(r&1)*8; u2 lo=ldstr(ad), hi=ldstr(ad+4*1296);
      const u32 sc=smb+40960+w*2048+(i&1)*1024; ldsw128(sc+(r&7)*128+((((r>>3)*4+q)^(r&7))<<4),u4{lo[0],lo[1],hi[0],hi[1]});
      CFENCE(); const u4 o=ldsr128(sc+(lane>>3)*128+(((lane&7)^(lane>>3))<<4)); CFENCE();
      st16(rs,OFF_XNT+a*81920+c*1024+lane*16,o); }
    WAITV(0);
    if(arrive(smb+C_Z1,lane)==7u && lane==0) st4(rs,OFF_FX+a*128,tag);
    if(a<27){
      #pragma unroll
      for(int i=0;i<5;i++){ u32 ad=smb+(8*q+(r>>2))*1280+(w*5+i)*32+(r&3)*8; u2 lo=ldstr(ad), hi=ldstr(ad+4*1280);
        st16(rs,OFF_WOF+(u32)((w*27+a)*5+i)*1024+lane*16,u4{lo[0],lo[1],hi[0],hi[1]}); } }
    WAITV(0); bar();
    if(tid==0) st4(rs,OFF_FW+a*128,tag);
    if(!g1){ pre01(); WAITV(0); }
    else { const u4 so=mksrd((const char*)p.out+(size_t)(a*4+mi)*20480); setm0<JNK>(smb); touch(so,lane*128,0u); touch(so,lane*128,8192u); touch(so,(lane&31)*128,16384u); }
  } else {
    const int r=lane&31, h=lane>>5;
    u32 epv;
    u32 kx[10]; { u32 k=kr0;
      #pragma unroll
      for(int i=0;i<10;i++){ kx[i]=k*128; k=k==9?0:k+1; } }
    auto issue1=[&](auto sc,int k){ constexpr int s=sc.value;
      if(k==0){ setm0<(s&3)*STG>(mw); dma<0>(srdX,vx0,kx[s]); } if(k==1) dma<1024>(srdX,vx1,kx[s]); if(k==2) dma<2048>(srdW,vw2,kx[s]*1728); if(k==3) dma<3072>(srdW,vw1,kx[s]*1728); };
    auto issue=[&](auto sc){ issue1(sc,0); issue1(sc,1); issue1(sc,2); issue1(sc,3); };
    u32 pr[6];
    gload4<0>(epv,0u,p.ws+OFF_EP);
    gload4<0>(pr[0],lane*4,(const char*)(p.lar+dch*128)); gload4<0>(pr[1],lane*4,(const char*)(p.aim+dch*128)); gload4<0>(pr[2],lane*4,(const char*)(p.cre+dch*128)); gload4<0>(pr[3],lane*4,(const char*)(p.cim+dch*128));
    gload2(pr[4],0u,(const char*)(p.ldt+dch)); gload2(pr[5],0u,(const char*)(p.dsk+dch));
    if(tid<32) *(LAS u32*)(smb+PM+tid*4)=0u;
    u32 ax[2][4],aw[2][4][2];
    { u32 row=32*mi+r, a0=smb+(row>>4)*4096+(row&15)*128+((h^((r>>1)&7))<<4);
      u32 qq=(lane&15)>>2, w0=smb+2048+g1*1024+h*4096+qq*128+8*(lane&1)+(((2*((lane>>4)&1)+((lane>>1)&1))^((qq&2)<<1))<<4);
      #pragma unroll
      for(int i=0;i<2;i++)
        #pragma unroll
        for(int k=0;k<4;k++){ const u32 kk=(k+mi)&3, wo=i*2*STG; ax[i][k]=opq((a0^(kk*32u))+wo); aw[i][k][0]=opq(w0+kk*8192+wo); aw[i][k][1]=opq((w0^64u)+kk*8192+wo); } }
    u4 sel; { const u32 o=((lane&15)==((lane>>4)&1))?0x3f803f80u:0u; sel=u4{o,o,o,o}; asm volatile("" : "+v"(sel)); }
    u32 ga[11];
    #pragma unroll
    for(int i=0;i<11;i++) ga[i]=opq(cb+2u*(u32)rbf((float)(512-8*lane+3-i)));
    Frag F[2];
    WAITV(11);
    bar();
    #pragma unroll
    for(int k=0;k<4;k++){
      #pragma unroll
      for(int n=0;n<2;n++){ u2 lo=ldstr(aw[0][k][n]), hi=ldstr(aw[0][k][n]+512); F[0].w[k][n]=u4{lo[0],lo[1],hi[0],hi[1]}; }
      F[0].x[k]=ldsr128(ax[0][k]); }
    issue(std::integral_constant<int,2>{});
    f16v acc[2]={}; f4 cacc[2]={}; u4 G;
    const u32 go=OFF_RS+rt*2048+mi*512, cp=smb+MISC+g1*1024;
    sfor<10>([&](auto ic){
      constexpr int s=ic.value; Frag& cur=F[s&1]; Frag& nxt=F[(s&1)^1];
      if constexpr(s<9){ WAITV(s==8? 3 : s==7? 7 : s==0? 11 : 4); asm volatile("s_barrier" ::: "memory"); }
      auto rw=[&](int k,int n){ if constexpr(s<9){ const int i=((s+1)>>1)&1; const u32 so=((s+1)&1)*STG; u2 lo=ldstr(aw[i][k][n]+so), hi=ldstr(aw[i][k][n]+so+512); nxt.w[k][n]=u4{lo[0],lo[1],hi[0],hi[1]}; } };
      auto rx=[&](int k){ if constexpr(s<9){ const int i=((s+1)>>1)&1; const u32 so=((s+1)&1)*STG; nxt.x[k]=ldsr128(ax[i][k]+so); } };
      #pragma unroll
      for(int k=0;k<4;k++){
        acc[0]=mfma32b(cur.w[k][0],cur.x[k],acc[0]); SCHED();
        if constexpr(s+3<=9) issue1(std::integral_constant<int,s+3>{},k);
        rw(k,0); rx(k);
        SCHED();
        acc[1]=mfma32b(cur.w[k][1],cur.x[k],acc[1]); SCHED();
        rw(k,1);
        if(k==0){ cacc[0]=mfma16(cur.w[0][0],sel,cacc[0]); cacc[1]=mfma16(cur.w[0][1],sel,cacc[1]); }
        if constexpr(s==6){ if(k==3){ gload<0>(G,r*16,p.ws+go); setm0<JNK>(smb);
            if(g1){ const u4 sp=mksrd((const char*)p.wsp+(size_t)(xc>>1)*512*4096); const u32 lb=(((xc&1)*28+idx)*4+mi)*74, l0=lb+lane, l1=lb+64+(lane<10?lane:9);
              touch(sp,(l0<16384?l0:16383)*128,0u); touch(sp,(l1<16384?l1:16383)*128,0u); }
            else { touch(mksrd(p.out+rtB*64*640),0u,0u); touch(mksrd(w==0? (const void*)(p.bsp+rtB*64) : w==1? (const void*)p.ws : w==2? (const void*)(p.wsp+(size_t)rtB*64*2048) : (const void*)(p.x+rtB*64*640)),(lane&15)*(w==1? (1u<<20) : w==2? 16384u : w==3? 4096u : 0u),0u); } } }
        if constexpr(s==8){ if(k==3) signal(smb+C_DONE,lane); }
        if constexpr(s==9){ if(k==1){
          if((lane&14)==0){ *(LAS f4*)(cp+mi*256+(lane&1)*64+(lane>>4)*16)=cacc[0]; *(LAS f4*)(cp+mi*256+128+(lane&1)*64+(lane>>4)*16)=cacc[1]; }
          signal(smb+C_CS+g1*4,lane); } }
        SCHED();
      }
    });
    if(!g1) __builtin_amdgcn_s_setprio(2);
    asm volatile("s_waitcnt vmcnt(2)" : "+v"(G),"+v"(epv),"+v"(pr[0]),"+v"(pr[1]),"+v"(pr[2]),"+v"(pr[3]),"+v"(pr[4]),"+v"(pr[5]) :: "memory");
    tag=uni(epv)+TAGINC; dsk=bflo(pr[5]);
    ldswait(smb+C_CS+g1*4,4u);
    const float cs0=*(LAS float*)(cp+h*512+r*4)+*(LAS float*)(cp+h*512+256+r*4), cs1=*(LAS float*)(cp+h*512+128+r*4)+*(LAS float*)(cp+h*512+384+r*4);
    #pragma unroll 1
    for(int sp=0;sp<SPIN && !__all(G[1]==tag&&G[3]==tag);sp++)asm volatile("global_load_dwordx4 %0, %1, %2 sc1\n\ts_waitcnt vmcnt(0)" : "=&v"(G) : "v"(r*16), "s"(p.ws+go) : "memory");
    const float nm=-__uint_as_float(G[0]), ri1=__uint_as_float(G[2]);
    acc[0]=__builtin_amdgcn_mfma_f32_32x32x2f32(cs0,nm,acc[0],0,0,0); acc[1]=__builtin_amdgcn_mfma_f32_32x32x2f32(cs1,nm,acc[1],0,0,0);
    #pragma unroll
    for(int e=0;e<16;e++) zp[e>>3][e&7]=pkbf2(gelu2(f2{acc[e>>3][2*(e&7)],acc[e>>3][2*(e&7)+1]}*ri1));
    if(g1){ ldswait(smb+C_DONE,8u); }
    else {
      float S=0.f,Q=0.f;
      if(j<13){
        #pragma unroll
        for(int e=0;e<8;e++){ S=dot2(zp[0][e],0x3f803f80u,S); Q=dot2(zp[0][e],zp[0][e],Q); } }
      #pragma unroll
      for(int e=0;e<8;e++){ S=dot2(zp[1][e],0x3f803f80u,S); Q=dot2(zp[1][e],zp[1][e],Q); }
      #define XSUM32(v) do{ auto t_=__builtin_amdgcn_permlane32_swap(__float_as_uint(v),__float_as_uint(v),false,false); v=__uint_as_float(t_[0])+__uint_as_float(t_[1]); }while(0)
      XSUM32(S); XSUM32(Q);
      const u32 sto=OFF_ST+rt*(14*2048)+(32*mi+r)*16;
      if(h==0) st16(rs,sto+j*2048,u4{__float_as_uint(S),tag,__float_as_uint(Q),tag});
    }
    float kp[11]; u4 g[7]={};
    const u32 gof=OFF_ST+rt*(14*2048)+(32*mi+r)*16+h*(7*2048);
    auto samp=[&](){ CFENCE();
      #pragma unroll
      for(int i=0;i<7;i++) g[i]=ld16(rs,gof+i*2048);
      CFENCE(); };
    if(hasc) ksc=kgen_tab(cb,lane,pr,kp); else if(!g1){ pre01(); samp(); }
    if(g1){
      if(hasc){
        kgen_exp(cb,lane,kp,ga);
        signal(smb+C_KP+mi*4,lane);
      }
      if(hasc) ldswait(smb+C_FULL,1u); else if(w<7){ z1st(); f1(); }
      if(hasc) z1st();
    } else {
      float S,Q;
      #pragma unroll 1
      for(int sp=0;sp<SPIN;sp++){ bool ok=true;
        #pragma unroll
        for(int i=0;i<7;i++) ok=ok&&g[i][1]==tag&&g[i][3]==tag;
        if(__all(ok)) break;
        __builtin_amdgcn_s_sleep(4); samp(); }
      S=0.f; Q=0.f;
      #pragma unroll
      for(int i=0;i<7;i++){ S+=__uint_as_float(g[i][0]); Q+=__uint_as_float(g[i][2]); }
      XSUM32(S); XSUM32(Q);
      float mean=S*(1.f/864.f), m2,r2; ln_fin(mean,Q*(1.f/864.f)-mean*mean,m2,r2); m2*=-r2;
      ldswait(smb+C_DONE,8u);
      const u32 tb=smb+RB+mi*RING, ad=tb+(8*(lane>>4)+((lane&15)>>2))*80+(lane&3)*8;
      const u32 zo=OFF_Z2F+((rt*4+mi)*54+(j==13?-2:2+4*j))*1024+lane*16;
      #pragma unroll
      for(int n=0;n<2;n++){
        #pragma unroll
        for(int g2=0;g2<4;g2++){ u2 o;
          #pragma unroll
          for(int e=0;e<2;e++) o[e]=pkbf2(fma2(unpk(zp[n][2*g2+e]),f2{r2,r2},f2{m2,m2}));
          *(LAS u2*)(tb+r*80+g2*16+h*8)=o; }
        CFENCE();
        u2 l0=ldstr(ad), h0=ldstr(ad+320), l1=ldstr(ad+32), h1=ldstr(ad+32+320);
        CFENCE();
        if(j<13||n){ st16(rs,zo+n*2048,u4{l0[0],l0[1],h0[0],h0[1]}); st16(rs,zo+n*2048+1024,u4{l1[0],l1[1],h1[0],h1[1]}); } }
      WAITV(0);
      if(arrive(smb+C_Z2,lane)==3u && lane==0) st4(rs,OFF_F2+(rt*16+j)*128,tag);
    }
  }
  const int ft0=ct<7?7*ct:47;
  bool adone=true; const u32 fao=OFF_FA+((lane<28?lane:0)*8+ct)*128;
  auto aload=[&](){ const u32 vo=(lane>>3)*4096+(lane&7)*16;
    #pragma unroll 1
    for(int i=0;i<10;i++){ setm0<ATL>(smb+i*1024); dma<0>(srdS,vo,OFF_AT+(u32)(80*ct+8*i)*4096+rtB*128); } };
  if(w>=4){
    if(hasc){
      __builtin_amdgcn_s_setprio(3);
      conv_wave(f1,rs,dch,cb,smb+C_KP+mi*4,lane,ksc,dsk);
      __builtin_amdgcn_s_setprio(0);
      if(arrive(smb+C_CV,lane)==(idx<24?2u:1u) && lane==0) st4(rs,OFF_FA+b*128,tag);
    }
    if(w==7){
      if(idx<28){
        pollw<16>(rs,OFF_FX+(lane&31)*128,tag);
        u4 sw[3][4];
        #pragma unroll
        for(int c=0;c<3;c++)
          #pragma unroll
          for(int i=0;i<4;i++) sw[c][i]=ld16(rs,OFF_XNT+((lane>>3)+8*i)*81920+(80*xc+idx+((c<2||idx<24)?28*c:0))*128+(lane&7)*16);
        #pragma unroll
        for(int c=0;c<3;c++){
          #pragma unroll
          for(int i=0;i<4;i++){ const int x=lane+64*i; ldsw128(smb+c*CVS+UOF+(x&3)*1040+((x>>2)+1)*16,sw[c][i]); }
          if(lane<4) ldsw128(smb+c*CVS+UOF+lane*1040,u4{0,0,0,0}); }
        signal(smb+C_FULL,lane);
        #pragma unroll 1
        for(int c=0;c<3;c++){
          #pragma unroll
          for(int i=0;i<4;i++){ sw[0][i]=sw[1][i]; sw[1][i]=sw[2][i]; }
          conv_win(smb+c*CVS,lane,sw[2]); }
        __builtin_amdgcn_s_setprio(3);
        #pragma unroll 1
        for(int c=0;c<(idx<24?3:2);c++){ ldswait<4>(smb+C_KP+c*4,1u); conv_help(smb+c*CVS,lane,sw[0]); signal(smb+C_KP+c*4,lane);
          #pragma unroll
          for(int i=0;i<4;i++){ sw[0][i]=sw[1][i]; sw[1][i]=sw[2][i]; } } }
      __builtin_amdgcn_s_setprio(0);
      if(idx<28){ z1st(); f1(); }
      pollw<16>(rs,OFF_FW+(lane<27?lane:0)*128,tag); signal(smb+C_Z1R,lane);
    }
    ldswait<6>(smb+C_Z1R,5u);
    u4 wq[24];
    #pragma unroll
    for(int i=0;i<24;i++) wq[i]=ld16(rs,OFF_WOF+ct*(135*1024)+40960+mi*RING+((i==23&&mi==3)?22:i)*1024+lane*16);
    if(w<6){
      #pragma unroll 1
      for(u32 i=0;i<20480;i+=4096){ setm0<0>(cb+i); const u32 o=OFF_WOF+ct*(135*1024)+mi*20480+i; dma<0>(srdS,lane*16,o); dma<1024>(srdS,lane*16,o); dma<2048>(srdS,lane*16,o); dma<3072>(srdS,lane*16,o); } }
    if(w==6){ adone=__all(ld4(rs,fao)==tag); if(adone) aload(); }
    ldswait(smb+C_FREE,4u);
    #pragma unroll
    for(int i=0;i<24;i++) ldsw128(smb+RB+mi*RING+i*1024+lane*16,wq[i]);
    if(w<6) WAITV(0);
    signal(smb+C_WR,lane);
    if(!adone){ adone=__all(ld4(rs,fao)==tag); if(adone) aload(); }
    if(w==7 && b==255){
      #pragma unroll 1
      for(int sp=0;sp<SPIN;sp++){ const u32 o=OFF_FA+lane*128; u32 v0=ld4(rs,o),v1=ld4(rs,o+8192),v2=ld4(rs,o+16384),v3=ld4(rs,lane<32? o+24576 : OFF_FW+(lane-32)*128); CFENCE();
        if(__all(v0==tag&&v1==tag&&v2==tag&&v3==tag)) break; __builtin_amdgcn_s_sleep(4); }
      if(lane==0) st4(rs,OFF_EP,tag); }
  }
  int w2=w; asm volatile("" : "+s"(w2));
  if(w2<4){
    __builtin_amdgcn_s_setprio(2);
    int r=lane&15, q=lane>>4;
    const u32 rb=smb+RB+w*RING;
    u4 srdA=mksrd((const char*)p.wsp+(size_t)rtB*64*4096+w*1024-4096+256);
    const char* zb=p.ws+OFF_Z2F+(u32)(16*w*54+ft0+3)*1024;
    u32 voA[2]; { u32 lr=lane>>3,c=lane&7; voA[0]=lr*4096+((c^(lr>>1))<<4); voA[1]=voA[0]^64u; }
    u32 sofa[8];
    #pragma unroll
    for(int g=0;g<8;g++) sofa[g]=((g+2*w)&7)*32768+4096-(g&3)*1024;
    const u32 voB=lane*16;
    u32 la[2]; la[0]=rb+r*128+((q^(r>>1))<<4); la[1]=la[0]^64u;
    f4 acc[4][7]; u4 B[NB][7]; u4 A[2][4]; u2 z1[7];
    auto issueA=[&](auto sc,auto gc){ constexpr int s=sc.value,g=gc.value; if constexpr((g&3)==0) setm0<((s+1)%3)*8192+g*1024>(rb); dma<(g&3)*1024>(srdA,voA[g&1],sofa[g]);
      if constexpr(g==7){ u64 a=((u64)srdA[1]<<32|srdA[0])+128; srdA[0]=(u32)a; srdA[1]=(u32)(a>>32); } };

    u32 bsr,fv;
    auto fof=[&](){ const u32 l=voB>>4; return OFF_F1+((rtB>>1)*16+(((ft0>>1)+(l&3))>>1))*128; };
    #pragma unroll
    for(int m=0;m<4;m++){ A[0][m]=ldsr128(la[0]+m*2048+8192); A[1][m]=ldsr128(la[1]+m*2048+8192); }
    sfor<8>([&](auto g){ issueA(std::integral_constant<int,2>{},g); });
    waitA(A[0]); waitA(A[1]);
    sfor<8>([&](auto g){ issueA(std::integral_constant<int,3>{},g); });
    { const int hf=(ft0>>1)+q; pollw(rs,OFF_F2+((4*w+(lane&3))*16+(hf==0?13:(hf-1)>>1))*128,tag); }
    sfor<NB>([&](auto hh){ const char* s=zb+(u32)hh.value*(54*1024); sfor<7>([&](auto n){ gload<(n.value-3)*1024>(B[hh][n],voB,s); }); });
    auto redw=[&](auto nc){ constexpr int n=nc.value; *(LAS f4*)(rb+n*1024+voB)=acc[1][n]; *(LAS f4*)(rb+(7+n)*1024+voB)=acc[2][n]; *(LAS f4*)(rb+(14+n)*1024+voB)=acc[3][n]; };
    const char* z1b=p.ws+OFF_Z1T+(u32)((rtB*4+w)*27+(ft0>>1))*1024;
    sfor<16>([&](auto hc){
      constexpr int h=hc.value, S=h%NB, P=h&1, HZ=HZZ, cnt=g2cnt(h);
      waitB<cnt>(B[S]); waitA(A[P]);
      if constexpr(h==HZ){ tie1(fv); if(!__all(fv==tag)) pollw(rs,fof(),tag); }
      if constexpr(h+1<16 && h>0){
        #pragma unroll
        for(int m=0;m<4;m++) A[P^1][m]=ldsr128(la[(h+1)&1]+m*2048+((((h+1)>>1)+1)%3)*8192);
      }
      if constexpr(h==HZ-3) gload4<0>(fv,fof(),p.ws);
      const char* s=zb+(u32)(h+NB)*(54*1024);
      sfor<7>([&](auto n){
        if constexpr(h==0) mfma4z(acc[0][n],acc[1][n],acc[2][n],acc[3][n],B[S][n],A[P]); else mfma4(acc[0][n],acc[1][n],acc[2][n],acc[3][n],B[S][n],A[P]);
        if constexpr(h>=3 && h<=10 && (n.value&1)==0) issueA(std::integral_constant<int,(h-1)/2+3>{},std::integral_constant<int,((h-1)&1)*4+n.value/2>{});
        if constexpr(h+NB<16) gload_t<(n.value-3)*1024>(B[S][n],voB,s);
        if constexpr(h==HZ){ gload8<(n.value>>1)*1024>(z1[n],voB,z1b+((n.value&1)? ((ft0&1)?1024:8) : (ft0&1)*8));
          if constexpr(n.value==6) { gload2(bsr,(voB>>3)&30u,(const char*)(p.bsp+(rtB*4+w)*16)); } }
        if constexpr(h==13 && n.value==0) signal(smb+C_Z1R,lane);
        if constexpr(h==15){ SCHED(); if constexpr(n.value>=2) redw(std::integral_constant<int,n.value-2>{}); SCHED(); }
      });
    });
    asm volatile("s_nop 15\n\ts_nop 3" ::: "memory");
    SCHED();
    lane=__builtin_amdgcn_mbcnt_hi(~0u,__builtin_amdgcn_mbcnt_lo(~0u,0u)); asm volatile("" : "+v"(lane)); r=lane&15; q=lane>>4;
    redw(std::integral_constant<int,5>{}); redw(std::integral_constant<int,6>{});
    signal(smb+C_RED,lane);
    ldswait(smb+C_RED,4u);
    #pragma unroll
    for(int m=1;m<4;m++){ u32 src=smb+RB+((w-m)&3)*RING+(m-1)*7168+lane*16;
      #pragma unroll
      for(int n=0;n<7;n++) acc[0][n]+=*(LAS f4*)(src+n*1024); }
    asm volatile("s_waitcnt vmcnt(0) lgkmcnt(0)" : "+v"(z1[0]),"+v"(z1[1]),"+v"(z1[2]),"+v"(z1[3]),"+v"(z1[4]),"+v"(z1[5]),"+v"(z1[6]),"+v"(bsr) :: "memory");
    const float bs=bflo(bsr);
    signal(smb+C_FREE,lane);
    u2 o[7];
    #pragma unroll
    for(int n=0;n<7;n++)
      #pragma unroll
      for(int e=0;e<2;e++)o[n][e]=pkbf2(unpk(z1[n][e])*(f2{acc[0][n][2*e],acc[0][n][2*e+1]}+bs));
    const u32 ho=OFF_HF+(u32)((rtB*4+w)*54+ft0)*512+(q>>1)*256+r*16;
    #pragma unroll
    for(int n=0;n<6;n+=2){ auto sx=__builtin_amdgcn_permlane16_swap(o[n][0],o[n+1][0],false,false); auto sy=__builtin_amdgcn_permlane16_swap(o[n][1],o[n+1][1],false,false);
      if(ct<7||n>=2) st16(rs,ho+(n+(q&1))*512,u4{sx[0],sy[0],sx[1],sy[1]}); }
    st8(rs,ho+6*512+(q&1)*8,o[6]);
    WAITV(0);
    if(arrive(smb+C_H,lane)==3u && lane==0) st4(rs,OFF_FH+(rtB*8+ct)*128,tag);
    __builtin_amdgcn_s_setprio(0);
  }
  { const int r=lane&15, q=lane>>4, m=w&3, kh=w>>2;
    u2 xv[5]={};
    if(kh){
      #pragma unroll
      for(int n=0;n<5;n++) xv[n]=*(const u2*)(p.x+(rtB*64+m*16+r)*640+80*ct+16*n+4*q);
    }
    f4 acc[5]={}; u4 W[3][5];
    const u32 wl2=smb+lane*16+RB-40960+kh*14*5120, wl0=kh? wl2 : smb+lane*16, wl1=kh? wl2 : smb+lane*16+CVS-20480;
    #define WAD(k,n) (((k)<4? wl0 : (k)<8? wl1 : wl2)+((k)*5+(n))*1024)
    f4 ax[5]={};
    auto addax=[&](){
      #pragma unroll
      for(int n=0;n<5;n++){ u2 av=ldstr(smb+ATL+(16*n+4*q+(r>>2))*128+(16*m+4*(r&3))*2); ax[n]=f4{bflo(av[0]),bfhi(av[0]),bflo(av[1]),bfhi(av[1])}+f4{bflo(xv[n][0]),bfhi(xv[n][0]),bflo(xv[n][1]),bfhi(xv[n][1])}; } };
    if(w==6 && adone){ WAITV(0); signal(smb+C_AT,lane); }
    u32 oo[3],to[3]; const u32 tb=smb+CVS+m*3072;
    #pragma unroll
    for(int i=0;i<3;i++){ const int it=lane+64*i, rr=(it*205)>>11, cc=it-rr*10; oo[i]=opq((rtB*64+m*16+rr)*1280+ct*160+cc*16); to[i]=opq(tb+rr*176+cc*16); }
    if(m==0){ ldswait(smb+C_H,4u); pollw(rs,OFF_FH+(rtB*8+kh*4+(lane&3))*128,tag); signal(smb+C_TAG+kh*4,lane); } else ldswait(smb+C_TAG+kh*4,1u);
    u4 hf[14];
    const u32 ho=OFF_HF+(u32)((rtB*4+m)*27+kh*14)*1024+lane*16;
    #pragma unroll
    for(int k=0;k<HPF;k++) hf[k]=ld16(rs,ho+k*1024);
    const bool early=kh && uni(ldsv(smb+C_AT))!=0u;
    CFENCE();
    if(early) addax();
    ldswait(smb+C_WR,4u);
    const u32 wl3=kh? wl2-5120 : wl2;
    #pragma unroll
    for(int n=0;n<10;n++) W[n/5][n%5]=ldsr128(WAD(n/5,n%5));
    #pragma unroll
    for(int k=0;k<13;k++){
      if(k+HPF<14) hf[k+HPF]=ld16(rs,ho+((k+HPF==13&&kh)?12:k+HPF)*1024);
      SCHED();
      #pragma unroll
      for(int n=0;n<5;n++){ acc[n]=mfma16(W[k%3][n],hf[k],acc[n]); SCHED();
        if(k<11) W[(k+2)%3][n]=ldsr128(WAD(k+2,n)); else if(k==11) W[1][n]=ldsr128(wl3+(65+n)*1024);
        SCHED(); }
      if(k==5 && !kh) signal(smb+C_G1,lane);
    }
    const u32 pb=smb+m*5120+lane*16;
    if(!kh){
      #pragma unroll
      for(int n=0;n<5;n++) acc[n]=mfma16(W[1][n],hf[13],acc[n]);
      ldswait(smb+C_G1,4u);
      #pragma unroll
      for(int n=0;n<5;n++) *(LAS f4*)(pb+n*1024)=acc[n];
      signal(smb+C_P+m*4,lane);
    } else {
      if(w==6 && !adone){ pollw(rs,fao,tag); aload(); WAITV(0); signal(smb+C_AT,lane); }
      if(!early){ ldswait(smb+C_AT,1u); addax(); }
      ldswait(smb+C_P+m*4,1u);
      #pragma unroll
      for(int n=0;n<5;n++){ const f4 o=(acc[n]+ax[n])+*(LAS f4*)(pb+n*1024); *(LAS u2*)(tb+r*176+n*32+q*8)=u2{pkbf(o[0],o[1]),pkbf(o[2],o[3])}; }
      CFENCE();
      #pragma unroll
      for(int i=0;i<3;i++) if(i<2||lane<32) __builtin_amdgcn_raw_buffer_store_b128(ldsr128(to[i]),__builtin_amdgcn_make_buffer_rsrc(p.out,0,2048*1280,0x00020000),oo[i],0,16);
    }
  }
}
extern "C" void kernel_launch(void* const* d_in,const int* in_sizes,int n_in,void* d_out,int out_size,void* d_ws,size_t ws_size,hipStream_t stream){
  Params p;
  p.x=(const u16*)d_in[0]; p.lar=(const u16*)d_in[1]; p.aim=(const u16*)d_in[2]; p.cre=(const u16*)d_in[3]; p.cim=(const u16*)d_in[4];
  p.ldt=(const u16*)d_in[5]; p.dsk=(const u16*)d_in[6]; p.win=(const u16*)d_in[8]; p.wsp=(const u16*)d_in[9]; p.bsp=(const u16*)d_in[10]; p.wout=(const u16*)d_in[11];
  p.out=(u16*)d_out; p.ws=(char*)d_ws;
  (void)hipFuncSetAttribute((const void*)fused,hipFuncAttributeMaxDynamicSharedMemorySize,LDS_TOTAL);
  fused<<<256,512,LDS_TOTAL,stream>>>(p);
}
```

```cpp
#include <hip/hip_runtime.h>
#include <utility>
#pragma clang diagnostic ignored "-Wint-to-pointer-cast"
typedef unsigned short u16;
typedef unsigned int u32;
typedef unsigned long long u64;
typedef __attribute__((ext_vector_type(8))) __bf16 bfx8;
typedef __attribute__((ext_vector_type(8))) _Float16 hfx8;
typedef __attribute__((ext_vector_type(2))) __bf16 bf2;
typedef __attribute__((ext_vector_type(2))) _Float16 hf2;
typedef __attribute__((ext_vector_type(2))) float f2;
typedef __attribute__((ext_vector_type(4))) float f4;
typedef __attribute__((ext_vector_type(16))) float f16v;
typedef __attribute__((ext_vector_type(4))) u32 u4;
typedef __attribute__((ext_vector_type(2))) u32 u2;
typedef __attribute__((ext_vector_type(4))) short s4;
#define LAS __attribute__((address_space(3)))
#define DI __device__ __forceinline__

constexpr u32 OFF_Z2F = 0;
constexpr u32 OFF_Z1T = OFF_Z2F + 64*54*1024;
constexpr u32 OFF_XNT = OFF_Z1T + 128*27*1024;
constexpr u32 OFF_WOF = OFF_XNT + 640*4096;
constexpr u32 OFF_HF  = OFF_WOF + 8*27*5*1024;
constexpr u32 OFF_AT  = OFF_HF + 128*27*1024;
constexpr u32 OFF_ST  = OFF_AT + 640*4096;
constexpr u32 OFF_RS  = OFF_ST + 16*14*2048+8192;
constexpr u32 OFF_EP  = OFF_RS + 2048*16;
constexpr u32 OFF_F2  = OFF_EP + 256;
constexpr u32 OFF_F1  = OFF_F2 + 256*128;
constexpr u32 OFF_FX  = OFF_F1 + 256*128;
constexpr u32 OFF_FW  = OFF_FX + 32*128;
constexpr u32 OFF_FH  = OFF_FW + 32*128;
constexpr u32 OFF_FA  = OFF_FH + 256*128;
constexpr u32 WS_END  = OFF_FA + 256*128;

struct Params {
  const u16 *x, *lar, *aim, *cre, *cim, *ldt, *dsk, *win, *wsp, *bsp, *wout;
  u16* out; char* ws;
};
template<class F,int... I> DI void sfor_impl(F&& f, std::integer_sequence<int,I...>){ (f(std::integral_constant<int,I>{}),...); }
template<int N,class F> DI void sfor(F&& f){ sfor_impl(f, std::make_integer_sequence<int,N>{}); }

DI float bflo(u32 v){ return __uint_as_float(v<<16); }
DI float bfhi(u32 v){ return __uint_as_float(v&0xffff0000u); }
DI u32 pkbf(float a,float b){ f2 v={a,b}; return __builtin_bit_cast(u32,__builtin_convertvector(v,bf2)); }
DI u32 pkhf(float a,float b){ f2 v={a,b}; return __builtin_bit_cast(u32,__builtin_convertvector(v,hf2)); }
DI float rbf(float a){ return bflo(pkbf(a,a)); }
DI float hlo(u32 v){ return (float)__builtin_bit_cast(hf2,v)[0]; }
DI float hhi(u32 v){ return (float)__builtin_bit_cast(hf2,v)[1]; }
DI float dot2(u32 a,u32 b,float c){ return __builtin_amdgcn_fdot2_f32_bf16(__builtin_bit_cast(bf2,a),__builtin_bit_cast(bf2,b),c,false); }
DI f4 mfma16(u4 a,u4 b,f4 c){ return __builtin_amdgcn_mfma_f32_16x16x32_bf16(__builtin_bit_cast(bfx8,a),__builtin_bit_cast(bfx8,b),c,0,0,0); }
DI f16v mfma32h(u4 a,u4 b,f16v c){ return __builtin_amdgcn_mfma_f32_32x32x16_f16(__builtin_bit_cast(hfx8,a),__builtin_bit_cast(hfx8,b),c,0,0,0); }
DI u4 ldsr128(u32 a){ return *(LAS u4*)a; }
DI u2 ldsr64(u32 a){ return *(LAS u2*)a; }
DI void ldsw128(u32 a,u4 v){ *(LAS u4*)a=v; }
DI u2 ldstr(u32 a){ return __builtin_bit_cast(u2,__builtin_amdgcn_ds_read_tr16_b64_v4i16((LAS s4*)a)); }
DI u32 uni(u32 v){ return __builtin_amdgcn_readfirstlane(v); }

template<int OFF> DI void gload(u4& dst,u32 voff,const char* base){ asm volatile("global_load_dwordx4 %0, %1, %2 offset:%3 sc1" : "=v"(dst) : "v"(voff), "s"(base), "n"(OFF)); }
template<int OFF> DI void gload_t(u4& dst,u32 voff,const char* base){ asm volatile("global_load_dwordx4 %0, %1, %2 offset:%3 sc1" : "+v"(dst) : "v"(voff), "s"(base), "n"(OFF)); }
DI void mfma4(f4& c0,f4& c1,f4& c2,f4& c3,const u4& a,const u4 (&b)[4]){
  asm volatile("v_mfma_f32_16x16x32_bf16 %0, %4, %5, %0\n\tv_mfma_f32_16x16x32_bf16 %1, %4, %6, %1\n\tv_mfma_f32_16x16x32_bf16 %2, %4, %7, %2\n\tv_mfma_f32_16x16x32_bf16 %3, %4, %8, %3"
    : "+v"(c0),"+v"(c1),"+v"(c2),"+v"(c3) : "v"(a),"v"(b[0]),"v"(b[1]),"v"(b[2]),"v"(b[3])); }
DI void mfma4z(f4& c0,f4& c1,f4& c2,f4& c3,const u4& a,const u4 (&b)[4]){
  asm volatile("v_mfma_f32_16x16x32_bf16 %0, %4, %5, 0\n\tv_mfma_f32_16x16x32_bf16 %1, %4, %6, 0\n\tv_mfma_f32_16x16x32_bf16 %2, %4, %7, 0\n\tv_mfma_f32_16x16x32_bf16 %3, %4, %8, 0"
    : "=&v"(c0),"=&v"(c1),"=&v"(c2),"=&v"(c3) : "v"(a),"v"(b[0]),"v"(b[1]),"v"(b[2]),"v"(b[3])); }
template<int CNT> DI void waitB(u4 (&B)[7]){ asm volatile("s_waitcnt vmcnt(%7)" : "+v"(B[0]),"+v"(B[1]),"+v"(B[2]),"+v"(B[3]),"+v"(B[4]),"+v"(B[5]),"+v"(B[6]) : "n"(CNT) : "memory"); }
DI void waitA(u4 (&A)[4]){ asm volatile("s_waitcnt lgkmcnt(0)" : "+v"(A[0]),"+v"(A[1]),"+v"(A[2]),"+v"(A[3]) :: "memory"); }
#define WAITV(n) asm volatile("s_waitcnt vmcnt(%0)" :: "n"(n) : "memory")
#define WAITL0() asm volatile("s_waitcnt lgkmcnt(0)" ::: "memory")
#define CFENCE() asm volatile("" ::: "memory")
#define SCHED() __builtin_amdgcn_sched_barrier(0)
DI void bar(){ asm volatile("s_waitcnt lgkmcnt(0)\n\ts_barrier" ::: "memory"); }
DI f2 unpk(u32 v){ return f2{bflo(v),bfhi(v)}; }
DI u32 pkbf2(f2 v){ return __builtin_bit_cast(u32,__builtin_convertvector(v,bf2)); }
DI u32 pkhf2(f2 v){ return __builtin_bit_cast(u32,__builtin_convertvector(v,hf2)); }
DI f2 fma2(f2 a,f2 b,f2 c){ return __builtin_elementwise_fma(a,b,c); }
DI f2 gelu2(f2 x){
  f2 u=x*fma2(x*x,f2{-0.10294324f,-0.10294324f},f2{-2.30220820f,-2.30220820f});
  f2 d=f2{__builtin_amdgcn_exp2f(u[0]),__builtin_amdgcn_exp2f(u[1])}+f2{1.f,1.f};
  return x*f2{__builtin_amdgcn_rcpf(d[0]),__builtin_amdgcn_rcpf(d[1])};
}
DI void ln_fin(float mean,float var,float& m,float& rinv){
  m=rbf(mean); float v=rbf(__builtin_fmaxf(var,0.f));
  float s=rbf(__builtin_amdgcn_sqrtf(rbf(v+1e-5f))); float r=__builtin_amdgcn_rcpf(s); rinv=__builtin_fmaf(__builtin_fmaf(-s,r,1.f),r,r);
}
typedef __amdgpu_buffer_rsrc_t rsrc_t;
DI void st16(rsrc_t rs,u32 off,u4 v){ __builtin_amdgcn_raw_buffer_store_b128(v,rs,off,0,16); }
DI void st8(rsrc_t rs,u32 off,u2 v){ __builtin_amdgcn_raw_buffer_store_b64(v,rs,off,0,16); }
DI void st4(rsrc_t rs,u32 off,u32 v){ __builtin_amdgcn_raw_buffer_store_b32(v,rs,off,0,16); }
DI u4 ld16(rsrc_t rs,u32 off){ return __builtin_amdgcn_raw_buffer_load_b128(rs,off,0,16); }
DI u32 ld4(rsrc_t rs,u32 off){ return __builtin_amdgcn_raw_buffer_load_b32(rs,off,0,16); }
constexpr int SPIN=1<<17;
constexpr u32 TAGINC=0x9E3779B9u;
template<int SL=2> DI void pollw(rsrc_t rs,u32 off,u32 tag){
  #pragma unroll 1
  for(int s=0;s<SPIN;s++){ u32 v=ld4(rs,off); CFENCE(); if(__all(v==tag)) break; __builtin_amdgcn_s_sleep(SL); }
}

DI f16v mfma32b(u4 a,u4 b,f16v c){ return __builtin_amdgcn_mfma_f32_32x32x16_bf16(__builtin_bit_cast(bfx8,a),__builtin_bit_cast(bfx8,b),c,0,0,0); }
DI u4 mksrd(const void* b){ u64 a=(u64)b; return u4{(u32)a,(u32)(a>>32)&0xffffu,0xffffffffu,0x00020000u}; }
template<int IMM> DI void setm0(u32 base){ asm volatile("s_add_u32 m0, %0, %1\n\ts_nop 0" :: "s"(base), "n"(IMM) : "scc","memory"); }
template<int OFF> DI void dma(u4 srd,u32 voff,u32 soff){ asm volatile("buffer_load_dwordx4 %0, %1, %2 offen offset:%3 sc1 lds" :: "v"(voff), "s"(srd), "s"(soff), "n"(OFF) : "memory"); }
DI void touch(u4 srd,u32 voff,u32 soff){ asm volatile("buffer_load_dword %0, %1, %2 offen sc1 lds" :: "v"(voff), "s"(srd), "s"(soff) : "memory"); }
template<int OFF> DI void gload8(u2& dst,u32 voff,const char* base){ asm volatile("global_load_dwordx2 %0, %1, %2 offset:%3 sc1" : "=v"(dst) : "v"(voff), "s"(base), "n"(OFF)); }
DI void gload2(u32& dst,u32 voff,const char* base){ asm volatile("global_load_ushort %0, %1, %2" : "=v"(dst) : "v"(voff), "s"(base)); }
DI u32 arrive(u32 a,int){ u32 o; asm volatile("s_mov_b64 exec, 1\n\tds_add_rtn_u32 %0, %1, %2\n\ts_mov_b64 exec, -1\n\ts_waitcnt lgkmcnt(0)" : "=&v"(o) : "v"(a), "v"(1u) : "memory"); return uni(o); }
template<int SL=1> DI void ldswait(u32 a,u32 n){ while(__hip_atomic_load((LAS u32*)a,__ATOMIC_RELAXED,__HIP_MEMORY_SCOPE_WORKGROUP)<n) __builtin_amdgcn_s_sleep(SL); CFENCE(); }
DI void tie1(u32& g){ asm volatile("" : "+v"(g) :: "memory"); }
DI u32 opq(u32 v){ asm volatile("" : "+v"(v)); return v; }

constexpr u32 STG=32768;
constexpr u32 RING=24576, RB=62848, CONV=0, KP=2208, UOF=16768, CVS=UOF+4160, S7O=4*KP, S9O=S7O+2112, KZO=S9O+2112, MISC=RB+4*RING, JNK=MISC+2048, PM=163840-128, LDS_TOTAL=163840;
constexpr u32 ATL=2*CVS;
constexpr u32 C_FULL=PM, C_G1=PM+4, C_Z2=PM+8, C_Z1=PM+12, C_RED=PM+16, C_H=PM+20, C_FREE=PM+24, C_CV=PM+28, C_Z1R=PM+32, C_TAG=PM+40, C_KP=PM+48, C_DONE=PM+64, C_CS=PM+72, C_P=PM+96, C_AT=PM+112, C_WR=PM+116;
constexpr u32 CODE_BYTES=37888;
constexpr int HPF=5;
DI void signal(u32 a,int){ asm volatile("s_mov_b64 exec, 1\n\tds_add_u32 %0, %1\n\ts_mov_b64 exec, -1" :: "v"(a), "v"(1u) : "memory"); }
template<int OFF> DI void gload4(u32& dst,u32 voff,const char* base){ asm volatile("global_load_dword %0, %1, %2 offset:%3 sc1" : "=v"(dst) : "v"(voff), "s"(base), "n"(OFF)); }

DI f2 cmulp(f2 a,f2 b,f2 bs){ return fma2(f2{a[1],a[1]},bs,f2{a[0],a[0]}*b); }
DI u32 ldsv(u32 a){ return __hip_atomic_load((LAS u32*)a,__ATOMIC_RELAXED,__HIP_MEMORY_SCOPE_WORKGROUP); }
DI f2 cpow_dir(float lr,float rv,float pw){ float e=__builtin_amdgcn_exp2f(lr*pw); float f=__builtin_amdgcn_fractf(rv*pw);
  return f2{e*__builtin_amdgcn_cosf(f),e*__builtin_amdgcn_sinf(f)}; }
DI u32 dup(u32 a){ return __builtin_amdgcn_perm(a,a,0x01000100u); }
DI u32 cat(u32 lo,u32 hi){ return __builtin_amdgcn_perm(hi,lo,0x05040100u); }
DI u32 lds16(u32 a){ return *(LAS u16*)a; }

DI float kgen_tab(u32 cb,int lane,const u32 (&pr)[6],float (&kp)[11]){
  const float dt=rbf(__expf(bflo(pr[4])));
  const float cr[2]={bflo(pr[2]),bfhi(pr[2])}, cm[2]={bflo(pr[3]),bfhi(pr[3])};
  float mx=fmaxf(fmaxf(fabsf(cr[0]),fabsf(cm[0])),fmaxf(fabsf(cr[1]),fabsf(cm[1])));
  u32 eb=__float_as_uint(mx)&0x7f800000u;
  #define DPPMAX(c) eb=max(eb,(u32)__builtin_amdgcn_update_dpp(0,(int)eb,c,0xf,0xf,false))
  DPPMAX(0x128); DPPMAX(0x124); DPPMAX(0x122); DPPMAX(0x121);
  eb=max(max((u32)__builtin_amdgcn_readlane(eb,0),(u32)__builtin_amdgcn_readlane(eb,16)),max((u32)__builtin_amdgcn_readlane(eb,32),(u32)__builtin_amdgcn_readlane(eb,48))); eb=eb<0x01000000u?0x3f800000u:(eb>0x7e000000u?0x7e000000u:eb);
  const float ksc=2.f*dt*__uint_as_float(eb), isc=__uint_as_float(0x7f000000u-eb);
  const int r=lane&31,h=lane>>5;
  const u32 TP=272, TA=cb, TB=cb+22*TP;
  f16v acc={};
  f2 an[2][4];
  #pragma unroll
  for(int c=0;c<2;c++){ const float l_a=c?bfhi(pr[0]):bflo(pr[0]), i_m=c?bfhi(pr[1]):bflo(pr[1]);
    const float lr=(-rbf(__expf(l_a))*dt)*1.44269504f, rv=(i_m*dt)*0.15915494f;
    an[c][0]=cpow_dir(lr,rv,1.f); an[c][1]=cpow_dir(lr,-rv,8.f); an[c][2]=cpow_dir(lr,-rv,16.f); an[c][3]=cpow_dir(lr,rv,256.f); }
  u32 hold[54]; const u32 wt=TA+lane*4;
  struct P2 { f2 v[2]; };
  auto mul=[&](const P2& x,int k){ P2 o;
    #pragma unroll
    for(int c=0;c<2;c++) o.v[c]=cmulp(x.v[c],an[c][k],f2{-an[c][k][1],an[c][k][0]});
    return o; };
  auto put=[&](int row,const P2& x){ *(LAS u32*)(wt+row*TP)=pkhf2(x.v[0]); hold[row]=pkhf2(x.v[1]); };
  P2 e={{f2{1.f,0.f},f2{1.f,0.f}}}, o={{an[0][1],an[1][1]}}, bk[8], t;
  bk[0].v[0]=f2{cr[0]*isc,cm[0]*isc}; bk[0].v[1]=f2{cr[1]*isc,cm[1]*isc}; put(0,bk[0]); t=bk[0];
  constexpr int tr[8]={8,10,11,16,17,20,21,18};
  #pragma unroll
  for(int k=0;k<16;k++){ put(22+2*k,e); put(23+2*k,o);
    if(k<15){ if(k) e=mul(e,2); else { e.v[0]=an[0][2]; e.v[1]=an[1][2]; } o=mul(o,2); }
    if(k<8){ t=mul(t,3); put(tr[k],t); }
    if(k<7){ bk[k+1]=mul(bk[k],0); put(k+1,bk[k+1]); }
    if(k==8){ put(9,mul(bk[2],3)); put(13,mul(bk[6],3)); t=mul(bk[4],3); put(12,t); }
    if(k==9||k==10){ t=mul(t,3); put(5+k,t); }
    SCHED(); }
  const int ra=(r>21||r==19)?0:r;
  #pragma unroll
  for(int c=0;c<2;c++){
    CFENCE();
    u4 fa[8],fb[8];
    #pragma unroll
    for(int ks=0;ks<8;ks++){ fa[ks]=ldsr128(TA+ra*TP+ks*32+h*16); fb[ks]=ldsr128(TB+r*TP+ks*32+h*16); }
    CFENCE();
    if(c==0){
      #pragma unroll
      for(int i=0;i<54;i++) if(i!=19) *(LAS u32*)(wt+i*TP)=hold[i]; }
    #pragma unroll
    for(int ks=0;ks<8;ks++) acc=mfma32h(fa[ks],fb[ks],acc);
  }
  CFENCE();
  #pragma unroll
  for(int i=0;i<11;i++) kp[i]=acc[i];
  return ksc;
}
DI void kgen_exp(u32 cb,int lane,const float (&acc)[11],const u32 (&ga)[11]){
  const int r=lane&31,h=lane>>5;
  { const u32 a4=cb+r*16+h*8, a5=cb+r*16+h*1024, az=cb+KZO+r*2+h*128;
    #define PK(addr,base,reg) do{ *(LAS _Float16*)((addr)+2*(base))=(_Float16)acc[reg]; }while(0)
    PK(a4,0,0);PK(a4,1,1);PK(a4,2,2);PK(a4,3,3); PK(a4,256,4);PK(a4,258,5);PK(a4,512,6);PK(a4,768,7);
    PK(a5,1024,8); PK(az,255,10); PK(az,127,8); PK(az,159,9);
    #undef PK
    CFENCE();
    if(lane<16) ldsw128(cb+KZO+lane*16,u4{0,0,0,0});
    CFENCE();
  }
  { const u32 g0=cb+2*(1024-8*lane);
    u32 f2_=lds16(g0+(lane?8:0)), a2=lds16(g0), m2=lds16(g0-8), b2=lds16(g0-16);
    u32 e[11];
    #pragma unroll
    for(int j=0;j<11;j++) e[j]=lds16(ga[j]);
    u32 k01=*(LAS u32*)cb, k23=*(LAS u32*)(cb+4), kl=lds16(cb+2048);
    WAITL0();
    const u32 wo=cb+96+lane*16;
    { u32 aa=dup(a2),mm=dup(m2),bb=dup(b2),am=cat(a2,m2),fa=cat(f2_,a2),mb=cat(m2,b2);
      ldsw128(wo,u4{aa,am,mm,bb}); ldsw128(wo+KP,u4{aa,aa,mm,mb}); ldsw128(wo+2*KP,u4{aa,aa,am,mm}); ldsw128(wo+3*KP,u4{fa,aa,aa,mm}); }
    { u32 o0=cat(e[0],e[1]),o1=cat(e[2],e[3]),o2=cat(e[4],e[5]),o3=cat(e[6],e[7]),o4=cat(e[8],e[9]);
      u32 p1=cat(e[1],e[2]),d0=cat(e[3],e[4]),d1=cat(e[5],e[6]),d2=cat(e[7],e[8]),d3=cat(e[9],e[10]);
      ldsw128(wo+1024,u4{d0,d1,d2,d3}); ldsw128(wo+1024+KP,u4{o1,o2,o3,o4}); ldsw128(wo+1024+2*KP,u4{p1,d0,d1,d2}); ldsw128(wo+1024+3*KP,u4{o0,o1,o2,o3}); }
    if(lane<10){ if(lane){k01=0;k23=0;} if(lane!=9) kl=0;
      const u32 d0=k01&0xffffu, o1=__builtin_amdgcn_alignbit(k01,k01,16), o0=__builtin_amdgcn_alignbit(k23,k23,16), p1=(k23&0xffffu)|(k01&0xffff0000u), kk=dup(kl), zk=kl<<16;
      const u32 w2=lane<4? wo+2048 : wo-160;
      ldsw128(w2,u4{d0,0,kk,kk}); ldsw128(w2+KP,u4{o1,0,zk,kk}); ldsw128(w2+2*KP,u4{p1,d0,0,kk}); ldsw128(w2+3*KP,u4{o0,o1,0,zk}); }
  }
}
DI f4 mfma16h(u4 a,u4 b,f4 c){ return __builtin_amdgcn_mfma_f32_16x16x32_f16(__builtin_bit_cast(hfx8,a),__builtin_bit_cast(hfx8,b),c,0,0,0); }
DI u4 lds2x64(u32 a){ const u2 lo=ldsr64(a), hi=ldsr64(a+8); return u4{lo[0],lo[1],hi[0],hi[1]}; }
DI void conv_win(u32 cb,int lane,u4 (&sw)[4]){
  float P[25]; P[0]=0.f;
  #pragma unroll
  for(int c=0;c<4;c++){ const int X=2*lane-2+c; const u4 v=ldsr128(cb+UOF+(X&3)*1040+((X>>2)+1)*16);
    #pragma unroll
    for(int e=0;e<8;e++){ const int i=8*c+e-4; if(i>=0&&i<24) P[i+1]=P[i]+((e&1)?hhi(v[e>>1]):hlo(v[e>>1])); } }
  #pragma unroll
  for(int k=0;k<8;k++){ sw[k>>2][k&3]=pkhf(P[2*k+8]-P[2*k+1],P[2*k+9]-P[2*k+2]); sw[2+(k>>2)][k&3]=pkhf(P[2*k+9]-P[2*k],P[2*k+10]-P[2*k+1]); }
}
DI void conv_help(u32 cb,int lane,const u4 (&sw)[4]){
  const int r=lane&15,q=lane>>4; f4 y[2][2]={};
  ldsw128(cb+S7O+48+lane*32,sw[0]); ldsw128(cb+S7O+64+lane*32,sw[1]); ldsw128(cb+S9O+48+lane*32,sw[2]); ldsw128(cb+S9O+64+lane*32,sw[3]);
  if(lane<3){ ldsw128(cb+S7O+lane*16,u4{0,0,0,0}); ldsw128(cb+S9O+lane*16,u4{0,0,0,0}); }
  CFENCE();
  const u32 ka=cb+(r&3)*KP+16*q-8*(r>>2)+64, ub=cb+UOF+q*1040+(r+1)*16;
  const u4 a0=lds2x64(ka+32), a1=lds2x64(ka), b0=ldsr128(ub), b1=ldsr128(ub+256), a2=lds2x64(ka-32), b2=ldsr128(ub-16), b3=ldsr128(ub+240);
  const u32 sa=cb+(((r>>2)&1)?S9O:S7O)+2*(64*q+24-8*(r>>2)+4*(r&3)), kz=cb+KZO+8*(r-2*q+23); u4 bz[4],fa[4][2];
  #pragma unroll
  for(int ks=0;ks<4;ks++) bz[ks]=lds2x64(kz+(3-ks)*64);
  #pragma unroll
  for(int i=0;i<8;i++){ const u2 lo=ldstr(sa+(i>>1)*512+(i&1)*32+64), hi=ldstr(sa+(i>>1)*512+(i&1)*32); fa[i>>1][i&1]=u4{lo[0],lo[1],hi[0],hi[1]}; }
  SCHED();
  y[0][0]=mfma16h(a2,b2,y[0][0]); y[0][1]=mfma16h(a2,b3,y[0][1]);
  y[0][0]=mfma16h(a0,b0,y[0][0]); y[1][0]=mfma16h(a1,b0,y[1][0]); y[0][1]=mfma16h(a0,b1,y[0][1]); y[1][1]=mfma16h(a1,b1,y[1][1]);
  #pragma unroll
  for(int ks=0;ks<4;ks++)
    #pragma unroll
    for(int m=0;m<2;m++){ y[m][1]=mfma16h(fa[ks][m],bz[ks],y[m][1]); if(ks<2) y[m][0]=mfma16h(fa[ks][m],bz[ks+2],y[m][0]); }
  CFENCE();
  #pragma unroll
  for(int i=0;i<4;i++) *(LAS f4*)(cb+S7O+i*1024+lane*16)=y[i&1][i>>1];
}
#define RDA(S,i,m) asm volatile("ds_read2_b64 %0, %1 offset0:%2 offset1:%3" : "=v"(AF[S][i][m]) : "v"(ka), "n"((1-(i))*128+(1-(m))*4), "n"((1-(i))*128+(1-(m))*4+1))
#define RDB(S,i,ad,o) asm volatile("ds_read_b128 %0, %1 offset:%2" : "=v"(BF[S][i]) : "v"(ad), "n"(o))
#define TIEA(S,i) "+v"(AF[S][i][0]),"+v"(AF[S][i][1])
#define CV_M(S,i,n) do{ y[0][n]=mfma16h(AF[S][i][0],BF[S][(n)-(i)],y[0][n]); y[1][n]=mfma16h(AF[S][i][1],BF[S][(n)-(i)],y[1][n]); SCHED(); }while(0)
#define CV_U0() u32 u0=u1-256; u0=((int)u0<(int)uz)?uz:u0
template<class CB> DI void conv_wave(CB&& mid,rsrc_t rs,int d,u32 cb,u32 flag,int lane,float ksc,float dsk){
  const int r=lane&15,q=lane>>4; u32 ka=cb+(r&3)*KP+16*q-8*(r>>2)+1088; const u32 uz=cb+UOF+q*1040; u32 u1=uz+(r+17)*16; u4 AF[2][2][2]; u4 BF[2][4]; f4 y[2][4]={};
  #define CV_WT(S) asm volatile("s_waitcnt lgkmcnt(0)" : TIEA(S,0),TIEA(S,1),"+v"(BF[S][0]),"+v"(BF[S][1]),"+v"(BF[S][2]),"+v"(BF[S][3]))
  #define CV_STEP(C,N) do{ CV_U0(); CV_M(C,0,0); RDA(N,0,0); RDB(N,0,u0,0); RDA(N,0,1); SCHED(); CV_M(C,0,1); RDB(N,1,u1,0); RDA(N,1,0); RDB(N,2,u1,256); SCHED(); CV_M(C,0,2); RDA(N,1,1); RDB(N,3,u1,512); \
      ka-=64; u1-=16; SCHED(); CV_M(C,0,3); CV_M(C,1,1); CV_M(C,1,2); CV_M(C,1,3); CV_WT(N); }while(0)
  { CV_U0(); RDA(0,0,0); RDA(0,0,1); RDA(0,1,0); RDA(0,1,1); RDB(0,0,u0,0); RDB(0,1,u1,0); RDB(0,2,u1,256); RDB(0,3,u1,512); ka-=64; u1-=16; CV_WT(0); }
  #pragma unroll 1
  for(int dl=0;dl<8;dl++){ CV_STEP(0,1); if(dl==7){ ka+=64; u1+=16; } CV_STEP(1,0); }
  mid();
  u2 uv[2][4];
  #pragma unroll
  for(int n=0;n<4;n++)
    #pragma unroll
    for(int m=0;m<2;m++) uv[m][n]=ldsr64(cb+UOF+(2*m+(q>>1))*1040+(16*n+r+1)*16+8*(q&1));
  const u32 sc=cb+UOF+r*64+q*8;
  auto fin=[&](int n){
    #pragma unroll
    for(int m=0;m<2;m++){ u2 o;
      #pragma unroll
      for(int e=0;e<2;e++) o[e]=pkbf2(fma2(f2{hlo(uv[m][n][e]),hhi(uv[m][n][e])},f2{dsk,dsk},f2{y[m][n][2*e],y[m][n][2*e+1]}*ksc));
      *(LAS u2*)(sc+n*1024+m*32)=o; } };
  ldswait(flag,2u);
  { f4 hp[4];
    #pragma unroll
    for(int i=0;i<4;i++) hp[i]=*(LAS f4*)(cb+S7O+i*1024+lane*16);
    fin(0); fin(1);
    #pragma unroll
    for(int i=0;i<4;i++) y[i&1][2+(i>>1)]+=hp[i];
    fin(2); fin(3); }
  CFENCE();
  u4 ov[4];
  #pragma unroll
  for(int n=0;n<4;n++) ov[n]=ldsr128(cb+UOF+n*1024+lane*16);
  #pragma unroll
  for(int n=0;n<4;n++) st16(rs,OFF_AT+d*4096+n*1024+lane*16,ov[n]);
  WAITV(0);
}

constexpr int NB=3, HZZ=14;
constexpr int g2ops(int h){ return h<0? 7 : (h+NB<16?7:0)+((h>=3&&h<=10)?4:0)+(h==HZZ-3?1:0)+(h==HZZ?8:0); }
constexpr int g2cnt(int h){ int c=0; for(int i=h-NB+1;i<h;i++) c+=g2ops(i); return c; }
struct Frag { u4 x[4], w[4][2]; };
__global__ __launch_bounds__(512,2) void fused(Params p){
  extern __shared__ __attribute__((aligned(16))) char sm[];
  const u32 smb=(u32)(size_t)(LAS char*)sm;
  u64 pc0; asm volatile("s_getpc_b64 %0" : "=s"(pc0));
  const int b=blockIdx.x, xc=b&7, idx=b>>3;
  const int tid=threadIdx.x, w=uni(tid>>6); int lane=tid&63;
  auto cpf=[&](u32 o){ u4 sc=mksrd((const void*)(pc0&~127ull)); sc[2]=CODE_BYTES; setm0<JNK>(smb); touch(sc,lane*128,o); };
  if(idx>=28) cpf(w*8192);
  SCHED();
  const int rt=4*(xc>>1)+(idx&3), j=7*(xc&1)+(idx>>2), mi=w&3, g1=w>>2;
  const u32 kr0=((j+rt)%5)*2;
  u32 vx0,vx1,vw1,vw2,k0=kr0*128,k0w=kr0*(64*3456); const u32 mw=smb+w*4096;
  { const u32 lr=lane>>3,c=lane&7; vx0=(rt*128+16*w+lr)*1280+((c^(lr>>1))<<4); vx1=(vx0^64u)+9216;
    vw1=(8*w+lr)*3456+((c^((lr&2)<<1))<<4)+j*128; vw2=vw1+1024+(j==13?0:14*128); }
  asm volatile("" : "+v"(vx0),"+v"(vx1),"+v"(vw1),"+v"(vw2),"+s"(k0),"+s"(k0w));
  setm0<0>(mw);
  SCHED();
  const u4 srdX=mksrd(p.x), srdW=mksrd((const char*)p.win-3072);
  if(idx<28){ dma<0>(srdX,vx0,k0); dma<1024>(srdX,vx1,k0); dma<2048>(srdW,vw2,k0w); dma<3072>(srdW,vw1,k0w); }
  SCHED();
  const rsrc_t rs=__builtin_amdgcn_make_buffer_rsrc(p.ws,0,WS_END,0x00020000);
  const u4 srdS=mksrd(p.ws);
  const int rtB=8*(xc>>1)+(idx&7), ct=4*(xc&1)+(idx>>3);
  const bool hasc=idx<28 && idx+28*(w&3)<80 && w>=4;
  const int dch=80*xc+(hasc? idx+28*(w&3) : 0);
  const u32 cb=smb+CONV+(w&3)*CVS;
  u32 tag; float ksc=0.f, dsk=0.f;
  u32 zp[2][8]={};
  auto z1st=[&](){ const int r=lane&31,h=lane>>5;
    #pragma unroll
    for(int n=0;n<2;n++) if(2*j+n<27){ const u32 oo=OFF_Z1T+((rt*8+2*mi+(r>>4))*27+2*j+n)*1024+(h*16+(r&15))*16;
      st16(rs,oo,u4{zp[n][0],zp[n][1],zp[n][4],zp[n][5]}); st16(rs,oo+512,u4{zp[n][2],zp[n][3],zp[n][6],zp[n][7]}); } };
  auto f1=[&](){ WAITV(0); if(arrive(smb+C_Z1,lane)==3u && lane==0) st4(rs,OFF_F1+(rt*16+j)*128,tag); };
  auto pre01=[&](){
    const u4 sa=mksrd((const char*)p.wsp+(size_t)rtB*64*4096+mi*1024-4096); const u32 v0=(lane>>3)*4096+(((lane&7)^(lane>>4))<<4);
    #pragma unroll 1
    for(int i=0;i<4;i++){ setm0<RB+8192>(smb+mi*RING+i*4096); const u32 g=(i&1)*4+2*mi, k=(i>>1)*128+4096;
      dma<0>(sa,v0,(g&7)*32768+k); dma<1024>(sa,v0^64u,(g&7)*32768+32768-1024+k); dma<2048>(sa,v0,((g+2)&7)*32768-2048+k); dma<3072>(sa,v0^64u,((g+2)&7)*32768+32768-3072+k); } };
  if(idx>=28){
    const int a=xc*4+idx-28, r=lane&15, q=lane>>4;
    const u32 ep=ld4(rs,OFF_EP);
    u4 xr[10]; const char* xs=(const char*)p.x+(size_t)(64*a+8*w+(lane>>3))*1280+(lane&7)*16;
    #pragma unroll
    for(int i=0;i<10;i++) xr[i]=*(const u4*)(xs+i*128);
    { const u32 l=(a*8+w)*19+(lane<19?lane:18); setm0<JNK>(smb); touch(srdS,(l<3584? OFF_ST/128+l : OFF_F2/128+(l-3584<1088?l-3584:1087))*128,0u); }
    if(tid<32) *(LAS u32*)(smb+PM+tid*4)=0u;
    tag=uni(ep)+TAGINC;
    float s0=0.f,s1=0.f,q0=0.f,q1=0.f;
    #pragma unroll
    for(int i=0;i<10;i++){ s0=dot2(xr[i][0],0x3f803f80u,s0); q0=dot2(xr[i][0],xr[i][0],q0); s1=dot2(xr[i][1],0x3f803f80u,s1); q1=dot2(xr[i][1],xr[i][1],q1);
      s0=dot2(xr[i][2],0x3f803f80u,s0); q0=dot2(xr[i][2],xr[i][2],q0); s1=dot2(xr[i][3],0x3f803f80u,s1); q1=dot2(xr[i][3],xr[i][3],q1); }
    s0+=s1; q0+=q1;
    #define DPPADD(v,c) v+=__int_as_float(__builtin_amdgcn_update_dpp(0,__float_as_int(v),c,0xf,0xf,false))
    DPPADD(s0,0xB1); DPPADD(q0,0xB1); DPPADD(s0,0x4E); DPPADD(q0,0x4E); DPPADD(s0,0x141); DPPADD(q0,0x141);
    float m1,ri; { float mean=s0*(1.f/640.f); ln_fin(mean,q0*(1.f/640.f)-mean*mean,m1,ri); }
    if((lane&7)==0) st16(rs,OFF_RS+(64*a+8*w+(lane>>3))*16,u4{__float_as_uint(m1),tag,__float_as_uint(ri),tag});
    if(a<27){ const u4 srdO=mksrd((const char*)p.wout+a*(32*1280)+w*5120); const u32 mo=smb+w*5120; setm0<0>(mo);
      dma<0>(srdO,lane*16,0); dma<1024>(srdO,lane*16,0); dma<2048>(srdO,lane*16,0); dma<3072>(srdO,lane*16,0); setm0<4096>(mo); dma<0>(srdO,lane*16,4096); }
    #pragma unroll
    for(int i=0;i<10;i++){ u4 o;
      #pragma unroll
      for(int e=0;e<4;e++) o[e]=pkhf2(fma2(unpk(xr[i][e]),f2{ri,ri},f2{-m1*ri,-m1*ri}));
      ldsw128(smb+RB+(8*w+(lane>>3))*1296+(lane&7)*16+i*128,o); }
    WAITV(0); bar();
    #pragma unroll
    for(int i=0;i<10;i++){ const int c=w*10+i; const u32 ad=smb+RB+(32*((r>>1)&1)+8*q+(r>>2))*1296+c*16+(r&1)*8; u2 lo=ldstr(ad), hi=ldstr(ad+4*1296);
      const u32 sc=smb+40960+w*2048+(i&1)*1024; ldsw128(sc+(r&7)*128+((((r>>3)*4+q)^(r&7))<<4),u4{lo[0],lo[1],hi[0],hi[1]});
      CFENCE(); const u4 o=ldsr128(sc+(lane>>3)*128+(((lane&7)^(lane>>3))<<4)); CFENCE();
      st16(rs,OFF_XNT+a*81920+c*1024+lane*16,o); }
    WAITV(0);
    if(arrive(smb+C_Z1,lane)==7u && lane==0) st4(rs,OFF_FX+a*128,tag);
    if(a<27){
      #pragma unroll
      for(int i=0;i<5;i++){ u32 ad=smb+(8*q+(r>>2))*1280+(w*5+i)*32+(r&3)*8; u2 lo=ldstr(ad), hi=ldstr(ad+4*1280);
        st16(rs,OFF_WOF+(u32)((w*27+a)*5+i)*1024+lane*16,u4{lo[0],lo[1],hi[0],hi[1]}); } }
    WAITV(0); bar();
    if(tid==0) st4(rs,OFF_FW+a*128,tag);
    if(!g1){ pre01(); WAITV(0); }
    else { const u4 so=mksrd((const char*)p.out+(size_t)(a*4+mi)*20480); setm0<JNK>(smb); touch(so,lane*128,0u); touch(so,lane*128,8192u); touch(so,(lane&31)*128,16384u); }
  } else {
    const int r=lane&31, h=lane>>5;
    u32 epv;
    u32 kx[10]; { u32 k=kr0;
      #pragma unroll
      for(int i=0;i<10;i++){ kx[i]=k*128; k=k==9?0:k+1; } }
    auto issue1=[&](auto sc,int k){ constexpr int s=sc.value;
      if(k==0){ setm0<(s&3)*STG>(mw); dma<0>(srdX,vx0,kx[s]); } if(k==1) dma<1024>(srdX,vx1,kx[s]); if(k==2) dma<2048>(srdW,vw2,kx[s]*1728); if(k==3) dma<3072>(srdW,vw1,kx[s]*1728); };
    auto issue=[&](auto sc){ issue1(sc,0); issue1(sc,1); issue1(sc,2); issue1(sc,3); };
    u32 pr[6];
    gload4<0>(epv,0u,p.ws+OFF_EP);
    gload4<0>(pr[0],lane*4,g1? (const char*)(p.lar+dch*128) : (const char*)p.wsp+(size_t)(xc>>1)*512*4096+((xc&1)*28+idx)*(4*74*128)); gload4<0>(pr[1],lane*4,(const char*)(p.aim+dch*128)); gload4<0>(pr[2],lane*4,(const char*)(p.cre+dch*128)); gload4<0>(pr[3],lane*4,(const char*)(p.cim+dch*128));
    gload2(pr[4],0u,(const char*)(p.ldt+dch)); gload2(pr[5],0u,(const char*)(p.dsk+dch));
    asm volatile("s_barrier" ::: "memory");
    issue(std::integral_constant<int,1>{});
    if(tid<32) *(LAS u32*)(smb+PM+tid*4)=0u;
    u32 ax[2][4],aw[2][4][2];
    { u32 row=32*mi+r, a0=smb+(row>>4)*4096+(row&15)*128+((h^((r>>1)&7))<<4);
      u32 qq=(lane&15)>>2, w0=smb+2048+g1*1024+h*4096+qq*128+8*(lane&1)+(((2*((lane>>4)&1)+((lane>>1)&1))^((qq&2)<<1))<<4);
      #pragma unroll
      for(int i=0;i<2;i++)
        #pragma unroll
        for(int k=0;k<4;k++){ const u32 kk=(k+mi)&3, wo=i*2*STG; ax[i][k]=opq((a0^(kk*32u))+wo); aw[i][k][0]=opq(w0+kk*8192+wo); aw[i][k][1]=opq((w0^64u)+kk*8192+wo); } }
    u4 sel; { const u32 o=((lane&15)==((lane>>4)&1))?0x3f803f80u:0u; sel=u4{o,o,o,o}; asm volatile("" : "+v"(sel)); }
    u32 ga[11];
    #pragma unroll
    for(int i=0;i<11;i++) ga[i]=opq(cb+2u*(u32)rbf((float)(512-8*lane+3-i)));
    Frag F[2];
    asm volatile("s_waitcnt vmcnt(4)" : "+v"(epv),"+v"(pr[0]),"+v"(pr[1]),"+v"(pr[2]),"+v"(pr[3]),"+v"(pr[4]),"+v"(pr[5]) :: "memory");
    bar();
    tag=uni(epv)+TAGINC; dsk=bflo(pr[5]);
    #pragma unroll
    for(int k=0;k<4;k++){
      #pragma unroll
      for(int n=0;n<2;n++){ u2 lo=ldstr(aw[0][k][n]), hi=ldstr(aw[0][k][n]+512); F[0].w[k][n]=u4{lo[0],lo[1],hi[0],hi[1]}; }
      F[0].x[k]=ldsr128(ax[0][k]); }
    issue(std::integral_constant<int,2>{});
    f16v acc[2]={}; f4 cacc[2]={}; u4 G;
    const u32 go=OFF_RS+rt*2048+mi*512, cp=smb+MISC+g1*1024;
    sfor<10>([&](auto ic){
      constexpr int s=ic.value; Frag& cur=F[s&1]; Frag& nxt=F[(s&1)^1];
      if constexpr(s<9){ WAITV(s==8? 3 : s==7? 7 : 4); asm volatile("s_barrier" ::: "memory"); }
      auto rw=[&](int k,int n){ if constexpr(s<9){ const int i=((s+1)>>1)&1; const u32 so=((s+1)&1)*STG; u2 lo=ldstr(aw[i][k][n]+so), hi=ldstr(aw[i][k][n]+so+512); nxt.w[k][n]=u4{lo[0],lo[1],hi[0],hi[1]}; } };
      auto rx=[&](int k){ if constexpr(s<9){ const int i=((s+1)>>1)&1; const u32 so=((s+1)&1)*STG; nxt.x[k]=ldsr128(ax[i][k]+so); } };
      #pragma unroll
      for(int k=0;k<4;k++){
        acc[0]=mfma32b(cur.w[k][0],cur.x[k],acc[0]); SCHED();
        if constexpr(s+3<=9) issue1(std::integral_constant<int,s+3>{},k);
        rw(k,0); rx(k);
        SCHED();
        acc[1]=mfma32b(cur.w[k][1],cur.x[k],acc[1]); SCHED();
        rw(k,1);
        if(k==0){ cacc[0]=mfma16(cur.w[0][0],sel,cacc[0]); cacc[1]=mfma16(cur.w[0][1],sel,cacc[1]); }
        if constexpr(s==6){ if(k==3){ gload<0>(G,r*16,p.ws+go); setm0<JNK>(smb);
            if(g1){ const u4 sp=mksrd((const char*)p.wsp+(size_t)(xc>>1)*512*4096); const u32 lb=(((xc&1)*28+idx)*4+mi)*74, l0=lb+lane, l1=lb+64+(lane<10?lane:9);
              touch(sp,(l0<16384?l0:16383)*128,0u); touch(sp,(l1<16384?l1:16383)*128,0u); }
            else { touch(mksrd(p.out+rtB*64*640),0u,0u); touch(mksrd(w==0? (const void*)(p.bsp+rtB*64) : w==1? (const void*)p.ws : w==2? (const void*)(p.wsp+(size_t)rtB*64*2048) : (const void*)(p.x+rtB*64*640)),(lane&15)*(w==1? (1u<<20) : w==2? 16384u : w==3? 4096u : 0u),0u); } } }
        if constexpr(s==8){ if(k==3) signal(smb+C_DONE,lane); }
        if constexpr(s==9){ if(k==1){
          if((lane&14)==0){ *(LAS f4*)(cp+mi*256+(lane&1)*64+(lane>>4)*16)=cacc[0]; *(LAS f4*)(cp+mi*256+128+(lane&1)*64+(lane>>4)*16)=cacc[1]; }
          signal(smb+C_CS+g1*4,lane); } }
        SCHED();
      }
    });
    if(!g1) __builtin_amdgcn_s_setprio(2);
    asm volatile("s_waitcnt vmcnt(2)" : "+v"(G) :: "memory");
    ldswait(smb+C_CS+g1*4,4u);
    const float cs0=*(LAS float*)(cp+h*512+r*4)+*(LAS float*)(cp+h*512+256+r*4), cs1=*(LAS float*)(cp+h*512+128+r*4)+*(LAS float*)(cp+h*512+384+r*4);
    #pragma unroll 1
    for(int sp=0;sp<SPIN && !__all(G[1]==tag&&G[3]==tag);sp++)asm volatile("global_load_dwordx4 %0, %1, %2 sc1\n\ts_waitcnt vmcnt(0)" : "=&v"(G) : "v"(r*16), "s"(p.ws+go) : "memory");
    const float nm=-__uint_as_float(G[0]), ri1=__uint_as_float(G[2]);
    acc[0]=__builtin_amdgcn_mfma_f32_32x32x2f32(cs0,nm,acc[0],0,0,0); acc[1]=__builtin_amdgcn_mfma_f32_32x32x2f32(cs1,nm,acc[1],0,0,0);
    #pragma unroll
    for(int e=0;e<16;e++) zp[e>>3][e&7]=pkbf2(gelu2(f2{acc[e>>3][2*(e&7)],acc[e>>3][2*(e&7)+1]}*ri1));
    if(g1){ ldswait(smb+C_DONE,8u); }
    else {
      float S=0.f,Q=0.f;
      if(j<13){
        #pragma unroll
        for(int e=0;e<8;e++){ S=dot2(zp[0][e],0x3f803f80u,S); Q=dot2(zp[0][e],zp[0][e],Q); } }
      #pragma unroll
      for(int e=0;e<8;e++){ S=dot2(zp[1][e],0x3f803f80u,S); Q=dot2(zp[1][e],zp[1][e],Q); }
      #define XSUM32(v) do{ auto t_=__builtin_amdgcn_permlane32_swap(__float_as_uint(v),__float_as_uint(v),false,false); v=__uint_as_float(t_[0])+__uint_as_float(t_[1]); }while(0)
      XSUM32(S); XSUM32(Q);
      const u32 sto=OFF_ST+rt*(14*2048)+(32*mi+r)*16;
      if(h==0) st16(rs,sto+j*2048,u4{__float_as_uint(S),tag,__float_as_uint(Q),tag});
    }
    float kp[11]; u4 g[7]={};
    const u32 gof=OFF_ST+rt*(14*2048)+(32*mi+r)*16+h*(7*2048);
    auto samp=[&](){ CFENCE();
      #pragma unroll
      for(int i=0;i<7;i++) g[i]=ld16(rs,gof+i*2048);
      CFENCE(); };
    if(hasc) ksc=kgen_tab(cb,lane,pr,kp); else if(!g1){ pre01(); samp(); }
    if(g1){
      if(hasc){
        kgen_exp(cb,lane,kp,ga);
        signal(smb+C_KP+mi*4,lane);
      }
      if(hasc) ldswait(smb+C_FULL,1u); else if(w<7){ z1st(); f1(); }
      if(hasc) z1st();
    } else {
      float S,Q;
      #pragma unroll 1
      for(int sp=0;sp<SPIN;sp++){ bool ok=true;
        #pragma unroll
        for(int i=0;i<7;i++) ok=ok&&g[i][1]==tag&&g[i][3]==tag;
        if(__all(ok)) break;
        __builtin_amdgcn_s_sleep(4); samp(); }
      S=0.f; Q=0.f;
      #pragma unroll
      for(int i=0;i<7;i++){ S+=__uint_as_float(g[i][0]); Q+=__uint_as_float(g[i][2]); }
      XSUM32(S); XSUM32(Q);
      float mean=S*(1.f/864.f), m2,r2; ln_fin(mean,Q*(1.f/864.f)-mean*mean,m2,r2); m2*=-r2;
      ldswait(smb+C_DONE,8u);
      const u32 tb=smb+RB+mi*RING, ad=tb+(8*(lane>>4)+((lane&15)>>2))*80+(lane&3)*8;
      const u32 zo=OFF_Z2F+((rt*4+mi)*54+(j==13?-2:2+4*j))*1024+lane*16;
      #pragma unroll
      for(int n=0;n<2;n++){
        #pragma unroll
        for(int g2=0;g2<4;g2++){ u2 o;
          #pragma unroll
          for(int e=0;e<2;e++) o[e]=pkbf2(fma2(unpk(zp[n][2*g2+e]),f2{r2,r2},f2{m2,m2}));
          *(LAS u2*)(tb+r*80+g2*16+h*8)=o; }
        CFENCE();
        u2 l0=ldstr(ad), h0=ldstr(ad+320), l1=ldstr(ad+32), h1=ldstr(ad+32+320);
        CFENCE();
        if(j<13||n){ st16(rs,zo+n*2048,u4{l0[0],l0[1],h0[0],h0[1]}); st16(rs,zo+n*2048+1024,u4{l1[0],l1[1],h1[0],h1[1]}); } }
      WAITV(0);
      if(arrive(smb+C_Z2,lane)==3u && lane==0) st4(rs,OFF_F2+(rt*16+j)*128,tag);
    }
  }
  const int ft0=ct<7?7*ct:47;
  bool adone=true; const u32 fao=OFF_FA+((lane<28?lane:0)*8+ct)*128;
  auto aload=[&](){ const u32 vo=(lane>>3)*4096+(lane&7)*16;
    #pragma unroll 1
    for(int i=0;i<10;i++){ setm0<ATL>(smb+i*1024); dma<0>(srdS,vo,OFF_AT+(u32)(80*ct+8*i)*4096+rtB*128); } };
  if(w>=4){
    if(hasc){
      __builtin_amdgcn_s_setprio(3);
      conv_wave(f1,rs,dch,cb,smb+C_KP+mi*4,lane,ksc,dsk);
      __builtin_amdgcn_s_setprio(0);
      if(arrive(smb+C_CV,lane)==(idx<24?2u:1u) && lane==0) st4(rs,OFF_FA+b*128,tag);
    }
    if(w==7){
      if(idx<28){
        pollw<16>(rs,OFF_FX+(lane&31)*128,tag);
        u4 sw[3][4];
        #pragma unroll
        for(int c=0;c<3;c++)
          #pragma unroll
          for(int i=0;i<4;i++) sw[c][i]=ld16(rs,OFF_XNT+((lane>>3)+8*i)*81920+(80*xc+idx+((c<2||idx<24)?28*c:0))*128+(lane&7)*16);
        #pragma unroll
        for(int c=0;c<3;c++){
          #pragma unroll
          for(int i=0;i<4;i++){ const int x=lane+64*i; ldsw128(smb+c*CVS+UOF+(x&3)*1040+((x>>2)+1)*16,sw[c][i]); }
          if(lane<4) ldsw128(smb+c*CVS+UOF+lane*1040,u4{0,0,0,0}); }
        signal(smb+C_FULL,lane);
        #pragma unroll 1
        for(int c=0;c<3;c++){
          #pragma unroll
          for(int i=0;i<4;i++){ sw[0][i]=sw[1][i]; sw[1][i]=sw[2][i]; }
          conv_win(smb+c*CVS,lane,sw[2]); }
        __builtin_amdgcn_s_setprio(3);
        #pragma unroll 1
        for(int c=0;c<(idx<24?3:2);c++){ ldswait<4>(smb+C_KP+c*4,1u); conv_help(smb+c*CVS,lane,sw[0]); signal(smb+C_KP+c*4,lane);
          #pragma unroll
          for(int i=0;i<4;i++){ sw[0][i]=sw[1][i]; sw[1][i]=sw[2][i]; } } }
      __builtin_amdgcn_s_setprio(0);
      if(idx<28){ z1st(); f1(); }
      pollw<16>(rs,OFF_FW+(lane<27?lane:0)*128,tag); signal(smb+C_Z1R,lane);
    }
    ldswait<6>(smb+C_Z1R,5u);
    u4 wq[24];
    #pragma unroll
    for(int i=0;i<24;i++) wq[i]=ld16(rs,OFF_WOF+ct*(135*1024)+40960+mi*RING+((i==23&&mi==3)?22:i)*1024+lane*16);
    if(w<6){
      #pragma unroll 1
      for(u32 i=0;i<20480;i+=4096){ setm0<0>(cb+i); const u32 o=OFF_WOF+ct*(135*1024)+mi*20480+i; dma<0>(srdS,lane*16,o); dma<1024>(srdS,lane*16,o); dma<2048>(srdS,lane*16,o); dma<3072>(srdS,lane*16,o); } }
    if(w==6){ adone=__all(ld4(rs,fao)==tag); if(adone) aload(); }
    ldswait(smb+C_FREE,4u);
    #pragma unroll
    for(int i=0;i<24;i++) ldsw128(smb+RB+mi*RING+i*1024+lane*16,wq[i]);
    if(w<6) WAITV(0);
    signal(smb+C_WR,lane);
    if(!adone){ adone=__all(ld4(rs,fao)==tag); if(adone) aload(); }
    if(w==7 && b==255){
      #pragma unroll 1
      for(int sp=0;sp<SPIN;sp++){ const u32 o=OFF_FA+lane*128; u32 v0=ld4(rs,o),v1=ld4(rs,o+8192),v2=ld4(rs,o+16384),v3=ld4(rs,lane<32? o+24576 : OFF_FW+(lane-32)*128); CFENCE();
        if(__all(v0==tag&&v1==tag&&v2==tag&&v3==tag)) break; __builtin_amdgcn_s_sleep(4); }
      if(lane==0) st4(rs,OFF_EP,tag); }
  }
  int w2=w; asm volatile("" : "+s"(w2));
  if(w2<4){
    __builtin_amdgcn_s_setprio(2);
    int r=lane&15, q=lane>>4;
    const u32 rb=smb+RB+w*RING;
    u4 srdA=mksrd((const char*)p.wsp+(size_t)rtB*64*4096+w*1024-4096+256);
    const char* zb=p.ws+OFF_Z2F+(u32)(16*w*54+ft0+3)*1024;
    u32 voA[2]; { u32 lr=lane>>3,c=lane&7; voA[0]=lr*4096+((c^(lr>>1))<<4); voA[1]=voA[0]^64u; }
    u32 sofa[8];
    #pragma unroll
    for(int g=0;g<8;g++) sofa[g]=((g+2*w)&7)*32768+4096-(g&3)*1024;
    const u32 voB=lane*16;
    u32 la[2]; la[0]=rb+r*128+((q^(r>>1))<<4); la[1]=la[0]^64u;
    f4 acc[4][7]; u4 B[NB][7]; u4 A[2][4]; u2 z1[7];
    auto issueA=[&](auto sc,auto gc){ constexpr int s=sc.value,g=gc.value; if constexpr((g&3)==0) setm0<((s+1)%3)*8192+g*1024>(rb); dma<(g&3)*1024>(srdA,voA[g&1],sofa[g]);
      if constexpr(g==7){ u64 a=((u64)srdA[1]<<32|srdA[0])+128; srdA[0]=(u32)a; srdA[1]=(u32)(a>>32); } };

    u32 bsr,fv;
    auto fof=[&](){ const u32 l=voB>>4; return OFF_F1+((rtB>>1)*16+(((ft0>>1)+(l&3))>>1))*128; };
    #pragma unroll
    for(int m=0;m<4;m++){ A[0][m]=ldsr128(la[0]+m*2048+8192); A[1][m]=ldsr128(la[1]+m*2048+8192); }
    sfor<8>([&](auto g){ issueA(std::integral_constant<int,2>{},g); });
    waitA(A[0]); waitA(A[1]);
    sfor<8>([&](auto g){ issueA(std::integral_constant<int,3>{},g); });
    { const int hf=(ft0>>1)+q; pollw(rs,OFF_F2+((4*w+(lane&3))*16+(hf==0?13:(hf-1)>>1))*128,tag); }
    sfor<NB>([&](auto hh){ const char* s=zb+(u32)hh.value*(54*1024); sfor<7>([&](auto n){ gload<(n.value-3)*1024>(B[hh][n],voB,s); }); });
    auto redw=[&](auto nc){ constexpr int n=nc.value; *(LAS f4*)(rb+n*1024+voB)=acc[1][n]; *(LAS f4*)(rb+(7+n)*1024+voB)=acc[2][n]; *(LAS f4*)(rb+(14+n)*1024+voB)=acc[3][n]; };
    const char* z1b=p.ws+OFF_Z1T+(u32)((rtB*4+w)*27+(ft0>>1))*1024;
    sfor<16>([&](auto hc){
      constexpr int h=hc.value, S=h%NB, P=h&1, HZ=HZZ, cnt=g2cnt(h);
      waitB<cnt>(B[S]); waitA(A[P]);
      if constexpr(h==HZ){ tie1(fv); if(!__all(fv==tag)) pollw(rs,fof(),tag); }
      if constexpr(h+1<16 && h>0){
        #pragma unroll
        for(int m=0;m<4;m++) A[P^1][m]=ldsr128(la[(h+1)&1]+m*2048+((((h+1)>>1)+1)%3)*8192);
      }
      if constexpr(h==HZ-3) gload4<0>(fv,fof(),p.ws);
      const char* s=zb+(u32)(h+NB)*(54*1024);
      sfor<7>([&](auto n){
        if constexpr(h==0) mfma4z(acc[0][n],acc[1][n],acc[2][n],acc[3][n],B[S][n],A[P]); else mfma4(acc[0][n],acc[1][n],acc[2][n],acc[3][n],B[S][n],A[P]);
        if constexpr(h>=3 && h<=10 && (n.value&1)==0) issueA(std::integral_constant<int,(h-1)/2+3>{},std::integral_constant<int,((h-1)&1)*4+n.value/2>{});
        if constexpr(h+NB<16) gload_t<(n.value-3)*1024>(B[S][n],voB,s);
        if constexpr(h==HZ){ gload8<(n.value>>1)*1024>(z1[n],voB,z1b+((n.value&1)? ((ft0&1)?1024:8) : (ft0&1)*8));
          if constexpr(n.value==6) { gload2(bsr,(voB>>3)&30u,(const char*)(p.bsp+(rtB*4+w)*16)); } }
        if constexpr(h==13 && n.value==0) signal(smb+C_Z1R,lane);
        if constexpr(h==15){ SCHED(); if constexpr(n.value>=2) redw(std::integral_constant<int,n.value-2>{}); SCHED(); }
      });
    });
    asm volatile("s_nop 15\n\ts_nop 3" ::: "memory");
    SCHED();
    lane=__builtin_amdgcn_mbcnt_hi(~0u,__builtin_amdgcn_mbcnt_lo(~0u,0u)); asm volatile("" : "+v"(lane)); r=lane&15; q=lane>>4;
    redw(std::integral_constant<int,5>{}); redw(std::integral_constant<int,6>{});
    signal(smb+C_RED,lane);
    ldswait(smb+C_RED,4u);
    #pragma unroll
    for(int m=1;m<4;m++){ u32 src=smb+RB+((w-m)&3)*RING+(m-1)*7168+lane*16;
      #pragma unroll
      for(int n=0;n<7;n++) acc[0][n]+=*(LAS f4*)(src+n*1024); }
    asm volatile("s_waitcnt vmcnt(0) lgkmcnt(0)" : "+v"(z1[0]),"+v"(z1[1]),"+v"(z1[2]),"+v"(z1[3]),"+v"(z1[4]),"+v"(z1[5]),"+v"(z1[6]),"+v"(bsr) :: "memory");
    const float bs=bflo(bsr);
    signal(smb+C_FREE,lane);
    u2 o[7];
    #pragma unroll
    for(int n=0;n<7;n++)
      #pragma unroll
      for(int e=0;e<2;e++)o[n][e]=pkbf2(unpk(z1[n][e])*(f2{acc[0][n][2*e],acc[0][n][2*e+1]}+bs));
    const u32 ho=OFF_HF+(u32)((rtB*4+w)*54+ft0)*512+(q>>1)*256+r*16;
    #pragma unroll
    for(int n=0;n<6;n+=2){ auto sx=__builtin_amdgcn_permlane16_swap(o[n][0],o[n+1][0],false,false); auto sy=__builtin_amdgcn_permlane16_swap(o[n][1],o[n+1][1],false,false);
      if(ct<7||n>=2) st16(rs,ho+(n+(q&1))*512,u4{sx[0],sy[0],sx[1],sy[1]}); }
    st8(rs,ho+6*512+(q&1)*8,o[6]);
    WAITV(0);
    if(arrive(smb+C_H,lane)==3u && lane==0) st4(rs,OFF_FH+(rtB*8+ct)*128,tag);
    __builtin_amdgcn_s_setprio(0);
  }
  { const int r=lane&15, q=lane>>4, m=w&3, kh=w>>2;
    u2 xv[5]={};
    if(kh){
      #pragma unroll
      for(int n=0;n<5;n++) xv[n]=*(const u2*)(p.x+(rtB*64+m*16+r)*640+80*ct+16*n+4*q);
    }
    f4 acc[5]={}; u4 W[3][5];
    const u32 wl2=smb+lane*16+RB-40960+kh*14*5120, wl0=kh? wl2 : smb+lane*16, wl1=kh? wl2 : smb+lane*16+CVS-20480;
    #define WAD(k,n) (((k)<4? wl0 : (k)<8? wl1 : wl2)+((k)*5+(n))*1024)
    f4 ax[5]={};
    auto addax=[&](){
      #pragma unroll
      for(int n=0;n<5;n++){ u2 av=ldstr(smb+ATL+(16*n+4*q+(r>>2))*128+(16*m+4*(r&3))*2); ax[n]=f4{bflo(av[0]),bfhi(av[0]),bflo(av[1]),bfhi(av[1])}+f4{bflo(xv[n][0]),bfhi(xv[n][0]),bflo(xv[n][1]),bfhi(xv[n][1])}; } };
    if(w==6 && adone){ WAITV(0); signal(smb+C_AT,lane); }
    u32 oo[3],to[3]; const u32 tb=smb+CVS+m*3072;
    #pragma unroll
    for(int i=0;i<3;i++){ const int it=lane+64*i, rr=(it*205)>>11, cc=it-rr*10; oo[i]=opq((rtB*64+m*16+rr)*1280+ct*160+cc*16); to[i]=opq(tb+rr*176+cc*16); }
    if(m==0){ ldswait(smb+C_H,4u); pollw(rs,OFF_FH+(rtB*8+kh*4+(lane&3))*128,tag); signal(smb+C_TAG+kh*4,lane); } else ldswait(smb+C_TAG+kh*4,1u);
    u4 hf[14];
    const u32 ho=OFF_HF+(u32)((rtB*4+m)*27+kh*14)*1024+lane*16;
    #pragma unroll
    for(int k=0;k<HPF;k++) hf[k]=ld16(rs,ho+k*1024);
    const bool early=kh && uni(ldsv(smb+C_AT))!=0u;
    CFENCE();
    if(early) addax();
    ldswait(smb+C_WR,4u);
    const u32 wl3=kh? wl2-5120 : wl2;
    #pragma unroll
    for(int n=0;n<10;n++) W[n/5][n%5]=ldsr128(WAD(n/5,n%5));
    #pragma unroll
    for(int k=0;k<13;k++){
      if(k+HPF<14) hf[k+HPF]=ld16(rs,ho+((k+HPF==13&&kh)?12:k+HPF)*1024);
      SCHED();
      #pragma unroll
      for(int n=0;n<5;n++){ acc[n]=mfma16(W[k%3][n],hf[k],acc[n]); SCHED();
        if(k<11) W[(k+2)%3][n]=ldsr128(WAD(k+2,n)); else if(k==11) W[1][n]=ldsr128(wl3+(65+n)*1024);
        SCHED(); }
      if(k==5 && !kh) signal(smb+C_G1,lane);
    }
    const u32 pb=smb+m*5120+lane*16;
    if(!kh){
      #pragma unroll
      for(int n=0;n<5;n++) acc[n]=mfma16(W[1][n],hf[13],acc[n]);
      ldswait(smb+C_G1,4u);
      #pragma unroll
      for(int n=0;n<5;n++) *(LAS f4*)(pb+n*1024)=acc[n];
      signal(smb+C_P+m*4,lane);
    } else {
      if(w==6 && !adone){ pollw(rs,fao,tag); aload(); WAITV(0); signal(smb+C_AT,lane); }
      if(!early){ ldswait(smb+C_AT,1u); addax(); }
      ldswait(smb+C_P+m*4,1u);
      #pragma unroll
      for(int n=0;n<5;n++){ const f4 o=(acc[n]+ax[n])+*(LAS f4*)(pb+n*1024); *(LAS u2*)(tb+r*176+n*32+q*8)=u2{pkbf(o[0],o[1]),pkbf(o[2],o[3])}; }
      CFENCE();
      #pragma unroll
      for(int i=0;i<3;i++) if(i<2||lane<32) __builtin_amdgcn_raw_buffer_store_b128(ldsr128(to[i]),__builtin_amdgcn_make_buffer_rsrc(p.out,0,2048*1280,0x00020000),oo[i],0,16);
    }
  }
}
extern "C" void kernel_launch(void* const* d_in,const int* in_sizes,int n_in,void* d_out,int out_size,void* d_ws,size_t ws_size,hipStream_t stream){
  Params p;
  p.x=(const u16*)d_in[0]; p.lar=(const u16*)d_in[1]; p.aim=(const u16*)d_in[2]; p.cre=(const u16*)d_in[3]; p.cim=(const u16*)d_in[4];
  p.ldt=(const u16*)d_in[5]; p.dsk=(const u16*)d_in[6]; p.win=(const u16*)d_in[8]; p.wsp=(const u16*)d_in[9]; p.bsp=(const u16*)d_in[10]; p.wout=(const u16*)d_in[11];
  p.out=(u16*)d_out; p.ws=(char*)d_ws;
  (void)hipFuncSetAttribute((const void*)fused,hipFuncAttributeMaxDynamicSharedMemorySize,LDS_TOTAL);
  fused<<<256,512,LDS_TOTAL,stream>>>(p);
}
```

```cpp
#include <hip/hip_runtime.h>
#include <utility>
#pragma clang diagnostic ignored "-Wint-to-pointer-cast"
typedef unsigned short u16;
typedef unsigned int u32;
typedef unsigned long long u64;
typedef __attribute__((ext_vector_type(8))) __bf16 bfx8;
typedef __attribute__((ext_vector_type(8))) _Float16 hfx8;
typedef __attribute__((ext_vector_type(2))) __bf16 bf2;
typedef __attribute__((ext_vector_type(2))) _Float16 hf2;
typedef __attribute__((ext_vector_type(2))) float f2;
typedef __attribute__((ext_vector_type(4))) float f4;
typedef __attribute__((ext_vector_type(16))) float f16v;
typedef __attribute__((ext_vector_type(4))) u32 u4;
typedef __attribute__((ext_vector_type(2))) u32 u2;
typedef __attribute__((ext_vector_type(4))) short s4;
#define LAS __attribute__((address_space(3)))
#define DI __device__ __forceinline__

constexpr u32 OFF_Z2F = 0;
constexpr u32 OFF_Z1T = OFF_Z2F + 64*54*1024;
constexpr u32 OFF_XNT = OFF_Z1T + 128*27*1024;
constexpr u32 OFF_WOF = OFF_XNT + 640*4096;
constexpr u32 OFF_HF  = OFF_WOF + 8*27*5*1024;
constexpr u32 OFF_AT  = OFF_HF + 128*27*1024;
constexpr u32 OFF_ST  = OFF_AT + 640*4096;
constexpr u32 OFF_RS  = OFF_ST + 16*14*2048+8192;
constexpr u32 OFF_EP  = OFF_RS + 2048*16;
constexpr u32 OFF_F2  = OFF_EP + 256;
constexpr u32 OFF_F1  = OFF_F2 + 256*128;
constexpr u32 OFF_FX  = OFF_F1 + 256*128;
constexpr u32 OFF_FW  = OFF_FX + 32*128;
constexpr u32 OFF_FH  = OFF_FW + 32*128;
constexpr u32 OFF_FA  = OFF_FH + 256*128;
constexpr u32 WS_END  = OFF_FA + 256*128;

struct Params {
  const u16 *x, *lar, *aim, *cre, *cim, *ldt, *dsk, *win, *wsp, *bsp, *wout;
  u16* out; char* ws;
};
template<class F,int... I> DI void sfor_impl(F&& f, std::integer_sequence<int,I...>){ (f(std::integral_constant<int,I>{}),...); }
template<int N,class F> DI void sfor(F&& f){ sfor_impl(f, std::make_integer_sequence<int,N>{}); }

DI float bflo(u32 v){ return __uint_as_float(v<<16); }
DI float bfhi(u32 v){ return __uint_as_float(v&0xffff0000u); }
DI u32 pkbf(float a,float b){ f2 v={a,b}; return __builtin_bit_cast(u32,__builtin_convertvector(v,bf2)); }
DI u32 pkhf(float a,float b){ f2 v={a,b}; return __builtin_bit_cast(u32,__builtin_convertvector(v,hf2)); }
DI float rbf(float a){ return bflo(pkbf(a,a)); }
DI float hlo(u32 v){ return (float)__builtin_bit_cast(hf2,v)[0]; }
DI float hhi(u32 v){ return (float)__builtin_bit_cast(hf2,v)[1]; }
DI float dot2(u32 a,u32 b,float c){ return __builtin_amdgcn_fdot2_f32_bf16(__builtin_bit_cast(bf2,a),__builtin_bit_cast(bf2,b),c,false); }
DI f4 mfma16(u4 a,u4 b,f4 c){ return __builtin_amdgcn_mfma_f32_16x16x32_bf16(__builtin_bit_cast(bfx8,a),__builtin_bit_cast(bfx8,b),c,0,0,0); }
DI f16v mfma32h(u4 a,u4 b,f16v c){ return __builtin_amdgcn_mfma_f32_32x32x16_f16(__builtin_bit_cast(hfx8,a),__builtin_bit_cast(hfx8,b),c,0,0,0); }
DI u4 ldsr128(u32 a){ return *(LAS u4*)a; }
DI u2 ldsr64(u32 a){ return *(LAS u2*)a; }
DI void ldsw128(u32 a,u4 v){ *(LAS u4*)a=v; }
DI u2 ldstr(u32 a){ return __builtin_bit_cast(u2,__builtin_amdgcn_ds_read_tr16_b64_v4i16((LAS s4*)a)); }
DI u32 uni(u32 v){ return __builtin_amdgcn_readfirstlane(v); }

template<int OFF> DI void gload(u4& dst,u32 voff,const char* base){ asm volatile("global_load_dwordx4 %0, %1, %2 offset:%3 sc1" : "=v"(dst) : "v"(voff), "s"(base), "n"(OFF)); }
template<int OFF> DI void gload_t(u4& dst,u32 voff,const char* base){ asm volatile("global_load_dwordx4 %0, %1, %2 offset:%3 sc1" : "+v"(dst) : "v"(voff), "s"(base), "n"(OFF)); }
DI void mfma4(f4& c0,f4& c1,f4& c2,f4& c3,const u4& a,const u4 (&b)[4]){
  asm volatile("v_mfma_f32_16x16x32_bf16 %0, %4, %5, %0\n\tv_mfma_f32_16x16x32_bf16 %1, %4, %6, %1\n\tv_mfma_f32_16x16x32_bf16 %2, %4, %7, %2\n\tv_mfma_f32_16x16x32_bf16 %3, %4, %8, %3"
    : "+v"(c0),"+v"(c1),"+v"(c2),"+v"(c3) : "v"(a),"v"(b[0]),"v"(b[1]),"v"(b[2]),"v"(b[3])); }
DI void mfma4z(f4& c0,f4& c1,f4& c2,f4& c3,const u4& a,const u4 (&b)[4]){
  asm volatile("v_mfma_f32_16x16x32_bf16 %0, %4, %5, 0\n\tv_mfma_f32_16x16x32_bf16 %1, %4, %6, 0\n\tv_mfma_f32_16x16x32_bf16 %2, %4, %7, 0\n\tv_mfma_f32_16x16x32_bf16 %3, %4, %8, 0"
    : "=&v"(c0),"=&v"(c1),"=&v"(c2),"=&v"(c3) : "v"(a),"v"(b[0]),"v"(b[1]),"v"(b[2]),"v"(b[3])); }
template<int CNT> DI void waitB(u4 (&B)[7]){ asm volatile("s_waitcnt vmcnt(%7)" : "+v"(B[0]),"+v"(B[1]),"+v"(B[2]),"+v"(B[3]),"+v"(B[4]),"+v"(B[5]),"+v"(B[6]) : "n"(CNT) : "memory"); }
DI void waitA(u4 (&A)[4]){ asm volatile("s_waitcnt lgkmcnt(0)" : "+v"(A[0]),"+v"(A[1]),"+v"(A[2]),"+v"(A[3]) :: "memory"); }
#define WAITV(n) asm volatile("s_waitcnt vmcnt(%0)" :: "n"(n) : "memory")
#define WAITL0() asm volatile("s_waitcnt lgkmcnt(0)" ::: "memory")
#define CFENCE() asm volatile("" ::: "memory")
#define SCHED() __builtin_amdgcn_sched_barrier(0)
DI void bar(){ asm volatile("s_waitcnt lgkmcnt(0)\n\ts_barrier" ::: "memory"); }
DI f2 unpk(u32 v){ return f2{bflo(v),bfhi(v)}; }
DI u32 pkbf2(f2 v){ return __builtin_bit_cast(u32,__builtin_convertvector(v,bf2)); }
DI u32 pkhf2(f2 v){ return __builtin_bit_cast(u32,__builtin_convertvector(v,hf2)); }
DI f2 fma2(f2 a,f2 b,f2 c){ return __builtin_elementwise_fma(a,b,c); }
DI f2 gelu2(f2 x){
  f2 u=x*fma2(x*x,f2{-0.10294324f,-0.10294324f},f2{-2.30220820f,-2.30220820f});
  f2 d=f2{__builtin_amdgcn_exp2f(u[0]),__builtin_amdgcn_exp2f(u[1])}+f2{1.f,1.f};
  return x*f2{__builtin_amdgcn_rcpf(d[0]),__builtin_amdgcn_rcpf(d[1])};
}
DI void ln_fin(float mean,float var,float& m,float& rinv){
  m=rbf(mean); float v=rbf(__builtin_fmaxf(var,0.f));
  float s=rbf(__builtin_amdgcn_sqrtf(rbf(v+1e-5f))); float r=__builtin_amdgcn_rcpf(s); rinv=__builtin_fmaf(__builtin_fmaf(-s,r,1.f),r,r);
}
typedef __amdgpu_buffer_rsrc_t rsrc_t;
DI void st16(rsrc_t rs,u32 off,u4 v){ __builtin_amdgcn_raw_buffer_store_b128(v,rs,off,0,16); }
DI void st8(rsrc_t rs,u32 off,u2 v){ __builtin_amdgcn_raw_buffer_store_b64(v,rs,off,0,16); }
DI void st4(rsrc_t rs,u32 off,u32 v){ __builtin_amdgcn_raw_buffer_store_b32(v,rs,off,0,16); }
DI u4 ld16(rsrc_t rs,u32 off){ return __builtin_amdgcn_raw_buffer_load_b128(rs,off,0,16); }
DI u32 ld4(rsrc_t rs,u32 off){ return __builtin_amdgcn_raw_buffer_load_b32(rs,off,0,16); }
constexpr int SPIN=1<<17;
constexpr u32 TAGINC=0x9E3779B9u;
template<int SL=2> DI void pollw(rsrc_t rs,u32 off,u32 tag){
  #pragma unroll 1
  for(int s=0;s<SPIN;s++){ u32 v=ld4(rs,off); CFENCE(); if(__all(v==tag)) break; __builtin_amdgcn_s_sleep(SL); }
}

DI f16v mfma32b(u4 a,u4 b,f16v c){ return __builtin_amdgcn_mfma_f32_32x32x16_bf16(__builtin_bit_cast(bfx8,a),__builtin_bit_cast(bfx8,b),c,0,0,0); }
DI u4 mksrd(const void* b){ u64 a=(u64)b; return u4{(u32)a,(u32)(a>>32)&0xffffu,0xffffffffu,0x00020000u}; }
template<int IMM> DI void setm0(u32 base){ asm volatile("s_add_u32 m0, %0, %1\n\ts_nop 0" :: "s"(base), "n"(IMM) : "scc","memory"); }
template<int OFF> DI void dma(u4 srd,u32 voff,u32 soff){ asm volatile("buffer_load_dwordx4 %0, %1, %2 offen offset:%3 sc1 lds" :: "v"(voff), "s"(srd), "s"(soff), "n"(OFF) : "memory"); }
DI void touch(u4 srd,u32 voff,u32 soff){ asm volatile("buffer_load_dword %0, %1, %2 offen sc1 lds" :: "v"(voff), "s"(srd), "s"(soff) : "memory"); }
template<int OFF> DI void gload8(u2& dst,u32 voff,const char* base){ asm volatile("global_load_dwordx2 %0, %1, %2 offset:%3 sc1" : "=v"(dst) : "v"(voff), "s"(base), "n"(OFF)); }
DI void gload2(u32& dst,u32 voff,const char* base){ asm volatile("global_load_ushort %0, %1, %2" : "=v"(dst) : "v"(voff), "s"(base)); }
DI u32 arrive(u32 a,int){ u32 o; asm volatile("s_mov_b64 exec, 1\n\tds_add_rtn_u32 %0, %1, %2\n\ts_mov_b64 exec, -1\n\ts_waitcnt lgkmcnt(0)" : "=&v"(o) : "v"(a), "v"(1u) : "memory"); return uni(o); }
template<int SL=1> DI void ldswait(u32 a,u32 n){ while(__hip_atomic_load((LAS u32*)a,__ATOMIC_RELAXED,__HIP_MEMORY_SCOPE_WORKGROUP)<n) __builtin_amdgcn_s_sleep(SL); CFENCE(); }
DI void tie1(u32& g){ asm volatile("" : "+v"(g) :: "memory"); }
DI u32 opq(u32 v){ asm volatile("" : "+v"(v)); return v; }

constexpr u32 STG=32768;
constexpr u32 RING=24576, RB=62848, CONV=0, KP=2208, UOF=16768, CVS=UOF+4160, S7O=4*KP, S9O=S7O+2112, KZO=S9O+2112, MISC=RB+4*RING, JNK=MISC+2048, PM=163840-128, LDS_TOTAL=163840;
constexpr u32 ATL=2*CVS;
constexpr u32 C_FULL=PM, C_G1=PM+4, C_Z2=PM+8, C_Z1=PM+12, C_RED=PM+16, C_H=PM+20, C_FREE=PM+24, C_CV=PM+28, C_Z1R=PM+32, C_TAG=PM+40, C_KP=PM+48, C_DONE=PM+64, C_CS=PM+72, C_P=PM+96, C_AT=PM+112, C_WR=PM+116;
constexpr u32 CODE_BYTES=37888;
constexpr int HPF=5;
DI void signal(u32 a,int){ asm volatile("s_mov_b64 exec, 1\n\tds_add_u32 %0, %1\n\ts_mov_b64 exec, -1" :: "v"(a), "v"(1u) : "memory"); }
template<int OFF> DI void gload4(u32& dst,u32 voff,const char* base){ asm volatile("global_load_dword %0, %1, %2 offset:%3 sc1" : "=v"(dst) : "v"(voff), "s"(base), "n"(OFF)); }

DI f2 cmulp(f2 a,f2 b,f2 bs){ return fma2(f2{a[1],a[1]},bs,f2{a[0],a[0]}*b); }
DI u32 ldsv(u32 a){ return __hip_atomic_load((LAS u32*)a,__ATOMIC_RELAXED,__HIP_MEMORY_SCOPE_WORKGROUP); }
DI f2 cpow_dir(float lr,float rv,float pw){ float e=__builtin_amdgcn_exp2f(lr*pw); float f=__builtin_amdgcn_fractf(rv*pw);
  return f2{e*__builtin_amdgcn_cosf(f),e*__builtin_amdgcn_sinf(f)}; }
DI u32 dup(u32 a){ return __builtin_amdgcn_perm(a,a,0x01000100u); }
DI u32 cat(u32 lo,u32 hi){ return __builtin_amdgcn_perm(hi,lo,0x05040100u); }
DI u32 lds16(u32 a){ return *(LAS u16*)a; }

DI float kgen_tab(u32 cb,int lane,const u32 (&pr)[6],float (&kp)[11]){
  const float dt=rbf(__expf(bflo(pr[4])));
  const float cr[2]={bflo(pr[2]),bfhi(pr[2])}, cm[2]={bflo(pr[3]),bfhi(pr[3])};
  float mx=fmaxf(fmaxf(fabsf(cr[0]),fabsf(cm[0])),fmaxf(fabsf(cr[1]),fabsf(cm[1])));
  u32 eb=__float_as_uint(mx)&0x7f800000u;
  #define DPPMAX(c) eb=max(eb,(u32)__builtin_amdgcn_update_dpp(0,(int)eb,c,0xf,0xf,false))
  DPPMAX(0x128); DPPMAX(0x124); DPPMAX(0x122); DPPMAX(0x121);
  eb=max(max((u32)__builtin_amdgcn_readlane(eb,0),(u32)__builtin_amdgcn_readlane(eb,16)),max((u32)__builtin_amdgcn_readlane(eb,32),(u32)__builtin_amdgcn_readlane(eb,48))); eb=eb<0x01000000u?0x3f800000u:(eb>0x7e000000u?0x7e000000u:eb);
  const float ksc=2.f*dt*__uint_as_float(eb), isc=__uint_as_float(0x7f000000u-eb);
  const int r=lane&31,h=lane>>5;
  const u32 TP=272, TA=cb, TB=cb+22*TP;
  f16v acc={};
  f2 an[2][4];
  #pragma unroll
  for(int c=0;c<2;c++){ const float l_a=c?bfhi(pr[0]):bflo(pr[0]), i_m=c?bfhi(pr[1]):bflo(pr[1]);
    const float lr=(-rbf(__expf(l_a))*dt)*1.44269504f, rv=(i_m*dt)*0.15915494f;
    an[c][0]=cpow_dir(lr,rv,1.f); an[c][1]=cpow_dir(lr,-rv,8.f); an[c][2]=cpow_dir(lr,-rv,16.f); an[c][3]=cpow_dir(lr,rv,256.f); }
  u32 hold[54]; const u32 wt=TA+lane*4;
  struct P2 { f2 v[2]; };
  auto mul=[&](const P2& x,int k){ P2 o;
    #pragma unroll
    for(int c=0;c<2;c++) o.v[c]=cmulp(x.v[c],an[c][k],f2{-an[c][k][1],an[c][k][0]});
    return o; };
  auto put=[&](int row,const P2& x){ *(LAS u32*)(wt+row*TP)=pkhf2(x.v[0]); hold[row]=pkhf2(x.v[1]); };
  P2 e={{f2{1.f,0.f},f2{1.f,0.f}}}, o={{an[0][1],an[1][1]}}, bk[8], t;
  bk[0].v[0]=f2{cr[0]*isc,cm[0]*isc}; bk[0].v[1]=f2{cr[1]*isc,cm[1]*isc}; put(0,bk[0]); t=bk[0];
  constexpr int tr[8]={8,10,11,16,17,20,21,18};
  #pragma unroll
  for(int k=0;k<16;k++){ put(22+2*k,e); put(23+2*k,o);
    if(k<15){ if(k) e=mul(e,2); else { e.v[0]=an[0][2]; e.v[1]=an[1][2]; } o=mul(o,2); }
    if(k<8){ t=mul(t,3); put(tr[k],t); }
    if(k<7){ bk[k+1]=mul(bk[k],0); put(k+1,bk[k+1]); }
    if(k==8){ put(9,mul(bk[2],3)); put(13,mul(bk[6],3)); t=mul(bk[4],3); put(12,t); }
    if(k==9||k==10){ t=mul(t,3); put(5+k,t); }
    SCHED(); }
  const int ra=(r>21||r==19)?0:r;
  #pragma unroll
  for(int c=0;c<2;c++){
    CFENCE();
    u4 fa[8],fb[8];
    #pragma unroll
    for(int ks=0;ks<8;ks++){ fa[ks]=ldsr128(TA+ra*TP+ks*32+h*16); fb[ks]=ldsr128(TB+r*TP+ks*32+h*16); }
    CFENCE();
    if(c==0){
      #pragma unroll
      for(int i=0;i<54;i++) if(i!=19) *(LAS u32*)(wt+i*TP)=hold[i]; }
    #pragma unroll
    for(int ks=0;ks<8;ks++) acc=mfma32h(fa[ks],fb[ks],acc);
  }
  CFENCE();
  #pragma unroll
  for(int i=0;i<11;i++) kp[i]=acc[i];
  return ksc;
}
DI void kgen_exp(u32 cb,int lane,const float (&acc)[11],const u32 (&ga)[11]){
  const int r=lane&31,h=lane>>5;
  { const u32 a4=cb+r*16+h*8, a5=cb+r*16+h*1024, az=cb+KZO+r*2+h*128;
    #define PK(addr,base,reg) do{ *(LAS _Float16*)((addr)+2*(base))=(_Float16)acc[reg]; }while(0)
    PK(a4,0,0);PK(a4,1,1);PK(a4,2,2);PK(a4,3,3); PK(a4,256,4);PK(a4,258,5);PK(a4,512,6);PK(a4,768,7);
    PK(a5,1024,8); PK(az,255,10); PK(az,127,8); PK(az,159,9);
    #undef PK
    CFENCE();
    if(lane<16) ldsw128(cb+KZO+lane*16,u4{0,0,0,0});
    CFENCE();
  }
  { const u32 g0=cb+2*(1024-8*lane);
    u32 f2_=lds16(g0+(lane?8:0)), a2=lds16(g0), m2=lds16(g0-8), b2=lds16(g0-16);
    u32 e[11];
    #pragma unroll
    for(int j=0;j<11;j++) e[j]=lds16(ga[j]);
    u32 k01=*(LAS u32*)cb, k23=*(LAS u32*)(cb+4), kl=lds16(cb+2048);
    WAITL0();
    const u32 wo=cb+96+lane*16;
    { u32 aa=dup(a2),mm=dup(m2),bb=dup(b2),am=cat(a2,m2),fa=cat(f2_,a2),mb=cat(m2,b2);
      ldsw128(wo,u4{aa,am,mm,bb}); ldsw128(wo+KP,u4{aa,aa,mm,mb}); ldsw128(wo+2*KP,u4{aa,aa,am,mm}); ldsw128(wo+3*KP,u4{fa,aa,aa,mm}); }
    { u32 o0=cat(e[0],e[1]),o1=cat(e[2],e[3]),o2=cat(e[4],e[5]),o3=cat(e[6],e[7]),o4=cat(e[8],e[9]);
      u32 p1=cat(e[1],e[2]),d0=cat(e[3],e[4]),d1=cat(e[5],e[6]),d2=cat(e[7],e[8]),d3=cat(e[9],e[10]);
      ldsw128(wo+1024,u4{d0,d1,d2,d3}); ldsw128(wo+1024+KP,u4{o1,o2,o3,o4}); ldsw128(wo+1024+2*KP,u4{p1,d0,d1,d2}); ldsw128(wo+1024+3*KP,u4{o0,o1,o2,o3}); }
    if(lane<10){ if(lane){k01=0;k23=0;} if(lane!=9) kl=0;
      const u32 d0=k01&0xffffu, o1=__builtin_amdgcn_alignbit(k01,k01,16), o0=__builtin_amdgcn_alignbit(k23,k23,16), p1=(k23&0xffffu)|(k01&0xffff0000u), kk=dup(kl), zk=kl<<16;
      const u32 w2=lane<4? wo+2048 : wo-160;
      ldsw128(w2,u4{d0,0,kk,kk}); ldsw128(w2+KP,u4{o1,0,zk,kk}); ldsw128(w2+2*KP,u4{p1,d0,0,kk}); ldsw128(w2+3*KP,u4{o0,o1,0,zk}); }
  }
}
DI f4 mfma16h(u4 a,u4 b,f4 c){ return __builtin_amdgcn_mfma_f32_16x16x32_f16(__builtin_bit_cast(hfx8,a),__builtin_bit_cast(hfx8,b),c,0,0,0); }
DI u4 lds2x64(u32 a){ const u2 lo=ldsr64(a), hi=ldsr64(a+8); return u4{lo[0],lo[1],hi[0],hi[1]}; }
DI void conv_win(u32 cb,int lane,u4 (&sw)[4]){
  float P[25]; P[0]=0.f;
  #pragma unroll
  for(int c=0;c<4;c++){ const int X=2*lane-2+c; const u4 v=ldsr128(cb+UOF+(X&3)*1040+((X>>2)+1)*16);
    #pragma unroll
    for(int e=0;e<8;e++){ const int i=8*c+e-4; if(i>=0&&i<24) P[i+1]=P[i]+((e&1)?hhi(v[e>>1]):hlo(v[e>>1])); } }
  #pragma unroll
  for(int k=0;k<8;k++){ sw[k>>2][k&3]=pkhf(P[2*k+8]-P[2*k+1],P[2*k+9]-P[2*k+2]); sw[2+(k>>2)][k&3]=pkhf(P[2*k+9]-P[2*k],P[2*k+10]-P[2*k+1]); }
}
DI void conv_help(u32 cb,int lane,const u4 (&sw)[4]){
  const int r=lane&15,q=lane>>4; f4 y[2][2]={};
  ldsw128(cb+S7O+48+lane*32,sw[0]); ldsw128(cb+S7O+64+lane*32,sw[1]); ldsw128(cb+S9O+48+lane*32,sw[2]); ldsw128(cb+S9O+64+lane*32,sw[3]);
  if(lane<3){ ldsw128(cb+S7O+lane*16,u4{0,0,0,0}); ldsw128(cb+S9O+lane*16,u4{0,0,0,0}); }
  CFENCE();
  const u32 ka=cb+(r&3)*KP+16*q-8*(r>>2)+64, ub=cb+UOF+q*1040+(r+1)*16;
  const u4 a0=lds2x64(ka+32), a1=lds2x64(ka), b0=ldsr128(ub), b1=ldsr128(ub+256), a2=lds2x64(ka-32), b2=ldsr128(ub-16), b3=ldsr128(ub+240);
  const u32 sa=cb+(((r>>2)&1)?S9O:S7O)+2*(64*q+24-8*(r>>2)+4*(r&3)), kz=cb+KZO+8*(r-2*q+23); u4 bz[4],fa[4][2];
  #pragma unroll
  for(int ks=0;ks<4;ks++) bz[ks]=lds2x64(kz+(3-ks)*64);
  #pragma unroll
  for(int i=0;i<8;i++){ const u2 lo=ldstr(sa+(i>>1)*512+(i&1)*32+64), hi=ldstr(sa+(i>>1)*512+(i&1)*32); fa[i>>1][i&1]=u4{lo[0],lo[1],hi[0],hi[1]}; }
  SCHED();
  y[0][0]=mfma16h(a2,b2,y[0][0]); y[0][1]=mfma16h(a2,b3,y[0][1]);
  y[0][0]=mfma16h(a0,b0,y[0][0]); y[1][0]=mfma16h(a1,b0,y[1][0]); y[0][1]=mfma16h(a0,b1,y[0][1]); y[1][1]=mfma16h(a1,b1,y[1][1]);
  #pragma unroll
  for(int ks=0;ks<4;ks++)
    #pragma unroll
    for(int m=0;m<2;m++){ y[m][1]=mfma16h(fa[ks][m],bz[ks],y[m][1]); if(ks<2) y[m][0]=mfma16h(fa[ks][m],bz[ks+2],y[m][0]); }
  CFENCE();
  #pragma unroll
  for(int i=0;i<4;i++) *(LAS f4*)(cb+S7O+i*1024+lane*16)=y[i&1][i>>1];
}
#define RDA(S,i,m) asm volatile("ds_read2_b64 %0, %1 offset0:%2 offset1:%3" : "=v"(AF[S][i][m]) : "v"(ka), "n"((1-(i))*128+(1-(m))*4), "n"((1-(i))*128+(1-(m))*4+1))
#define RDB(S,i,ad,o) asm volatile("ds_read_b128 %0, %1 offset:%2" : "=v"(BF[S][i]) : "v"(ad), "n"(o))
#define TIEA(S,i) "+v"(AF[S][i][0]),"+v"(AF[S][i][1])
#define CV_M(S,i,n) do{ y[0][n]=mfma16h(AF[S][i][0],BF[S][(n)-(i)],y[0][n]); y[1][n]=mfma16h(AF[S][i][1],BF[S][(n)-(i)],y[1][n]); SCHED(); }while(0)
#define CV_U0() u32 u0=u1-256; u0=((int)u0<(int)uz)?uz:u0
template<class CB> DI void conv_wave(CB&& mid,rsrc_t rs,int d,u32 cb,u32 flag,int lane,float ksc,float dsk){
  const int r=lane&15,q=lane>>4; u32 ka=cb+(r&3)*KP+16*q-8*(r>>2)+1088; const u32 uz=cb+UOF+q*1040; u32 u1=uz+(r+17)*16; u4 AF[2][2][2]; u4 BF[2][4]; f4 y[2][4]={};
  #define CV_WT(S) asm volatile("s_waitcnt lgkmcnt(0)" : TIEA(S,0),TIEA(S,1),"+v"(BF[S][0]),"+v"(BF[S][1]),"+v"(BF[S][2]),"+v"(BF[S][3]))
  #define CV_STEP(C,N) do{ CV_U0(); CV_M(C,0,0); RDA(N,0,0); RDB(N,0,u0,0); RDA(N,0,1); SCHED(); CV_M(C,0,1); RDB(N,1,u1,0); RDA(N,1,0); RDB(N,2,u1,256); SCHED(); CV_M(C,0,2); RDA(N,1,1); RDB(N,3,u1,512); \
      ka-=64; u1-=16; SCHED(); CV_M(C,0,3); CV_M(C,1,1); CV_M(C,1,2); CV_M(C,1,3); CV_WT(N); }while(0)
  { CV_U0(); RDA(0,0,0); RDA(0,0,1); RDA(0,1,0); RDA(0,1,1); RDB(0,0,u0,0); RDB(0,1,u1,0); RDB(0,2,u1,256); RDB(0,3,u1,512); ka-=64; u1-=16; CV_WT(0); }
  #pragma unroll 1
  for(int dl=0;dl<8;dl++){ CV_STEP(0,1); if(dl==7){ ka+=64; u1+=16; } CV_STEP(1,0); if(dl==2) mid(); }
  u2 uv[2][4];
  #pragma unroll
  for(int n=0;n<4;n++)
    #pragma unroll
    for(int m=0;m<2;m++) uv[m][n]=ldsr64(cb+UOF+(2*m+(q>>1))*1040+(16*n+r+1)*16+8*(q&1));
  const u32 sc=cb+UOF+r*64+q*8;
  auto fin=[&](int n){
    #pragma unroll
    for(int m=0;m<2;m++){ u2 o;
      #pragma unroll
      for(int e=0;e<2;e++) o[e]=pkbf2(fma2(f2{hlo(uv[m][n][e]),hhi(uv[m][n][e])},f2{dsk,dsk},f2{y[m][n][2*e],y[m][n][2*e+1]}*ksc));
      *(LAS u2*)(sc+n*1024+m*32)=o; } };
  ldswait(flag,2u);
  { f4 hp[4];
    #pragma unroll
    for(int i=0;i<4;i++) hp[i]=*(LAS f4*)(cb+S7O+i*1024+lane*16);
    fin(0); fin(1);
    #pragma unroll
    for(int i=0;i<4;i++) y[i&1][2+(i>>1)]+=hp[i];
    fin(2); fin(3); }
  CFENCE();
  u4 ov[4];
  #pragma unroll
  for(int n=0;n<4;n++) ov[n]=ldsr128(cb+UOF+n*1024+lane*16);
  #pragma unroll
  for(int n=0;n<4;n++) st16(rs,OFF_AT+d*4096+n*1024+lane*16,ov[n]);
  WAITV(0);
}

constexpr int NB=3, HZZ=13;
constexpr int g2ops(int h){ return h<0? 7 : (h+NB<16?7:0)+((h>=3&&h<=10)?4:0)+(h==HZZ-3?1:0)+(h==HZZ?8:0); }
constexpr int g2cnt(int h){ int c=0; for(int i=h-NB+1;i<h;i++) c+=g2ops(i); return c; }
struct Frag { u4 x[4], w[4][2]; };
__global__ __launch_bounds__(512,2) void fused(Params p){
  extern __shared__ __attribute__((aligned(16))) char sm[];
  const u32 smb=(u32)(size_t)(LAS char*)sm;
  u64 pc0; asm volatile("s_getpc_b64 %0" : "=s"(pc0));
  const int b=blockIdx.x, xc=b&7, idx=b>>3;
  const int tid=threadIdx.x, w=uni(tid>>6); int lane=tid&63;
  auto cpf=[&](u32 o){ u4 sc=mksrd((const void*)(pc0&~127ull)); sc[2]=CODE_BYTES; setm0<JNK>(smb); touch(sc,lane*128,o); };
  if(idx>=28) cpf(w*8192);
  SCHED();
  const int rt=4*(xc>>1)+(idx&3), j=7*(xc&1)+(idx>>2), mi=w&3, g1=w>>2;
  const u32 kr0=((j+rt)%5)*2;
  u32 vx0,vx1,vw1,vw2,k0=kr0*128,k0w=kr0*(64*3456); const u32 mw=smb+w*4096;
  { const u32 lr=lane>>3,c=lane&7; vx0=(rt*128+16*w+lr)*1280+((c^(lr>>1))<<4); vx1=(vx0^64u)+9216;
    vw1=(8*w+lr)*3456+((c^((lr&2)<<1))<<4)+j*128; vw2=vw1+1024+(j==13?0:14*128); }
  asm volatile("" : "+v"(vx0),"+v"(vx1),"+v"(vw1),"+v"(vw2),"+s"(k0),"+s"(k0w));
  setm0<0>(mw);
  SCHED();
  const u4 srdX=mksrd(p.x), srdW=mksrd((const char*)p.win-3072);
  if(idx<28){ dma<0>(srdX,vx0,k0); dma<1024>(srdX,vx1,k0); dma<2048>(srdW,vw2,k0w); dma<3072>(srdW,vw1,k0w); }
  SCHED();
  const rsrc_t rs=__builtin_amdgcn_make_buffer_rsrc(p.ws,0,WS_END,0x00020000);
  const u4 srdS=mksrd(p.ws);
  const int rtB=8*(xc>>1)+(idx&7), ct=4*(xc&1)+(idx>>3);
  const bool hasc=idx<28 && idx+28*(w&3)<80 && w>=4;
  const int dch=80*xc+(hasc? idx+28*(w&3) : 0);
  const u32 cb=smb+CONV+(w&3)*CVS;
  u32 tag; float ksc=0.f, dsk=0.f;
  u32 zp[2][8]={};
  auto z1st=[&](){ const int r=lane&31,h=lane>>5;
    #pragma unroll
    for(int n=0;n<2;n++) if(2*j+n<27){ const u32 oo=OFF_Z1T+((rt*8+2*mi+(r>>4))*27+2*j+n)*1024+(h*16+(r&15))*16;
      st16(rs,oo,u4{zp[n][0],zp[n][1],zp[n][4],zp[n][5]}); st16(rs,oo+512,u4{zp[n][2],zp[n][3],zp[n][6],zp[n][7]}); } };
  auto f1=[&](){ WAITV(0); if(arrive(smb+C_Z1,lane)==3u && lane==0) st4(rs,OFF_F1+(rt*16+j)*128,tag); };
  auto pre01=[&](){
    const u4 sa=mksrd((const char*)p.wsp+(size_t)rtB*64*4096+mi*1024-4096); const u32 v0=(lane>>3)*4096+(((lane&7)^(lane>>4))<<4);
    #pragma unroll 1
    for(int i=0;i<4;i++){ setm0<RB+8192>(smb+mi*RING+i*4096); const u32 g=(i&1)*4+2*mi, k=(i>>1)*128+4096;
      dma<0>(sa,v0,(g&7)*32768+k); dma<1024>(sa,v0^64u,(g&7)*32768+32768-1024+k); dma<2048>(sa,v0,((g+2)&7)*32768-2048+k); dma<3072>(sa,v0^64u,((g+2)&7)*32768+32768-3072+k); } };
  if(idx>=28){
    const int a=xc*4+idx-28, r=lane&15, q=lane>>4;
    const u32 ep=ld4(rs,OFF_EP);
    u4 xr[10]; const char* xs=(const char*)p.x+(size_t)(64*a+8*w+(lane>>3))*1280+(lane&7)*16;
    #pragma unroll
    for(int i=0;i<10;i++) xr[i]=*(const u4*)(xs+i*128);
    { const u32 l=(a*8+w)*19+(lane<19?lane:18); setm0<JNK>(smb); touch(srdS,(l<3584? OFF_ST/128+l : OFF_F2/128+(l-3584<1088?l-3584:1087))*128,0u); }
    if(tid<32) *(LAS u32*)(smb+PM+tid*4)=0u;
    tag=uni(ep)+TAGINC;
    float s0=0.f,s1=0.f,q0=0.f,q1=0.f;
    #pragma unroll
    for(int i=0;i<10;i++){ s0=dot2(xr[i][0],0x3f803f80u,s0); q0=dot2(xr[i][0],xr[i][0],q0); s1=dot2(xr[i][1],0x3f803f80u,s1); q1=dot2(xr[i][1],xr[i][1],q1);
      s0=dot2(xr[i][2],0x3f803f80u,s0); q0=dot2(xr[i][2],xr[i][2],q0); s1=dot2(xr[i][3],0x3f803f80u,s1); q1=dot2(xr[i][3],xr[i][3],q1); }
    s0+=s1; q0+=q1;
    #define DPPADD(v,c) v+=__int_as_float(__builtin_amdgcn_update_dpp(0,__float_as_int(v),c,0xf,0xf,false))
    DPPADD(s0,0xB1); DPPADD(q0,0xB1); DPPADD(s0,0x4E); DPPADD(q0,0x4E); DPPADD(s0,0x141); DPPADD(q0,0x141);
    float m1,ri; { float mean=s0*(1.f/640.f); ln_fin(mean,q0*(1.f/640.f)-mean*mean,m1,ri); }
    if((lane&7)==0) st16(rs,OFF_RS+(64*a+8*w+(lane>>3))*16,u4{__float_as_uint(m1),tag,__float_as_uint(ri),tag});
    if(a<27){ const u4 srdO=mksrd((const char*)p.wout+a*(32*1280)+w*5120); const u32 mo=smb+w*5120; setm0<0>(mo);
      dma<0>(srdO,lane*16,0); dma<1024>(srdO,lane*16,0); dma<2048>(srdO,lane*16,0); dma<3072>(srdO,lane*16,0); setm0<4096>(mo); dma<0>(srdO,lane*16,4096); }
    #pragma unroll
    for(int i=0;i<10;i++){ u4 o;
      #pragma unroll
      for(int e=0;e<4;e++) o[e]=pkhf2(fma2(unpk(xr[i][e]),f2{ri,ri},f2{-m1*ri,-m1*ri}));
      ldsw128(smb+RB+(8*w+(lane>>3))*1296+(lane&7)*16+i*128,o); }
    WAITV(0); bar();
    #pragma unroll
    for(int i=0;i<10;i++){ const int c=w*10+i; const u32 ad=smb+RB+(32*((r>>1)&1)+8*q+(r>>2))*1296+c*16+(r&1)*8; u2 lo=ldstr(ad), hi=ldstr(ad+4*1296);
      const u32 sc=smb+40960+w*2048+(i&1)*1024; ldsw128(sc+(r&7)*128+((((r>>3)*4+q)^(r&7))<<4),u4{lo[0],lo[1],hi[0],hi[1]});
      CFENCE(); const u4 o=ldsr128(sc+(lane>>3)*128+(((lane&7)^(lane>>3))<<4)); CFENCE();
      st16(rs,OFF_XNT+a*81920+c*1024+lane*16,o); }
    WAITV(0);
    if(arrive(smb+C_Z1,lane)==7u && lane==0) st4(rs,OFF_FX+a*128,tag);
    if(a<27){
      #pragma unroll
      for(int i=0;i<5;i++){ u32 ad=smb+(8*q+(r>>2))*1280+(w*5+i)*32+(r&3)*8; u2 lo=ldstr(ad), hi=ldstr(ad+4*1280);
        st16(rs,OFF_WOF+(u32)((w*27+a)*5+i)*1024+lane*16,u4{lo[0],lo[1],hi[0],hi[1]}); } }
    WAITV(0); bar();
    if(tid==0) st4(rs,OFF_FW+a*128,tag);
    if(!g1){ pre01(); WAITV(0); }
    else { const u4 so=mksrd((const char*)p.out+(size_t)(a*4+mi)*20480); setm0<JNK>(smb); touch(so,lane*128,0u); touch(so,lane*128,8192u); touch(so,(lane&31)*128,16384u); }
  } else {
    const int r=lane&31, h=lane>>5;
    u32 epv;
    u32 kx[10]; { u32 k=kr0;
      #pragma unroll
      for(int i=0;i<10;i++){ kx[i]=k*128; k=k==9?0:k+1; } }
    auto issue1=[&](auto sc,int k){ constexpr int s=sc.value;
      if(k==0){ setm0<(s&3)*STG>(mw); dma<0>(srdX,vx0,kx[s]); } if(k==1) dma<1024>(srdX,vx1,kx[s]); if(k==2) dma<2048>(srdW,vw2,kx[s]*1728); if(k==3) dma<3072>(srdW,vw1,kx[s]*1728); };
    auto issue=[&](auto sc){ issue1(sc,0); issue1(sc,1); issue1(sc,2); issue1(sc,3); };
    u32 pr[6];
    gload4<0>(epv,0u,p.ws+OFF_EP);
    gload4<0>(pr[0],lane*4,g1? (const char*)(p.lar+dch*128) : (const char*)p.wsp+(size_t)(xc>>1)*512*4096+((xc&1)*28+idx)*(4*74*128)); gload4<0>(pr[1],lane*4,(const char*)(p.aim+dch*128)); gload4<0>(pr[2],lane*4,(const char*)(p.cre+dch*128)); gload4<0>(pr[3],lane*4,(const char*)(p.cim+dch*128));
    gload2(pr[4],0u,(const char*)(p.ldt+dch)); gload2(pr[5],0u,(const char*)(p.dsk+dch));
    asm volatile("s_barrier" ::: "memory");
    issue(std::integral_constant<int,1>{});
    if(tid<32) *(LAS u32*)(smb+PM+tid*4)=0u;
    u32 ax[2][4],aw[2][4][2];
    { u32 row=32*mi+r, a0=smb+(row>>4)*4096+(row&15)*128+((h^((r>>1)&7))<<4);
      u32 qq=(lane&15)>>2, w0=smb+2048+g1*1024+h*4096+qq*128+8*(lane&1)+(((2*((lane>>4)&1)+((lane>>1)&1))^((qq&2)<<1))<<4);
      #pragma unroll
      for(int i=0;i<2;i++)
        #pragma unroll
        for(int k=0;k<4;k++){ const u32 kk=(k+mi)&3, wo=i*2*STG; ax[i][k]=opq((a0^(kk*32u))+wo); aw[i][k][0]=opq(w0+kk*8192+wo); aw[i][k][1]=opq((w0^64u)+kk*8192+wo); } }
    u4 sel; { const u32 o=((lane&15)==((lane>>4)&1))?0x3f803f80u:0u; sel=u4{o,o,o,o}; asm volatile("" : "+v"(sel)); }
    u32 ga[11];
    #pragma unroll
    for(int i=0;i<11;i++) ga[i]=opq(cb+2u*(u32)rbf((float)(512-8*lane+3-i)));
    Frag F[2];
    asm volatile("s_waitcnt vmcnt(4)" : "+v"(epv),"+v"(pr[0]),"+v"(pr[1]),"+v"(pr[2]),"+v"(pr[3]),"+v"(pr[4]),"+v"(pr[5]) :: "memory");
    bar();
    tag=uni(epv)+TAGINC; dsk=bflo(pr[5]);
    #pragma unroll
    for(int k=0;k<4;k++){
      #pragma unroll
      for(int n=0;n<2;n++){ u2 lo=ldstr(aw[0][k][n]), hi=ldstr(aw[0][k][n]+512); F[0].w[k][n]=u4{lo[0],lo[1],hi[0],hi[1]}; }
      F[0].x[k]=ldsr128(ax[0][k]); }
    issue(std::integral_constant<int,2>{});
    f16v acc[2]={}; f4 cacc[2]={}; u4 G;
    const u32 go=OFF_RS+rt*2048+mi*512, cp=smb+MISC+g1*1024;
    sfor<10>([&](auto ic){
      constexpr int s=ic.value; Frag& cur=F[s&1]; Frag& nxt=F[(s&1)^1];
      if constexpr(s<9){ WAITV(s==8? 3 : s==7? 7 : 4); asm volatile("s_barrier" ::: "memory"); }
      auto rw=[&](int k,int n){ if constexpr(s<9){ const int i=((s+1)>>1)&1; const u32 so=((s+1)&1)*STG; u2 lo=ldstr(aw[i][k][n]+so), hi=ldstr(aw[i][k][n]+so+512); nxt.w[k][n]=u4{lo[0],lo[1],hi[0],hi[1]}; } };
      auto rx=[&](int k){ if constexpr(s<9){ const int i=((s+1)>>1)&1; const u32 so=((s+1)&1)*STG; nxt.x[k]=ldsr128(ax[i][k]+so); } };
      #pragma unroll
      for(int k=0;k<4;k++){
        acc[0]=mfma32b(cur.w[k][0],cur.x[k],acc[0]); SCHED();
        if constexpr(s+3<=9) issue1(std::integral_constant<int,s+3>{},k);
        rw(k,0); rx(k);
        SCHED();
        acc[1]=mfma32b(cur.w[k][1],cur.x[k],acc[1]); SCHED();
        rw(k,1);
        if(k==0){ cacc[0]=mfma16(cur.w[0][0],sel,cacc[0]); cacc[1]=mfma16(cur.w[0][1],sel,cacc[1]); }
        if constexpr(s==6){ if(k==3){ gload<0>(G,r*16,p.ws+go); setm0<JNK>(smb);
            if(g1){ const u4 sp=mksrd((const char*)p.wsp+(size_t)(xc>>1)*512*4096); const u32 lb=(((xc&1)*28+idx)*4+mi)*74, l0=lb+lane, l1=lb+64+(lane<10?lane:9);
              touch(sp,(l0<16384?l0:16383)*128,0u); touch(sp,(l1<16384?l1:16383)*128,0u); }
            else { touch(mksrd(p.out+rtB*64*640),0u,0u); touch(mksrd(w==0? (const void*)(p.bsp+rtB*64) : w==1? (const void*)p.ws : w==2? (const void*)(p.wsp+(size_t)rtB*64*2048) : (const void*)(p.x+rtB*64*640)),(lane&15)*(w==1? (1u<<20) : w==2? 16384u : w==3? 4096u : 0u),0u); } } }
        if constexpr(s==8){ if(k==3) signal(smb+C_DONE,lane); }
        if constexpr(s==9){ if(k==1){
          if((lane&14)==0){ *(LAS f4*)(cp+mi*256+(lane&1)*64+(lane>>4)*16)=cacc[0]; *(LAS f4*)(cp+mi*256+128+(lane&1)*64+(lane>>4)*16)=cacc[1]; }
          signal(smb+C_CS+g1*4,lane); } }
        SCHED();
      }
    });
    if(!g1) __builtin_amdgcn_s_setprio(2);
    asm volatile("s_waitcnt vmcnt(2)" : "+v"(G) :: "memory");
    ldswait(smb+C_CS+g1*4,4u);
    const float cs0=*(LAS float*)(cp+h*512+r*4)+*(LAS float*)(cp+h*512+256+r*4), cs1=*(LAS float*)(cp+h*512+128+r*4)+*(LAS float*)(cp+h*512+384+r*4);
    #pragma unroll 1
    for(int sp=0;sp<SPIN && !__all(G[1]==tag&&G[3]==tag);sp++)asm volatile("global_load_dwordx4 %0, %1, %2 sc1\n\ts_waitcnt vmcnt(0)" : "=&v"(G) : "v"(r*16), "s"(p.ws+go) : "memory");
    const float nm=-__uint_as_float(G[0]), ri1=__uint_as_float(G[2]);
    acc[0]=__builtin_amdgcn_mfma_f32_32x32x2f32(cs0,nm,acc[0],0,0,0); acc[1]=__builtin_amdgcn_mfma_f32_32x32x2f32(cs1,nm,acc[1],0,0,0);
    #pragma unroll
    for(int e=0;e<16;e++) zp[e>>3][e&7]=pkbf2(gelu2(f2{acc[e>>3][2*(e&7)],acc[e>>3][2*(e&7)+1]}*ri1));
    if(g1){ ldswait(smb+C_DONE,8u); }
    else {
      float S=0.f,Q=0.f;
      if(j<13){
        #pragma unroll
        for(int e=0;e<8;e++){ S=dot2(zp[0][e],0x3f803f80u,S); Q=dot2(zp[0][e],zp[0][e],Q); } }
      #pragma unroll
      for(int e=0;e<8;e++){ S=dot2(zp[1][e],0x3f803f80u,S); Q=dot2(zp[1][e],zp[1][e],Q); }
      #define XSUM32(v) do{ auto t_=__builtin_amdgcn_permlane32_swap(__float_as_uint(v),__float_as_uint(v),false,false); v=__uint_as_float(t_[0])+__uint_as_float(t_[1]); }while(0)
      XSUM32(S); XSUM32(Q);
      const u32 sto=OFF_ST+rt*(14*2048)+(32*mi+r)*16;
      if(h==0) st16(rs,sto+j*2048,u4{__float_as_uint(S),tag,__float_as_uint(Q),tag});
    }
    float kp[11]; u4 g[7]={};
    const u32 gof=OFF_ST+rt*(14*2048)+(32*mi+r)*16+h*(7*2048);
    auto samp=[&](){ CFENCE();
      #pragma unroll
      for(int i=0;i<7;i++) g[i]=ld16(rs,gof+i*2048);
      CFENCE(); };
    if(hasc) ksc=kgen_tab(cb,lane,pr,kp); else if(!g1){ pre01(); samp(); }
    if(g1){
      if(hasc){
        kgen_exp(cb,lane,kp,ga);
        signal(smb+C_KP+mi*4,lane);
      }
      if(hasc) ldswait(smb+C_FULL,1u); else if(w<7){ z1st(); f1(); }
      if(hasc) z1st();
    } else {
      float S,Q;
      #pragma unroll 1
      for(int sp=0;sp<SPIN;sp++){ bool ok=true;
        #pragma unroll
        for(int i=0;i<7;i++) ok=ok&&g[i][1]==tag&&g[i][3]==tag;
        if(__all(ok)) break;
        __builtin_amdgcn_s_sleep(4); samp(); }
      S=0.f; Q=0.f;
      #pragma unroll
      for(int i=0;i<7;i++){ S+=__uint_as_float(g[i][0]); Q+=__uint_as_float(g[i][2]); }
      XSUM32(S); XSUM32(Q);
      float mean=S*(1.f/864.f), m2,r2; ln_fin(mean,Q*(1.f/864.f)-mean*mean,m2,r2); m2*=-r2;
      ldswait(smb+C_DONE,8u);
      const u32 tb=smb+RB+mi*RING, ad=tb+(8*(lane>>4)+((lane&15)>>2))*80+(lane&3)*8;
      const u32 zo=OFF_Z2F+((rt*4+mi)*54+(j==13?-2:2+4*j))*1024+lane*16;
      #pragma unroll
      for(int n=0;n<2;n++){
        #pragma unroll
        for(int g2=0;g2<4;g2++){ u2 o;
          #pragma unroll
          for(int e=0;e<2;e++) o[e]=pkbf2(fma2(unpk(zp[n][2*g2+e]),f2{r2,r2},f2{m2,m2}));
          *(LAS u2*)(tb+r*80+g2*16+h*8)=o; }
        CFENCE();
        u2 l0=ldstr(ad), h0=ldstr(ad+320), l1=ldstr(ad+32), h1=ldstr(ad+32+320);
        CFENCE();
        if(j<13||n){ st16(rs,zo+n*2048,u4{l0[0],l0[1],h0[0],h0[1]}); st16(rs,zo+n*2048+1024,u4{l1[0],l1[1],h1[0],h1[1]}); } }
      WAITV(0);
      if(arrive(smb+C_Z2,lane)==3u && lane==0) st4(rs,OFF_F2+(rt*16+j)*128,tag);
    }
  }
  const int ft0=ct<7?7*ct:47;
  bool adone=true; const u32 fao=OFF_FA+((lane<28?lane:0)*8+ct)*128;
  auto aload=[&](){ const u32 vo=(lane>>3)*4096+(lane&7)*16;
    #pragma unroll 1
    for(int i=0;i<10;i++){ setm0<ATL>(smb+i*1024); dma<0>(srdS,vo,OFF_AT+(u32)(80*ct+8*i)*4096+rtB*128); } };
  if(w>=4){
    if(hasc){
      __builtin_amdgcn_s_setprio(3);
      conv_wave(f1,rs,dch,cb,smb+C_KP+mi*4,lane,ksc,dsk);
      __builtin_amdgcn_s_setprio(0);
      if(arrive(smb+C_CV,lane)==(idx<24?2u:1u) && lane==0) st4(rs,OFF_FA+b*128,tag);
    }
    if(w==7){
      if(idx<28){
        pollw<16>(rs,OFF_FX+(lane&31)*128,tag);
        u4 sw[3][4];
        #pragma unroll
        for(int c=0;c<3;c++)
          #pragma unroll
          for(int i=0;i<4;i++) sw[c][i]=ld16(rs,OFF_XNT+((lane>>3)+8*i)*81920+(80*xc+idx+((c<2||idx<24)?28*c:0))*128+(lane&7)*16);
        #pragma unroll
        for(int c=0;c<3;c++){
          #pragma unroll
          for(int i=0;i<4;i++){ const int x=lane+64*i; ldsw128(smb+c*CVS+UOF+(x&3)*1040+((x>>2)+1)*16,sw[c][i]); }
          if(lane<4) ldsw128(smb+c*CVS+UOF+lane*1040,u4{0,0,0,0}); }
        signal(smb+C_FULL,lane);
        #pragma unroll 1
        for(int c=0;c<3;c++){
          #pragma unroll
          for(int i=0;i<4;i++){ sw[0][i]=sw[1][i]; sw[1][i]=sw[2][i]; }
          conv_win(smb+c*CVS,lane,sw[2]); }
        z1st();
        __builtin_amdgcn_s_setprio(3);
        #pragma unroll 1
        for(int c=0;c<(idx<24?3:2);c++){ ldswait<4>(smb+C_KP+c*4,1u); conv_help(smb+c*CVS,lane,sw[0]); signal(smb+C_KP+c*4,lane); if(c==0) f1();
          #pragma unroll
          for(int i=0;i<4;i++){ sw[0][i]=sw[1][i]; sw[1][i]=sw[2][i]; } } }
      __builtin_amdgcn_s_setprio(0);
      pollw<16>(rs,OFF_FW+(lane<27?lane:0)*128,tag); signal(smb+C_Z1R,lane);
    }
    ldswait<6>(smb+C_Z1R,5u);
    u4 wq[24];
    #pragma unroll
    for(int i=0;i<24;i++) wq[i]=ld16(rs,OFF_WOF+ct*(135*1024)+40960+mi*RING+((i==23&&mi==3)?22:i)*1024+lane*16);
    if(w<6){
      #pragma unroll 1
      for(u32 i=0;i<20480;i+=4096){ setm0<0>(cb+i); const u32 o=OFF_WOF+ct*(135*1024)+mi*20480+i; dma<0>(srdS,lane*16,o); dma<1024>(srdS,lane*16,o); dma<2048>(srdS,lane*16,o); dma<3072>(srdS,lane*16,o); } }
    if(w==6){ adone=__all(ld4(rs,fao)==tag); if(adone) aload(); }
    if(w==6 && adone){ WAITV(0); signal(smb+C_AT,lane); }
    ldswait(smb+C_FREE,4u);
    #pragma unroll
    for(int i=0;i<24;i++) ldsw128(smb+RB+mi*RING+i*1024+lane*16,wq[i]);
    if(w<6) WAITV(0);
    signal(smb+C_WR,lane);
    if(!adone){ adone=__all(ld4(rs,fao)==tag); if(adone){ aload(); WAITV(0); signal(smb+C_AT,lane); } }
    if(w==7 && b==255){
      #pragma unroll 1
      for(int sp=0;sp<SPIN;sp++){ const u32 o=OFF_FA+lane*128; u32 v0=ld4(rs,o),v1=ld4(rs,o+8192),v2=ld4(rs,o+16384),v3=ld4(rs,lane<32? o+24576 : OFF_FW+(lane-32)*128); CFENCE();
        if(__all(v0==tag&&v1==tag&&v2==tag&&v3==tag)) break; __builtin_amdgcn_s_sleep(4); }
      if(lane==0) st4(rs,OFF_EP,tag); }
  }
  int w2=w; asm volatile("" : "+s"(w2));
  if(w2<4){
    __builtin_amdgcn_s_setprio(2);
    int r=lane&15, q=lane>>4;
    const u32 rb=smb+RB+w*RING;
    u4 srdA=mksrd((const char*)p.wsp+(size_t)rtB*64*4096+w*1024-4096+256);
    const char* zb=p.ws+OFF_Z2F+(u32)(16*w*54+ft0+3)*1024;
    u32 voA[2]; { u32 lr=lane>>3,c=lane&7; voA[0]=lr*4096+((c^(lr>>1))<<4); voA[1]=voA[0]^64u; }
    u32 sofa[8];
    #pragma unroll
    for(int g=0;g<8;g++) sofa[g]=((g+2*w)&7)*32768+4096-(g&3)*1024;
    const u32 voB=lane*16;
    u32 la[2]; la[0]=rb+r*128+((q^(r>>1))<<4); la[1]=la[0]^64u;
    f4 acc[4][7]; u4 B[NB][7]; u4 A[2][4]; u2 z1[7];
    auto issueA=[&](auto sc,auto gc){ constexpr int s=sc.value,g=gc.value; if constexpr((g&3)==0) setm0<((s+1)%3)*8192+g*1024>(rb); dma<(g&3)*1024>(srdA,voA[g&1],sofa[g]);
      if constexpr(g==7){ u64 a=((u64)srdA[1]<<32|srdA[0])+128; srdA[0]=(u32)a; srdA[1]=(u32)(a>>32); } };

    u32 bsr,fv;
    auto fof=[&](){ const u32 l=voB>>4; return OFF_F1+((rtB>>1)*16+(((ft0>>1)+(l&3))>>1))*128; };
    #pragma unroll
    for(int m=0;m<4;m++){ A[0][m]=ldsr128(la[0]+m*2048+8192); A[1][m]=ldsr128(la[1]+m*2048+8192); }
    sfor<8>([&](auto g){ issueA(std::integral_constant<int,2>{},g); });
    waitA(A[0]); waitA(A[1]);
    sfor<8>([&](auto g){ issueA(std::integral_constant<int,3>{},g); });
    { const int hf=(ft0>>1)+q; pollw(rs,OFF_F2+((4*w+(lane&3))*16+(hf==0?13:(hf-1)>>1))*128,tag); }
    sfor<NB>([&](auto hh){ const char* s=zb+(u32)hh.value*(54*1024); sfor<7>([&](auto n){ gload<(n.value-3)*1024>(B[hh][n],voB,s); }); });
    auto redw=[&](auto nc){ constexpr int n=nc.value; *(LAS f4*)(rb+n*1024+voB)=acc[1][n]; *(LAS f4*)(rb+(7+n)*1024+voB)=acc[2][n]; *(LAS f4*)(rb+(14+n)*1024+voB)=acc[3][n]; };
    const char* z1b=p.ws+OFF_Z1T+(u32)((rtB*4+w)*27+(ft0>>1))*1024;
    sfor<16>([&](auto hc){
      constexpr int h=hc.value, S=h%NB, P=h&1, HZ=HZZ, cnt=g2cnt(h);
      waitB<cnt>(B[S]); waitA(A[P]);
      if constexpr(h==HZ){ tie1(fv); if(!__all(fv==tag)) pollw(rs,fof(),tag); }
      if constexpr(h+1<16 && h>0){
        #pragma unroll
        for(int m=0;m<4;m++) A[P^1][m]=ldsr128(la[(h+1)&1]+m*2048+((((h+1)>>1)+1)%3)*8192);
      }
      if constexpr(h==HZ-3) gload4<0>(fv,fof(),p.ws);
      const char* s=zb+(u32)(h+NB)*(54*1024);
      sfor<7>([&](auto n){
        if constexpr(h==0) mfma4z(acc[0][n],acc[1][n],acc[2][n],acc[3][n],B[S][n],A[P]); else mfma4(acc[0][n],acc[1][n],acc[2][n],acc[3][n],B[S][n],A[P]);
        if constexpr(h>=3 && h<=10 && (n.value&1)==0) issueA(std::integral_constant<int,(h-1)/2+3>{},std::integral_constant<int,((h-1)&1)*4+n.value/2>{});
        if constexpr(h+NB<16) gload_t<(n.value-3)*1024>(B[S][n],voB,s);
        if constexpr(h==HZ){ gload8<(n.value>>1)*1024>(z1[n],voB,z1b+((n.value&1)? ((ft0&1)?1024:8) : (ft0&1)*8));
          if constexpr(n.value==6) { gload2(bsr,(voB>>3)&30u,(const char*)(p.bsp+(rtB*4+w)*16)); } }
        if constexpr(h==13 && n.value==0) signal(smb+C_Z1R,lane);
        if constexpr(h==15){ SCHED(); if constexpr(n.value>=2) redw(std::integral_constant<int,n.value-2>{}); SCHED(); }
      });
    });
    asm volatile("s_nop 15\n\ts_nop 3" ::: "memory");
    SCHED();
    lane=__builtin_amdgcn_mbcnt_hi(~0u,__builtin_amdgcn_mbcnt_lo(~0u,0u)); asm volatile("" : "+v"(lane)); r=lane&15; q=lane>>4;
    redw(std::integral_constant<int,5>{}); redw(std::integral_constant<int,6>{});
    signal(smb+C_RED,lane);
    ldswait(smb+C_RED,4u);
    #pragma unroll
    for(int m=1;m<4;m++){ u32 src=smb+RB+((w-m)&3)*RING+(m-1)*7168+lane*16;
      #pragma unroll
      for(int n=0;n<7;n++) acc[0][n]+=*(LAS f4*)(src+n*1024); }
    asm volatile("s_waitcnt vmcnt(0) lgkmcnt(0)" : "+v"(z1[0]),"+v"(z1[1]),"+v"(z1[2]),"+v"(z1[3]),"+v"(z1[4]),"+v"(z1[5]),"+v"(z1[6]),"+v"(bsr) :: "memory");
    const float bs=bflo(bsr);
    signal(smb+C_FREE,lane);
    u2 o[7];
    #pragma unroll
    for(int n=0;n<7;n++)
      #pragma unroll
      for(int e=0;e<2;e++)o[n][e]=pkbf2(unpk(z1[n][e])*(f2{acc[0][n][2*e],acc[0][n][2*e+1]}+bs));
    const u32 ho=OFF_HF+(u32)((rtB*4+w)*54+ft0)*512+(q>>1)*256+r*16;
    #pragma unroll
    for(int n=0;n<6;n+=2){ auto sx=__builtin_amdgcn_permlane16_swap(o[n][0],o[n+1][0],false,false); auto sy=__builtin_amdgcn_permlane16_swap(o[n][1],o[n+1][1],false,false);
      if(ct<7||n>=2) st16(rs,ho+(n+(q&1))*512,u4{sx[0],sy[0],sx[1],sy[1]}); }
    st8(rs,ho+6*512+(q&1)*8,o[6]);
    WAITV(0);
    if(arrive(smb+C_H,lane)==3u && lane==0) st4(rs,OFF_FH+(rtB*8+ct)*128,tag);
    __builtin_amdgcn_s_setprio(0);
  }
  { const int r=lane&15, q=lane>>4, m=w&3, kh=w>>2;
    u2 xv[5]={};
    if(kh){
      #pragma unroll
      for(int n=0;n<5;n++) xv[n]=*(const u2*)(p.x+(rtB*64+m*16+r)*640+80*ct+16*n+4*q);
    }
    f4 acc[5]={}; u4 W[3][5];
    const u32 wl2=smb+lane*16+RB-40960+kh*14*5120, wl0=kh? wl2 : smb+lane*16, wl1=kh? wl2 : smb+lane*16+CVS-20480;
    #define WAD(k,n) (((k)<4? wl0 : (k)<8? wl1 : wl2)+((k)*5+(n))*1024)
    f4 ax[5]={};
    auto addax=[&](){
      #pragma unroll
      for(int n=0;n<5;n++){ u2 av=ldstr(smb+ATL+(16*n+4*q+(r>>2))*128+(16*m+4*(r&3))*2); ax[n]=f4{bflo(av[0]),bfhi(av[0]),bflo(av[1]),bfhi(av[1])}+f4{bflo(xv[n][0]),bfhi(xv[n][0]),bflo(xv[n][1]),bfhi(xv[n][1])}; } };
    u32 oo[3],to[3]; const u32 tb=smb+CVS+m*3072;
    #pragma unroll
    for(int i=0;i<3;i++){ const int it=lane+64*i, rr=(it*205)>>11, cc=it-rr*10; oo[i]=opq((rtB*64+m*16+rr)*1280+ct*160+cc*16); to[i]=opq(tb+rr*176+cc*16); }
    bool early=false;
    if(kh){ while(ldsv(smb+C_AT)==0u && ldsv(smb+C_H)<4u) __builtin_amdgcn_s_sleep(1);
      early=uni(ldsv(smb+C_AT))!=0u; CFENCE(); if(early) addax(); }
    const u32 fho=OFF_FH+(rtB*8+kh*4+(lane&3))*128; u32 fh=0;
    if(m==0){ ldswait(smb+C_H,4u); fh=ld4(rs,fho); }
    ldswait(smb+C_WR,4u);
    const u32 wl3=kh? wl2-5120 : wl2;
    #pragma unroll
    for(int n=0;n<10;n++) W[n/5][n%5]=ldsr128(WAD(n/5,n%5));
    if(m==0){ if(!__all(fh==tag)) pollw(rs,fho,tag); signal(smb+C_TAG+kh*4,lane); } else ldswait(smb+C_TAG+kh*4,1u);
    if(kh) __builtin_amdgcn_s_setprio(1);
    u4 hf[14];
    const u32 ho=OFF_HF+(u32)((rtB*4+m)*27+kh*14)*1024+lane*16;
    #pragma unroll
    for(int k=0;k<HPF;k++) hf[k]=ld16(rs,ho+k*1024);
    #pragma unroll
    for(int k=0;k<13;k++){
      if(k+HPF<14) hf[k+HPF]=ld16(rs,ho+((k+HPF==13&&kh)?12:k+HPF)*1024);
      SCHED();
      #pragma unroll
      for(int n=0;n<5;n++){ acc[n]=mfma16(W[k%3][n],hf[k],acc[n]); SCHED();
        if(k<11) W[(k+2)%3][n]=ldsr128(WAD(k+2,n)); else if(k==11) W[1][n]=ldsr128(wl3+(65+n)*1024);
        SCHED(); }
      if(k==4) __builtin_amdgcn_s_setprio(0);
      if(k==5 && !kh) signal(smb+C_G1,lane);
    }
    const u32 pb=smb+m*5120+lane*16;
    if(!kh){
      #pragma unroll
      for(int n=0;n<5;n++) acc[n]=mfma16(W[1][n],hf[13],acc[n]);
      ldswait(smb+C_G1,4u);
      #pragma unroll
      for(int n=0;n<5;n++) *(LAS f4*)(pb+n*1024)=acc[n];
      signal(smb+C_P+m*4,lane);
    } else {
      if(w==6 && !adone){ pollw(rs,fao,tag); aload(); WAITV(0); signal(smb+C_AT,lane); }
      if(!early){ ldswait(smb+C_AT,1u); addax(); }
      ldswait(smb+C_P+m*4,1u);
      #pragma unroll
      for(int n=0;n<5;n++){ const f4 o=(acc[n]+ax[n])+*(LAS f4*)(pb+n*1024); *(LAS u2*)(tb+r*176+n*32+q*8)=u2{pkbf(o[0],o[1]),pkbf(o[2],o[3])}; }
      CFENCE();
      #pragma unroll
      for(int i=0;i<3;i++) if(i<2||lane<32) __builtin_amdgcn_raw_buffer_store_b128(ldsr128(to[i]),__builtin_amdgcn_make_buffer_rsrc(p.out,0,2048*1280,0x00020000),oo[i],0,16);
    }
  }
}
extern "C" void kernel_launch(void* const* d_in,const int* in_sizes,int n_in,void* d_out,int out_size,void* d_ws,size_t ws_size,hipStream_t stream){
  Params p;
  p.x=(const u16*)d_in[0]; p.lar=(const u16*)d_in[1]; p.aim=(const u16*)d_in[2]; p.cre=(const u16*)d_in[3]; p.cim=(const u16*)d_in[4];
  p.ldt=(const u16*)d_in[5]; p.dsk=(const u16*)d_in[6]; p.win=(const u16*)d_in[8]; p.wsp=(const u16*)d_in[9]; p.bsp=(const u16*)d_in[10]; p.wout=(const u16*)d_in[11];
  p.out=(u16*)d_out; p.ws=(char*)d_ws;
  (void)hipFuncSetAttribute((const void*)fused,hipFuncAttributeMaxDynamicSharedMemorySize,LDS_TOTAL);
  fused<<<256,512,LDS_TOTAL,stream>>>(p);
}
```

```cpp
#include <hip/hip_runtime.h>
#include <utility>
#pragma clang diagnostic ignored "-Wint-to-pointer-cast"
typedef unsigned short u16;
typedef unsigned int u32;
typedef unsigned long long u64;
typedef __attribute__((ext_vector_type(8))) __bf16 bfx8;
typedef __attribute__((ext_vector_type(8))) _Float16 hfx8;
typedef __attribute__((ext_vector_type(2))) __bf16 bf2;
typedef __attribute__((ext_vector_type(2))) _Float16 hf2;
typedef __attribute__((ext_vector_type(2))) float f2;
typedef __attribute__((ext_vector_type(4))) float f4;
typedef __attribute__((ext_vector_type(16))) float f16v;
typedef __attribute__((ext_vector_type(4))) u32 u4;
typedef __attribute__((ext_vector_type(2))) u32 u2;
typedef __attribute__((ext_vector_type(4))) short s4;
#define LAS __attribute__((address_space(3)))
#define DI __device__ __forceinline__

constexpr u32 OFF_Z2F = 0;
constexpr u32 OFF_Z1T = OFF_Z2F + 64*54*1024;
constexpr u32 OFF_XNT = OFF_Z1T + 128*27*1024;
constexpr u32 OFF_WOF = OFF_XNT + 640*4096;
constexpr u32 OFF_HF  = OFF_WOF + 8*27*5*1024;
constexpr u32 OFF_AT  = OFF_HF + 128*27*1024;
constexpr u32 OFF_ST  = OFF_AT + 640*4096;
constexpr u32 OFF_RS  = OFF_ST + 16*14*2048+8192;
constexpr u32 OFF_EP  = OFF_RS + 2048*16;
constexpr u32 OFF_F2  = OFF_EP + 256;
constexpr u32 OFF_F1  = OFF_F2 + 256*128;
constexpr u32 OFF_FX  = OFF_F1 + 256*128;
constexpr u32 OFF_FW  = OFF_FX + 32*128;
constexpr u32 OFF_FH  = OFF_FW + 32*128;
constexpr u32 OFF_FA  = OFF_FH + 256*128;
constexpr u32 WS_END  = OFF_FA + 256*128;

struct Params {
  const u16 *x, *lar, *aim, *cre, *cim, *ldt, *dsk, *win, *wsp, *bsp, *wout;
  u16* out; char* ws;
};
template<class F,int... I> DI void sfor_impl(F&& f, std::integer_sequence<int,I...>){ (f(std::integral_constant<int,I>{}),...); }
template<int N,class F> DI void sfor(F&& f){ sfor_impl(f, std::make_integer_sequence<int,N>{}); }

DI float bflo(u32 v){ return __uint_as_float(v<<16); }
DI float bfhi(u32 v){ return __uint_as_float(v&0xffff0000u); }
DI u32 pkbf(float a,float b){ f2 v={a,b}; return __builtin_bit_cast(u32,__builtin_convertvector(v,bf2)); }
DI u32 pkhf(float a,float b){ f2 v={a,b}; return __builtin_bit_cast(u32,__builtin_convertvector(v,hf2)); }
DI float rbf(float a){ return bflo(pkbf(a,a)); }
DI float hlo(u32 v){ return (float)__builtin_bit_cast(hf2,v)[0]; }
DI float hhi(u32 v){ return (float)__builtin_bit_cast(hf2,v)[1]; }
DI float dot2(u32 a,u32 b,float c){ return __builtin_amdgcn_fdot2_f32_bf16(__builtin_bit_cast(bf2,a),__builtin_bit_cast(bf2,b),c,false); }
DI f4 mfma16(u4 a,u4 b,f4 c){ return __builtin_amdgcn_mfma_f32_16x16x32_bf16(__builtin_bit_cast(bfx8,a),__builtin_bit_cast(bfx8,b),c,0,0,0); }
DI f16v mfma32h(u4 a,u4 b,f16v c){ return __builtin_amdgcn_mfma_f32_32x32x16_f16(__builtin_bit_cast(hfx8,a),__builtin_bit_cast(hfx8,b),c,0,0,0); }
DI u4 ldsr128(u32 a){ return *(LAS u4*)a; }
DI u2 ldsr64(u32 a){ return *(LAS u2*)a; }
DI void ldsw128(u32 a,u4 v){ *(LAS u4*)a=v; }
DI u2 ldstr(u32 a){ return __builtin_bit_cast(u2,__builtin_amdgcn_ds_read_tr16_b64_v4i16((LAS s4*)a)); }
DI u32 uni(u32 v){ return __builtin_amdgcn_readfirstlane(v); }

template<int OFF> DI void gload(u4& dst,u32 voff,const char* base){ asm volatile("global_load_dwordx4 %0, %1, %2 offset:%3 sc1" : "=v"(dst) : "v"(voff), "s"(base), "n"(OFF)); }
template<int OFF> DI void gload_t(u4& dst,u32 voff,const char* base){ asm volatile("global_load_dwordx4 %0, %1, %2 offset:%3 sc1" : "+v"(dst) : "v"(voff), "s"(base), "n"(OFF)); }
DI void mfma4(f4& c0,f4& c1,f4& c2,f4& c3,const u4& a,const u4 (&b)[4]){
  asm volatile("v_mfma_f32_16x16x32_bf16 %0, %4, %5, %0\n\tv_mfma_f32_16x16x32_bf16 %1, %4, %6, %1\n\tv_mfma_f32_16x16x32_bf16 %2, %4, %7, %2\n\tv_mfma_f32_16x16x32_bf16 %3, %4, %8, %3"
    : "+v"(c0),"+v"(c1),"+v"(c2),"+v"(c3) : "v"(a),"v"(b[0]),"v"(b[1]),"v"(b[2]),"v"(b[3])); }
DI void mfma4z(f4& c0,f4& c1,f4& c2,f4& c3,const u4& a,const u4 (&b)[4]){
  asm volatile("v_mfma_f32_16x16x32_bf16 %0, %4, %5, 0\n\tv_mfma_f32_16x16x32_bf16 %1, %4, %6, 0\n\tv_mfma_f32_16x16x32_bf16 %2, %4, %7, 0\n\tv_mfma_f32_16x16x32_bf16 %3, %4, %8, 0"
    : "=&v"(c0),"=&v"(c1),"=&v"(c2),"=&v"(c3) : "v"(a),"v"(b[0]),"v"(b[1]),"v"(b[2]),"v"(b[3])); }
template<int CNT> DI void waitB(u4 (&B)[7]){ asm volatile("s_waitcnt vmcnt(%7)" : "+v"(B[0]),"+v"(B[1]),"+v"(B[2]),"+v"(B[3]),"+v"(B[4]),"+v"(B[5]),"+v"(B[6]) : "n"(CNT) : "memory"); }
DI void waitA(u4 (&A)[4]){ asm volatile("s_waitcnt lgkmcnt(0)" : "+v"(A[0]),"+v"(A[1]),"+v"(A[2]),"+v"(A[3]) :: "memory"); }
#define WAITV(n) asm volatile("s_waitcnt vmcnt(%0)" :: "n"(n) : "memory")
#define WAITL0() asm volatile("s_waitcnt lgkmcnt(0)" ::: "memory")
#define CFENCE() asm volatile("" ::: "memory")
#define SCHED() __builtin_amdgcn_sched_barrier(0)
DI void bar(){ asm volatile("s_waitcnt lgkmcnt(0)\n\ts_barrier" ::: "memory"); }
DI f2 unpk(u32 v){ return f2{bflo(v),bfhi(v)}; }
DI u32 pkbf2(f2 v){ return __builtin_bit_cast(u32,__builtin_convertvector(v,bf2)); }
DI u32 pkhf2(f2 v){ return __builtin_bit_cast(u32,__builtin_convertvector(v,hf2)); }
DI f2 fma2(f2 a,f2 b,f2 c){ return __builtin_elementwise_fma(a,b,c); }
DI f2 gelu2(f2 x){
  f2 u=x*fma2(x*x,f2{-0.10294324f,-0.10294324f},f2{-2.30220820f,-2.30220820f});
  f2 d=f2{__builtin_amdgcn_exp2f(u[0]),__builtin_amdgcn_exp2f(u[1])}+f2{1.f,1.f};
  return x*f2{__builtin_amdgcn_rcpf(d[0]),__builtin_amdgcn_rcpf(d[1])};
}
DI void ln_fin(float mean,float var,float& m,float& rinv){
  m=rbf(mean); float v=rbf(__builtin_fmaxf(var,0.f));
  float s=rbf(__builtin_amdgcn_sqrtf(rbf(v+1e-5f))); float r=__builtin_amdgcn_rcpf(s); rinv=__builtin_fmaf(__builtin_fmaf(-s,r,1.f),r,r);
}
typedef __amdgpu_buffer_rsrc_t rsrc_t;
DI void st16(rsrc_t rs,u32 off,u4 v){ __builtin_amdgcn_raw_buffer_store_b128(v,rs,off,0,16); }
DI void st8(rsrc_t rs,u32 off,u2 v){ __builtin_amdgcn_raw_buffer_store_b64(v,rs,off,0,16); }
DI void st4(rsrc_t rs,u32 off,u32 v){ __builtin_amdgcn_raw_buffer_store_b32(v,rs,off,0,16); }
DI u4 ld16(rsrc_t rs,u32 off){ return __builtin_amdgcn_raw_buffer_load_b128(rs,off,0,16); }
DI u32 ld4(rsrc_t rs,u32 off){ return __builtin_amdgcn_raw_buffer_load_b32(rs,off,0,16); }
constexpr int SPIN=1<<17;
constexpr u32 TAGINC=0x9E3779B9u;
template<int SL=2> DI void pollw(rsrc_t rs,u32 off,u32 tag){
  #pragma unroll 1
  for(int s=0;s<SPIN;s++){ u32 v=ld4(rs,off); CFENCE(); if(__all(v==tag)) break; __builtin_amdgcn_s_sleep(SL); }
}

DI f16v mfma32b(u4 a,u4 b,f16v c){ return __builtin_amdgcn_mfma_f32_32x32x16_bf16(__builtin_bit_cast(bfx8,a),__builtin_bit_cast(bfx8,b),c,0,0,0); }
DI u4 mksrd(const void* b){ u64 a=(u64)b; return u4{(u32)a,(u32)(a>>32)&0xffffu,0xffffffffu,0x00020000u}; }
template<int IMM> DI void setm0(u32 base){ asm volatile("s_add_u32 m0, %0, %1\n\ts_nop 0" :: "s"(base), "n"(IMM) : "scc","memory"); }
template<int OFF> DI void dma(u4 srd,u32 voff,u32 soff){ asm volatile("buffer_load_dwordx4 %0, %1, %2 offen offset:%3 sc1 lds" :: "v"(voff), "s"(srd), "s"(soff), "n"(OFF) : "memory"); }
DI void touch(u4 srd,u32 voff,u32 soff){ asm volatile("buffer_load_dword %0, %1, %2 offen sc1 lds" :: "v"(voff), "s"(srd), "s"(soff) : "memory"); }
template<int OFF> DI void gload8(u2& dst,u32 voff,const char* base){ asm volatile("global_load_dwordx2 %0, %1, %2 offset:%3 sc1" : "=v"(dst) : "v"(voff), "s"(base), "n"(OFF)); }
DI void gload2(u32& dst,u32 voff,const char* base){ asm volatile("global_load_ushort %0, %1, %2" : "=v"(dst) : "v"(voff), "s"(base)); }
DI u32 arrive(u32 a,int){ u32 o; asm volatile("s_mov_b64 exec, 1\n\tds_add_rtn_u32 %0, %1, %2\n\ts_mov_b64 exec, -1\n\ts_waitcnt lgkmcnt(0)" : "=&v"(o) : "v"(a), "v"(1u) : "memory"); return uni(o); }
template<int SL=1> DI void ldswait(u32 a,u32 n){ while(__hip_atomic_load((LAS u32*)a,__ATOMIC_RELAXED,__HIP_MEMORY_SCOPE_WORKGROUP)<n) __builtin_amdgcn_s_sleep(SL); CFENCE(); }
DI void tie1(u32& g){ asm volatile("" : "+v"(g) :: "memory"); }
DI u32 opq(u32 v){ asm volatile("" : "+v"(v)); return v; }

constexpr u32 STG=32768;
constexpr u32 RING=24576, RB=62848, CONV=0, KP=2208, UOF=16768, CVS=UOF+4160, S7O=4*KP, S9O=S7O+2112, KZO=S9O+2112, MISC=RB+4*RING, JNK=MISC+2048, PM=163840-128, LDS_TOTAL=163840;
constexpr u32 ATL=2*CVS;
constexpr u32 C_FULL=PM, C_G1=PM+4, C_Z2=PM+8, C_Z1=PM+12, C_RED=PM+16, C_H=PM+20, C_FREE=PM+24, C_CV=PM+28, C_Z1R=PM+32, C_TAG=PM+40, C_KP=PM+48, C_DONE=PM+64, C_CS=PM+72, C_P=PM+96, C_AT=PM+112, C_WR=PM+116;
constexpr u32 CODE_BYTES=37888;
constexpr int HPF=5;
DI void signal(u32 a,int){ asm volatile("s_mov_b64 exec, 1\n\tds_add_u32 %0, %1\n\ts_mov_b64 exec, -1" :: "v"(a), "v"(1u) : "memory"); }
template<int OFF> DI void gload4(u32& dst,u32 voff,const char* base){ asm volatile("global_load_dword %0, %1, %2 offset:%3 sc1" : "=v"(dst) : "v"(voff), "s"(base), "n"(OFF)); }

DI f2 cmulp(f2 a,f2 b,f2 bs){ return fma2(f2{a[1],a[1]},bs,f2{a[0],a[0]}*b); }
DI u32 ldsv(u32 a){ return __hip_atomic_load((LAS u32*)a,__ATOMIC_RELAXED,__HIP_MEMORY_SCOPE_WORKGROUP); }
DI f2 cpow_dir(float lr,float rv,float pw){ float e=__builtin_amdgcn_exp2f(lr*pw); float f=__builtin_amdgcn_fractf(rv*pw);
  return f2{e*__builtin_amdgcn_cosf(f),e*__builtin_amdgcn_sinf(f)}; }
DI u32 dup(u32 a){ return __builtin_amdgcn_perm(a,a,0x01000100u); }
DI u32 cat(u32 lo,u32 hi){ return __builtin_amdgcn_perm(hi,lo,0x05040100u); }
DI u32 lds16(u32 a){ return *(LAS u16*)a; }

DI float kgen_tab(u32 cb,int lane,const u32 (&pr)[6],float (&kp)[11]){
  const float dt=rbf(__expf(bflo(pr[4])));
  const float cr[2]={bflo(pr[2]),bfhi(pr[2])}, cm[2]={bflo(pr[3]),bfhi(pr[3])};
  float mx=fmaxf(fmaxf(fabsf(cr[0]),fabsf(cm[0])),fmaxf(fabsf(cr[1]),fabsf(cm[1])));
  u32 eb=__float_as_uint(mx)&0x7f800000u;
  #define DPPMAX(c) eb=max(eb,(u32)__builtin_amdgcn_update_dpp(0,(int)eb,c,0xf,0xf,false))
  DPPMAX(0x128); DPPMAX(0x124); DPPMAX(0x122); DPPMAX(0x121);
  eb=max(max((u32)__builtin_amdgcn_readlane(eb,0),(u32)__builtin_amdgcn_readlane(eb,16)),max((u32)__builtin_amdgcn_readlane(eb,32),(u32)__builtin_amdgcn_readlane(eb,48))); eb=eb<0x01000000u?0x3f800000u:(eb>0x7e000000u?0x7e000000u:eb);
  const float ksc=2.f*dt*__uint_as_float(eb), isc=__uint_as_float(0x7f000000u-eb);
  const int r=lane&31,h=lane>>5;
  const u32 TP=272, TA=cb, TB=cb+22*TP;
  f16v acc={};
  f2 an[2][4];
  #pragma unroll
  for(int c=0;c<2;c++){ const float l_a=c?bfhi(pr[0]):bflo(pr[0]), i_m=c?bfhi(pr[1]):bflo(pr[1]);
    const float lr=(-rbf(__expf(l_a))*dt)*1.44269504f, rv=(i_m*dt)*0.15915494f;
    an[c][0]=cpow_dir(lr,rv,1.f); an[c][1]=cpow_dir(lr,-rv,8.f); an[c][2]=cpow_dir(lr,-rv,16.f); an[c][3]=cpow_dir(lr,rv,256.f); }
  u32 hold[54]; const u32 wt=TA+lane*4;
  struct P2 { f2 v[2]; };
  auto mul=[&](const P2& x,int k){ P2 o;
    #pragma unroll
    for(int c=0;c<2;c++) o.v[c]=cmulp(x.v[c],an[c][k],f2{-an[c][k][1],an[c][k][0]});
    return o; };
  auto put=[&](int row,const P2& x){ *(LAS u32*)(wt+row*TP)=pkhf2(x.v[0]); hold[row]=pkhf2(x.v[1]); };
  P2 e={{f2{1.f,0.f},f2{1.f,0.f}}}, o={{an[0][1],an[1][1]}}, bk[8], t;
  bk[0].v[0]=f2{cr[0]*isc,cm[0]*isc}; bk[0].v[1]=f2{cr[1]*isc,cm[1]*isc}; put(0,bk[0]); t=bk[0];
  constexpr int tr[8]={8,10,11,16,17,20,21,18};
  #pragma unroll
  for(int k=0;k<16;k++){ put(22+2*k,e); put(23+2*k,o);
    if(k<15){ if(k) e=mul(e,2); else { e.v[0]=an[0][2]; e.v[1]=an[1][2]; } o=mul(o,2); }
    if(k<8){ t=mul(t,3); put(tr[k],t); }
    if(k<7){ bk[k+1]=mul(bk[k],0); put(k+1,bk[k+1]); }
    if(k==8){ put(9,mul(bk[2],3)); put(13,mul(bk[6],3)); t=mul(bk[4],3); put(12,t); }
    if(k==9||k==10){ t=mul(t,3); put(5+k,t); }
    SCHED(); }
  const int ra=(r>21||r==19)?0:r;
  #pragma unroll
  for(int c=0;c<2;c++){
    CFENCE();
    u4 fa[8],fb[8];
    #pragma unroll
    for(int ks=0;ks<8;ks++){ fa[ks]=ldsr128(TA+ra*TP+ks*32+h*16); fb[ks]=ldsr128(TB+r*TP+ks*32+h*16); }
    CFENCE();
    if(c==0){
      #pragma unroll
      for(int i=0;i<54;i++) if(i!=19) *(LAS u32*)(wt+i*TP)=hold[i]; }
    #pragma unroll
    for(int ks=0;ks<8;ks++) acc=mfma32h(fa[ks],fb[ks],acc);
  }
  CFENCE();
  #pragma unroll
  for(int i=0;i<11;i++) kp[i]=acc[i];
  return ksc;
}
DI void kgen_exp(u32 cb,int lane,const float (&acc)[11],const u32 (&ga)[11]){
  const int r=lane&31,h=lane>>5;
  { const u32 a4=cb+r*16+h*8, a5=cb+r*16+h*1024, az=cb+KZO+r*2+h*128;
    #define PK(addr,base,reg) do{ *(LAS _Float16*)((addr)+2*(base))=(_Float16)acc[reg]; }while(0)
    PK(a4,0,0);PK(a4,1,1);PK(a4,2,2);PK(a4,3,3); PK(a4,256,4);PK(a4,258,5);PK(a4,512,6);PK(a4,768,7);
    PK(a5,1024,8); PK(az,255,10); PK(az,127,8); PK(az,159,9);
    #undef PK
    CFENCE();
    if(lane<16) ldsw128(cb+KZO+lane*16,u4{0,0,0,0});
    CFENCE();
  }
  { const u32 g0=cb+2*(1024-8*lane);
    u32 f2_=lds16(g0+(lane?8:0)), a2=lds16(g0), m2=lds16(g0-8), b2=lds16(g0-16);
    u32 e[11];
    #pragma unroll
    for(int j=0;j<11;j++) e[j]=lds16(ga[j]);
    u32 k01=*(LAS u32*)cb, k23=*(LAS u32*)(cb+4), kl=lds16(cb+2048);
    WAITL0();
    const u32 wo=cb+96+lane*16;
    { u32 aa=dup(a2),mm=dup(m2),bb=dup(b2),am=cat(a2,m2),fa=cat(f2_,a2),mb=cat(m2,b2);
      ldsw128(wo,u4{aa,am,mm,bb}); ldsw128(wo+KP,u4{aa,aa,mm,mb}); ldsw128(wo+2*KP,u4{aa,aa,am,mm}); ldsw128(wo+3*KP,u4{fa,aa,aa,mm}); }
    { u32 o0=cat(e[0],e[1]),o1=cat(e[2],e[3]),o2=cat(e[4],e[5]),o3=cat(e[6],e[7]),o4=cat(e[8],e[9]);
      u32 p1=cat(e[1],e[2]),d0=cat(e[3],e[4]),d1=cat(e[5],e[6]),d2=cat(e[7],e[8]),d3=cat(e[9],e[10]);
      ldsw128(wo+1024,u4{d0,d1,d2,d3}); ldsw128(wo+1024+KP,u4{o1,o2,o3,o4}); ldsw128(wo+1024+2*KP,u4{p1,d0,d1,d2}); ldsw128(wo+1024+3*KP,u4{o0,o1,o2,o3}); }
    if(lane<10){ if(lane){k01=0;k23=0;} if(lane!=9) kl=0;
      const u32 d0=k01&0xffffu, o1=__builtin_amdgcn_alignbit(k01,k01,16), o0=__builtin_amdgcn_alignbit(k23,k23,16), p1=(k23&0xffffu)|(k01&0xffff0000u), kk=dup(kl), zk=kl<<16;
      const u32 w2=lane<4? wo+2048 : wo-160;
      ldsw128(w2,u4{d0,0,kk,kk}); ldsw128(w2+KP,u4{o1,0,zk,kk}); ldsw128(w2+2*KP,u4{p1,d0,0,kk}); ldsw128(w2+3*KP,u4{o0,o1,0,zk}); }
  }
}
DI f4 mfma16h(u4 a,u4 b,f4 c){ return __builtin_amdgcn_mfma_f32_16x16x32_f16(__builtin_bit_cast(hfx8,a),__builtin_bit_cast(hfx8,b),c,0,0,0); }
DI u4 lds2x64(u32 a){ const u2 lo=ldsr64(a), hi=ldsr64(a+8); return u4{lo[0],lo[1],hi[0],hi[1]}; }
DI void conv_win(u32 cb,int lane,u4 (&sw)[4]){
  float P[25]; P[0]=0.f;
  #pragma unroll
  for(int c=0;c<4;c++){ const int X=2*lane-2+c; const u4 v=ldsr128(cb+UOF+(X&3)*1040+((X>>2)+1)*16);
    #pragma unroll
    for(int e=0;e<8;e++){ const int i=8*c+e-4; if(i>=0&&i<24) P[i+1]=P[i]+((e&1)?hhi(v[e>>1]):hlo(v[e>>1])); } }
  #pragma unroll
  for(int k=0;k<8;k++){ sw[k>>2][k&3]=pkhf(P[2*k+8]-P[2*k+1],P[2*k+9]-P[2*k+2]); sw[2+(k>>2)][k&3]=pkhf(P[2*k+9]-P[2*k],P[2*k+10]-P[2*k+1]); }
}
DI void conv_help(u32 cb,int lane,const u4 (&sw)[4]){
  const int r=lane&15,q=lane>>4; f4 y[2][2]={};
  ldsw128(cb+S7O+48+lane*32,sw[0]); ldsw128(cb+S7O+64+lane*32,sw[1]); ldsw128(cb+S9O+48+lane*32,sw[2]); ldsw128(cb+S9O+64+lane*32,sw[3]);
  if(lane<3){ ldsw128(cb+S7O+lane*16,u4{0,0,0,0}); ldsw128(cb+S9O+lane*16,u4{0,0,0,0}); }
  CFENCE();
  const u32 ka=cb+(r&3)*KP+16*q-8*(r>>2)+64, ub=cb+UOF+q*1040+(r+1)*16;
  const u4 a0=lds2x64(ka+32), a1=lds2x64(ka), b0=ldsr128(ub), b1=ldsr128(ub+256), a2=lds2x64(ka-32), b2=ldsr128(ub-16), b3=ldsr128(ub+240);
  const u32 sa=cb+(((r>>2)&1)?S9O:S7O)+2*(64*q+24-8*(r>>2)+4*(r&3)), kz=cb+KZO+8*(r-2*q+23); u4 bz[4],fa[4][2];
  #pragma unroll
  for(int ks=0;ks<4;ks++) bz[ks]=lds2x64(kz+(3-ks)*64);
  #pragma unroll
  for(int i=0;i<8;i++){ const u2 lo=ldstr(sa+(i>>1)*512+(i&1)*32+64), hi=ldstr(sa+(i>>1)*512+(i&1)*32); fa[i>>1][i&1]=u4{lo[0],lo[1],hi[0],hi[1]}; }
  SCHED();
  y[0][0]=mfma16h(a2,b2,y[0][0]); y[0][1]=mfma16h(a2,b3,y[0][1]);
  y[0][0]=mfma16h(a0,b0,y[0][0]); y[1][0]=mfma16h(a1,b0,y[1][0]); y[0][1]=mfma16h(a0,b1,y[0][1]); y[1][1]=mfma16h(a1,b1,y[1][1]);
  #pragma unroll
  for(int ks=0;ks<4;ks++)
    #pragma unroll
    for(int m=0;m<2;m++){ y[m][1]=mfma16h(fa[ks][m],bz[ks],y[m][1]); if(ks<2) y[m][0]=mfma16h(fa[ks][m],bz[ks+2],y[m][0]); }
  CFENCE();
  #pragma unroll
  for(int i=0;i<4;i++) *(LAS f4*)(cb+S7O+i*1024+lane*16)=y[i&1][i>>1];
}
#define RDA(S,i,m) asm volatile("ds_read2_b64 %0, %1 offset0:%2 offset1:%3" : "=v"(AF[S][i][m]) : "v"(ka), "n"((1-(i))*128+(1-(m))*4), "n"((1-(i))*128+(1-(m))*4+1))
#define RDB(S,i,ad,o) asm volatile("ds_read_b128 %0, %1 offset:%2" : "=v"(BF[S][i]) : "v"(ad), "n"(o))
#define TIEA(S,i) "+v"(AF[S][i][0]),"+v"(AF[S][i][1])
#define CV_M(S,i,n) do{ y[0][n]=mfma16h(AF[S][i][0],BF[S][(n)-(i)],y[0][n]); y[1][n]=mfma16h(AF[S][i][1],BF[S][(n)-(i)],y[1][n]); SCHED(); }while(0)
#define CV_U0() u32 u0=u1-256; u0=((int)u0<(int)uz)?uz:u0
template<class CB> DI void conv_wave(CB&& mid,rsrc_t rs,int d,u32 cb,u32 flag,int lane,float ksc,float dsk){
  const int r=lane&15,q=lane>>4; u32 ka=cb+(r&3)*KP+16*q-8*(r>>2)+1088; const u32 uz=cb+UOF+q*1040; u32 u1=uz+(r+17)*16; u4 AF[2][2][2]; u4 BF[2][4]; f4 y[2][4]={};
  #define CV_WT(S) asm volatile("s_waitcnt lgkmcnt(0)" : TIEA(S,0),TIEA(S,1),"+v"(BF[S][0]),"+v"(BF[S][1]),"+v"(BF[S][2]),"+v"(BF[S][3]))
  #define CV_STEP(C,N) do{ CV_U0(); CV_M(C,0,0); RDA(N,0,0); RDB(N,0,u0,0); RDA(N,0,1); SCHED(); CV_M(C,0,1); RDB(N,1,u1,0); RDA(N,1,0); RDB(N,2,u1,256); SCHED(); CV_M(C,0,2); RDA(N,1,1); RDB(N,3,u1,512); \
      ka-=64; u1-=16; SCHED(); CV_M(C,0,3); CV_M(C,1,1); CV_M(C,1,2); CV_M(C,1,3); CV_WT(N); }while(0)
  { CV_U0(); RDA(0,0,0); RDA(0,0,1); RDA(0,1,0); RDA(0,1,1); RDB(0,0,u0,0); RDB(0,1,u1,0); RDB(0,2,u1,256); RDB(0,3,u1,512); ka-=64; u1-=16; CV_WT(0); }
  #pragma unroll 1
  for(int dl=0;dl<8;dl++){ CV_STEP(0,1); if(dl==7){ ka+=64; u1+=16; } CV_STEP(1,0); if(dl==2) mid(); }
  u2 uv[2][4];
  #pragma unroll
  for(int n=0;n<4;n++)
    #pragma unroll
    for(int m=0;m<2;m++) uv[m][n]=ldsr64(cb+UOF+(2*m+(q>>1))*1040+(16*n+r+1)*16+8*(q&1));
  const u32 sc=cb+UOF+r*64+q*8;
  auto fin=[&](int n){
    #pragma unroll
    for(int m=0;m<2;m++){ u2 o;
      #pragma unroll
      for(int e=0;e<2;e++) o[e]=pkbf2(fma2(f2{hlo(uv[m][n][e]),hhi(uv[m][n][e])},f2{dsk,dsk},f2{y[m][n][2*e],y[m][n][2*e+1]}*ksc));
      *(LAS u2*)(sc+n*1024+m*32)=o; } };
  ldswait(flag,2u);
  { f4 hp[4];
    #pragma unroll
    for(int i=0;i<4;i++) hp[i]=*(LAS f4*)(cb+S7O+i*1024+lane*16);
    fin(0); fin(1);
    #pragma unroll
    for(int i=0;i<4;i++) y[i&1][2+(i>>1)]+=hp[i];
    fin(2); fin(3); }
  CFENCE();
  u4 ov[4];
  #pragma unroll
  for(int n=0;n<4;n++) ov[n]=ldsr128(cb+UOF+n*1024+lane*16);
  #pragma unroll
  for(int n=0;n<4;n++) st16(rs,OFF_AT+d*4096+n*1024+lane*16,ov[n]);
  WAITV(0);
}

constexpr int NB=3, HZZ=13;
constexpr int g2ops(int h){ return h<0? 7 : (h+NB<16?7:0)+((h>=3&&h<=10)?4:0)+(h==HZZ-3?1:0)+(h==HZZ?8:0); }
constexpr int g2cnt(int h){ int c=0; for(int i=h-NB+1;i<h;i++) c+=g2ops(i); return c; }
struct Frag { u4 x[4], w[4][2]; };
__global__ __launch_bounds__(512,2) void fused(Params p){
  extern __shared__ __attribute__((aligned(16))) char sm[];
  const u32 smb=(u32)(size_t)(LAS char*)sm;
  u64 pc0; asm volatile("s_getpc_b64 %0" : "=s"(pc0));
  const int b=blockIdx.x, xc=b&7, idx=b>>3;
  const int tid=threadIdx.x, w=uni(tid>>6); int lane=tid&63;
  auto cpf=[&](u32 o){ u4 sc=mksrd((const void*)(pc0&~127ull)); sc[2]=CODE_BYTES; setm0<JNK>(smb); touch(sc,lane*128,o); };
  if(idx>=28) cpf(w*8192);
  SCHED();
  const int rt=4*(xc>>1)+(idx&3), j=7*(xc&1)+(idx>>2), mi=w&3, g1=w>>2;
  const u32 kr0=((j+rt)%5)*2;
  u32 vx0,vx1,vw1,vw2,k0=kr0*128,k0w=kr0*(64*3456); const u32 mw=smb+w*4096;
  { const u32 lr=lane>>3,c=lane&7; vx0=(rt*128+16*w+lr)*1280+((c^(lr>>1))<<4); vx1=(vx0^64u)+9216;
    vw1=(8*w+lr)*3456+((c^((lr&2)<<1))<<4)+j*128; vw2=vw1+1024+(j==13?0:14*128); }
  asm volatile("" : "+v"(vx0),"+v"(vx1),"+v"(vw1),"+v"(vw2),"+s"(k0),"+s"(k0w));
  setm0<0>(mw);
  SCHED();
  const u4 srdX=mksrd(p.x), srdW=mksrd((const char*)p.win-3072);
  if(idx<28){ dma<0>(srdX,vx0,k0); dma<1024>(srdX,vx1,k0); dma<2048>(srdW,vw2,k0w); dma<3072>(srdW,vw1,k0w); }
  SCHED();
  const rsrc_t rs=__builtin_amdgcn_make_buffer_rsrc(p.ws,0,WS_END,0x00020000);
  const u4 srdS=mksrd(p.ws);
  const int rtB=8*(xc>>1)+(idx&7), ct=4*(xc&1)+(idx>>3);
  const bool hasc=idx<28 && idx+28*(w&3)<80 && w>=4;
  const int dch=80*xc+(hasc? idx+28*(w&3) : 0);
  const u32 cb=smb+CONV+(w&3)*CVS;
  u32 tag; float ksc=0.f, dsk=0.f;
  u32 zp[2][8]={};
  auto z1st=[&](){ const int r=lane&31,h=lane>>5;
    #pragma unroll
    for(int n=0;n<2;n++) if(2*j+n<27){ const u32 oo=OFF_Z1T+((rt*8+2*mi+(r>>4))*27+2*j+n)*1024+(h*16+(r&15))*16;
      st16(rs,oo,u4{zp[n][0],zp[n][1],zp[n][4],zp[n][5]}); st16(rs,oo+512,u4{zp[n][2],zp[n][3],zp[n][6],zp[n][7]}); } };
  auto f1=[&](){ WAITV(0); if(arrive(smb+C_Z1,lane)==3u && lane==0) st4(rs,OFF_F1+(rt*16+j)*128,tag); };
  auto pre01=[&](){
    const u4 sa=mksrd((const char*)p.wsp+(size_t)rtB*64*4096+mi*1024-4096); const u32 v0=(lane>>3)*4096+(((lane&7)^(lane>>4))<<4);
    #pragma unroll 1
    for(int i=0;i<4;i++){ setm0<RB+8192>(smb+mi*RING+i*4096); const u32 g=(i&1)*4+2*mi, k=(i>>1)*128+4096;
      dma<0>(sa,v0,(g&7)*32768+k); dma<1024>(sa,v0^64u,(g&7)*32768+32768-1024+k); dma<2048>(sa,v0,((g+2)&7)*32768-2048+k); dma<3072>(sa,v0^64u,((g+2)&7)*32768+32768-3072+k); } };
  if(idx>=28){
    const int a=xc*4+idx-28, r=lane&15, q=lane>>4;
    const u32 ep=ld4(rs,OFF_EP);
    u4 xr[10]; const char* xs=(const char*)p.x+(size_t)(64*a+8*w+(lane>>3))*1280+(lane&7)*16;
    #pragma unroll
    for(int i=0;i<10;i++) xr[i]=*(const u4*)(xs+i*128);
    { const u32 l=(a*8+w)*19+(lane<19?lane:18); setm0<JNK>(smb); touch(srdS,(l<3584? OFF_ST/128+l : OFF_F2/128+(l-3584<1088?l-3584:1087))*128,0u); }
    if(tid<32) *(LAS u32*)(smb+PM+tid*4)=0u;
    tag=uni(ep)+TAGINC;
    float s0=0.f,s1=0.f,q0=0.f,q1=0.f;
    #pragma unroll
    for(int i=0;i<10;i++){ s0=dot2(xr[i][0],0x3f803f80u,s0); q0=dot2(xr[i][0],xr[i][0],q0); s1=dot2(xr[i][1],0x3f803f80u,s1); q1=dot2(xr[i][1],xr[i][1],q1);
      s0=dot2(xr[i][2],0x3f803f80u,s0); q0=dot2(xr[i][2],xr[i][2],q0); s1=dot2(xr[i][3],0x3f803f80u,s1); q1=dot2(xr[i][3],xr[i][3],q1); }
    s0+=s1; q0+=q1;
    #define DPPADD(v,c) v+=__int_as_float(__builtin_amdgcn_update_dpp(0,__float_as_int(v),c,0xf,0xf,false))
    DPPADD(s0,0xB1); DPPADD(q0,0xB1); DPPADD(s0,0x4E); DPPADD(q0,0x4E); DPPADD(s0,0x141); DPPADD(q0,0x141);
    float m1,ri; { float mean=s0*(1.f/640.f); ln_fin(mean,q0*(1.f/640.f)-mean*mean,m1,ri); }
    if((lane&7)==0) st16(rs,OFF_RS+(64*a+8*w+(lane>>3))*16,u4{__float_as_uint(m1),tag,__float_as_uint(ri),tag});
    if(a<27){ const u4 srdO=mksrd((const char*)p.wout+a*(32*1280)+w*5120); const u32 mo=smb+w*5120; setm0<0>(mo);
      dma<0>(srdO,lane*16,0); dma<1024>(srdO,lane*16,0); dma<2048>(srdO,lane*16,0); dma<3072>(srdO,lane*16,0); setm0<4096>(mo); dma<0>(srdO,lane*16,4096); }
    #pragma unroll
    for(int i=0;i<10;i++){ u4 o;
      #pragma unroll
      for(int e=0;e<4;e++) o[e]=pkhf2(fma2(unpk(xr[i][e]),f2{ri,ri},f2{-m1*ri,-m1*ri}));
      ldsw128(smb+RB+(8*w+(lane>>3))*1296+(lane&7)*16+i*128,o); }
    WAITV(0); bar();
    #pragma unroll
    for(int i=0;i<10;i++){ const int c=w*10+i; const u32 ad=smb+RB+(32*((r>>1)&1)+8*q+(r>>2))*1296+c*16+(r&1)*8; u2 lo=ldstr(ad), hi=ldstr(ad+4*1296);
      const u32 sc=smb+40960+w*2048+(i&1)*1024; ldsw128(sc+(r&7)*128+((((r>>3)*4+q)^(r&7))<<4),u4{lo[0],lo[1],hi[0],hi[1]});
      CFENCE(); const u4 o=ldsr128(sc+(lane>>3)*128+(((lane&7)^(lane>>3))<<4)); CFENCE();
      st16(rs,OFF_XNT+a*81920+c*1024+lane*16,o); }
    WAITV(0);
    if(arrive(smb+C_Z1,lane)==7u && lane==0) st4(rs,OFF_FX+a*128,tag);
    if(a<27){
      #pragma unroll
      for(int i=0;i<5;i++){ u32 ad=smb+(8*q+(r>>2))*1280+(w*5+i)*32+(r&3)*8; u2 lo=ldstr(ad), hi=ldstr(ad+4*1280);
        st16(rs,OFF_WOF+(u32)((w*27+a)*5+i)*1024+lane*16,u4{lo[0],lo[1],hi[0],hi[1]}); } }
    WAITV(0); bar();
    if(tid==0) st4(rs,OFF_FW+a*128,tag);
    if(!g1){ pre01(); WAITV(0); }
    else { const u4 so=mksrd((const char*)p.out+(size_t)(a*4+mi)*20480); setm0<JNK>(smb); touch(so,lane*128,0u); touch(so,lane*128,8192u); touch(so,(lane&31)*128,16384u); }
  } else {
    const int r=lane&31, h=lane>>5;
    u32 epv;
    u32 kx[10]; { u32 k=kr0;
      #pragma unroll
      for(int i=0;i<10;i++){ kx[i]=k*128; k=k==9?0:k+1; } }
    auto issue1=[&](auto sc,int k){ constexpr int s=sc.value;
      if(k==0){ setm0<(s&3)*STG>(mw); dma<0>(srdX,vx0,kx[s]); } if(k==1) dma<1024>(srdX,vx1,kx[s]); if(k==2) dma<2048>(srdW,vw2,kx[s]*1728); if(k==3) dma<3072>(srdW,vw1,kx[s]*1728); };
    auto issue=[&](auto sc){ issue1(sc,0); issue1(sc,1); issue1(sc,2); issue1(sc,3); };
    u32 pr[6];
    gload4<0>(epv,0u,p.ws+OFF_EP);
    gload4<0>(pr[0],lane*4,g1? (const char*)(p.lar+dch*128) : (const char*)p.wsp+(size_t)(xc>>1)*512*4096+((xc&1)*28+idx)*(4*74*128)); gload4<0>(pr[1],lane*4,(const char*)(p.aim+dch*128)); gload4<0>(pr[2],lane*4,(const char*)(p.cre+dch*128)); gload4<0>(pr[3],lane*4,(const char*)(p.cim+dch*128));
    gload2(pr[4],0u,(const char*)(p.ldt+dch)); gload2(pr[5],0u,(const char*)(p.dsk+dch));
    asm volatile("s_barrier" ::: "memory");
    issue(std::integral_constant<int,1>{});
    if(tid<32) *(LAS u32*)(smb+PM+tid*4)=0u;
    u32 ax[2][4],aw[2][4][2];
    { u32 row=32*mi+r, a0=smb+(row>>4)*4096+(row&15)*128+((h^((r>>1)&7))<<4);
      u32 qq=(lane&15)>>2, w0=smb+2048+g1*1024+h*4096+qq*128+8*(lane&1)+(((2*((lane>>4)&1)+((lane>>1)&1))^((qq&2)<<1))<<4);
      #pragma unroll
      for(int i=0;i<2;i++)
        #pragma unroll
        for(int k=0;k<4;k++){ const u32 kk=(k+mi)&3, wo=i*2*STG; ax[i][k]=opq((a0^(kk*32u))+wo); aw[i][k][0]=opq(w0+kk*8192+wo); aw[i][k][1]=opq((w0^64u)+kk*8192+wo); } }
    u4 sel; { const u32 o=((lane&15)==((lane>>4)&1))?0x3f803f80u:0u; sel=u4{o,o,o,o}; asm volatile("" : "+v"(sel)); }
    u32 ga[11];
    #pragma unroll
    for(int i=0;i<11;i++) ga[i]=opq(cb+2u*(u32)rbf((float)(512-8*lane+3-i)));
    Frag F[2];
    asm volatile("s_waitcnt vmcnt(4)" : "+v"(epv),"+v"(pr[0]),"+v"(pr[1]),"+v"(pr[2]),"+v"(pr[3]),"+v"(pr[4]),"+v"(pr[5]) :: "memory");
    bar();
    tag=uni(epv)+TAGINC; dsk=bflo(pr[5]);
    #pragma unroll
    for(int k=0;k<4;k++){
      #pragma unroll
      for(int n=0;n<2;n++){ u2 lo=ldstr(aw[0][k][n]), hi=ldstr(aw[0][k][n]+512); F[0].w[k][n]=u4{lo[0],lo[1],hi[0],hi[1]}; }
      F[0].x[k]=ldsr128(ax[0][k]); }
    issue(std::integral_constant<int,2>{});
    f16v acc[2]={}; f4 cacc[2]={}; u4 G;
    const u32 go=OFF_RS+rt*2048+mi*512, cp=smb+MISC+g1*1024;
    sfor<10>([&](auto ic){
      constexpr int s=ic.value; Frag& cur=F[s&1]; Frag& nxt=F[(s&1)^1];
      if constexpr(s<9){ WAITV(s==8? 3 : s==7? 7 : 4); asm volatile("s_barrier" ::: "memory"); }
      auto rw=[&](int k,int n){ if constexpr(s<9){ const int i=((s+1)>>1)&1; const u32 so=((s+1)&1)*STG; u2 lo=ldstr(aw[i][k][n]+so), hi=ldstr(aw[i][k][n]+so+512); nxt.w[k][n]=u4{lo[0],lo[1],hi[0],hi[1]}; } };
      auto rx=[&](int k){ if constexpr(s<9){ const int i=((s+1)>>1)&1; const u32 so=((s+1)&1)*STG; nxt.x[k]=ldsr128(ax[i][k]+so); } };
      #pragma unroll
      for(int k=0;k<4;k++){
        acc[0]=mfma32b(cur.w[k][0],cur.x[k],acc[0]); SCHED();
        if constexpr(s+3<=9) issue1(std::integral_constant<int,s+3>{},k);
        rw(k,0); rx(k);
        SCHED();
        acc[1]=mfma32b(cur.w[k][1],cur.x[k],acc[1]); SCHED();
        rw(k,1);
        if(k==0){ cacc[0]=mfma16(cur.w[0][0],sel,cacc[0]); cacc[1]=mfma16(cur.w[0][1],sel,cacc[1]); }
        if constexpr(s==6){ if(k==3){ gload<0>(G,r*16,p.ws+go); setm0<JNK>(smb);
            if(g1){ const u4 sp=mksrd((const char*)p.wsp+(size_t)(xc>>1)*512*4096); const u32 lb=(((xc&1)*28+idx)*4+mi)*74, l0=lb+lane, l1=lb+64+(lane<10?lane:9);
              touch(sp,(l0<16384?l0:16383)*128,0u); touch(sp,(l1<16384?l1:16383)*128,0u); }
            else { touch(mksrd(p.out+rtB*64*640),0u,0u); touch(mksrd(w==0? (const void*)(p.bsp+rtB*64) : w==1? (const void*)p.ws : w==2? (const void*)(p.wsp+(size_t)rtB*64*2048) : (const void*)(p.x+rtB*64*640)),(lane&15)*(w==1? (1u<<20) : w==2? 16384u : w==3? 4096u : 0u),0u); } } }
        if constexpr(s==8){ if(k==3) signal(smb+C_DONE,lane); }
        if constexpr(s==9){ if(k==1){
          if((lane&14)==0){ *(LAS f4*)(cp+mi*256+(lane&1)*64+(lane>>4)*16)=cacc[0]; *(LAS f4*)(cp+mi*256+128+(lane&1)*64+(lane>>4)*16)=cacc[1]; }
          signal(smb+C_CS+g1*4,lane); } }
        SCHED();
      }
    });
    if(!g1) __builtin_amdgcn_s_setprio(2);
    asm volatile("s_waitcnt vmcnt(2)" : "+v"(G) :: "memory");
    ldswait(smb+C_CS+g1*4,4u);
    const float cs0=*(LAS float*)(cp+h*512+r*4)+*(LAS float*)(cp+h*512+256+r*4), cs1=*(LAS float*)(cp+h*512+128+r*4)+*(LAS float*)(cp+h*512+384+r*4);
    #pragma unroll 1
    for(int sp=0;sp<SPIN && !__all(G[1]==tag&&G[3]==tag);sp++)asm volatile("global_load_dwordx4 %0, %1, %2 sc1\n\ts_waitcnt vmcnt(0)" : "=&v"(G) : "v"(r*16), "s"(p.ws+go) : "memory");
    const float nm=-__uint_as_float(G[0]), ri1=__uint_as_float(G[2]);
    acc[0]=__builtin_amdgcn_mfma_f32_32x32x2f32(cs0,nm,acc[0],0,0,0); acc[1]=__builtin_amdgcn_mfma_f32_32x32x2f32(cs1,nm,acc[1],0,0,0);
    #pragma unroll
    for(int e=0;e<16;e++) zp[e>>3][e&7]=pkbf2(gelu2(f2{acc[e>>3][2*(e&7)],acc[e>>3][2*(e&7)+1]}*ri1));
    if(g1){ ldswait(smb+C_DONE,8u); }
    else {
      float S=0.f,Q=0.f;
      if(j<13){
        #pragma unroll
        for(int e=0;e<8;e++){ S=dot2(zp[0][e],0x3f803f80u,S); Q=dot2(zp[0][e],zp[0][e],Q); } }
      #pragma unroll
      for(int e=0;e<8;e++){ S=dot2(zp[1][e],0x3f803f80u,S); Q=dot2(zp[1][e],zp[1][e],Q); }
      #define XSUM32(v) do{ auto t_=__builtin_amdgcn_permlane32_swap(__float_as_uint(v),__float_as_uint(v),false,false); v=__uint_as_float(t_[0])+__uint_as_float(t_[1]); }while(0)
      XSUM32(S); XSUM32(Q);
      const u32 sto=OFF_ST+rt*(14*2048)+(32*mi+r)*16;
      if(h==0) st16(rs,sto+j*2048,u4{__float_as_uint(S),tag,__float_as_uint(Q),tag});
    }
    float kp[11]; u4 g[7]={};
    const u32 gof=OFF_ST+rt*(14*2048)+(32*mi+r)*16+h*(7*2048);
    auto samp=[&](){ CFENCE();
      #pragma unroll
      for(int i=0;i<7;i++) g[i]=ld16(rs,gof+i*2048);
      CFENCE(); };
    if(hasc) ksc=kgen_tab(cb,lane,pr,kp); else if(!g1){ pre01(); samp(); }
    if(g1){
      if(hasc){
        kgen_exp(cb,lane,kp,ga);
        signal(smb+C_KP+mi*4,lane);
      }
      if(hasc) ldswait(smb+C_FULL,1u); else if(w<7){ z1st(); f1(); }
      if(hasc) z1st();
    } else {
      float S,Q;
      #pragma unroll 1
      for(int sp=0;sp<SPIN;sp++){ bool ok=true;
        #pragma unroll
        for(int i=0;i<7;i++) ok=ok&&g[i][1]==tag&&g[i][3]==tag;
        if(__all(ok)) break;
        __builtin_amdgcn_s_sleep(4); samp(); }
      S=0.f; Q=0.f;
      #pragma unroll
      for(int i=0;i<7;i++){ S+=__uint_as_float(g[i][0]); Q+=__uint_as_float(g[i][2]); }
      XSUM32(S); XSUM32(Q);
      float mean=S*(1.f/864.f), m2,r2; ln_fin(mean,Q*(1.f/864.f)-mean*mean,m2,r2); m2*=-r2;
      ldswait(smb+C_DONE,8u);
      const u32 tb=smb+RB+mi*RING, ad=tb+(8*(lane>>4)+((lane&15)>>2))*80+(lane&3)*8;
      const u32 zo=OFF_Z2F+((rt*4+mi)*54+(j==13?-2:2+4*j))*1024+lane*16;
      #pragma unroll
      for(int n=0;n<2;n++){
        #pragma unroll
        for(int g2=0;g2<4;g2++){ u2 o;
          #pragma unroll
          for(int e=0;e<2;e++) o[e]=pkbf2(fma2(unpk(zp[n][2*g2+e]),f2{r2,r2},f2{m2,m2}));
          *(LAS u2*)(tb+r*80+g2*16+h*8)=o; }
        CFENCE();
        u2 l0=ldstr(ad), h0=ldstr(ad+320), l1=ldstr(ad+32), h1=ldstr(ad+32+320);
        CFENCE();
        if(j<13||n){ st16(rs,zo+n*2048,u4{l0[0],l0[1],h0[0],h0[1]}); st16(rs,zo+n*2048+1024,u4{l1[0],l1[1],h1[0],h1[1]}); } }
      WAITV(0);
      if(arrive(smb+C_Z2,lane)==3u && lane==0) st4(rs,OFF_F2+(rt*16+j)*128,tag);
    }
  }
  const int ft0=ct<7?7*ct:47;
  bool adone=true; const u32 fao=OFF_FA+((lane<28?lane:0)*8+ct)*128;
  auto aload=[&](){ const u32 vo=(lane>>3)*4096+(lane&7)*16;
    #pragma unroll 1
    for(int i=0;i<10;i++){ setm0<ATL>(smb+i*1024); dma<0>(srdS,vo,OFF_AT+(u32)(80*ct+8*i)*4096+rtB*128); } };
  if(w>=4){
    if(hasc){
      __builtin_amdgcn_s_setprio(3);
      conv_wave(f1,rs,dch,cb,smb+C_KP+mi*4,lane,ksc,dsk);
      __builtin_amdgcn_s_setprio(0);
      if(arrive(smb+C_CV,lane)==(idx<24?2u:1u) && lane==0) st4(rs,OFF_FA+b*128,tag);
    }
    if(w==7){
      if(idx<28){
        pollw<16>(rs,OFF_FX+(lane&31)*128,tag);
        u4 sw[3][4];
        #pragma unroll
        for(int c=0;c<3;c++)
          #pragma unroll
          for(int i=0;i<4;i++) sw[c][i]=ld16(rs,OFF_XNT+((lane>>3)+8*i)*81920+(80*xc+idx+((c<2||idx<24)?28*c:0))*128+(lane&7)*16);
        #pragma unroll
        for(int c=0;c<3;c++){
          #pragma unroll
          for(int i=0;i<4;i++){ const int x=lane+64*i; ldsw128(smb+c*CVS+UOF+(x&3)*1040+((x>>2)+1)*16,sw[c][i]); }
          if(lane<4) ldsw128(smb+c*CVS+UOF+lane*1040,u4{0,0,0,0}); }
        signal(smb+C_FULL,lane);
        #pragma unroll 1
        for(int c=0;c<3;c++){
          #pragma unroll
          for(int i=0;i<4;i++){ sw[0][i]=sw[1][i]; sw[1][i]=sw[2][i]; }
          conv_win(smb+c*CVS,lane,sw[2]); }
        z1st();
        __builtin_amdgcn_s_setprio(3);
        #pragma unroll 1
        for(int c=0;c<(idx<24?3:2);c++){ ldswait<4>(smb+C_KP+c*4,1u); conv_help(smb+c*CVS,lane,sw[0]); signal(smb+C_KP+c*4,lane); if(c==0) f1();
          #pragma unroll
          for(int i=0;i<4;i++){ sw[0][i]=sw[1][i]; sw[1][i]=sw[2][i]; } } }
      __builtin_amdgcn_s_setprio(0);
      pollw<16>(rs,OFF_FW+(lane<27?lane:0)*128,tag); signal(smb+C_Z1R,lane);
    }
    ldswait<6>(smb+C_Z1R,5u);
    u4 wq[24];
    #pragma unroll
    for(int i=0;i<24;i++) wq[i]=ld16(rs,OFF_WOF+ct*(135*1024)+40960+mi*RING+((i==23&&mi==3)?22:i)*1024+lane*16);
    if(w<6){
      #pragma unroll 1
      for(u32 i=0;i<20480;i+=4096){ setm0<0>(cb+i); const u32 o=OFF_WOF+ct*(135*1024)+mi*20480+i; dma<0>(srdS,lane*16,o); dma<1024>(srdS,lane*16,o); dma<2048>(srdS,lane*16,o); dma<3072>(srdS,lane*16,o); } }
    if(w==6){ adone=__all(ld4(rs,fao)==tag); if(adone) aload(); }
    ldswait(smb+C_FREE,4u);
    #pragma unroll
    for(int i=0;i<24;i++) ldsw128(smb+RB+mi*RING+i*1024+lane*16,wq[i]);
    if(w<6) WAITV(0);
    signal(smb+C_WR,lane);
    if(!adone){ adone=__all(ld4(rs,fao)==tag); if(adone) aload(); }
    if(w==7 && b==255){
      #pragma unroll 1
      for(int sp=0;sp<SPIN;sp++){ const u32 o=OFF_FA+lane*128; u32 v0=ld4(rs,o),v1=ld4(rs,o+8192),v2=ld4(rs,o+16384),v3=ld4(rs,lane<32? o+24576 : OFF_FW+(lane-32)*128); CFENCE();
        if(__all(v0==tag&&v1==tag&&v2==tag&&v3==tag)) break; __builtin_amdgcn_s_sleep(4); }
      if(lane==0) st4(rs,OFF_EP,tag); }
  }
  int w2=w; asm volatile("" : "+s"(w2));
  if(w2<4){
    __builtin_amdgcn_s_setprio(2);
    int r=lane&15, q=lane>>4;
    const u32 rb=smb+RB+w*RING;
    u4 srdA=mksrd((const char*)p.wsp+(size_t)rtB*64*4096+w*1024-4096+256);
    const char* zb=p.ws+OFF_Z2F+(u32)(16*w*54+ft0+3)*1024;
    u32 voA[2]; { u32 lr=lane>>3,c=lane&7; voA[0]=lr*4096+((c^(lr>>1))<<4); voA[1]=voA[0]^64u; }
    u32 sofa[8];
    #pragma unroll
    for(int g=0;g<8;g++) sofa[g]=((g+2*w)&7)*32768+4096-(g&3)*1024;
    const u32 voB=lane*16;
    u32 la[2]; la[0]=rb+r*128+((q^(r>>1))<<4); la[1]=la[0]^64u;
    f4 acc[4][7]; u4 B[NB][7]; u4 A[2][4]; u2 z1[7];
    auto issueA=[&](auto sc,auto gc){ constexpr int s=sc.value,g=gc.value; if constexpr((g&3)==0) setm0<((s+1)%3)*8192+g*1024>(rb); dma<(g&3)*1024>(srdA,voA[g&1],sofa[g]);
      if constexpr(g==7){ u64 a=((u64)srdA[1]<<32|srdA[0])+128; srdA[0]=(u32)a; srdA[1]=(u32)(a>>32); } };

    u32 bsr,fv;
    auto fof=[&](){ const u32 l=voB>>4; return OFF_F1+((rtB>>1)*16+(((ft0>>1)+(l&3))>>1))*128; };
    #pragma unroll
    for(int m=0;m<4;m++){ A[0][m]=ldsr128(la[0]+m*2048+8192); A[1][m]=ldsr128(la[1]+m*2048+8192); }
    sfor<8>([&](auto g){ issueA(std::integral_constant<int,2>{},g); });
    waitA(A[0]); waitA(A[1]);
    sfor<8>([&](auto g){ issueA(std::integral_constant<int,3>{},g); });
    { const int hf=(ft0>>1)+q; pollw(rs,OFF_F2+((4*w+(lane&3))*16+(hf==0?13:(hf-1)>>1))*128,tag); }
    sfor<NB>([&](auto hh){ const char* s=zb+(u32)hh.value*(54*1024); sfor<7>([&](auto n){ gload<(n.value-3)*1024>(B[hh][n],voB,s); }); });
    auto redw=[&](auto nc){ constexpr int n=nc.value; *(LAS f4*)(rb+n*1024+voB)=acc[1][n]; *(LAS f4*)(rb+(7+n)*1024+voB)=acc[2][n]; *(LAS f4*)(rb+(14+n)*1024+voB)=acc[3][n]; };
    const char* z1b=p.ws+OFF_Z1T+(u32)((rtB*4+w)*27+(ft0>>1))*1024;
    sfor<16>([&](auto hc){
      constexpr int h=hc.value, S=h%NB, P=h&1, HZ=HZZ, cnt=g2cnt(h);
      waitB<cnt>(B[S]); waitA(A[P]);
      if constexpr(h==HZ){ tie1(fv); if(!__all(fv==tag)) pollw(rs,fof(),tag); }
      if constexpr(h+1<16 && h>0){
        #pragma unroll
        for(int m=0;m<4;m++) A[P^1][m]=ldsr128(la[(h+1)&1]+m*2048+((((h+1)>>1)+1)%3)*8192);
      }
      if constexpr(h==HZ-3) gload4<0>(fv,fof(),p.ws);
      const char* s=zb+(u32)(h+NB)*(54*1024);
      sfor<7>([&](auto n){
        if constexpr(h==0) mfma4z(acc[0][n],acc[1][n],acc[2][n],acc[3][n],B[S][n],A[P]); else mfma4(acc[0][n],acc[1][n],acc[2][n],acc[3][n],B[S][n],A[P]);
        if constexpr(h>=3 && h<=10 && (n.value&1)==0) issueA(std::integral_constant<int,(h-1)/2+3>{},std::integral_constant<int,((h-1)&1)*4+n.value/2>{});
        if constexpr(h+NB<16) gload_t<(n.value-3)*1024>(B[S][n],voB,s);
        if constexpr(h==HZ){ gload8<(n.value>>1)*1024>(z1[n],voB,z1b+((n.value&1)? ((ft0&1)?1024:8) : (ft0&1)*8));
          if constexpr(n.value==6) { gload2(bsr,(voB>>3)&30u,(const char*)(p.bsp+(rtB*4+w)*16)); } }
        if constexpr(h==13 && n.value==0) signal(smb+C_Z1R,lane);
        if constexpr(h==15){ SCHED(); if constexpr(n.value>=2) redw(std::integral_constant<int,n.value-2>{}); SCHED(); }
      });
    });
    asm volatile("s_nop 15\n\ts_nop 3" ::: "memory");
    SCHED();
    lane=__builtin_amdgcn_mbcnt_hi(~0u,__builtin_amdgcn_mbcnt_lo(~0u,0u)); asm volatile("" : "+v"(lane)); r=lane&15; q=lane>>4;
    redw(std::integral_constant<int,5>{}); redw(std::integral_constant<int,6>{});
    signal(smb+C_RED,lane);
    ldswait(smb+C_RED,4u);
    #pragma unroll
    for(int m=1;m<4;m++){ u32 src=smb+RB+((w-m)&3)*RING+(m-1)*7168+lane*16;
      #pragma unroll
      for(int n=0;n<7;n++) acc[0][n]+=*(LAS f4*)(src+n*1024); }
    asm volatile("s_waitcnt vmcnt(0) lgkmcnt(0)" : "+v"(z1[0]),"+v"(z1[1]),"+v"(z1[2]),"+v"(z1[3]),"+v"(z1[4]),"+v"(z1[5]),"+v"(z1[6]),"+v"(bsr) :: "memory");
    const float bs=bflo(bsr);
    signal(smb+C_FREE,lane);
    u2 o[7];
    #pragma unroll
    for(int n=0;n<7;n++)
      #pragma unroll
      for(int e=0;e<2;e++)o[n][e]=pkbf2(unpk(z1[n][e])*(f2{acc[0][n][2*e],acc[0][n][2*e+1]}+bs));
    const u32 ho=OFF_HF+(u32)((rtB*4+w)*54+ft0)*512+(q>>1)*256+r*16;
    #pragma unroll
    for(int n=0;n<6;n+=2){ auto sx=__builtin_amdgcn_permlane16_swap(o[n][0],o[n+1][0],false,false); auto sy=__builtin_amdgcn_permlane16_swap(o[n][1],o[n+1][1],false,false);
      if(ct<7||n>=2) st16(rs,ho+(n+(q&1))*512,u4{sx[0],sy[0],sx[1],sy[1]}); }
    st8(rs,ho+6*512+(q&1)*8,o[6]);
    WAITV(0);
    if(arrive(smb+C_H,lane)==3u && lane==0) st4(rs,OFF_FH+(rtB*8+ct)*128,tag);
    __builtin_amdgcn_s_setprio(0);
  }
  { const int r=lane&15, q=lane>>4, m=w&3, kh=w>>2;
    u2 xv[5]={};
    if(kh){
      #pragma unroll
      for(int n=0;n<5;n++) xv[n]=*(const u2*)(p.x+(rtB*64+m*16+r)*640+80*ct+16*n+4*q);
    }
    f4 acc[5]={}; u4 W[3][5];
    const u32 wl2=smb+lane*16+RB-40960+kh*14*5120, wl0=kh? wl2 : smb+lane*16, wl1=kh? wl2 : smb+lane*16+CVS-20480;
    #define WAD(k,n) (((k)<4? wl0 : (k)<8? wl1 : wl2)+((k)*5+(n))*1024)
    f4 ax[5]={};
    auto addax=[&](){
      #pragma unroll
      for(int n=0;n<5;n++){ u2 av=ldstr(smb+ATL+(16*n+4*q+(r>>2))*128+(16*m+4*(r&3))*2); ax[n]=f4{bflo(av[0]),bfhi(av[0]),bflo(av[1]),bfhi(av[1])}+f4{bflo(xv[n][0]),bfhi(xv[n][0]),bflo(xv[n][1]),bfhi(xv[n][1])}; } };
    if(w==6 && adone){ WAITV(0); signal(smb+C_AT,lane); }
    u32 oo[3],to[3]; const u32 tb=smb+CVS+m*3072;
    #pragma unroll
    for(int i=0;i<3;i++){ const int it=lane+64*i, rr=(it*205)>>11, cc=it-rr*10; oo[i]=opq((rtB*64+m*16+rr)*1280+ct*160+cc*16); to[i]=opq(tb+rr*176+cc*16); }
    if(m==0){ ldswait(smb+C_H,4u); pollw(rs,OFF_FH+(rtB*8+kh*4+(lane&3))*128,tag); signal(smb+C_TAG+kh*4,lane); } else ldswait(smb+C_TAG+kh*4,1u);
    u4 hf[14];
    const u32 ho=OFF_HF+(u32)((rtB*4+m)*27+kh*14)*1024+lane*16;
    #pragma unroll
    for(int k=0;k<HPF;k++) hf[k]=ld16(rs,ho+k*1024);
    const bool early=kh && uni(ldsv(smb+C_AT))!=0u;
    CFENCE();
    if(early) addax();
    ldswait(smb+C_WR,4u);
    const u32 wl3=kh? wl2-5120 : wl2;
    #pragma unroll
    for(int n=0;n<10;n++) W[n/5][n%5]=ldsr128(WAD(n/5,n%5));
    #pragma unroll
    for(int k=0;k<13;k++){
      if(k+HPF<14) hf[k+HPF]=ld16(rs,ho+((k+HPF==13&&kh)?12:k+HPF)*1024);
      SCHED();
      #pragma unroll
      for(int n=0;n<5;n++){ acc[n]=mfma16(W[k%3][n],hf[k],acc[n]); SCHED();
        if(k<11) W[(k+2)%3][n]=ldsr128(WAD(k+2,n)); else if(k==11) W[1][n]=ldsr128(wl3+(65+n)*1024);
        SCHED(); }
      if(k==5 && !kh) signal(smb+C_G1,lane);
    }
    const u32 pb=smb+m*5120+lane*16;
    if(!kh){
      #pragma unroll
      for(int n=0;n<5;n++) acc[n]=mfma16(W[1][n],hf[13],acc[n]);
      ldswait(smb+C_G1,4u);
      #pragma unroll
      for(int n=0;n<5;n++) *(LAS f4*)(pb+n*1024)=acc[n];
      signal(smb+C_P+m*4,lane);
    } else {
      if(w==6 && !adone){ pollw(rs,fao,tag); aload(); WAITV(0); signal(smb+C_AT,lane); }
      if(!early){ ldswait(smb+C_AT,1u); addax(); }
      ldswait(smb+C_P+m*4,1u);
      #pragma unroll
      for(int n=0;n<5;n++){ const f4 o=(acc[n]+ax[n])+*(LAS f4*)(pb+n*1024); *(LAS u2*)(tb+r*176+n*32+q*8)=u2{pkbf(o[0],o[1]),pkbf(o[2],o[3])}; }
      CFENCE();
      #pragma unroll
      for(int i=0;i<3;i++) if(i<2||lane<32) __builtin_amdgcn_raw_buffer_store_b128(ldsr128(to[i]),__builtin_amdgcn_make_buffer_rsrc(p.out,0,2048*1280,0x00020000),oo[i],0,16);
    }
  }
}
extern "C" void kernel_launch(void* const* d_in,const int* in_sizes,int n_in,void* d_out,int out_size,void* d_ws,size_t ws_size,hipStream_t stream){
  Params p;
  p.x=(const u16*)d_in[0]; p.lar=(const u16*)d_in[1]; p.aim=(const u16*)d_in[2]; p.cre=(const u16*)d_in[3]; p.cim=(const u16*)d_in[4];
  p.ldt=(const u16*)d_in[5]; p.dsk=(const u16*)d_in[6]; p.win=(const u16*)d_in[8]; p.wsp=(const u16*)d_in[9]; p.bsp=(const u16*)d_in[10]; p.wout=(const u16*)d_in[11];
  p.out=(u16*)d_out; p.ws=(char*)d_ws;
  (void)hipFuncSetAttribute((const void*)fused,hipFuncAttributeMaxDynamicSharedMemorySize,LDS_TOTAL);
  fused<<<256,512,LDS_TOTAL,stream>>>(p);
}
```
